```python
import torch

def _softmax(x, axis=-1):
    return torch.softmax(x, dim=axis)

def _gelu(x):
    return torch.nn.functional.gelu(x, approximate='tanh')

def _silu(x):
    return torch.nn.functional.silu(x)

def _sigmoid(x):
    return torch.sigmoid(x)

def _layernorm(x, axis=-1, eps=1e-05):
    m = x.mean(dim=axis, keepdim=True)
    v = x.var(dim=axis, keepdim=True, unbiased=False)
    return (x - m) / torch.sqrt(v + eps)

def _scatter_add(tgt, idx, src, axis=0):
    return tgt.clone().index_add_(axis, idx, src)

def _segment_sum(src, seg, n):
    out = torch.zeros((n,) + src.shape[1:], dtype=src.dtype)
    return out.index_add_(0, seg, src)

def _pad(x, pw):
    flat = tuple((v for pair in reversed(pw) for v in pair))
    return torch.nn.functional.pad(x, flat)
kernel_name = 'adaLN_cond__gqa_block_cached__mixer_gmlp__prompt'

def setup_inputs():

    _w = lambda a: (lambda t: t.to(torch.bfloat16) if t.is_floating_point() else t)(torch.as_tensor(a))

    def _A_setup():
        B, Sq, D = (1, 2048, 576)
        Hq, Hkv, Sk, Dh = (32, 16, 2048, 128)
        assert Hq % Hkv == 0, (Hq, Hkv)
        return {'x': _w((lambda *s: torch.randn(*s, dtype=torch.bfloat16) * 0.05)(B, Sq, D)), 'k_cache': _w((lambda *s: torch.randn(*s, dtype=torch.bfloat16) * 3.2)(B, Hkv, Sk, Dh)), 'v_cache': _w((lambda *s: torch.randn(*s, dtype=torch.bfloat16) * 0.4)(B, Hkv, Sk, Dh)), 'wq': _w((lambda *s: torch.randn(*s, dtype=torch.bfloat16) * 3.2)(D, Hq * Dh) * D ** (-0.5)), 'wo': _w((lambda *s: torch.randn(*s, dtype=torch.bfloat16) * 0.4)(Hq * Dh, D) * (Hq * Dh) ** (-0.5))}
    _A_in = _A_setup()

    def _B_setup():
        B, S, D, F = (1, 2048, 576, 4608)
        Fh = F // 2
        return {'x': _w((lambda *s: torch.randn(*s, dtype=torch.bfloat16) * 0.05)(B, S, D)), 'w_in': _w((lambda *s: torch.randn(*s, dtype=torch.bfloat16) * 1.6)(D, 2 * Fh)), 'w_sp': _w(0.01 * (lambda *s: torch.randn(*s, dtype=torch.bfloat16) * 1.6)(S, S)), 'b_sp': _w(1.0 + 0.01 * (lambda *s: torch.randn(*s, dtype=torch.bfloat16) * 0.05)(S)), 'w_out': _w((lambda *s: torch.randn(*s, dtype=torch.bfloat16) * 0.05)(Fh, D))}
    _B_in = _B_setup()
    return {'A_x': _A_in['x'], 'A_k_cache': _A_in['k_cache'], 'A_v_cache': _A_in['v_cache'], 'A_wq': _A_in['wq'], 'A_wo': _A_in['wo'], 'B_x': _B_in['x'], 'B_w_in': _B_in['w_in'], 'B_w_sp': _B_in['w_sp'], 'B_b_sp': _B_in['b_sp'], 'B_w_out': _B_in['w_out'], 'c': _w((lambda *s: torch.randn(*s, dtype=torch.bfloat16) * 0.2)(1, 576) * 8.0), 'w_mod': _w((lambda *s: torch.randn(*s, dtype=torch.bfloat16) * 0.2)(576, 6 * 576) * 576 ** (-0.5) * 8.0)}

def reference(A_x, A_k_cache, A_v_cache, A_wq, A_wo, B_x, B_w_in, B_w_sp, B_b_sp, B_w_out, c, w_mod):

    def _A_ref(x, k_cache, v_cache, wq, wo):
        B, Sq, D = x.shape
        Hq, Dh = (32, 128)
        Hkv = k_cache.shape[1]
        rep = Hq // Hkv
        q = (lambda x, *d: x.permute(*d))((x @ wq).reshape(B, Sq, Hq, Dh), 0, 2, 1, 3)
        k = (lambda x, n, axis: torch.repeat_interleave(x, n, dim=axis))(k_cache, rep, axis=1)
        v = (lambda x, n, axis: torch.repeat_interleave(x, n, dim=axis))(v_cache, rep, axis=1)
        scores = torch.einsum('bhqd,bhkd->bhqk', q, k) * Dh ** (-0.5)
        p = _softmax(scores, axis=-1)
        o = torch.einsum('bhqk,bhkd->bhqd', p, v)
        return (lambda x, *d: x.permute(*d))(o, 0, 2, 1, 3).reshape(B, Sq, Hq * Dh) @ wo

    def _B_ref(x, w_in, w_sp, b_sp, w_out):
        B, S, D = x.shape
        F = w_in.shape[-1]
        Fh = F // 2
        z = _gelu(torch.einsum('bsd,df->bsf', x, w_in))
        z1 = z[..., :Fh].clone()
        z2 = _layernorm(z[..., Fh:], axis=-1)
        g = torch.einsum('ts,bsf->btf', w_sp, z2) + b_sp[None, :, None]
        return torch.einsum('bsf,fd->bsd', z1 * g, w_out)
    x = A_x
    mod = (_silu(c) @ w_mod).reshape(c.shape[0], 6, 1, -1)
    sa, ca, ga, sb, cb, gb = (mod[:, 0].clone(), mod[:, 1].clone(), mod[:, 2].clone(), mod[:, 3].clone(), mod[:, 4].clone(), mod[:, 5].clone())
    h = _layernorm(x, axis=-1) * (1 + ca) + sa
    x = x + ga * _A_ref(h, A_k_cache, A_v_cache, A_wq, A_wo)
    h = _layernorm(x, axis=-1) * (1 + cb) + sb
    x = x + gb * _B_ref(h, B_w_in, B_w_sp, B_b_sp, B_w_out)
    return x
```

```cpp
#include <hip/hip_runtime.h>
#include <hip/hip_bf16.h>
#include <cstdio>

typedef unsigned short u16;
using bf16x8 = __attribute__((ext_vector_type(8))) short;
using f32x4 = __attribute__((ext_vector_type(4))) float;

#define MIMIC 1

__device__ __forceinline__ float bf2f(u16 b) { return __uint_as_float(((unsigned)b) << 16); }
__device__ __forceinline__ u16 f2bf(float f) { unsigned u = __float_as_uint(f); u += 0x7FFFu + ((u >> 16) & 1u); return (u16)(u >> 16); }
__device__ __forceinline__ float rbf(float f) { return bf2f(f2bf(f)); }

constexpr int S = 2048, D = 576, HQ = 32, HKV = 16, DH = 128, F = 4608, FH = 2304;

__global__ void k_mod(const u16* c, const u16* wmod, float* mod) {
  int n = blockIdx.x * blockDim.x + threadIdx.x;
  if (n >= 6 * D) return;
  float acc = 0.f;
  for (int k = 0; k < D; ++k) { float cv = bf2f(c[k]); float s = cv / (1.f + __expf(-cv)); s = rbf(s); acc += s * bf2f(wmod[(size_t)k * 6 * D + n]); }
  mod[n] = rbf(acc);
}

__global__ void k_ln(const u16* x, int ldx, int ncols, const float* scale, const float* shift, u16* out, int ldo) {
  int row = blockIdx.x; int t = threadIdx.x;
  __shared__ float red[256];
  float s = 0.f; for (int i = t; i < ncols; i += 256) s += bf2f(x[(size_t)row * ldx + i]);
  red[t] = s; __syncthreads(); for (int o = 128; o > 0; o >>= 1) { if (t < o) red[t] += red[t + o]; __syncthreads(); }
  float mean = red[0] / ncols; __syncthreads();
  float q = 0.f; for (int i = t; i < ncols; i += 256) { float d = bf2f(x[(size_t)row * ldx + i]) - mean; q += d * d; }
  red[t] = q; __syncthreads(); for (int o = 128; o > 0; o >>= 1) { if (t < o) red[t] += red[t + o]; __syncthreads(); }
  float rstd = rsqrtf(red[0] / ncols + 1e-5f);
  for (int i = t; i < ncols; i += 256) { float v = (bf2f(x[(size_t)row * ldx + i]) - mean) * rstd; if (scale) v = v * (1.f + scale[i]) + shift[i]; out[(size_t)row * ldo + i] = f2bf(v); }
}

template <int EPI>
__global__ __launch_bounds__(256) void k_gemm(const u16* A, int lda, const u16* B, int ldb, u16* C, int ldc, int K, const u16* res, int ldr, const float* gate, const u16* bsp) {
  __shared__ __attribute__((aligned(16))) u16 As[64][40];
  __shared__ __attribute__((aligned(16))) u16 Bs[64][40];
  int bm = blockIdx.y * 64, bn = blockIdx.x * 64;
  int t = threadIdx.x, w = t >> 6, lane = t & 63, wr = w >> 1, wc = w & 1, fr = lane & 15, fq = lane >> 4;
  f32x4 acc[2][2] = {};
  for (int k0 = 0; k0 < K; k0 += 32) {
    { int r = t >> 2, c = (t & 3) * 8; *(bf16x8*)&As[r][c] = *(const bf16x8*)&A[(size_t)(bm + r) * lda + k0 + c]; }
    { int kk = t >> 3, n = (t & 7) * 8; bf16x8 v = *(const bf16x8*)&B[(size_t)(k0 + kk) * ldb + bn + n]; for (int j = 0; j < 8; ++j) Bs[n + j][kk] = (u16)v[j]; }
    __syncthreads();
    bf16x8 a[2], b[2];
    for (int m = 0; m < 2; ++m) a[m] = *(const bf16x8*)&As[wr * 32 + m * 16 + fr][fq * 8];
    for (int n = 0; n < 2; ++n) b[n] = *(const bf16x8*)&Bs[wc * 32 + n * 16 + fr][fq * 8];
    for (int m = 0; m < 2; ++m) for (int n = 0; n < 2; ++n) acc[m][n] = __builtin_amdgcn_mfma_f32_16x16x32_bf16(a[m], b[n], acc[m][n], 0, 0, 0);
    __syncthreads();
  }
  for (int m = 0; m < 2; ++m) for (int n = 0; n < 2; ++n) for (int j = 0; j < 4; ++j) {
    int row = bm + wr * 32 + m * 16 + fq * 4 + j, col = bn + wc * 32 + n * 16 + fr; float v = acc[m][n][j];
    if (EPI == 1) { float u = 0.7978845608028654f * (v + 0.044715f * v * v * v); float th = 1.f - 2.f / (1.f + __expf(2.f * u)); v = 0.5f * v * (1.f + th); }
    if (EPI == 2) { v = bf2f(res[(size_t)row * ldr + col]) + gate[col] * v; }
    if (EPI == 3) { v = bf2f(res[(size_t)row * ldr + col]) * (v + bf2f(bsp[row])); }
    C[(size_t)row * ldc + col] = f2bf(v);
  }
}

__global__ __launch_bounds__(256) void k_attn(const u16* Q, const u16* Kc, const u16* Vc, u16* O) {
  int w = threadIdx.x >> 6, lane = threadIdx.x & 63;
  int row = blockIdx.x * 4 + w, h = blockIdx.y, kvh = h >> 1;
  __shared__ float qs[4][DH]; __shared__ float ps[4][S];
  for (int d = lane; d < DH; d += 64) qs[w][d] = bf2f(Q[(size_t)row * (HQ * DH) + h * DH + d]);
  __syncthreads();
  float mx = -1e30f;
  for (int k = lane; k < S; k += 64) { const u16* kr = Kc + ((size_t)kvh * S + k) * DH; float s = 0.f;
    for (int d = 0; d < DH; d += 8) { bf16x8 kv = *(const bf16x8*)&kr[d]; for (int j = 0; j < 8; ++j) s += qs[w][d + j] * bf2f((u16)kv[j]); }
    s = rbf(rbf(s) * 0.08838834764831845f);
    ps[w][k] = s; mx = fmaxf(mx, s); }
  for (int o = 32; o > 0; o >>= 1) mx = fmaxf(mx, __shfl_xor(mx, o));
  float l = 0.f;
  for (int k = lane; k < S; k += 64) { float p = __expf(ps[w][k] - mx); ps[w][k] = p; l += p; }
  for (int o = 32; o > 0; o >>= 1) l += __shfl_xor(l, o);
  __syncthreads();
  float il = 1.f / l;
  float o0 = 0.f, o1 = 0.f;
  for (int k = 0; k < S; ++k) { float p = rbf(ps[w][k] * il); const u16* vr = Vc + ((size_t)kvh * S + k) * DH; o0 += p * bf2f(vr[lane]); o1 += p * bf2f(vr[lane + 64]); }
  O[(size_t)row * (HQ * DH) + h * DH + lane] = f2bf(o0); O[(size_t)row * (HQ * DH) + h * DH + lane + 64] = f2bf(o1);
}

extern "C" void kernel_launch(void* const* d_in, const int* in_sizes, int n_in, void* d_out, int out_size, void* d_ws, size_t ws_size, hipStream_t stream) {
  const u16* x = (const u16*)d_in[0]; const u16* kc = (const u16*)d_in[1]; const u16* vc = (const u16*)d_in[2];
  const u16* wq = (const u16*)d_in[3]; const u16* wo = (const u16*)d_in[4]; const u16* w_in = (const u16*)d_in[6];
  const u16* w_sp = (const u16*)d_in[7]; const u16* b_sp = (const u16*)d_in[8]; const u16* w_out = (const u16*)d_in[9];
  const u16* c = (const u16*)d_in[10]; const u16* w_mod = (const u16*)d_in[11];
  char* ws = (char*)d_ws;
  float* mod = (float*)ws; ws += 65536;
  u16* h = (u16*)ws; ws += (size_t)S * D * 2;
  u16* q = (u16*)ws; ws += (size_t)S * 4096 * 2;
  u16* o = (u16*)ws; ws += (size_t)S * 4096 * 2;
  u16* x1 = (u16*)ws; ws += (size_t)S * D * 2;
  u16* z = (u16*)ws; ws += (size_t)S * F * 2;
  u16* z2 = (u16*)ws; ws += (size_t)S * FH * 2;
  u16* u = (u16*)ws; ws += (size_t)S * FH * 2;
  u16* out = (u16*)d_out;
  const float *sa = mod, *ca = mod + D, *ga = mod + 2 * D, *sb = mod + 3 * D, *cb = mod + 4 * D, *gb = mod + 5 * D;
  k_mod<<<(6 * D + 63) / 64, 64, 0, stream>>>(c, w_mod, mod);
  k_ln<<<S, 256, 0, stream>>>(x, D, D, ca, sa, h, D);
  k_gemm<0><<<dim3(4096 / 64, S / 64), 256, 0, stream>>>(h, D, wq, 4096, q, 4096, D, nullptr, 0, nullptr, nullptr);
  k_attn<<<dim3(S / 4, HQ), 256, 0, stream>>>(q, kc, vc, o);
  k_gemm<2><<<dim3(D / 64, S / 64), 256, 0, stream>>>(o, 4096, wo, D, x1, D, 4096, x, D, ga, nullptr);
  k_ln<<<S, 256, 0, stream>>>(x1, D, D, cb, sb, h, D);
  k_gemm<1><<<dim3(F / 64, S / 64), 256, 0, stream>>>(h, D, w_in, F, z, F, D, nullptr, 0, nullptr, nullptr);
  k_ln<<<S, 256, 0, stream>>>(z + FH, F, FH, nullptr, nullptr, z2, FH);
  k_gemm<3><<<dim3(FH / 64, S / 64), 256, 0, stream>>>(w_sp, S, z2, FH, u, FH, S, z, F, nullptr, b_sp);
  k_gemm<2><<<dim3(D / 64, S / 64), 256, 0, stream>>>(u, FH, w_out, D, out, D, FH, x1, D, gb, nullptr);
}
```

```cpp
#include <hip/hip_runtime.h>
typedef unsigned short u16;
using u32x4 = __attribute__((ext_vector_type(4))) unsigned;
__device__ __forceinline__ float blo(unsigned w) { return __uint_as_float(w << 16); }
__device__ __forceinline__ float bhi(unsigned w) { return __uint_as_float(w & 0xffff0000u); }

extern "C" __global__ __launch_bounds__(256, 1) __attribute__((aligned(4096))) void k_main(char* ws, const u16* x, const u16* wq, const u16* kc, const u16* vc, const u16* wo, const u16* w_in, const u16* w_sp, const u16* b_sp,
                                                            const u16* w_out, u16* out, const u16* c, const u16* w_mod) {
  asm volatile(
"s_load_dwordx2 s[84:85],%1,96\n"
"s_load_dwordx8 s[24:31],%1,64\n"
"s_load_dwordx16 s[8:23],%1,0\n"
"s_getpc_b64 s[56:57]\n"
"s_and_b32 s57,s57,0xffff\n"
"s_mov_b32 s58,8192\n"
"s_mov_b32 s59,0x20000\n"
"v_lshlrev_b32 v13,7,%0\n"
"buffer_load_dword a231,v13,s[56:59],0 offen\n"
"v_and_b32 v8,63,%0\n"
"v_lshrrev_b32 v10,6,%0\n"
"v_lshlrev_b32 v9,4,v8\n"
"v_readfirstlane_b32 s37,v10\n"
"v_lshlrev_b32 v10,2,v8\n"
"s_bfe_u32 s36,%2,0x20001\n"
"s_lshr_b32 s35,%2,6\n"
"s_bfe_u32 s34,%2,0x30003\n"
"s_and_b32 s39,%2,1\n"
"s_lshl_b32 s39,s39,3\n"
"s_add_u32 s34,s34,s39\n"
"s_lshl_b32 s39,%2,2\n"
"s_add_u32 s39,s39,s37\n"
"v_cmp_ne_u32 vcc,0,v8\n"
"v_bfrev_b32 v11,1\n"
"s_nop 0\n"
"v_cndmask_b32 v11,0,v11,vcc\n"
"s_lshr_b32 s61,s39,5\n"
"s_bfe_u32 s68,s39,0x20002\n"
"s_lshl2_add_u32 s61,s61,s68\n"
"s_and_b32 s68,s39,16\n"
"s_lshl2_add_u32 s61,s68,s61\n"
"s_cmp_lt_u32 s61,54\n"
"s_cselect_b32 s75,331776,0\n"
"s_cmp_ge_u32 s61,18\n"
"s_cselect_b32 s60,1,0\n"
"s_cmp_ge_u32 s61,36\n"
"s_addc_u32 s60,s60,0\n"
"s_mul_i32 s68,s60,18\n"
"s_sub_u32 s61,s61,s68\n"
"s_mov_b32 s74,0\n"
"s_mul_i32 s69,s60,192\n"
"s_add_u32 s74,s74,s69\n"
"s_mul_i32 s69,s37,48\n"
"s_add_u32 s74,s74,s69\n"
"s_mov_b32 s68,0\n"
"s_mul_i32 s69,s74,6912\n"
"s_add_u32 s68,s68,s69\n"
"s_mul_i32 s69,s61,128\n"
"s_add_u32 s68,s68,s69\n"
"s_mov_b32 s50,s75\n"
"s_mov_b32 s51,0x20000\n"
"s_lshl_b32 s74,s74,1\n"
"s_mov_b32 s54,96\n"
"s_mov_b32 s55,0x20000\n"
"v_lshrrev_b32 v234,3,v8\n"
"v_lshlrev_b32 v237,1,v234\n"
"v_mul_u32_u24 v236,6912,v234\n"
"v_and_b32 v235,7,v8\n"
"v_lshl_add_u32 v192,v235,4,v236\n"
"v_add_u32 v193,55296,v192\n"
"v_add_u32 v194,110592,v192\n"
"v_add_u32 v195,165888,v192\n"
"v_add_u32 v196,221184,v192\n"
"v_add_u32 v197,276480,v192\n"
"s_waitcnt lgkmcnt(0)\n"
"s_add_u32 s48,s84,s68\n"
"s_addc_u32 s49,s85,0\n"
"s_and_b32 s49,s49,0xffff\n"
"s_add_u32 s52,s30,s74\n"
"s_addc_u32 s53,s31,0\n"
"s_and_b32 s53,s53,0xffff\n"
"buffer_load_ushort v184,v237,s[52:55],0 offen offset:0\n"
"buffer_load_ushort v185,v237,s[52:55],0 offen offset:16\n"
"buffer_load_ushort v186,v237,s[52:55],0 offen offset:32\n"
"buffer_load_ushort v187,v237,s[52:55],0 offen offset:48\n"
"buffer_load_ushort v188,v237,s[52:55],0 offen offset:64\n"
"buffer_load_ushort v189,v237,s[52:55],0 offen offset:80\n"
"buffer_load_dwordx4 v[160:163],v192,s[48:51],0 offen\n"
"buffer_load_dwordx4 v[164:167],v193,s[48:51],0 offen\n"
"buffer_load_dwordx4 v[168:171],v194,s[48:51],0 offen\n"
"buffer_load_dwordx4 v[172:175],v195,s[48:51],0 offen\n"
"buffer_load_dwordx4 v[176:179],v196,s[48:51],0 offen\n"
"buffer_load_dwordx4 v[180:183],v197,s[48:51],0 offen\n"
"s_load_dword s38,s[8:9],262144\n"
"s_add_u32 s88,s8,0\n"
"s_addc_u32 s89,s9,0\n"
"s_and_b32 s89,s89,0xffff\n"
"s_mov_b32 s90,0x10000000\n"
"s_mov_b32 s91,0x20000\n"
"s_mov_b32 s68,0\n"
"s_mul_i32 s69,s34,147456\n"
"s_add_u32 s68,s68,s69\n"
"s_mul_i32 s69,s37,36864\n"
"s_add_u32 s68,s68,s69\n"
"s_add_u32 s40,s10,s68\n"
"s_addc_u32 s41,s11,0\n"
"s_and_b32 s41,s41,0xffff\n"
"s_mov_b32 s42,36864\n"
"s_mov_b32 s43,0x20000\n"
"s_mov_b32 s68,0\n"
"s_mul_i32 s69,s36,9216\n"
"s_add_u32 s68,s68,s69\n"
"s_mul_i32 s69,s35,2304\n"
"s_add_u32 s68,s68,s69\n"
"v_lshrrev_b32 v238,5,v8\n"
"v_and_b32 v239,31,v8\n"
"v_mul_u32_u24 v236,1152,v238\n"
"v_mad_u32_u24 v236,v239,12,v236\n"
"buffer_load_dwordx3 v[112:114],v236,s[40:43],s68 offen offset:0\n"
"buffer_load_dwordx3 v[116:118],v236,s[40:43],s68 offen offset:384\n"
"buffer_load_dwordx3 v[120:122],v236,s[40:43],s68 offen offset:768\n"
"v_lshlrev_b32 v140,4,v239\n"
"v_sub_u32 v236,0,v239\n"
"v_and_b32 v236,0x80000000,v236\n"
"v_lshl_or_b32 v141,v238,4,v236\n"
"s_lshl_b32 s66,s37,12\n"
"s_and_b32 s79,s34,7\n"
"s_lshl1_add_u32 s79,s35,s79\n"
"s_sub_u32 s68,s79,9\n"
"s_min_u32 s79,s79,s68\n"
"v_mul_u32_u24 v234,1152,v234\n"
"v_lshrrev_b32 v238,4,v8\n"
"v_xor_b32 v235,v235,v238\n"
"v_lshl_add_u32 v208,v235,4,v234\n"
"v_xor_b32 v209,64,v208\n"
"v_add_u32 v209,8192,v209\n"
"v_add_u32 v210,16384,v208\n"
"v_add_u32 v211,16384,v209\n"
"s_add_u32 m0,s66,65536\n"
"s_lshl_b32 s77,s79,7\n"
"buffer_load_dwordx4 v208,s[40:43],s77 offen offset:0 lds\n"
"buffer_load_dwordx4 v209,s[40:43],s77 offen offset:1024 lds\n"
"buffer_load_dwordx4 v210,s[40:43],s77 offen offset:2048 lds\n"
"buffer_load_dwordx4 v211,s[40:43],s77 offen offset:3072 lds\n"
"s_add_u32 s68,s66,65536\n"
"v_and_b32 v234,31,v8\n"
"v_lshlrev_b32 v234,7,v234\n"
"v_bfe_u32 v235,v8,1,3\n"
"v_lshrrev_b32 v238,5,v8\n"
"v_xor_b32 v235,v235,v238\n"
"v_lshl_add_u32 v234,v235,4,v234\n"
"v_xor_b32 v235,0,v234\n"
"v_add_u32 v243,s68,v235\n"
"v_add_u32 v212,65536,v243\n"
"v_xor_b32 v235,32,v234\n"
"v_add_u32 v244,s68,v235\n"
"v_add_u32 v213,65536,v244\n"
"v_xor_b32 v235,64,v234\n"
"v_add_u32 v245,s68,v235\n"
"v_add_u32 v214,65536,v245\n"
"v_xor_b32 v235,96,v234\n"
"v_add_u32 v255,s68,v235\n"
"v_add_u32 v215,65536,v255\n"
"s_mov_b32 s76,98304\n"
"s_mul_i32 s69,s34,2048\n"
"s_add_u32 s76,s76,s69\n"
"s_mul_i32 s69,s37,512\n"
"s_add_u32 s76,s76,s69\n"
"s_mov_b32 s87,s76\n"
"s_mul_i32 s69,s36,128\n"
"s_add_u32 s87,s87,s69\n"
"s_mul_i32 s69,s35,32\n"
"s_add_u32 s87,s87,s69\n"
"s_mov_b32 s68,0\n"
"s_mul_i32 s69,s36,2048\n"
"s_add_u32 s68,s68,s69\n"
"s_mul_i32 s69,s35,512\n"
"s_add_u32 s68,s68,s69\n"
"s_add_u32 s44,s12,s68\n"
"s_addc_u32 s45,s13,0\n"
"s_and_b32 s45,s45,0xffff\n"
"s_mov_b32 s46,4710912\n"
"s_mov_b32 s47,0x20000\n"
"s_lshl_b32 s65,s37,11\n"
"s_lshl_b32 s68,s37,17\n"
"v_bfe_u32 v234,v8,2,2\n"
"v_lshrrev_b32 v235,4,v8\n"
"v_lshlrev_b32 v235,6,v235\n"
"v_lshl_add_u32 v234,v234,13,v235\n"
"v_and_b32 v235,3,v8\n"
"v_lshl_add_u32 v234,v235,4,v234\n"
"v_add_u32 v230,s68,v234\n"
"v_add_u32 v231,31744,v230\n"
"v_add_u32 v216,63488,v230\n"
"v_add_u32 v217,95232,v230\n"
"s_mov_b32 s72,0x3f803f80\n"
"s_mov_b32 s73,0xffff0000\n"
"v_mov_b32 v220,0\n"
"v_mov_b32 v221,0\n"
"v_mov_b32 v222,0\n"
"v_mov_b32 v223,0\n"
"v_mov_b32 v200,0\n"
"v_mov_b32 v201,0\n"
"v_mov_b32 v202,0\n"
"v_mov_b32 v203,0\n"
"v_mov_b32 v204,0\n"
"v_mov_b32 v205,0\n"
"v_mov_b32 v206,0\n"
"v_mov_b32 v207,0\n"
"s_sub_u32 s68,s61,9\n"
"s_lshl_b32 s69,s60,8\n"
"s_or_b32 s68,s68,s69\n"
"s_cmp_lt_u32 s68,9\n"
"s_cselect_b32 s63,1.0,0\n"
"s_mov_b32 s71,65536\n"
"s_mul_i32 s69,s60,9216\n"
"s_add_u32 s71,s71,s69\n"
"s_mul_i32 s69,s61,512\n"
"s_add_u32 s71,s71,s69\n"
"s_cmp_eq_u32 s75,0\n"
"s_cselect_b32 s68,1,s37\n"
"s_mov_b32 s69,0x7ffffff0\n"
"s_cmp_eq_u32 s68,0\n"
"s_cselect_b32 s71,s71,s69\n"
"v_and_b32 v234,7,v8\n"
"v_lshrrev_b32 v235,4,v8\n"
"v_lshlrev_b32 v235,3,v235\n"
"v_lshl_add_u32 v234,v234,5,v235\n"
"s_lshl_b32 s68,s37,8\n"
"s_add_u32 s68,s68,152576\n"
"v_add_u32 v238,s68,v234\n"
"v_add_u32 v239,152576,v10\n"
"v_lshlrev_b32 v237,3,v8\n"
"s_mov_b64 exec,0\n"
"s_cmp_eq_u32 s37,1\n"
"s_cbranch_scc1 L_hra_%=\n"
"s_cmp_eq_u32 s37,2\n"
"s_cbranch_scc1 L_hra_%=\n"
"s_cmp_eq_u32 s37,3\n"
"s_cbranch_scc1 L_hra_%=\n"
"s_branch L_pga16_%=\n"
"L_hra_%=:\n"
"s_mov_b64 exec,-1\n"
"s_waitcnt vmcnt(13)\n"
"v_lshlrev_b32 v184,16,v184\n"
"v_lshlrev_b32 v185,16,v185\n"
"v_lshlrev_b32 v186,16,v186\n"
"v_lshlrev_b32 v187,16,v187\n"
"v_lshlrev_b32 v188,16,v188\n"
"v_lshlrev_b32 v189,16,v189\n"
"v_mul_f32 v192,0xbfb8aa3b,v184\n"
"v_mul_f32 v193,0xbfb8aa3b,v185\n"
"v_mul_f32 v194,0xbfb8aa3b,v186\n"
"v_mul_f32 v195,0xbfb8aa3b,v187\n"
"v_mul_f32 v196,0xbfb8aa3b,v188\n"
"v_mul_f32 v197,0xbfb8aa3b,v189\n"
"v_exp_f32 v192,v192\n"
"v_exp_f32 v193,v193\n"
"v_exp_f32 v194,v194\n"
"v_exp_f32 v195,v195\n"
"v_exp_f32 v196,v196\n"
"v_exp_f32 v197,v197\n"
"v_pk_add_f32 v[192:193],1.0,v[192:193] op_sel_hi:[0,1]\n"
"v_pk_add_f32 v[194:195],1.0,v[194:195] op_sel_hi:[0,1]\n"
"v_pk_add_f32 v[196:197],1.0,v[196:197] op_sel_hi:[0,1]\n"
"v_rcp_f32 v192,v192\n"
"v_rcp_f32 v193,v193\n"
"v_rcp_f32 v194,v194\n"
"v_rcp_f32 v195,v195\n"
"v_rcp_f32 v196,v196\n"
"v_rcp_f32 v197,v197\n"
"v_pk_mul_f32 v[184:185],v[184:185],v[192:193]\n"
"v_pk_mul_f32 v[186:187],v[186:187],v[194:195]\n"
"v_pk_mul_f32 v[188:189],v[188:189],v[196:197]\n"
"s_waitcnt vmcnt(12)\n"
"v_lshlrev_b32 v192,16,v160\n"
"v_and_b32 v193,s73,v160\n"
"v_lshlrev_b32 v194,16,v161\n"
"v_and_b32 v195,s73,v161\n"
"v_lshlrev_b32 v196,16,v162\n"
"v_and_b32 v197,s73,v162\n"
"v_lshlrev_b32 v198,16,v163\n"
"v_and_b32 v199,s73,v163\n"
"v_pk_fma_f32 v[200:201],v[184:185],v[192:193],v[200:201] op_sel_hi:[0,1,1]\n"
"v_pk_fma_f32 v[202:203],v[184:185],v[194:195],v[202:203] op_sel_hi:[0,1,1]\n"
"v_pk_fma_f32 v[204:205],v[184:185],v[196:197],v[204:205] op_sel_hi:[0,1,1]\n"
"v_pk_fma_f32 v[206:207],v[184:185],v[198:199],v[206:207] op_sel_hi:[0,1,1]\n"
"s_waitcnt vmcnt(11)\n"
"v_lshlrev_b32 v192,16,v164\n"
"v_and_b32 v193,s73,v164\n"
"v_lshlrev_b32 v194,16,v165\n"
"v_and_b32 v195,s73,v165\n"
"v_lshlrev_b32 v196,16,v166\n"
"v_and_b32 v197,s73,v166\n"
"v_lshlrev_b32 v198,16,v167\n"
"v_and_b32 v199,s73,v167\n"
"v_pk_fma_f32 v[200:201],v[184:185],v[192:193],v[200:201] op_sel:[1,0,0]\n"
"v_pk_fma_f32 v[202:203],v[184:185],v[194:195],v[202:203] op_sel:[1,0,0]\n"
"v_pk_fma_f32 v[204:205],v[184:185],v[196:197],v[204:205] op_sel:[1,0,0]\n"
"v_pk_fma_f32 v[206:207],v[184:185],v[198:199],v[206:207] op_sel:[1,0,0]\n"
"s_waitcnt vmcnt(10)\n"
"v_lshlrev_b32 v192,16,v168\n"
"v_and_b32 v193,s73,v168\n"
"v_lshlrev_b32 v194,16,v169\n"
"v_and_b32 v195,s73,v169\n"
"v_lshlrev_b32 v196,16,v170\n"
"v_and_b32 v197,s73,v170\n"
"v_lshlrev_b32 v198,16,v171\n"
"v_and_b32 v199,s73,v171\n"
"v_pk_fma_f32 v[200:201],v[186:187],v[192:193],v[200:201] op_sel_hi:[0,1,1]\n"
"v_pk_fma_f32 v[202:203],v[186:187],v[194:195],v[202:203] op_sel_hi:[0,1,1]\n"
"v_pk_fma_f32 v[204:205],v[186:187],v[196:197],v[204:205] op_sel_hi:[0,1,1]\n"
"v_pk_fma_f32 v[206:207],v[186:187],v[198:199],v[206:207] op_sel_hi:[0,1,1]\n"
"s_waitcnt vmcnt(9)\n"
"v_lshlrev_b32 v192,16,v172\n"
"v_and_b32 v193,s73,v172\n"
"v_lshlrev_b32 v194,16,v173\n"
"v_and_b32 v195,s73,v173\n"
"v_lshlrev_b32 v196,16,v174\n"
"v_and_b32 v197,s73,v174\n"
"v_lshlrev_b32 v198,16,v175\n"
"v_and_b32 v199,s73,v175\n"
"v_pk_fma_f32 v[200:201],v[186:187],v[192:193],v[200:201] op_sel:[1,0,0]\n"
"v_pk_fma_f32 v[202:203],v[186:187],v[194:195],v[202:203] op_sel:[1,0,0]\n"
"v_pk_fma_f32 v[204:205],v[186:187],v[196:197],v[204:205] op_sel:[1,0,0]\n"
"v_pk_fma_f32 v[206:207],v[186:187],v[198:199],v[206:207] op_sel:[1,0,0]\n"
"s_waitcnt vmcnt(8)\n"
"v_lshlrev_b32 v192,16,v176\n"
"v_and_b32 v193,s73,v176\n"
"v_lshlrev_b32 v194,16,v177\n"
"v_and_b32 v195,s73,v177\n"
"v_lshlrev_b32 v196,16,v178\n"
"v_and_b32 v197,s73,v178\n"
"v_lshlrev_b32 v198,16,v179\n"
"v_and_b32 v199,s73,v179\n"
"v_pk_fma_f32 v[200:201],v[188:189],v[192:193],v[200:201] op_sel_hi:[0,1,1]\n"
"v_pk_fma_f32 v[202:203],v[188:189],v[194:195],v[202:203] op_sel_hi:[0,1,1]\n"
"v_pk_fma_f32 v[204:205],v[188:189],v[196:197],v[204:205] op_sel_hi:[0,1,1]\n"
"v_pk_fma_f32 v[206:207],v[188:189],v[198:199],v[206:207] op_sel_hi:[0,1,1]\n"
"s_waitcnt vmcnt(7)\n"
"v_lshlrev_b32 v192,16,v180\n"
"v_and_b32 v193,s73,v180\n"
"v_lshlrev_b32 v194,16,v181\n"
"v_and_b32 v195,s73,v181\n"
"v_lshlrev_b32 v196,16,v182\n"
"v_and_b32 v197,s73,v182\n"
"v_lshlrev_b32 v198,16,v183\n"
"v_and_b32 v199,s73,v183\n"
"v_pk_fma_f32 v[200:201],v[188:189],v[192:193],v[200:201] op_sel:[1,0,0]\n"
"v_pk_fma_f32 v[202:203],v[188:189],v[194:195],v[202:203] op_sel:[1,0,0]\n"
"v_pk_fma_f32 v[204:205],v[188:189],v[196:197],v[204:205] op_sel:[1,0,0]\n"
"v_pk_fma_f32 v[206:207],v[188:189],v[198:199],v[206:207] op_sel:[1,0,0]\n"
"s_add_u32 m0,s66,81920\n"
"s_add_u32 s77,s77,128\n"
"s_cmp_eq_u32 s77,1152\n"
"s_cselect_b32 s77,0,s77\n"
"buffer_load_dwordx4 v208,s[40:43],s77 offen offset:0 lds\n"
"buffer_load_dwordx4 v209,s[40:43],s77 offen offset:1024 lds\n"
"buffer_load_dwordx4 v210,s[40:43],s77 offen offset:2048 lds\n"
"buffer_load_dwordx4 v211,s[40:43],s77 offen offset:3072 lds\n"
"v_permlane32_swap_b32 v200,v204\n"
"v_permlane32_swap_b32 v201,v205\n"
"v_permlane32_swap_b32 v202,v206\n"
"v_permlane32_swap_b32 v203,v207\n"
"v_pk_add_f32 v[200:201],v[200:201],v[204:205]\n"
"v_pk_add_f32 v[202:203],v[202:203],v[206:207]\n"
"s_nop 1\n"
"v_permlane16_swap_b32 v200,v202\n"
"v_permlane16_swap_b32 v201,v203\n"
"v_pk_add_f32 v[200:201],v[200:201],v[202:203]\n"
"s_nop 1\n"
"v_add_f32_dpp v200,v200,v200 row_ror:8 row_mask:0xf bank_mask:0xf\n"
"v_add_f32_dpp v201,v201,v201 row_ror:8 row_mask:0xf bank_mask:0xf\n"
"ds_write_b64 v238,v[200:201]\n"
"s_waitcnt lgkmcnt(0)\n"
"s_add_u32 s38,s38,1\n"
"s_barrier\n"
"ds_read2st64_b32 v[200:201],v239 offset0:0 offset1:1\n"
"ds_read2st64_b32 v[202:203],v239 offset0:2 offset1:3\n"
"s_waitcnt lgkmcnt(0)\n"
"v_add_f32 v200,v200,v201\n"
"v_add_f32 v202,v202,v203\n"
"v_add_f32 v200,v200,v202\n"
"v_add_f32 v200,s63,v200\n"
"v_mul_f32 v200,0x3e0293ee,v200\n"
"v_mov_b32 v201,s38\n"
"buffer_store_dwordx2 v[200:201],v237,s[88:91],s71 offen sc1\n"
"s_waitcnt vmcnt(9)\n"
"v_dot2c_f32_bf16 v220,s72,v112\n"
"v_dot2c_f32_bf16 v222,v112,v112\n"
"v_dot2c_f32_bf16 v221,s72,v113\n"
"v_dot2c_f32_bf16 v223,v113,v113\n"
"v_dot2c_f32_bf16 v220,s72,v114\n"
"v_dot2c_f32_bf16 v222,v114,v114\n"
"v_dot2c_f32_bf16 v221,s72,v116\n"
"v_dot2c_f32_bf16 v223,v116,v116\n"
"v_dot2c_f32_bf16 v220,s72,v117\n"
"v_dot2c_f32_bf16 v222,v117,v117\n"
"v_dot2c_f32_bf16 v221,s72,v118\n"
"v_dot2c_f32_bf16 v223,v118,v118\n"
"v_dot2c_f32_bf16 v220,s72,v120\n"
"v_dot2c_f32_bf16 v222,v120,v120\n"
"v_dot2c_f32_bf16 v221,s72,v121\n"
"v_dot2c_f32_bf16 v223,v121,v121\n"
"v_dot2c_f32_bf16 v220,s72,v122\n"
"v_dot2c_f32_bf16 v222,v122,v122\n"
"v_mov_b32 v234,0x3ae38e39\n"
"v_mov_b32 v125,s38\n"
"v_add_f32 v220,v220,v221\n"
"v_add_f32 v222,v222,v223\n"
"s_nop 1\n"
"v_permlane16_swap_b32 v220,v222\n"
"v_add_f32 v220,v220,v222\n"
"v_mov_b32 v127,s38\n"
"s_nop 0\n"
"v_add_f32_dpp v220,v220,v220 row_ror:8 row_mask:0xf bank_mask:0xf\n"
"s_nop 1\n"
"v_add_f32_dpp v220,v220,v220 row_ror:4 row_mask:0xf bank_mask:0xf\n"
"s_nop 1\n"
"v_add_f32_dpp v220,v220,v220 row_ror:2 row_mask:0xf bank_mask:0xf\n"
"s_nop 1\n"
"v_add_f32_dpp v220,v220,v220 row_ror:1 row_mask:0xf bank_mask:0xf\n"
"v_mov_b32 v222,v220\n"
"s_nop 1\n"
"v_permlane16_swap_b32 v220,v222\n"
"v_mul_f32 v220,0xbae38e39,v220\n"
"v_fmaak_f32 v222,v234,v222,0x3727c5ac\n"
"v_fma_f32 v222,-v220,v220,v222\n"
"v_rsq_f32 v124,v222\n"
"s_nop 0\n"
"v_mul_f32 v126,v220,v124\n"
"buffer_store_dwordx4 v[124:127],v141,s[88:91],s87 offen sc1\n"
"s_mul_i32 s74,s37,288\n"
"s_add_u32 s52,s30,s74\n"
"s_addc_u32 s53,s31,0\n"
"s_and_b32 s53,s53,0xffff\n"
"s_mov_b32 s54,288\n"
"s_mov_b32 s55,0x20000\n"
"v_and_b32 v234,62,v8\n"
"buffer_load_ushort v128,v234,s[52:55],0 offen offset:0\n"
"buffer_load_ushort v129,v234,s[52:55],0 offen offset:64\n"
"buffer_load_ushort v130,v234,s[52:55],0 offen offset:128\n"
"buffer_load_ushort v131,v234,s[52:55],0 offen offset:192\n"
"buffer_load_ushort v132,v234,s[52:55],0 offen offset:256\n"
"s_add_u32 m0,s66,98304\n"
"s_add_u32 s77,s77,128\n"
"s_cmp_eq_u32 s77,1152\n"
"s_cselect_b32 s77,0,s77\n"
"buffer_load_dwordx4 v208,s[40:43],s77 offen offset:0 lds\n"
"buffer_load_dwordx4 v209,s[40:43],s77 offen offset:1024 lds\n"
"buffer_load_dwordx4 v210,s[40:43],s77 offen offset:2048 lds\n"
"buffer_load_dwordx4 v211,s[40:43],s77 offen offset:3072 lds\n"
"s_add_u32 m0,s66,114688\n"
"s_add_u32 s77,s77,128\n"
"s_cmp_eq_u32 s77,1152\n"
"s_cselect_b32 s77,0,s77\n"
"buffer_load_dwordx4 v208,s[40:43],s77 offen offset:0 lds\n"
"buffer_load_dwordx4 v209,s[40:43],s77 offen offset:1024 lds\n"
"buffer_load_dwordx4 v210,s[40:43],s77 offen offset:2048 lds\n"
"buffer_load_dwordx4 v211,s[40:43],s77 offen offset:3072 lds\n"
"s_add_u32 m0,s66,131072\n"
"s_add_u32 s77,s77,128\n"
"s_cmp_eq_u32 s77,1152\n"
"s_cselect_b32 s77,0,s77\n"
"buffer_load_dwordx4 v208,s[40:43],s77 offen offset:0 lds\n"
"buffer_load_dwordx4 v209,s[40:43],s77 offen offset:1024 lds\n"
"buffer_load_dwordx4 v210,s[40:43],s77 offen offset:2048 lds\n"
"buffer_load_dwordx4 v211,s[40:43],s77 offen offset:3072 lds\n"
"s_getpc_b64 s[56:57]\n"
"s_add_u32 s56,s56,k_main@rel32@lo+4\n"
"s_addc_u32 s57,s57,k_main@rel32@hi+12\n"
"s_and_b32 s57,s57,0xffff\n"
"s_mov_b32 s58,k_main_e-k_main\n"
"s_mov_b32 s59,0x20000\n"
"s_lshl2_add_u32 s68,s34,s37\n"
"s_and_b32 s68,s68,15\n"
"s_lshl_b32 s68,s68,13\n"
"v_lshlrev_b32 v13,7,v8\n"
"buffer_load_dword a228,v13,s[56:59],s68 offen\n"
"s_mov_b32 s62,65536\n"
"s_mul_i32 s74,s37,1024\n"
"s_add_u32 s62,s62,s74\n"
"v_and_b32 v142,0xf0,v9\n"
"v_lshl_add_u32 v142,s37,8,v142\n"
"s_mov_b32 s68,0\n"
"s_mul_i32 s74,s36,2097152\n"
"s_add_u32 s68,s68,s74\n"
"s_mul_i32 s74,s35,524288\n"
"s_add_u32 s68,s68,s74\n"
"s_add_u32 s48,s14,s68\n"
"s_addc_u32 s49,s15,0\n"
"s_and_b32 s49,s49,0xffff\n"
"s_mov_b32 s50,524288\n"
"s_mov_b32 s51,0x20000\n"
"s_add_u32 s52,s16,s68\n"
"s_addc_u32 s53,s17,0\n"
"s_and_b32 s53,s53,0xffff\n"
"s_mov_b32 s54,524288\n"
"s_mov_b32 s55,0x20000\n"
"v_accvgpr_write_b32 a232,-1\n"
"v_accvgpr_write_b32 a233,-1\n"
"v_accvgpr_write_b32 a234,-1\n"
"v_accvgpr_write_b32 a235,-1\n"
"s_mov_b32 s64,0x6f800000\n"
"s_mov_b32 s70,0x42cc0000\n"
"v_bfe_u32 v234,v8,2,2\n"
"v_and_b32 v235,3,v8\n"
"v_lshlrev_b32 v238,11,v234\n"
"v_lshl_add_u32 v238,v235,6,v238\n"
"v_bfe_u32 v236,v8,3,2\n"
"v_lshrrev_b32 v237,5,v8\n"
"v_xor_b32 v236,v236,v237\n"
"v_lshl_add_u32 v225,v236,4,v238\n"
"v_bfe_u32 v236,v8,4,1\n"
"v_lshlrev_b32 v236,5,v236\n"
"v_lshl_add_u32 v236,v234,6,v236\n"
"v_bfrev_b32 v238,v235\n"
"v_lshrrev_b32 v238,27,v238\n"
"v_add_u32 v232,v238,v236\n"
"v_lshl_add_u32 v232,v237,11,v232\n"
"v_lshrrev_b32 v238,4,v8\n"
"v_lshl_add_u32 v226,v235,3,v236\n"
"v_lshl_add_u32 v226,v238,11,v226\n"
"v_add_u32 v226,65536,v226\n"
"v_xor_b32 v227,32,v226\n"
"v_lshlrev_b32 v239,6,v238\n"
"v_lshl_add_u32 v236,v234,8,v239\n"
"s_waitcnt vmcnt(24)\n"
"ds_read_b128 v[32:35],v243 offset:0\n"
"ds_read_b128 v[36:39],v244 offset:0\n"
"ds_read_b128 v[40:43],v245 offset:0\n"
"ds_read_b128 v[44:47],v255 offset:0\n"
"v_add_u32 v236,s65,v236\n"
"v_lshl_add_u32 v236,v235,4,v236\n"
"s_lshl_b32 s68,s37,5\n"
"s_and_b32 s68,s68,32\n"
"v_xor_b32 v229,s68,v236\n"
"s_lshl_b32 s68,s37,3\n"
"s_and_b32 s68,s68,16\n"
"s_add_u32 s68,s68,8192\n"
"v_xor_b32 v228,s68,v236\n"
"v_and_b32 v236,1,v8\n"
"v_mul_u32_u24 v236,0xaaaa,v236\n"
"v_add_u32 v236,0x4444,v236\n"
"v_accvgpr_write_b32 a248,v236\n"
"v_bfe_u32 v235,v8,1,2\n"
"v_xor_b32 v235,v235,v237\n"
"v_cmp_eq_u32 vcc,0,v235\n"
"v_mov_b32 v236,s72\n"
"s_nop 0\n"
"v_cndmask_b32 v236,0,v236,vcc\n"
"v_accvgpr_write_b32 a240,v236\n"
"v_accvgpr_write_b32 a241,v236\n"
"v_accvgpr_write_b32 a242,v236\n"
"v_accvgpr_write_b32 a243,v236\n"
"v_cmp_eq_u32 vcc,2,v235\n"
"v_mov_b32 v236,s72\n"
"s_nop 0\n"
"v_cndmask_b32 v236,0,v236,vcc\n"
"v_accvgpr_write_b32 a244,v236\n"
"v_accvgpr_write_b32 a245,v236\n"
"v_accvgpr_write_b32 a246,v236\n"
"v_accvgpr_write_b32 a247,v236\n"
"v_and_b32 v234,31,v8\n"
"v_lshlrev_b32 v234,4,v234\n"
"v_lshl_add_u32 v240,v237,12,v234\n"
"v_bfrev_b32 v238,v238\n"
"v_lshrrev_b32 v238,26,v238\n"
"v_and_b32 v239,15,v8\n"
"v_lshl_add_u32 v241,v239,9,v238\n"
"s_lshl_b32 s68,s37,13\n"
"s_add_u32 s68,s68,131072\n"
"v_add_u32 v241,s68,v241\n"
"v_lshlrev_b32 v238,5,v237\n"
"v_add_u32 v242,147456,v238\n"
"v_and_b32 v238,28,v10\n"
"v_bfe_u32 v239,v8,3,1\n"
"v_lshl_add_u32 v238,v239,6,v238\n"
"s_waitcnt vmcnt(20)\n"
"ds_read_b128 v[48:51],v243 offset:16384\n"
"ds_read_b128 v[52:55],v244 offset:16384\n"
"ds_read_b128 v[56:59],v245 offset:16384\n"
"ds_read_b128 v[60:63],v255 offset:16384\n"
"v_add_u32 v223,v242,v238\n"
"s_and_b32 s98,s35,1\n"
"s_lshl_b32 s98,s98,1\n"
"v_lshl_add_u32 v254,s37,6,v8\n"
"v_lshl_add_u32 v253,s37,7,v8\n"
"v_add_u32 v234,0,v253\n"
"v_mul_u32_u24 v235,3641,v234\n"
"v_lshrrev_b32 v235,16,v235\n"
"v_mul_u32_u24 v236,18,v235\n"
"v_sub_u32 v234,v234,v236\n"
"v_add_u32 v234,s98,v234\n"
"v_subrev_u32 v236,18,v234\n"
"v_min_u32 v234,v234,v236\n"
"v_mul_u32_u24 v235,1152,v235\n"
"v_lshl_add_u32 v246,v234,4,v235\n"
"v_add_u32 v234,64,v253\n"
"v_mul_u32_u24 v235,3641,v234\n"
"v_lshrrev_b32 v235,16,v235\n"
"v_mul_u32_u24 v236,18,v235\n"
"v_sub_u32 v234,v234,v236\n"
"v_add_u32 v234,s98,v234\n"
"v_subrev_u32 v236,18,v234\n"
"v_min_u32 v234,v234,v236\n"
"v_mul_u32_u24 v235,1152,v235\n"
"v_lshl_add_u32 v247,v234,4,v235\n"
"v_subrev_u32 v247,1024,v247\n"
"v_add_u32 v234,512,v253\n"
"v_mul_u32_u24 v235,3641,v234\n"
"v_lshrrev_b32 v235,16,v235\n"
"v_mul_u32_u24 v236,18,v235\n"
"v_sub_u32 v234,v234,v236\n"
"v_add_u32 v234,s98,v234\n"
"v_subrev_u32 v236,18,v234\n"
"v_min_u32 v234,v234,v236\n"
"v_mul_u32_u24 v235,1152,v235\n"
"v_lshl_add_u32 v248,v234,4,v235\n"
"v_add_u32 v234,576,v253\n"
"v_mul_u32_u24 v235,3641,v234\n"
"v_lshrrev_b32 v235,16,v235\n"
"v_mul_u32_u24 v236,18,v235\n"
"v_sub_u32 v234,v234,v236\n"
"v_add_u32 v234,s98,v234\n"
"v_subrev_u32 v236,18,v234\n"
"v_min_u32 v234,v234,v236\n"
"v_mul_u32_u24 v235,1152,v235\n"
"v_lshl_add_u32 v249,v234,4,v235\n"
"s_waitcnt vmcnt(9)\n"
"ds_read_b128 v[64:67],v243 offset:32768\n"
"ds_read_b128 v[68:71],v244 offset:32768\n"
"ds_read_b128 v[72:75],v245 offset:32768\n"
"ds_read_b128 v[76:79],v255 offset:32768\n"
"v_subrev_u32 v249,1024,v249\n"
"v_add_u32 v234,1024,v253\n"
"v_mul_u32_u24 v235,3641,v234\n"
"v_lshrrev_b32 v235,16,v235\n"
"v_mul_u32_u24 v236,18,v235\n"
"v_sub_u32 v234,v234,v236\n"
"v_add_u32 v234,s98,v234\n"
"v_subrev_u32 v236,18,v234\n"
"v_min_u32 v234,v234,v236\n"
"v_mul_u32_u24 v235,1152,v235\n"
"v_lshl_add_u32 v250,v234,4,v235\n"
"v_add_u32 v234,1088,v253\n"
"v_mul_u32_u24 v235,3641,v234\n"
"v_lshrrev_b32 v235,16,v235\n"
"v_mul_u32_u24 v236,18,v235\n"
"v_sub_u32 v234,v234,v236\n"
"v_add_u32 v234,s98,v234\n"
"v_subrev_u32 v236,18,v234\n"
"v_min_u32 v234,v234,v236\n"
"v_mul_u32_u24 v235,1152,v235\n"
"v_lshl_add_u32 v251,v234,4,v235\n"
"v_subrev_u32 v251,1024,v251\n"
"v_add_u32 v234,1536,v253\n"
"v_mul_u32_u24 v235,3641,v234\n"
"v_lshrrev_b32 v235,16,v235\n"
"v_mul_u32_u24 v236,18,v235\n"
"v_sub_u32 v234,v234,v236\n"
"v_add_u32 v234,s98,v234\n"
"v_subrev_u32 v236,18,v234\n"
"v_min_u32 v234,v234,v236\n"
"v_mul_u32_u24 v235,1152,v235\n"
"v_lshl_add_u32 v252,v234,4,v235\n"
"v_add_u32 v234,1600,v253\n"
"v_mul_u32_u24 v235,3641,v234\n"
"v_lshrrev_b32 v235,16,v235\n"
"v_mul_u32_u24 v236,18,v235\n"
"v_sub_u32 v234,v234,v236\n"
"v_add_u32 v234,s98,v234\n"
"v_subrev_u32 v236,18,v234\n"
"v_min_u32 v234,v234,v236\n"
"v_mul_u32_u24 v235,1152,v235\n"
"v_lshl_add_u32 v253,v234,4,v235\n"
"v_subrev_u32 v253,1024,v253\n"
"v_add_u32 v234,2048,v254\n"
"v_mul_u32_u24 v235,3641,v234\n"
"v_lshrrev_b32 v235,16,v235\n"
"v_mul_u32_u24 v236,18,v235\n"
"s_waitcnt vmcnt(5)\n"
"ds_read_b128 v[80:83],v243 offset:49152\n"
"ds_read_b128 v[84:87],v244 offset:49152\n"
"ds_read_b128 v[88:91],v245 offset:49152\n"
"ds_read_b128 v[92:95],v255 offset:49152\n"
"s_add_u32 s69,s62,0\n"
"buffer_load_dwordx4 v[160:163],v9,s[88:91],s69 offen sc1\n"
"s_add_u32 s69,s62,4096\n"
"buffer_load_dwordx4 v[164:167],v9,s[88:91],s69 offen sc1\n"
"s_mov_b32 s69,73728\n"
"buffer_load_dwordx4 v[168:171],v142,s[88:91],s69 offen sc1\n"
"s_add_u32 s69,s62,9216\n"
"buffer_load_dwordx4 v[172:175],v9,s[88:91],s69 offen sc1\n"
"s_add_u32 s69,s62,13312\n"
"buffer_load_dwordx4 v[176:179],v9,s[88:91],s69 offen sc1\n"
"s_mov_b32 s69,82944\n"
"buffer_load_dwordx4 v[180:183],v142,s[88:91],s69 offen sc1\n"
"s_add_u32 s69,s62,18432\n"
"buffer_load_dwordx4 v[184:187],v9,s[88:91],s69 offen sc1\n"
"s_add_u32 s69,s62,22528\n"
"buffer_load_dwordx4 v[188:191],v9,s[88:91],s69 offen sc1\n"
"s_mov_b32 s69,92160\n"
"buffer_load_dwordx4 v[192:195],v142,s[88:91],s69 offen sc1\n"
"buffer_load_dwordx4 v[196:199],v140,s[88:91],s76 offen sc1\n"
"v_sub_u32 v234,v234,v236\n"
"v_add_u32 v234,s98,v234\n"
"v_subrev_u32 v236,18,v234\n"
"v_min_u32 v234,v234,v236\n"
"v_mul_u32_u24 v235,1152,v235\n"
"v_lshl_add_u32 v254,v234,4,v235\n"
"s_mov_b32 s99,0\n"
"s_mul_i32 s56,s36,1179648\n"
"s_add_u32 s99,s99,s56\n"
"s_mul_i32 s56,s35,288\n"
"L_pga16_%=:\n"
"s_cbranch_execz L_hra_%=\n"
"s_add_u32 s99,s99,s56\n"
"s_add_u32 s56,s18,s99\n"
"s_addc_u32 s57,s19,0\n"
"s_and_b32 s57,s57,0xffff\n"
"s_mov_b32 s58,1178784\n"
"s_mov_b32 s59,0x20000\n"
"s_mul_i32 s99,s35,294912\n"
"s_lshl_b32 s68,s37,7\n"
"s_lshl_b32 s74,s79,6\n"
"v_lshl_add_u32 v234,v8,1,s68\n"
"v_subrev_u32 v235,576,v234\n"
"v_min_u32 v235,v234,v235\n"
"s_add_u32 m0,s66,32768\n"
"s_lshl_b32 s78,s79,19\n"
"buffer_load_dwordx4 v230,s[44:47],s78 offen offset:0 lds\n"
"v_sub_u32 v236,v234,v235\n"
"v_subrev_u32 v235,s74,v235\n"
"v_add_u32 v237,576,v235\n"
"v_min_u32 v235,v235,v237\n"
"v_sub_u32 v235,v235,v236\n"
"v_lshlrev_b32 v235,2,v235\n"
"v_add_u32 v12,149760,v235\n"
"v_add_u32 v234,512,v234\n"
"v_subrev_u32 v235,576,v234\n"
"v_min_u32 v235,v234,v235\n"
"v_sub_u32 v236,v234,v235\n"
"v_subrev_u32 v235,s74,v235\n"
"v_add_u32 v237,576,v235\n"
"v_min_u32 v235,v235,v237\n"
"v_sub_u32 v235,v235,v236\n"
"v_lshlrev_b32 v235,2,v235\n"
"v_add_u32 v13,149760,v235\n"
"v_lshrrev_b32 v234,3,v142\n"
"v_add_u32 v234,1024,v234\n"
"v_subrev_u32 v235,576,v234\n"
"v_min_u32 v235,v234,v235\n"
"buffer_load_dwordx4 v231,s[44:47],s78 offen offset:1024 lds\n"
"v_sub_u32 v236,v234,v235\n"
"v_subrev_u32 v235,s74,v235\n"
"v_add_u32 v237,576,v235\n"
"v_min_u32 v235,v235,v237\n"
"v_sub_u32 v235,v235,v236\n"
"v_lshlrev_b32 v235,2,v235\n"
"v_add_u32 v14,149760,v235\n"
"s_lshr_b32 s68,s39,7\n"
"s_bfe_u32 s74,s39,0x30002\n"
"s_lshl3_add_u32 s68,s68,s74\n"
"s_bfe_u32 s74,s39,0x20005\n"
"s_lshl2_add_u32 s61,s68,s74\n"
"s_cmp_lt_u32 s61,144\n"
"s_cselect_b32 s75,988448,0\n"
"s_mov_b32 s68,2304\n"
"s_mul_i32 s74,s37,995328\n"
"s_add_u32 s68,s68,s74\n"
"s_mul_i32 s74,s61,32\n"
"s_add_u32 s68,s68,s74\n"
"s_add_u32 s80,s84,s68\n"
"s_addc_u32 s81,s85,0\n"
"s_and_b32 s81,s81,0xffff\n"
"buffer_load_dwordx4 v216,s[44:47],s78 offen offset:2048 lds\n"
"s_mov_b32 s82,s75\n"
"s_mov_b32 s83,0x20000\n"
"v_lshrrev_b32 v234,1,v8\n"
"v_mul_u32_u24 v236,6912,v234\n"
"v_and_b32 v235,1,v8\n"
"v_lshl_add_u32 v218,v235,4,v236\n"
"v_lshrrev_b32 v236,4,v8\n"
"v_lshlrev_b32 v234,28,v236\n"
"v_lshl_or_b32 v219,v8,2,v234\n"
"v_lshlrev_b32 v236,3,v236\n"
"v_lshl_add_u32 v234,v235,5,v236\n"
"s_lshl_b32 s68,s37,6\n"
"s_add_u32 s68,s68,152576\n"
"v_add_u32 v224,s68,v234\n"
"v_and_b32 v234,60,v10\n"
"v_add_u32 v221,152576,v234\n"
"s_sub_u32 s68,s61,72\n"
"s_cmp_lt_u32 s68,36\n"
"s_cselect_b32 s71,1.0,0\n"
"s_mov_b32 s67,7344640\n"
"s_mul_i32 s74,s61,64\n"
"s_add_u32 s67,s67,s74\n"
"buffer_load_dwordx4 v217,s[44:47],s78 offen offset:3072 lds\n"
"s_cmp_eq_u32 s75,0\n"
"s_cselect_b32 s68,1,s37\n"
"s_mov_b32 s74,0x3ffffff0\n"
"s_cmp_eq_u32 s68,0\n"
"s_cselect_b32 s67,s67,s74\n"
"v_lshlrev_b32 v128,16,v128\n"
"v_lshlrev_b32 v129,16,v129\n"
"v_lshlrev_b32 v130,16,v130\n"
"v_lshlrev_b32 v131,16,v131\n"
"v_lshlrev_b32 v132,16,v132\n"
"v_mul_f32 v133,0xbfb8aa3b,v128\n"
"v_mul_f32 v134,0xbfb8aa3b,v129\n"
"v_mul_f32 v135,0xbfb8aa3b,v130\n"
"v_mul_f32 v136,0xbfb8aa3b,v131\n"
"v_mul_f32 v137,0xbfb8aa3b,v132\n"
"v_exp_f32 v133,v133\n"
"v_exp_f32 v134,v134\n"
"v_exp_f32 v135,v135\n"
"v_exp_f32 v136,v136\n"
"v_exp_f32 v137,v137\n"
"v_add_f32 v133,1.0,v133\n"
"v_add_f32 v134,1.0,v134\n"
"s_add_u32 m0,s66,49152\n"
"s_add_u32 s86,s78,256\n"
"buffer_load_dwordx4 v230,s[44:47],s86 offen offset:0 lds\n"
"v_add_f32 v135,1.0,v135\n"
"v_pk_add_f32 v[136:137],1.0,v[136:137] op_sel_hi:[0,1]\n"
"v_rcp_f32 v133,v133\n"
"v_rcp_f32 v134,v134\n"
"v_rcp_f32 v135,v135\n"
"v_rcp_f32 v136,v136\n"
"v_rcp_f32 v137,v137\n"
"v_mul_f32 v128,v128,v133\n"
"v_mul_f32 v129,v129,v134\n"
"v_mul_f32 v130,v130,v135\n"
"v_mul_f32 v131,v131,v136\n"
"v_mul_f32 v132,v132,v137\n"
"v_accvgpr_write_b32 a224,v128\n"
"v_accvgpr_write_b32 a225,v129\n"
"v_accvgpr_write_b32 a226,v130\n"
"v_accvgpr_write_b32 a227,v131\n"
"v_accvgpr_write_b32 a229,v132\n"
"v_add_u32 v233,32768,v232\n"
"s_mov_b32 s63,0x40000\n"
"buffer_load_dwordx4 v231,s[44:47],s86 offen offset:1024 lds\n"
"s_mov_b64 exec,0\n"
"s_cmp_eq_u32 s37,1\n"
"s_cbranch_scc1 L_pgg48_%=\n"
"s_cmp_eq_u32 s37,2\n"
"s_cbranch_scc1 L_pgg64_%=\n"
"s_cmp_eq_u32 s37,3\n"
"s_cbranch_scc1 L_pgg80_%=\n"
"s_branch L_pgg32_%=\n"
"L_hrg_%=:\n"
"s_mov_b64 exec,-1\n"
"s_waitcnt lgkmcnt(12)\n"
"v_lshlrev_b32 v16,16,v32\n"
"v_and_b32 v17,s73,v32\n"
"v_lshlrev_b32 v18,16,v33\n"
"v_and_b32 v19,s73,v33\n"
"v_lshlrev_b32 v20,16,v34\n"
"v_and_b32 v21,s73,v34\n"
"v_lshlrev_b32 v22,16,v35\n"
"v_and_b32 v23,s73,v35\n"
"v_lshlrev_b32 v24,16,v36\n"
"v_and_b32 v25,s73,v36\n"
"v_lshlrev_b32 v26,16,v37\n"
"v_and_b32 v27,s73,v37\n"
"v_lshlrev_b32 v28,16,v38\n"
"v_and_b32 v29,s73,v38\n"
"v_lshlrev_b32 v30,16,v39\n"
"v_and_b32 v31,s73,v39\n"
"v_lshlrev_b32 v96,16,v40\n"
"v_and_b32 v97,s73,v40\n"
"v_lshlrev_b32 v98,16,v41\n"
"v_and_b32 v99,s73,v41\n"
"v_lshlrev_b32 v100,16,v42\n"
"v_and_b32 v101,s73,v42\n"
"v_lshlrev_b32 v102,16,v43\n"
"v_and_b32 v103,s73,v43\n"
"v_lshlrev_b32 v104,16,v44\n"
"v_and_b32 v105,s73,v44\n"
"v_lshlrev_b32 v106,16,v45\n"
"v_and_b32 v107,s73,v45\n"
"v_lshlrev_b32 v108,16,v46\n"
"v_and_b32 v109,s73,v46\n"
"v_lshlrev_b32 v110,16,v47\n"
"v_and_b32 v111,s73,v47\n"
"s_waitcnt lgkmcnt(8)\n"
"v_lshlrev_b32 v112,16,v48\n"
"v_and_b32 v113,s73,v48\n"
"v_lshlrev_b32 v114,16,v49\n"
"v_and_b32 v115,s73,v49\n"
"v_lshlrev_b32 v116,16,v50\n"
"v_and_b32 v117,s73,v50\n"
"v_lshlrev_b32 v118,16,v51\n"
"v_and_b32 v119,s73,v51\n"
"buffer_load_dwordx4 v216,s[44:47],s86 offen offset:2048 lds\n"
"v_lshlrev_b32 v120,16,v52\n"
"v_and_b32 v121,s73,v52\n"
"v_lshlrev_b32 v122,16,v53\n"
"v_and_b32 v123,s73,v53\n"
"v_lshlrev_b32 v124,16,v54\n"
"v_and_b32 v125,s73,v54\n"
"v_lshlrev_b32 v126,16,v55\n"
"v_and_b32 v127,s73,v55\n"
"v_lshlrev_b32 v128,16,v56\n"
"v_and_b32 v129,s73,v56\n"
"v_lshlrev_b32 v130,16,v57\n"
"v_and_b32 v131,s73,v57\n"
"v_lshlrev_b32 v132,16,v58\n"
"v_and_b32 v133,s73,v58\n"
"v_lshlrev_b32 v134,16,v59\n"
"v_and_b32 v135,s73,v59\n"
"v_lshlrev_b32 v144,16,v60\n"
"v_and_b32 v145,s73,v60\n"
"v_lshlrev_b32 v146,16,v61\n"
"v_and_b32 v147,s73,v61\n"
"v_lshlrev_b32 v148,16,v62\n"
"v_and_b32 v149,s73,v62\n"
"v_lshlrev_b32 v150,16,v63\n"
"v_and_b32 v151,s73,v63\n"
"s_waitcnt lgkmcnt(4)\n"
"v_lshlrev_b32 v152,16,v64\n"
"v_and_b32 v153,s73,v64\n"
"v_lshlrev_b32 v154,16,v65\n"
"v_and_b32 v155,s73,v65\n"
"v_lshlrev_b32 v156,16,v66\n"
"v_and_b32 v157,s73,v66\n"
"v_lshlrev_b32 v158,16,v67\n"
"v_and_b32 v159,s73,v67\n"
"v_lshlrev_b32 v200,16,v68\n"
"v_and_b32 v201,s73,v68\n"
"v_lshlrev_b32 v202,16,v69\n"
"v_and_b32 v203,s73,v69\n"
"v_lshlrev_b32 v204,16,v70\n"
"v_and_b32 v205,s73,v70\n"
"v_lshlrev_b32 v206,16,v71\n"
"v_and_b32 v207,s73,v71\n"
"buffer_load_dwordx4 v217,s[44:47],s86 offen offset:3072 lds\n"
"s_waitcnt vmcnt(8)\n"
"L_mc_%=:\n"
"s_sub_u32 s63,s63,1\n"
"v_min3_u32 v234,v161,v163,s38\n"
"v_max3_u32 v235,v161,v163,s38\n"
"v_min3_u32 v234,v165,v167,v234\n"
"v_max3_u32 v235,v165,v167,v235\n"
"v_min3_u32 v234,v169,v171,v234\n"
"v_max3_u32 v235,v169,v171,v235\n"
"v_min3_u32 v234,v173,v175,v234\n"
"v_max3_u32 v235,v173,v175,v235\n"
"v_min3_u32 v234,v177,v179,v234\n"
"v_max3_u32 v235,v177,v179,v235\n"
"v_min3_u32 v234,v181,v183,v234\n"
"v_max3_u32 v235,v181,v183,v235\n"
"v_min3_u32 v234,v185,v187,v234\n"
"v_max3_u32 v235,v185,v187,v235\n"
"v_min3_u32 v234,v189,v191,v234\n"
"v_max3_u32 v235,v189,v191,v235\n"
"v_min3_u32 v234,v193,v195,v234\n"
"v_max3_u32 v235,v193,v195,v235\n"
"v_min3_u32 v234,v197,v199,v234\n"
"v_max3_u32 v235,v197,v199,v235\n"
"v_cmp_ne_u32 vcc,v234,v235\n"
"s_cbranch_scc1 L_mo_%=\n"
"s_cbranch_vccnz L_mg_%=\n"
"L_mo_%=:\n"
"v_mov_b32 v222,v196\n"
"v_mov_b32 v220,v198\n"
"v_add_f32 v160,v160,v172\n"
"v_add_f32 v162,v162,v174\n"
"v_add_f32 v160,v160,v184\n"
"v_add_f32 v161,v162,v186\n"
"ds_write_b64 v12,v[160:161]\n"
"v_add_f32 v164,v164,v176\n"
"v_add_f32 v166,v166,v178\n"
"v_add_f32 v164,v164,v188\n"
"v_add_f32 v165,v166,v190\n"
"ds_write_b64 v13,v[164:165]\n"
"v_add_f32 v168,v168,v180\n"
"v_add_f32 v170,v170,v182\n"
"v_add_f32 v168,v168,v192\n"
"v_add_f32 v169,v170,v194\n"
"ds_write_b64 v14,v[168:169]\n"
"s_waitcnt lgkmcnt(0)\n"
"s_barrier\n"
"ds_read_b128 v[160:163],v242 offset:0\n"
"ds_read_b128 v[164:167],v242 offset:16\n"
"ds_read_b128 v[168:171],v242 offset:2304\n"
"ds_read_b128 v[172:175],v242 offset:2320\n"
"ds_read_b128 v[176:179],v242 offset:64\n"
"ds_read_b128 v[180:183],v242 offset:80\n"
"ds_read_b128 v[184:187],v242 offset:2368\n"
"ds_read_b128 v[188:191],v242 offset:2384\n"
"v_pk_fma_f32 v[16:17],v[16:17],v[222:223],v[220:221] op_sel_hi:[1,0,0]\n"
"v_pk_fma_f32 v[18:19],v[18:19],v[222:223],v[220:221] op_sel_hi:[1,0,0]\n"
"v_pk_fma_f32 v[20:21],v[20:21],v[222:223],v[220:221] op_sel_hi:[1,0,0]\n"
"v_pk_fma_f32 v[22:23],v[22:23],v[222:223],v[220:221] op_sel_hi:[1,0,0]\n"
"s_waitcnt lgkmcnt(4)\n"
"v_pk_fma_f32 v[16:17],v[16:17],v[160:161],v[168:169]\n"
"v_pk_fma_f32 v[18:19],v[18:19],v[162:163],v[170:171]\n"
"v_pk_fma_f32 v[20:21],v[20:21],v[164:165],v[172:173]\n"
"v_pk_fma_f32 v[22:23],v[22:23],v[166:167],v[174:175]\n"
"v_cvt_pk_bf16_f32 v32,v16,v17\n"
"v_cvt_pk_bf16_f32 v33,v18,v19\n"
"v_cvt_pk_bf16_f32 v34,v20,v21\n"
"v_cvt_pk_bf16_f32 v35,v22,v23\n"
"s_add_u32 m0,s66,0\n"
"s_add_u32 s78,s78,524288\n"
"s_cmp_eq_u32 s78,4718592\n"
"s_cselect_b32 s78,0,s78\n"
"buffer_load_dwordx4 v230,s[44:47],s78 offen offset:0 lds\n"
"ds_read_b128 v[160:163],v242 offset:128\n"
"ds_read_b128 v[164:167],v242 offset:144\n"
"ds_read_b128 v[168:171],v242 offset:2432\n"
"ds_read_b128 v[172:175],v242 offset:2448\n"
"v_pk_fma_f32 v[24:25],v[24:25],v[222:223],v[220:221] op_sel_hi:[1,0,0]\n"
"v_pk_fma_f32 v[26:27],v[26:27],v[222:223],v[220:221] op_sel_hi:[1,0,0]\n"
"v_pk_fma_f32 v[28:29],v[28:29],v[222:223],v[220:221] op_sel_hi:[1,0,0]\n"
"v_pk_fma_f32 v[30:31],v[30:31],v[222:223],v[220:221] op_sel_hi:[1,0,0]\n"
"s_waitcnt lgkmcnt(4)\n"
"v_pk_fma_f32 v[24:25],v[24:25],v[176:177],v[184:185]\n"
"v_pk_fma_f32 v[26:27],v[26:27],v[178:179],v[186:187]\n"
"v_pk_fma_f32 v[28:29],v[28:29],v[180:181],v[188:189]\n"
"v_pk_fma_f32 v[30:31],v[30:31],v[182:183],v[190:191]\n"
"v_cvt_pk_bf16_f32 v36,v24,v25\n"
"v_cvt_pk_bf16_f32 v37,v26,v27\n"
"v_cvt_pk_bf16_f32 v38,v28,v29\n"
"v_cvt_pk_bf16_f32 v39,v30,v31\n"
"buffer_load_dwordx4 v231,s[44:47],s78 offen offset:1024 lds\n"
"buffer_load_dwordx4 v216,s[44:47],s78 offen offset:2048 lds\n"
"ds_read_b128 v[176:179],v242 offset:192\n"
"ds_read_b128 v[180:183],v242 offset:208\n"
"ds_read_b128 v[184:187],v242 offset:2496\n"
"ds_read_b128 v[188:191],v242 offset:2512\n"
"v_pk_fma_f32 v[96:97],v[96:97],v[222:223],v[220:221] op_sel_hi:[1,0,0]\n"
"v_pk_fma_f32 v[98:99],v[98:99],v[222:223],v[220:221] op_sel_hi:[1,0,0]\n"
"v_pk_fma_f32 v[100:101],v[100:101],v[222:223],v[220:221] op_sel_hi:[1,0,0]\n"
"v_pk_fma_f32 v[102:103],v[102:103],v[222:223],v[220:221] op_sel_hi:[1,0,0]\n"
"s_waitcnt lgkmcnt(4)\n"
"v_pk_fma_f32 v[96:97],v[96:97],v[160:161],v[168:169]\n"
"v_pk_fma_f32 v[98:99],v[98:99],v[162:163],v[170:171]\n"
"v_pk_fma_f32 v[100:101],v[100:101],v[164:165],v[172:173]\n"
"v_pk_fma_f32 v[102:103],v[102:103],v[166:167],v[174:175]\n"
"v_cvt_pk_bf16_f32 v40,v96,v97\n"
"v_cvt_pk_bf16_f32 v41,v98,v99\n"
"v_cvt_pk_bf16_f32 v42,v100,v101\n"
"v_cvt_pk_bf16_f32 v43,v102,v103\n"
"buffer_load_dwordx4 v217,s[44:47],s78 offen offset:3072 lds\n"
"ds_read_b128 v[160:163],v242 offset:256\n"
"ds_read_b128 v[164:167],v242 offset:272\n"
"ds_read_b128 v[168:171],v242 offset:2560\n"
"ds_read_b128 v[172:175],v242 offset:2576\n"
"v_pk_fma_f32 v[104:105],v[104:105],v[222:223],v[220:221] op_sel_hi:[1,0,0]\n"
"v_pk_fma_f32 v[106:107],v[106:107],v[222:223],v[220:221] op_sel_hi:[1,0,0]\n"
"v_pk_fma_f32 v[108:109],v[108:109],v[222:223],v[220:221] op_sel_hi:[1,0,0]\n"
"v_pk_fma_f32 v[110:111],v[110:111],v[222:223],v[220:221] op_sel_hi:[1,0,0]\n"
"s_waitcnt lgkmcnt(4)\n"
"v_pk_fma_f32 v[104:105],v[104:105],v[176:177],v[184:185]\n"
"v_pk_fma_f32 v[106:107],v[106:107],v[178:179],v[186:187]\n"
"v_pk_fma_f32 v[108:109],v[108:109],v[180:181],v[188:189]\n"
"v_pk_fma_f32 v[110:111],v[110:111],v[182:183],v[190:191]\n"
"v_cvt_pk_bf16_f32 v44,v104,v105\n"
"v_cvt_pk_bf16_f32 v45,v106,v107\n"
"v_cvt_pk_bf16_f32 v46,v108,v109\n"
"v_cvt_pk_bf16_f32 v47,v110,v111\n"
"s_add_u32 m0,s66,16384\n"
"s_add_u32 s86,s78,256\n"
"buffer_load_dwordx4 v230,s[44:47],s86 offen offset:0 lds\n"
"buffer_load_dwordx4 v231,s[44:47],s86 offen offset:1024 lds\n"
"ds_read_b128 v[176:179],v242 offset:320\n"
"ds_read_b128 v[180:183],v242 offset:336\n"
"ds_read_b128 v[184:187],v242 offset:2624\n"
"ds_read_b128 v[188:191],v242 offset:2640\n"
"v_pk_fma_f32 v[112:113],v[112:113],v[222:223],v[220:221] op_sel_hi:[1,0,0]\n"
"v_pk_fma_f32 v[114:115],v[114:115],v[222:223],v[220:221] op_sel_hi:[1,0,0]\n"
"v_pk_fma_f32 v[116:117],v[116:117],v[222:223],v[220:221] op_sel_hi:[1,0,0]\n"
"v_pk_fma_f32 v[118:119],v[118:119],v[222:223],v[220:221] op_sel_hi:[1,0,0]\n"
"s_waitcnt lgkmcnt(4)\n"
"v_pk_fma_f32 v[112:113],v[112:113],v[160:161],v[168:169]\n"
"v_pk_fma_f32 v[114:115],v[114:115],v[162:163],v[170:171]\n"
"v_pk_fma_f32 v[116:117],v[116:117],v[164:165],v[172:173]\n"
"v_pk_fma_f32 v[118:119],v[118:119],v[166:167],v[174:175]\n"
"v_cvt_pk_bf16_f32 v48,v112,v113\n"
"v_cvt_pk_bf16_f32 v49,v114,v115\n"
"v_cvt_pk_bf16_f32 v50,v116,v117\n"
"v_cvt_pk_bf16_f32 v51,v118,v119\n"
"buffer_load_dwordx4 v216,s[44:47],s86 offen offset:2048 lds\n"
"buffer_load_dwordx4 v217,s[44:47],s86 offen offset:3072 lds\n"
"ds_read_b128 v[160:163],v242 offset:384\n"
"ds_read_b128 v[164:167],v242 offset:400\n"
"ds_read_b128 v[168:171],v242 offset:2688\n"
"ds_read_b128 v[172:175],v242 offset:2704\n"
"v_pk_fma_f32 v[120:121],v[120:121],v[222:223],v[220:221] op_sel_hi:[1,0,0]\n"
"v_pk_fma_f32 v[122:123],v[122:123],v[222:223],v[220:221] op_sel_hi:[1,0,0]\n"
"v_pk_fma_f32 v[124:125],v[124:125],v[222:223],v[220:221] op_sel_hi:[1,0,0]\n"
"v_pk_fma_f32 v[126:127],v[126:127],v[222:223],v[220:221] op_sel_hi:[1,0,0]\n"
"s_waitcnt lgkmcnt(4)\n"
"v_pk_fma_f32 v[120:121],v[120:121],v[176:177],v[184:185]\n"
"v_pk_fma_f32 v[122:123],v[122:123],v[178:179],v[186:187]\n"
"v_pk_fma_f32 v[124:125],v[124:125],v[180:181],v[188:189]\n"
"v_pk_fma_f32 v[126:127],v[126:127],v[182:183],v[190:191]\n"
"v_cvt_pk_bf16_f32 v52,v120,v121\n"
"v_cvt_pk_bf16_f32 v53,v122,v123\n"
"v_cvt_pk_bf16_f32 v54,v124,v125\n"
"v_cvt_pk_bf16_f32 v55,v126,v127\n"
"s_add_u32 m0,s66,98304\n"
"s_add_u32 s77,s77,128\n"
"s_cmp_eq_u32 s77,1152\n"
"s_cselect_b32 s77,0,s77\n"
"buffer_load_dwordx4 v208,s[40:43],s77 offen offset:0 lds\n"
"ds_read_b128 v[176:179],v242 offset:448\n"
"ds_read_b128 v[180:183],v242 offset:464\n"
"ds_read_b128 v[184:187],v242 offset:2752\n"
"ds_read_b128 v[188:191],v242 offset:2768\n"
"v_pk_fma_f32 v[128:129],v[128:129],v[222:223],v[220:221] op_sel_hi:[1,0,0]\n"
"v_pk_fma_f32 v[130:131],v[130:131],v[222:223],v[220:221] op_sel_hi:[1,0,0]\n"
"v_pk_fma_f32 v[132:133],v[132:133],v[222:223],v[220:221] op_sel_hi:[1,0,0]\n"
"v_pk_fma_f32 v[134:135],v[134:135],v[222:223],v[220:221] op_sel_hi:[1,0,0]\n"
"s_waitcnt lgkmcnt(4)\n"
"v_pk_fma_f32 v[128:129],v[128:129],v[160:161],v[168:169]\n"
"v_pk_fma_f32 v[130:131],v[130:131],v[162:163],v[170:171]\n"
"v_pk_fma_f32 v[132:133],v[132:133],v[164:165],v[172:173]\n"
"v_pk_fma_f32 v[134:135],v[134:135],v[166:167],v[174:175]\n"
"v_cvt_pk_bf16_f32 v56,v128,v129\n"
"v_cvt_pk_bf16_f32 v57,v130,v131\n"
"v_cvt_pk_bf16_f32 v58,v132,v133\n"
"v_cvt_pk_bf16_f32 v59,v134,v135\n"
"buffer_load_dwordx4 v209,s[40:43],s77 offen offset:1024 lds\n"
"buffer_load_dwordx4 v210,s[40:43],s77 offen offset:2048 lds\n"
"ds_read_b128 v[160:163],v242 offset:512\n"
"ds_read_b128 v[164:167],v242 offset:528\n"
"ds_read_b128 v[168:171],v242 offset:2816\n"
"ds_read_b128 v[172:175],v242 offset:2832\n"
"v_pk_fma_f32 v[144:145],v[144:145],v[222:223],v[220:221] op_sel_hi:[1,0,0]\n"
"v_pk_fma_f32 v[146:147],v[146:147],v[222:223],v[220:221] op_sel_hi:[1,0,0]\n"
"v_pk_fma_f32 v[148:149],v[148:149],v[222:223],v[220:221] op_sel_hi:[1,0,0]\n"
"v_pk_fma_f32 v[150:151],v[150:151],v[222:223],v[220:221] op_sel_hi:[1,0,0]\n"
"s_waitcnt lgkmcnt(4)\n"
"v_pk_fma_f32 v[144:145],v[144:145],v[176:177],v[184:185]\n"
"v_pk_fma_f32 v[146:147],v[146:147],v[178:179],v[186:187]\n"
"v_pk_fma_f32 v[148:149],v[148:149],v[180:181],v[188:189]\n"
"v_pk_fma_f32 v[150:151],v[150:151],v[182:183],v[190:191]\n"
"v_cvt_pk_bf16_f32 v60,v144,v145\n"
"v_cvt_pk_bf16_f32 v61,v146,v147\n"
"v_cvt_pk_bf16_f32 v62,v148,v149\n"
"v_cvt_pk_bf16_f32 v63,v150,v151\n"
"buffer_load_dwordx4 v211,s[40:43],s77 offen offset:3072 lds\n"
"s_add_u32 m0,s66,65536\n"
"s_add_u32 s78,s78,524288\n"
"s_cmp_eq_u32 s78,4718592\n"
"s_cselect_b32 s78,0,s78\n"
"buffer_load_dwordx4 v230,s[44:47],s78 offen offset:0 lds\n"
"ds_read_b128 v[176:179],v242 offset:576\n"
"ds_read_b128 v[180:183],v242 offset:592\n"
"ds_read_b128 v[184:187],v242 offset:2880\n"
"ds_read_b128 v[188:191],v242 offset:2896\n"
"v_pk_fma_f32 v[152:153],v[152:153],v[222:223],v[220:221] op_sel_hi:[1,0,0]\n"
"v_pk_fma_f32 v[154:155],v[154:155],v[222:223],v[220:221] op_sel_hi:[1,0,0]\n"
"v_pk_fma_f32 v[156:157],v[156:157],v[222:223],v[220:221] op_sel_hi:[1,0,0]\n"
"v_pk_fma_f32 v[158:159],v[158:159],v[222:223],v[220:221] op_sel_hi:[1,0,0]\n"
"s_waitcnt lgkmcnt(4)\n"
"v_pk_fma_f32 v[152:153],v[152:153],v[160:161],v[168:169]\n"
"v_pk_fma_f32 v[154:155],v[154:155],v[162:163],v[170:171]\n"
"v_pk_fma_f32 v[156:157],v[156:157],v[164:165],v[172:173]\n"
"v_pk_fma_f32 v[158:159],v[158:159],v[166:167],v[174:175]\n"
"v_cvt_pk_bf16_f32 v64,v152,v153\n"
"v_cvt_pk_bf16_f32 v65,v154,v155\n"
"v_cvt_pk_bf16_f32 v66,v156,v157\n"
"v_cvt_pk_bf16_f32 v67,v158,v159\n"
"buffer_load_dwordx4 v231,s[44:47],s78 offen offset:1024 lds\n"
"ds_read_b128 v[160:163],v242 offset:640\n"
"ds_read_b128 v[164:167],v242 offset:656\n"
"ds_read_b128 v[168:171],v242 offset:2944\n"
"ds_read_b128 v[172:175],v242 offset:2960\n"
"v_pk_fma_f32 v[200:201],v[200:201],v[222:223],v[220:221] op_sel_hi:[1,0,0]\n"
"v_pk_fma_f32 v[202:203],v[202:203],v[222:223],v[220:221] op_sel_hi:[1,0,0]\n"
"v_pk_fma_f32 v[204:205],v[204:205],v[222:223],v[220:221] op_sel_hi:[1,0,0]\n"
"v_pk_fma_f32 v[206:207],v[206:207],v[222:223],v[220:221] op_sel_hi:[1,0,0]\n"
"s_waitcnt lgkmcnt(4)\n"
"v_pk_fma_f32 v[200:201],v[200:201],v[176:177],v[184:185]\n"
"v_pk_fma_f32 v[202:203],v[202:203],v[178:179],v[186:187]\n"
"v_pk_fma_f32 v[204:205],v[204:205],v[180:181],v[188:189]\n"
"v_pk_fma_f32 v[206:207],v[206:207],v[182:183],v[190:191]\n"
"v_cvt_pk_bf16_f32 v68,v200,v201\n"
"v_cvt_pk_bf16_f32 v69,v202,v203\n"
"v_cvt_pk_bf16_f32 v70,v204,v205\n"
"v_cvt_pk_bf16_f32 v71,v206,v207\n"
"buffer_load_dwordx4 v216,s[44:47],s78 offen offset:2048 lds\n"
"buffer_load_dwordx4 v217,s[44:47],s78 offen offset:3072 lds\n"
"ds_read_b128 v[176:179],v242 offset:704\n"
"ds_read_b128 v[180:183],v242 offset:720\n"
"ds_read_b128 v[184:187],v242 offset:3008\n"
"ds_read_b128 v[188:191],v242 offset:3024\n"
"v_lshlrev_b32 v192,16,v72\n"
"v_and_b32 v193,s73,v72\n"
"v_lshlrev_b32 v194,16,v73\n"
"v_and_b32 v195,s73,v73\n"
"v_lshlrev_b32 v196,16,v74\n"
"v_and_b32 v197,s73,v74\n"
"v_lshlrev_b32 v198,16,v75\n"
"v_and_b32 v199,s73,v75\n"
"v_pk_fma_f32 v[192:193],v[192:193],v[222:223],v[220:221] op_sel_hi:[1,0,0]\n"
"v_pk_fma_f32 v[194:195],v[194:195],v[222:223],v[220:221] op_sel_hi:[1,0,0]\n"
"v_pk_fma_f32 v[196:197],v[196:197],v[222:223],v[220:221] op_sel_hi:[1,0,0]\n"
"v_pk_fma_f32 v[198:199],v[198:199],v[222:223],v[220:221] op_sel_hi:[1,0,0]\n"
"s_waitcnt lgkmcnt(4)\n"
"v_pk_fma_f32 v[192:193],v[192:193],v[160:161],v[168:169]\n"
"v_pk_fma_f32 v[194:195],v[194:195],v[162:163],v[170:171]\n"
"v_pk_fma_f32 v[196:197],v[196:197],v[164:165],v[172:173]\n"
"v_pk_fma_f32 v[198:199],v[198:199],v[166:167],v[174:175]\n"
"v_cvt_pk_bf16_f32 v72,v192,v193\n"
"v_cvt_pk_bf16_f32 v73,v194,v195\n"
"v_cvt_pk_bf16_f32 v74,v196,v197\n"
"v_cvt_pk_bf16_f32 v75,v198,v199\n"
"s_add_u32 m0,s66,81920\n"
"s_add_u32 s86,s78,256\n"
"buffer_load_dwordx4 v230,s[44:47],s86 offen offset:0 lds\n"
"buffer_load_dwordx4 v231,s[44:47],s86 offen offset:1024 lds\n"
"ds_read_b128 v[160:163],v242 offset:768\n"
"ds_read_b128 v[164:167],v242 offset:784\n"
"ds_read_b128 v[168:171],v242 offset:3072\n"
"ds_read_b128 v[172:175],v242 offset:3088\n"
"v_lshlrev_b32 v200,16,v76\n"
"v_and_b32 v201,s73,v76\n"
"v_lshlrev_b32 v202,16,v77\n"
"v_and_b32 v203,s73,v77\n"
"v_lshlrev_b32 v204,16,v78\n"
"v_and_b32 v205,s73,v78\n"
"v_lshlrev_b32 v206,16,v79\n"
"v_and_b32 v207,s73,v79\n"
"v_pk_fma_f32 v[200:201],v[200:201],v[222:223],v[220:221] op_sel_hi:[1,0,0]\n"
"v_pk_fma_f32 v[202:203],v[202:203],v[222:223],v[220:221] op_sel_hi:[1,0,0]\n"
"v_pk_fma_f32 v[204:205],v[204:205],v[222:223],v[220:221] op_sel_hi:[1,0,0]\n"
"v_pk_fma_f32 v[206:207],v[206:207],v[222:223],v[220:221] op_sel_hi:[1,0,0]\n"
"s_waitcnt lgkmcnt(4)\n"
"v_pk_fma_f32 v[200:201],v[200:201],v[176:177],v[184:185]\n"
"v_pk_fma_f32 v[202:203],v[202:203],v[178:179],v[186:187]\n"
"v_pk_fma_f32 v[204:205],v[204:205],v[180:181],v[188:189]\n"
"v_pk_fma_f32 v[206:207],v[206:207],v[182:183],v[190:191]\n"
"v_cvt_pk_bf16_f32 v76,v200,v201\n"
"v_cvt_pk_bf16_f32 v77,v202,v203\n"
"v_cvt_pk_bf16_f32 v78,v204,v205\n"
"v_cvt_pk_bf16_f32 v79,v206,v207\n"
"buffer_load_dwordx4 v216,s[44:47],s86 offen offset:2048 lds\n"
"ds_read_b128 v[176:179],v242 offset:832\n"
"ds_read_b128 v[180:183],v242 offset:848\n"
"ds_read_b128 v[184:187],v242 offset:3136\n"
"ds_read_b128 v[188:191],v242 offset:3152\n"
"v_lshlrev_b32 v192,16,v80\n"
"v_and_b32 v193,s73,v80\n"
"v_lshlrev_b32 v194,16,v81\n"
"v_and_b32 v195,s73,v81\n"
"v_lshlrev_b32 v196,16,v82\n"
"v_and_b32 v197,s73,v82\n"
"v_lshlrev_b32 v198,16,v83\n"
"v_and_b32 v199,s73,v83\n"
"v_pk_fma_f32 v[192:193],v[192:193],v[222:223],v[220:221] op_sel_hi:[1,0,0]\n"
"v_pk_fma_f32 v[194:195],v[194:195],v[222:223],v[220:221] op_sel_hi:[1,0,0]\n"
"v_pk_fma_f32 v[196:197],v[196:197],v[222:223],v[220:221] op_sel_hi:[1,0,0]\n"
"v_pk_fma_f32 v[198:199],v[198:199],v[222:223],v[220:221] op_sel_hi:[1,0,0]\n"
"s_waitcnt lgkmcnt(4)\n"
"v_pk_fma_f32 v[192:193],v[192:193],v[160:161],v[168:169]\n"
"v_pk_fma_f32 v[194:195],v[194:195],v[162:163],v[170:171]\n"
"v_pk_fma_f32 v[196:197],v[196:197],v[164:165],v[172:173]\n"
"v_pk_fma_f32 v[198:199],v[198:199],v[166:167],v[174:175]\n"
"v_cvt_pk_bf16_f32 v80,v192,v193\n"
"v_cvt_pk_bf16_f32 v81,v194,v195\n"
"v_cvt_pk_bf16_f32 v82,v196,v197\n"
"v_cvt_pk_bf16_f32 v83,v198,v199\n"
"buffer_load_dwordx4 v217,s[44:47],s86 offen offset:3072 lds\n"
"s_add_u32 m0,s66,114688\n"
"s_add_u32 s77,s77,128\n"
"s_cmp_eq_u32 s77,1152\n"
"s_cselect_b32 s77,0,s77\n"
"buffer_load_dwordx4 v208,s[40:43],s77 offen offset:0 lds\n"
"ds_read_b128 v[96:99],v212 offset:0\n"
"ds_read_b128 v[100:103],v213 offset:0\n"
"ds_read_b128 v[104:107],v214 offset:0\n"
"ds_read_b128 v[108:111],v215 offset:0\n"
"ds_read_b128 v[160:163],v242 offset:896\n"
"ds_read_b128 v[164:167],v242 offset:912\n"
"ds_read_b128 v[168:171],v242 offset:3200\n"
"ds_read_b128 v[172:175],v242 offset:3216\n"
"v_lshlrev_b32 v200,16,v84\n"
"v_and_b32 v201,s73,v84\n"
"v_lshlrev_b32 v202,16,v85\n"
"v_and_b32 v203,s73,v85\n"
"v_lshlrev_b32 v204,16,v86\n"
"v_and_b32 v205,s73,v86\n"
"v_lshlrev_b32 v206,16,v87\n"
"v_and_b32 v207,s73,v87\n"
"v_pk_fma_f32 v[200:201],v[200:201],v[222:223],v[220:221] op_sel_hi:[1,0,0]\n"
"v_pk_fma_f32 v[202:203],v[202:203],v[222:223],v[220:221] op_sel_hi:[1,0,0]\n"
"v_pk_fma_f32 v[204:205],v[204:205],v[222:223],v[220:221] op_sel_hi:[1,0,0]\n"
"v_pk_fma_f32 v[206:207],v[206:207],v[222:223],v[220:221] op_sel_hi:[1,0,0]\n"
"s_waitcnt lgkmcnt(8)\n"
"v_pk_fma_f32 v[200:201],v[200:201],v[176:177],v[184:185]\n"
"v_pk_fma_f32 v[202:203],v[202:203],v[178:179],v[186:187]\n"
"v_pk_fma_f32 v[204:205],v[204:205],v[180:181],v[188:189]\n"
"v_pk_fma_f32 v[206:207],v[206:207],v[182:183],v[190:191]\n"
"v_cvt_pk_bf16_f32 v84,v200,v201\n"
"v_cvt_pk_bf16_f32 v85,v202,v203\n"
"v_cvt_pk_bf16_f32 v86,v204,v205\n"
"v_cvt_pk_bf16_f32 v87,v206,v207\n"
"buffer_load_dwordx4 v209,s[40:43],s77 offen offset:1024 lds\n"
"buffer_load_dwordx4 v210,s[40:43],s77 offen offset:2048 lds\n"
"ds_read_b128 v[176:179],v242 offset:960\n"
"ds_read_b128 v[180:183],v242 offset:976\n"
"ds_read_b128 v[184:187],v242 offset:3264\n"
"ds_read_b128 v[188:191],v242 offset:3280\n"
"v_lshlrev_b32 v192,16,v88\n"
"v_and_b32 v193,s73,v88\n"
"v_lshlrev_b32 v194,16,v89\n"
"v_and_b32 v195,s73,v89\n"
"v_lshlrev_b32 v196,16,v90\n"
"v_and_b32 v197,s73,v90\n"
"v_lshlrev_b32 v198,16,v91\n"
"v_and_b32 v199,s73,v91\n"
"v_pk_fma_f32 v[192:193],v[192:193],v[222:223],v[220:221] op_sel_hi:[1,0,0]\n"
"v_pk_fma_f32 v[194:195],v[194:195],v[222:223],v[220:221] op_sel_hi:[1,0,0]\n"
"v_pk_fma_f32 v[196:197],v[196:197],v[222:223],v[220:221] op_sel_hi:[1,0,0]\n"
"v_pk_fma_f32 v[198:199],v[198:199],v[222:223],v[220:221] op_sel_hi:[1,0,0]\n"
"s_waitcnt lgkmcnt(4)\n"
"v_pk_fma_f32 v[192:193],v[192:193],v[160:161],v[168:169]\n"
"v_pk_fma_f32 v[194:195],v[194:195],v[162:163],v[170:171]\n"
"v_pk_fma_f32 v[196:197],v[196:197],v[164:165],v[172:173]\n"
"v_pk_fma_f32 v[198:199],v[198:199],v[166:167],v[174:175]\n"
"v_cvt_pk_bf16_f32 v88,v192,v193\n"
"v_cvt_pk_bf16_f32 v89,v194,v195\n"
"v_cvt_pk_bf16_f32 v90,v196,v197\n"
"v_cvt_pk_bf16_f32 v91,v198,v199\n"
"buffer_load_dwordx4 v211,s[40:43],s77 offen offset:3072 lds\n"
"s_waitcnt vmcnt(24)\n"
"s_barrier\n"
"ds_read_b64_tr_b16 a[128:129],v233 offset:0\n"
"ds_read_b64_tr_b16 a[130:131],v233 offset:1024\n"
"ds_read_b64_tr_b16 a[144:145],v233 offset:16384\n"
"ds_read_b64_tr_b16 a[146:147],v233 offset:17408\n"
"ds_read_b64_tr_b16 a[132:133],v233 offset:256\n"
"ds_read_b64_tr_b16 a[134:135],v233 offset:1280\n"
"ds_read_b64_tr_b16 a[148:149],v233 offset:16640\n"
"ds_read_b64_tr_b16 a[150:151],v233 offset:17664\n"
"ds_read_b64_tr_b16 a[136:137],v233 offset:512\n"
"ds_read_b64_tr_b16 a[138:139],v233 offset:1536\n"
"ds_read_b64_tr_b16 a[152:153],v233 offset:16896\n"
"ds_read_b64_tr_b16 a[154:155],v233 offset:17920\n"
"ds_read_b64_tr_b16 a[140:141],v233 offset:768\n"
"ds_read_b64_tr_b16 a[142:143],v233 offset:1792\n"
"ds_read_b64_tr_b16 a[156:157],v233 offset:17152\n"
"ds_read_b64_tr_b16 a[158:159],v233 offset:18176\n"
"ds_read_b32 v160,v223 offset:1024\n"
"ds_read_b128 v[168:171],v242 offset:3328\n"
"ds_read_b128 v[172:175],v242 offset:3344\n"
"v_lshlrev_b32 v200,16,v92\n"
"v_and_b32 v201,s73,v92\n"
"L_pgg32_%=:\n"
"s_cbranch_execz L_pgg96_%=\n"
"v_lshlrev_b32 v202,16,v93\n"
"v_and_b32 v203,s73,v93\n"
"v_lshlrev_b32 v204,16,v94\n"
"v_and_b32 v205,s73,v94\n"
"v_lshlrev_b32 v206,16,v95\n"
"v_and_b32 v207,s73,v95\n"
"v_pk_fma_f32 v[200:201],v[200:201],v[222:223],v[220:221] op_sel_hi:[1,0,0]\n"
"v_pk_fma_f32 v[202:203],v[202:203],v[222:223],v[220:221] op_sel_hi:[1,0,0]\n"
"v_pk_fma_f32 v[204:205],v[204:205],v[222:223],v[220:221] op_sel_hi:[1,0,0]\n"
"v_pk_fma_f32 v[206:207],v[206:207],v[222:223],v[220:221] op_sel_hi:[1,0,0]\n"
"s_waitcnt lgkmcnt(15)\n"
"v_pk_fma_f32 v[200:201],v[200:201],v[176:177],v[184:185]\n"
"v_pk_fma_f32 v[202:203],v[202:203],v[178:179],v[186:187]\n"
"v_pk_fma_f32 v[204:205],v[204:205],v[180:181],v[188:189]\n"
"v_pk_fma_f32 v[206:207],v[206:207],v[182:183],v[190:191]\n"
"v_cvt_pk_bf16_f32 v92,v200,v201\n"
"v_cvt_pk_bf16_f32 v93,v202,v203\n"
"v_cvt_pk_bf16_f32 v94,v204,v205\n"
"v_cvt_pk_bf16_f32 v95,v206,v207\n"
"s_add_u32 m0,s66,131072\n"
"s_add_u32 s77,s77,128\n"
"s_cmp_eq_u32 s77,1152\n"
"s_cselect_b32 s77,0,s77\n"
"buffer_load_dwordx4 v208,s[40:43],s77 offen offset:0 lds\n"
"buffer_load_dwordx4 v209,s[40:43],s77 offen offset:1024 lds\n"
"ds_read_b64_tr_b16 a[160:161],v233 offset:4096\n"
"ds_read_b64_tr_b16 a[162:163],v233 offset:5120\n"
"ds_read_b64_tr_b16 a[176:177],v233 offset:20480\n"
"ds_read_b64_tr_b16 a[178:179],v233 offset:21504\n"
"ds_read_b64_tr_b16 a[164:165],v233 offset:4352\n"
"ds_read_b64_tr_b16 a[166:167],v233 offset:5376\n"
"ds_read_b64_tr_b16 a[180:181],v233 offset:20736\n"
"ds_read_b64_tr_b16 a[182:183],v233 offset:21760\n"
"ds_read_b64_tr_b16 a[168:169],v233 offset:4608\n"
"ds_read_b64_tr_b16 a[170:171],v233 offset:5632\n"
"ds_read_b64_tr_b16 a[184:185],v233 offset:20992\n"
"ds_read_b64_tr_b16 a[186:187],v233 offset:22016\n"
"ds_read_b64_tr_b16 a[172:173],v233 offset:4864\n"
"ds_read_b64_tr_b16 a[174:175],v233 offset:5888\n"
"ds_read_b64_tr_b16 a[188:189],v233 offset:21248\n"
"ds_read_b64_tr_b16 a[190:191],v233 offset:22272\n"
"ds_read_b128 v[184:187],v242 offset:3392\n"
"ds_read_b128 v[188:191],v242 offset:3408\n"
"v_lshlrev_b32 v192,16,v96\n"
"v_and_b32 v193,s73,v96\n"
"v_lshlrev_b32 v194,16,v97\n"
"v_and_b32 v195,s73,v97\n"
"v_lshlrev_b32 v196,16,v98\n"
"v_and_b32 v197,s73,v98\n"
"v_lshlrev_b32 v198,16,v99\n"
"v_and_b32 v199,s73,v99\n"
"v_pk_fma_f32 v[192:193],v[192:193],v[222:223],v[220:221] op_sel_hi:[1,0,0]\n"
"v_pk_fma_f32 v[194:195],v[194:195],v[222:223],v[220:221] op_sel_hi:[1,0,0]\n"
"v_pk_fma_f32 v[196:197],v[196:197],v[222:223],v[220:221] op_sel_hi:[1,0,0]\n"
"v_pk_fma_f32 v[198:199],v[198:199],v[222:223],v[220:221] op_sel_hi:[1,0,0]\n"
"s_waitcnt lgkmcnt(15)\n"
"v_fmac_f32_dpp v168,v160,v192 row_newbcast:0 row_mask:0xf bank_mask:0xf\n"
"v_fmac_f32_dpp v169,v160,v193 row_newbcast:1 row_mask:0xf bank_mask:0xf\n"
"v_fmac_f32_dpp v170,v160,v194 row_newbcast:2 row_mask:0xf bank_mask:0xf\n"
"v_fmac_f32_dpp v171,v160,v195 row_newbcast:3 row_mask:0xf bank_mask:0xf\n"
"v_fmac_f32_dpp v172,v160,v196 row_newbcast:4 row_mask:0xf bank_mask:0xf\n"
"v_fmac_f32_dpp v173,v160,v197 row_newbcast:5 row_mask:0xf bank_mask:0xf\n"
"v_fmac_f32_dpp v174,v160,v198 row_newbcast:6 row_mask:0xf bank_mask:0xf\n"
"v_fmac_f32_dpp v175,v160,v199 row_newbcast:7 row_mask:0xf bank_mask:0xf\n"
"v_cvt_pk_bf16_f32 v96,v168,v169\n"
"v_cvt_pk_bf16_f32 v97,v170,v171\n"
"v_cvt_pk_bf16_f32 v98,v172,v173\n"
"v_cvt_pk_bf16_f32 v99,v174,v175\n"
"buffer_load_dwordx4 v210,s[40:43],s77 offen offset:2048 lds\n"
"buffer_load_dwordx4 v211,s[40:43],s77 offen offset:3072 lds\n"
"s_waitcnt vmcnt(16)\n"
"ds_read_b128 v[112:115],v243 offset:32768\n"
"ds_read_b128 v[116:119],v244 offset:32768\n"
"ds_read_b128 v[120:123],v245 offset:32768\n"
"ds_read_b128 v[124:127],v255 offset:32768\n"
"v_mfma_f32_32x32x16_bf16 a[0:15],a[128:131],v[32:35],0\n"
"ds_read_b64_tr_b16 a[192:193],v233 offset:8192\n"
"ds_read_b64_tr_b16 a[194:195],v233 offset:9216\n"
"ds_read_b32 v161,v223 offset:1152\n"
"ds_read_b128 v[168:171],v242 offset:3456\n"
"ds_read_b128 v[172:175],v242 offset:3472\n"
"v_mfma_f32_32x32x16_bf16 a[16:31],a[144:147],v[32:35],0\n"
"ds_read_b64_tr_b16 a[208:209],v233 offset:24576\n"
"ds_read_b64_tr_b16 a[210:211],v233 offset:25600\n"
"v_lshlrev_b32 v200,16,v100\n"
"v_and_b32 v201,s73,v100\n"
"v_lshlrev_b32 v202,16,v101\n"
"v_and_b32 v203,s73,v101\n"
"v_mfma_f32_32x32x16_bf16 a[32:47],a[132:135],v[32:35],0\n"
"ds_read_b64_tr_b16 a[196:197],v233 offset:8448\n"
"ds_read_b64_tr_b16 a[198:199],v233 offset:9472\n"
"v_lshlrev_b32 v204,16,v102\n"
"v_and_b32 v205,s73,v102\n"
"v_lshlrev_b32 v206,16,v103\n"
"v_and_b32 v207,s73,v103\n"
"v_mfma_f32_32x32x16_bf16 a[48:63],a[148:151],v[32:35],0\n"
"ds_read_b64_tr_b16 a[212:213],v233 offset:24832\n"
"ds_read_b64_tr_b16 a[214:215],v233 offset:25856\n"
"v_fma_f32 v200,v200,v222,v220\n"
"v_fma_f32 v201,v201,v222,v220\n"
"v_fma_f32 v202,v202,v222,v220\n"
"v_fma_f32 v203,v203,v222,v220\n"
"v_mfma_f32_32x32x16_bf16 a[64:79],a[136:139],v[32:35],0\n"
"ds_read_b64_tr_b16 a[200:201],v233 offset:8704\n"
"ds_read_b64_tr_b16 a[202:203],v233 offset:9728\n"
"v_fma_f32 v204,v204,v222,v220\n"
"v_fma_f32 v205,v205,v222,v220\n"
"v_fma_f32 v206,v206,v222,v220\n"
"v_mfma_f32_32x32x16_bf16 a[80:95],a[152:155],v[32:35],0\n"
"ds_read_b64_tr_b16 a[216:217],v233 offset:25088\n"
"ds_read_b64_tr_b16 a[218:219],v233 offset:26112\n"
"v_fma_f32 v207,v207,v222,v220\n"
"s_waitcnt lgkmcnt(15)\n"
"v_fmac_f32_dpp v184,v160,v200 row_newbcast:8 row_mask:0xf bank_mask:0xf\n"
"v_fmac_f32_dpp v185,v160,v201 row_newbcast:9 row_mask:0xf bank_mask:0xf\n"
"v_mfma_f32_32x32x16_bf16 a[96:111],a[140:143],v[32:35],0\n"
"ds_read_b64_tr_b16 a[204:205],v233 offset:8960\n"
"ds_read_b64_tr_b16 a[206:207],v233 offset:9984\n"
"v_fmac_f32_dpp v186,v160,v202 row_newbcast:10 row_mask:0xf bank_mask:0xf\n"
"v_fmac_f32_dpp v187,v160,v203 row_newbcast:11 row_mask:0xf bank_mask:0xf\n"
"v_fmac_f32_dpp v188,v160,v204 row_newbcast:12 row_mask:0xf bank_mask:0xf\n"
"v_fmac_f32_dpp v189,v160,v205 row_newbcast:13 row_mask:0xf bank_mask:0xf\n"
"v_mfma_f32_32x32x16_bf16 a[112:127],a[156:159],v[32:35],0\n"
"ds_read_b64_tr_b16 a[220:221],v233 offset:25344\n"
"ds_read_b64_tr_b16 a[222:223],v233 offset:26368\n"
"v_fmac_f32_dpp v190,v160,v206 row_newbcast:14 row_mask:0xf bank_mask:0xf\n"
"v_fmac_f32_dpp v191,v160,v207 row_newbcast:15 row_mask:0xf bank_mask:0xf\n"
"v_cvt_pk_bf16_f32 v100,v184,v185\n"
"v_cvt_pk_bf16_f32 v101,v186,v187\n"
"v_mfma_f32_32x32x16_bf16 a[0:15],a[160:163],v[36:39],a[0:15]\n"
"ds_read_b64_tr_b16 a[128:129],v233 offset:12288\n"
"ds_read_b64_tr_b16 a[130:131],v233 offset:13312\n"
"ds_read_b64_tr_b16 a[144:145],v233 offset:28672\n"
"s_waitcnt vmcnt(4)\n"
"ds_read_b128 v[128:131],v243 offset:49152\n"
"v_mfma_f32_32x32x16_bf16 a[16:31],a[176:179],v[36:39],a[16:31]\n"
"ds_read_b64_tr_b16 a[146:147],v233 offset:29696\n"
"ds_read_b64_tr_b16 a[132:133],v233 offset:12544\n"
"ds_read_b128 v[132:135],v244 offset:49152\n"
"ds_read_b128 v[136:139],v245 offset:49152\n"
"v_cvt_pk_bf16_f32 v102,v188,v189\n"
"v_cvt_pk_bf16_f32 v103,v190,v191\n"
"v_mfma_f32_32x32x16_bf16 a[32:47],a[164:167],v[36:39],a[32:47]\n"
"ds_read_b64_tr_b16 a[134:135],v233 offset:13568\n"
"ds_read_b64_tr_b16 a[148:149],v233 offset:28928\n"
"ds_read_b128 v[140:143],v255 offset:49152\n"
"ds_read_b128 v[184:187],v242 offset:3520\n"
"ds_read_b128 v[188:191],v242 offset:3536\n"
"v_lshlrev_b32 v192,16,v104\n"
"v_mfma_f32_32x32x16_bf16 a[48:63],a[180:183],v[36:39],a[48:63]\n"
"ds_read_b64_tr_b16 a[150:151],v233 offset:29952\n"
"ds_read_b64_tr_b16 a[136:137],v233 offset:12800\n"
"ds_read_b64_tr_b16 a[138:139],v233 offset:13824\n"
"v_and_b32 v193,s73,v104\n"
"v_lshlrev_b32 v194,16,v105\n"
"v_and_b32 v195,s73,v105\n"
"v_mfma_f32_32x32x16_bf16 a[64:79],a[168:171],v[36:39],a[64:79]\n"
"ds_read_b64_tr_b16 a[152:153],v233 offset:29184\n"
"ds_read_b64_tr_b16 a[154:155],v233 offset:30208\n"
"v_lshlrev_b32 v196,16,v106\n"
"v_and_b32 v197,s73,v106\n"
"v_lshlrev_b32 v198,16,v107\n"
"v_mfma_f32_32x32x16_bf16 a[80:95],a[184:187],v[36:39],a[80:95]\n"
"ds_read_b64_tr_b16 a[140:141],v233 offset:13056\n"
"ds_read_b64_tr_b16 a[142:143],v233 offset:14080\n"
"v_and_b32 v199,s73,v107\n"
"v_fma_f32 v192,v192,v222,v220\n"
"v_fma_f32 v193,v193,v222,v220\n"
"v_fma_f32 v194,v194,v222,v220\n"
"v_mfma_f32_32x32x16_bf16 a[96:111],a[172:175],v[36:39],a[96:111]\n"
"ds_read_b64_tr_b16 a[156:157],v233 offset:29440\n"
"ds_read_b64_tr_b16 a[158:159],v233 offset:30464\n"
"s_waitcnt lgkmcnt(11)\n"
"s_add_u32 m0,s66,98304\n"
"s_add_u32 s78,s78,524288\n"
"s_cmp_eq_u32 s78,4718592\n"
"s_cselect_b32 s78,0,s78\n"
"v_mfma_f32_32x32x16_bf16 a[112:127],a[188:191],v[36:39],a[112:127]\n"
"buffer_load_dwordx4 v230,s[44:47],s78 offen offset:0 lds\n"
"v_fma_f32 v195,v195,v222,v220\n"
"v_fma_f32 v196,v196,v222,v220\n"
"v_fma_f32 v197,v197,v222,v220\n"
"v_fma_f32 v198,v198,v222,v220\n"
"v_mfma_f32_32x32x16_bf16 a[0:15],a[192:195],v[40:43],a[0:15]\n"
"v_fma_f32 v199,v199,v222,v220\n"
"v_fmac_f32_dpp v168,v161,v192 row_newbcast:0 row_mask:0xf bank_mask:0xf\n"
"v_fmac_f32_dpp v169,v161,v193 row_newbcast:1 row_mask:0xf bank_mask:0xf\n"
"v_fmac_f32_dpp v170,v161,v194 row_newbcast:2 row_mask:0xf bank_mask:0xf\n"
"v_mfma_f32_32x32x16_bf16 a[16:31],a[208:211],v[40:43],a[16:31]\n"
"s_waitcnt lgkmcnt(0)\n"
"s_barrier\n"
"v_add_u32 v233,0,v232\n"
"v_fmac_f32_dpp v171,v161,v195 row_newbcast:3 row_mask:0xf bank_mask:0xf\n"
"v_fmac_f32_dpp v172,v161,v196 row_newbcast:4 row_mask:0xf bank_mask:0xf\n"
"v_mfma_f32_32x32x16_bf16 a[32:47],a[196:199],v[40:43],a[32:47]\n"
"ds_read_b64_tr_b16 a[160:161],v233 offset:0\n"
"ds_read_b64_tr_b16 a[162:163],v233 offset:1024\n"
"ds_read_b64_tr_b16 a[176:177],v233 offset:16384\n"
"buffer_load_dwordx4 v231,s[44:47],s78 offen offset:1024 lds\n"
"v_fmac_f32_dpp v173,v161,v197 row_newbcast:5 row_mask:0xf bank_mask:0xf\n"
"v_mfma_f32_32x32x16_bf16 a[48:63],a[212:215],v[40:43],a[48:63]\n"
"ds_read_b64_tr_b16 a[178:179],v233 offset:17408\n"
"ds_read_b64_tr_b16 a[164:165],v233 offset:256\n"
"ds_read_b64_tr_b16 a[166:167],v233 offset:1280\n"
"v_fmac_f32_dpp v174,v161,v198 row_newbcast:6 row_mask:0xf bank_mask:0xf\n"
"v_fmac_f32_dpp v175,v161,v199 row_newbcast:7 row_mask:0xf bank_mask:0xf\n"
"v_cvt_pk_bf16_f32 v104,v168,v169\n"
"v_mfma_f32_32x32x16_bf16 a[64:79],a[200:203],v[40:43],a[64:79]\n"
"ds_read_b64_tr_b16 a[180:181],v233 offset:16640\n"
"ds_read_b64_tr_b16 a[182:183],v233 offset:17664\n"
"v_cvt_pk_bf16_f32 v105,v170,v171\n"
"v_cvt_pk_bf16_f32 v106,v172,v173\n"
"v_cvt_pk_bf16_f32 v107,v174,v175\n"
"v_mfma_f32_32x32x16_bf16 a[80:95],a[216:219],v[40:43],a[80:95]\n"
"ds_read_b64_tr_b16 a[168:169],v233 offset:512\n"
"ds_read_b64_tr_b16 a[170:171],v233 offset:1536\n"
"ds_read_b64_tr_b16 a[184:185],v233 offset:16896\n"
"buffer_load_dwordx4 v216,s[44:47],s78 offen offset:2048 lds\n"
"ds_read_b32 v160,v223 offset:1280\n"
"v_mfma_f32_32x32x16_bf16 a[96:111],a[204:207],v[40:43],a[96:111]\n"
"ds_read_b64_tr_b16 a[186:187],v233 offset:17920\n"
"ds_read_b64_tr_b16 a[172:173],v233 offset:768\n"
"ds_read_b64_tr_b16 a[174:175],v233 offset:1792\n"
"ds_read_b128 v[168:171],v242 offset:3584\n"
"ds_read_b128 v[172:175],v242 offset:3600\n"
"v_lshlrev_b32 v200,16,v108\n"
"v_mfma_f32_32x32x16_bf16 a[112:127],a[220:223],v[40:43],a[112:127]\n"
"ds_read_b64_tr_b16 a[188:189],v233 offset:17152\n"
"ds_read_b64_tr_b16 a[190:191],v233 offset:18176\n"
"v_and_b32 v201,s73,v108\n"
"v_lshlrev_b32 v202,16,v109\n"
"v_and_b32 v203,s73,v109\n"
"v_lshlrev_b32 v204,16,v110\n"
"v_mfma_f32_32x32x16_bf16 a[0:15],a[128:131],v[44:47],a[0:15]\n"
"ds_read_b64_tr_b16 a[192:193],v233 offset:4096\n"
"ds_read_b64_tr_b16 a[194:195],v233 offset:5120\n"
"s_waitcnt vmcnt(3)\n"
"ds_read_b128 v[144:147],v212 offset:0\n"
"buffer_load_dwordx4 v217,s[44:47],s78 offen offset:3072 lds\n"
"v_mfma_f32_32x32x16_bf16 a[16:31],a[144:147],v[44:47],a[16:31]\n"
"ds_read_b64_tr_b16 a[208:209],v233 offset:20480\n"
"ds_read_b64_tr_b16 a[210:211],v233 offset:21504\n"
"ds_read_b128 v[148:151],v213 offset:0\n"
"ds_read_b128 v[152:155],v214 offset:0\n"
"v_and_b32 v205,s73,v110\n"
"v_lshlrev_b32 v206,16,v111\n"
"v_mfma_f32_32x32x16_bf16 a[32:47],a[132:135],v[44:47],a[32:47]\n"
"ds_read_b64_tr_b16 a[196:197],v233 offset:4352\n"
"ds_read_b64_tr_b16 a[198:199],v233 offset:5376\n"
"ds_read_b128 v[156:159],v215 offset:0\n"
"s_add_u32 m0,s66,114688\n"
"s_add_u32 s86,s78,256\n"
"v_and_b32 v207,s73,v111\n"
"v_mfma_f32_32x32x16_bf16 a[48:63],a[148:151],v[44:47],a[48:63]\n"
"ds_read_b64_tr_b16 a[212:213],v233 offset:20736\n"
"ds_read_b64_tr_b16 a[214:215],v233 offset:21760\n"
"buffer_load_dwordx4 v230,s[44:47],s86 offen offset:0 lds\n"
"v_fma_f32 v200,v200,v222,v220\n"
"v_fma_f32 v201,v201,v222,v220\n"
"v_mfma_f32_32x32x16_bf16 a[64:79],a[136:139],v[44:47],a[64:79]\n"
"ds_read_b64_tr_b16 a[200:201],v233 offset:4608\n"
"ds_read_b64_tr_b16 a[202:203],v233 offset:5632\n"
"v_fma_f32 v202,v202,v222,v220\n"
"v_fma_f32 v203,v203,v222,v220\n"
"v_fma_f32 v204,v204,v222,v220\n"
"v_mfma_f32_32x32x16_bf16 a[80:95],a[152:155],v[44:47],a[80:95]\n"
"ds_read_b64_tr_b16 a[216:217],v233 offset:20992\n"
"ds_read_b64_tr_b16 a[218:219],v233 offset:22016\n"
"v_fma_f32 v205,v205,v222,v220\n"
"v_fma_f32 v206,v206,v222,v220\n"
"v_fma_f32 v207,v207,v222,v220\n"
"v_mfma_f32_32x32x16_bf16 a[96:111],a[140:143],v[44:47],a[96:111]\n"
"ds_read_b64_tr_b16 a[204:205],v233 offset:4864\n"
"ds_read_b64_tr_b16 a[206:207],v233 offset:5888\n"
"buffer_load_dwordx4 v231,s[44:47],s86 offen offset:1024 lds\n"
"v_fmac_f32_dpp v184,v161,v200 row_newbcast:8 row_mask:0xf bank_mask:0xf\n"
"v_fmac_f32_dpp v185,v161,v201 row_newbcast:9 row_mask:0xf bank_mask:0xf\n"
"v_mfma_f32_32x32x16_bf16 a[112:127],a[156:159],v[44:47],a[112:127]\n"
"ds_read_b64_tr_b16 a[220:221],v233 offset:21248\n"
"ds_read_b64_tr_b16 a[222:223],v233 offset:22272\n"
"v_fmac_f32_dpp v186,v161,v202 row_newbcast:10 row_mask:0xf bank_mask:0xf\n"
"v_fmac_f32_dpp v187,v161,v203 row_newbcast:11 row_mask:0xf bank_mask:0xf\n"
"v_fmac_f32_dpp v188,v161,v204 row_newbcast:12 row_mask:0xf bank_mask:0xf\n"
"v_fmac_f32_dpp v189,v161,v205 row_newbcast:13 row_mask:0xf bank_mask:0xf\n"
"s_waitcnt lgkmcnt(15)\n"
"v_mfma_f32_32x32x16_bf16 a[0:15],a[160:163],v[48:51],a[0:15]\n"
"ds_read_b64_tr_b16 a[128:129],v233 offset:8192\n"
"ds_read_b64_tr_b16 a[130:131],v233 offset:9216\n"
"v_fmac_f32_dpp v190,v161,v206 row_newbcast:14 row_mask:0xf bank_mask:0xf\n"
"v_fmac_f32_dpp v191,v161,v207 row_newbcast:15 row_mask:0xf bank_mask:0xf\n"
"v_cvt_pk_bf16_f32 v108,v184,v185\n"
"v_mfma_f32_32x32x16_bf16 a[16:31],a[176:179],v[48:51],a[16:31]\n"
"ds_read_b64_tr_b16 a[144:145],v233 offset:24576\n"
"ds_read_b64_tr_b16 a[146:147],v233 offset:25600\n"
"buffer_load_dwordx4 v216,s[44:47],s86 offen offset:2048 lds\n"
"v_cvt_pk_bf16_f32 v109,v186,v187\n"
"v_cvt_pk_bf16_f32 v110,v188,v189\n"
"v_mfma_f32_32x32x16_bf16 a[32:47],a[164:167],v[48:51],a[32:47]\n"
"ds_read_b64_tr_b16 a[132:133],v233 offset:8448\n"
"ds_read_b64_tr_b16 a[134:135],v233 offset:9472\n"
"v_cvt_pk_bf16_f32 v111,v190,v191\n"
"ds_read_b128 v[184:187],v242 offset:3648\n"
"ds_read_b128 v[188:191],v242 offset:3664\n"
"v_mfma_f32_32x32x16_bf16 a[48:63],a[180:183],v[48:51],a[48:63]\n"
"ds_read_b64_tr_b16 a[148:149],v233 offset:24832\n"
"ds_read_b64_tr_b16 a[150:151],v233 offset:25856\n"
"v_lshlrev_b32 v192,16,v112\n"
"v_and_b32 v193,s73,v112\n"
"v_lshlrev_b32 v194,16,v113\n"
"v_and_b32 v195,s73,v113\n"
"v_mfma_f32_32x32x16_bf16 a[64:79],a[168:171],v[48:51],a[64:79]\n"
"ds_read_b64_tr_b16 a[136:137],v233 offset:8704\n"
"ds_read_b64_tr_b16 a[138:139],v233 offset:9728\n"
"buffer_load_dwordx4 v217,s[44:47],s86 offen offset:3072 lds\n"
"v_lshlrev_b32 v196,16,v114\n"
"v_mfma_f32_32x32x16_bf16 a[80:95],a[184:187],v[48:51],a[80:95]\n"
"ds_read_b64_tr_b16 a[152:153],v233 offset:25088\n"
"ds_read_b64_tr_b16 a[154:155],v233 offset:26112\n"
"v_and_b32 v197,s73,v114\n"
"v_lshlrev_b32 v198,16,v115\n"
"v_and_b32 v199,s73,v115\n"
"v_fma_f32 v192,v192,v222,v220\n"
"v_mfma_f32_32x32x16_bf16 a[96:111],a[172:175],v[48:51],a[96:111]\n"
"ds_read_b64_tr_b16 a[140:141],v233 offset:8960\n"
"ds_read_b64_tr_b16 a[142:143],v233 offset:9984\n"
"s_waitcnt lgkmcnt(15)\n"
"s_add_u32 m0,s66,131072\n"
"s_add_u32 s77,s77,128\n"
"s_cmp_eq_u32 s77,1152\n"
"s_cselect_b32 s77,0,s77\n"
"v_mfma_f32_32x32x16_bf16 a[112:127],a[188:191],v[48:51],a[112:127]\n"
"ds_read_b64_tr_b16 a[156:157],v233 offset:25344\n"
"ds_read_b64_tr_b16 a[158:159],v233 offset:26368\n"
"buffer_load_dwordx4 v208,s[40:43],s77 offen offset:0 lds\n"
"v_fma_f32 v193,v193,v222,v220\n"
"v_fma_f32 v194,v194,v222,v220\n"
"v_mfma_f32_32x32x16_bf16 a[0:15],a[192:195],v[52:55],a[0:15]\n"
"ds_read_b64_tr_b16 a[160:161],v233 offset:12288\n"
"ds_read_b64_tr_b16 a[162:163],v233 offset:13312\n"
"ds_read_b64_tr_b16 a[176:177],v233 offset:28672\n"
"v_fma_f32 v195,v195,v222,v220\n"
"v_fma_f32 v196,v196,v222,v220\n"
"v_mfma_f32_32x32x16_bf16 a[16:31],a[208:211],v[52:55],a[16:31]\n"
"ds_read_b64_tr_b16 a[178:179],v233 offset:29696\n"
"ds_read_b64_tr_b16 a[164:165],v233 offset:12544\n"
"v_fma_f32 v197,v197,v222,v220\n"
"v_fma_f32 v198,v198,v222,v220\n"
"v_fma_f32 v199,v199,v222,v220\n"
"v_mfma_f32_32x32x16_bf16 a[32:47],a[196:199],v[52:55],a[32:47]\n"
"ds_read_b64_tr_b16 a[166:167],v233 offset:13568\n"
"ds_read_b64_tr_b16 a[180:181],v233 offset:28928\n"
"buffer_load_dwordx4 v209,s[40:43],s77 offen offset:1024 lds\n"
"s_mov_b32 s61,0\n"
"buffer_load_dwordx4 v[32:35],v218,s[80:83],s61 offen\n"
"v_mfma_f32_32x32x16_bf16 a[48:63],a[212:215],v[52:55],a[48:63]\n"
"ds_read_b64_tr_b16 a[182:183],v233 offset:29952\n"
"ds_read_b64_tr_b16 a[168:169],v233 offset:12800\n"
"ds_read_b64_tr_b16 a[170:171],v233 offset:13824\n"
"v_fmac_f32_dpp v168,v160,v192 row_newbcast:0 row_mask:0xf bank_mask:0xf\n"
"v_fmac_f32_dpp v169,v160,v193 row_newbcast:1 row_mask:0xf bank_mask:0xf\n"
"v_fmac_f32_dpp v170,v160,v194 row_newbcast:2 row_mask:0xf bank_mask:0xf\n"
"v_mfma_f32_32x32x16_bf16 a[64:79],a[200:203],v[52:55],a[64:79]\n"
"ds_read_b64_tr_b16 a[184:185],v233 offset:29184\n"
"ds_read_b64_tr_b16 a[186:187],v233 offset:30208\n"
"v_fmac_f32_dpp v171,v160,v195 row_newbcast:3 row_mask:0xf bank_mask:0xf\n"
"v_fmac_f32_dpp v172,v160,v196 row_newbcast:4 row_mask:0xf bank_mask:0xf\n"
"v_fmac_f32_dpp v173,v160,v197 row_newbcast:5 row_mask:0xf bank_mask:0xf\n"
"v_mfma_f32_32x32x16_bf16 a[80:95],a[216:219],v[52:55],a[80:95]\n"
"ds_read_b64_tr_b16 a[172:173],v233 offset:13056\n"
"ds_read_b64_tr_b16 a[174:175],v233 offset:14080\n"
"buffer_load_dwordx4 v210,s[40:43],s77 offen offset:2048 lds\n"
"v_fmac_f32_dpp v174,v160,v198 row_newbcast:6 row_mask:0xf bank_mask:0xf\n"
"v_fmac_f32_dpp v175,v160,v199 row_newbcast:7 row_mask:0xf bank_mask:0xf\n"
"v_mfma_f32_32x32x16_bf16 a[96:111],a[204:207],v[52:55],a[96:111]\n"
"ds_read_b64_tr_b16 a[188:189],v233 offset:29440\n"
"ds_read_b64_tr_b16 a[190:191],v233 offset:30464\n"
"v_cvt_pk_bf16_f32 v112,v168,v169\n"
"v_cvt_pk_bf16_f32 v113,v170,v171\n"
"v_cvt_pk_bf16_f32 v114,v172,v173\n"
"v_cvt_pk_bf16_f32 v115,v174,v175\n"
"v_mfma_f32_32x32x16_bf16 a[112:127],a[220:223],v[52:55],a[112:127]\n"
"ds_read_b32 v161,v223 offset:1408\n"
"ds_read_b128 v[168:171],v242 offset:3712\n"
"ds_read_b128 v[172:175],v242 offset:3728\n"
"v_lshlrev_b32 v200,16,v116\n"
"v_and_b32 v201,s73,v116\n"
"v_lshlrev_b32 v202,16,v117\n"
"s_waitcnt lgkmcnt(15)\n"
"v_mfma_f32_32x32x16_bf16 a[0:15],a[128:131],v[56:59],a[0:15]\n"
"buffer_load_dwordx4 v211,s[40:43],s77 offen offset:3072 lds\n"
"v_and_b32 v203,s73,v117\n"
"v_lshlrev_b32 v204,16,v118\n"
"v_and_b32 v205,s73,v118\n"
"v_mfma_f32_32x32x16_bf16 a[16:31],a[144:147],v[56:59],a[16:31]\n"
"s_waitcnt lgkmcnt(0)\n"
"s_barrier\n"
"v_add_u32 v233,65536,v232\n"
"v_lshlrev_b32 v206,16,v119\n"
"v_and_b32 v207,s73,v119\n"
"v_mfma_f32_32x32x16_bf16 a[32:47],a[132:135],v[56:59],a[32:47]\n"
"ds_read_b64_tr_b16 a[192:193],v233 offset:0\n"
"ds_read_b64_tr_b16 a[194:195],v233 offset:1024\n"
"ds_read_b64_tr_b16 a[208:209],v233 offset:16384\n"
"s_add_u32 m0,s66,32768\n"
"s_add_u32 s78,s78,524288\n"
"s_cmp_eq_u32 s78,4718592\n"
"s_cselect_b32 s78,0,s78\n"
"v_mfma_f32_32x32x16_bf16 a[48:63],a[148:151],v[56:59],a[48:63]\n"
"ds_read_b64_tr_b16 a[210:211],v233 offset:17408\n"
"ds_read_b64_tr_b16 a[196:197],v233 offset:256\n"
"ds_read_b64_tr_b16 a[198:199],v233 offset:1280\n"
"buffer_load_dwordx4 v230,s[44:47],s78 offen offset:0 lds\n"
"v_fma_f32 v200,v200,v222,v220\n"
"v_mfma_f32_32x32x16_bf16 a[64:79],a[136:139],v[56:59],a[64:79]\n"
"ds_read_b64_tr_b16 a[212:213],v233 offset:16640\n"
"ds_read_b64_tr_b16 a[214:215],v233 offset:17664\n"
"v_fma_f32 v201,v201,v222,v220\n"
"v_fma_f32 v202,v202,v222,v220\n"
"v_fma_f32 v203,v203,v222,v220\n"
"v_mfma_f32_32x32x16_bf16 a[80:95],a[152:155],v[56:59],a[80:95]\n"
"ds_read_b64_tr_b16 a[200:201],v233 offset:512\n"
"ds_read_b64_tr_b16 a[202:203],v233 offset:1536\n"
"ds_read_b64_tr_b16 a[216:217],v233 offset:16896\n"
"v_fma_f32 v204,v204,v222,v220\n"
"v_fma_f32 v205,v205,v222,v220\n"
"v_fma_f32 v206,v206,v222,v220\n"
"v_mfma_f32_32x32x16_bf16 a[96:111],a[140:143],v[56:59],a[96:111]\n"
"ds_read_b64_tr_b16 a[218:219],v233 offset:17920\n"
"ds_read_b64_tr_b16 a[204:205],v233 offset:768\n"
"ds_read_b64_tr_b16 a[206:207],v233 offset:1792\n"
"buffer_load_dwordx4 v231,s[44:47],s78 offen offset:1024 lds\n"
"v_fma_f32 v207,v207,v222,v220\n"
"v_mfma_f32_32x32x16_bf16 a[112:127],a[156:159],v[56:59],a[112:127]\n"
"ds_read_b64_tr_b16 a[220:221],v233 offset:17152\n"
"ds_read_b64_tr_b16 a[222:223],v233 offset:18176\n"
"v_fmac_f32_dpp v184,v160,v200 row_newbcast:8 row_mask:0xf bank_mask:0xf\n"
"v_fmac_f32_dpp v185,v160,v201 row_newbcast:9 row_mask:0xf bank_mask:0xf\n"
"v_fmac_f32_dpp v186,v160,v202 row_newbcast:10 row_mask:0xf bank_mask:0xf\n"
"v_mfma_f32_32x32x16_bf16 a[0:15],a[160:163],v[60:63],a[0:15]\n"
"ds_read_b64_tr_b16 a[128:129],v233 offset:4096\n"
"ds_read_b64_tr_b16 a[130:131],v233 offset:5120\n"
"v_fmac_f32_dpp v187,v160,v203 row_newbcast:11 row_mask:0xf bank_mask:0xf\n"
"v_fmac_f32_dpp v188,v160,v204 row_newbcast:12 row_mask:0xf bank_mask:0xf\n"
"v_fmac_f32_dpp v189,v160,v205 row_newbcast:13 row_mask:0xf bank_mask:0xf\n"
"v_mfma_f32_32x32x16_bf16 a[16:31],a[176:179],v[60:63],a[16:31]\n"
"ds_read_b64_tr_b16 a[144:145],v233 offset:20480\n"
"ds_read_b64_tr_b16 a[146:147],v233 offset:21504\n"
"buffer_load_dwordx4 v216,s[44:47],s78 offen offset:2048 lds\n"
"v_fmac_f32_dpp v190,v160,v206 row_newbcast:14 row_mask:0xf bank_mask:0xf\n"
"v_fmac_f32_dpp v191,v160,v207 row_newbcast:15 row_mask:0xf bank_mask:0xf\n"
"v_mfma_f32_32x32x16_bf16 a[32:47],a[164:167],v[60:63],a[32:47]\n"
"ds_read_b64_tr_b16 a[132:133],v233 offset:4352\n"
"ds_read_b64_tr_b16 a[134:135],v233 offset:5376\n"
"v_cvt_pk_bf16_f32 v116,v184,v185\n"
"v_cvt_pk_bf16_f32 v117,v186,v187\n"
"v_cvt_pk_bf16_f32 v118,v188,v189\n"
"v_cvt_pk_bf16_f32 v119,v190,v191\n"
"v_mfma_f32_32x32x16_bf16 a[48:63],a[180:183],v[60:63],a[48:63]\n"
"ds_read_b64_tr_b16 a[148:149],v233 offset:20736\n"
"ds_read_b64_tr_b16 a[150:151],v233 offset:21760\n"
"ds_read_b128 v[184:187],v242 offset:3776\n"
"ds_read_b128 v[188:191],v242 offset:3792\n"
"v_lshlrev_b32 v192,16,v120\n"
"v_and_b32 v193,s73,v120\n"
"v_mfma_f32_32x32x16_bf16 a[64:79],a[168:171],v[60:63],a[64:79]\n"
"ds_read_b64_tr_b16 a[136:137],v233 offset:4608\n"
"ds_read_b64_tr_b16 a[138:139],v233 offset:5632\n"
"buffer_load_dwordx4 v217,s[44:47],s78 offen offset:3072 lds\n"
"v_lshlrev_b32 v194,16,v121\n"
"v_mfma_f32_32x32x16_bf16 a[80:95],a[184:187],v[60:63],a[80:95]\n"
"ds_read_b64_tr_b16 a[152:153],v233 offset:20992\n"
"ds_read_b64_tr_b16 a[154:155],v233 offset:22016\n"
"v_and_b32 v195,s73,v121\n"
"v_lshlrev_b32 v196,16,v122\n"
"v_and_b32 v197,s73,v122\n"
"v_lshlrev_b32 v198,16,v123\n"
"v_mfma_f32_32x32x16_bf16 a[96:111],a[172:175],v[60:63],a[96:111]\n"
"ds_read_b64_tr_b16 a[140:141],v233 offset:4864\n"
"ds_read_b64_tr_b16 a[142:143],v233 offset:5888\n"
"s_add_u32 m0,s66,49152\n"
"s_add_u32 s86,s78,256\n"
"v_and_b32 v199,s73,v123\n"
"v_fma_f32 v192,v192,v222,v220\n"
"v_mfma_f32_32x32x16_bf16 a[112:127],a[188:191],v[60:63],a[112:127]\n"
"ds_read_b64_tr_b16 a[156:157],v233 offset:21248\n"
"ds_read_b64_tr_b16 a[158:159],v233 offset:22272\n"
"buffer_load_dwordx4 v230,s[44:47],s86 offen offset:0 lds\n"
"s_mov_b32 s61,221184\n"
"buffer_load_dwordx4 v[36:39],v218,s[80:83],s61 offen\n"
"s_waitcnt lgkmcnt(15)\n"
"v_mfma_f32_32x32x16_bf16 a[0:15],a[192:195],v[64:67],a[0:15]\n"
"ds_read_b64_tr_b16 a[160:161],v233 offset:8192\n"
"ds_read_b64_tr_b16 a[162:163],v233 offset:9216\n"
"v_fma_f32 v193,v193,v222,v220\n"
"v_fma_f32 v194,v194,v222,v220\n"
"v_fma_f32 v195,v195,v222,v220\n"
"v_mfma_f32_32x32x16_bf16 a[16:31],a[208:211],v[64:67],a[16:31]\n"
"ds_read_b64_tr_b16 a[176:177],v233 offset:24576\n"
"ds_read_b64_tr_b16 a[178:179],v233 offset:25600\n"
"v_fma_f32 v196,v196,v222,v220\n"
"v_fma_f32 v197,v197,v222,v220\n"
"v_fma_f32 v198,v198,v222,v220\n"
"v_fma_f32 v199,v199,v222,v220\n"
"v_mfma_f32_32x32x16_bf16 a[32:47],a[196:199],v[64:67],a[32:47]\n"
"ds_read_b64_tr_b16 a[164:165],v233 offset:8448\n"
"ds_read_b64_tr_b16 a[166:167],v233 offset:9472\n"
"buffer_load_dwordx4 v231,s[44:47],s86 offen offset:1024 lds\n"
"v_fmac_f32_dpp v168,v161,v192 row_newbcast:0 row_mask:0xf bank_mask:0xf\n"
"v_mfma_f32_32x32x16_bf16 a[48:63],a[212:215],v[64:67],a[48:63]\n"
"ds_read_b64_tr_b16 a[180:181],v233 offset:24832\n"
"ds_read_b64_tr_b16 a[182:183],v233 offset:25856\n"
"v_fmac_f32_dpp v169,v161,v193 row_newbcast:1 row_mask:0xf bank_mask:0xf\n"
"v_fmac_f32_dpp v170,v161,v194 row_newbcast:2 row_mask:0xf bank_mask:0xf\n"
"v_fmac_f32_dpp v171,v161,v195 row_newbcast:3 row_mask:0xf bank_mask:0xf\n"
"v_fmac_f32_dpp v172,v161,v196 row_newbcast:4 row_mask:0xf bank_mask:0xf\n"
"v_mfma_f32_32x32x16_bf16 a[64:79],a[200:203],v[64:67],a[64:79]\n"
"ds_read_b64_tr_b16 a[168:169],v233 offset:8704\n"
"ds_read_b64_tr_b16 a[170:171],v233 offset:9728\n"
"v_fmac_f32_dpp v173,v161,v197 row_newbcast:5 row_mask:0xf bank_mask:0xf\n"
"v_fmac_f32_dpp v174,v161,v198 row_newbcast:6 row_mask:0xf bank_mask:0xf\n"
"v_fmac_f32_dpp v175,v161,v199 row_newbcast:7 row_mask:0xf bank_mask:0xf\n"
"v_mfma_f32_32x32x16_bf16 a[80:95],a[216:219],v[64:67],a[80:95]\n"
"ds_read_b64_tr_b16 a[184:185],v233 offset:25088\n"
"ds_read_b64_tr_b16 a[186:187],v233 offset:26112\n"
"buffer_load_dwordx4 v216,s[44:47],s86 offen offset:2048 lds\n"
"v_cvt_pk_bf16_f32 v120,v168,v169\n"
"v_cvt_pk_bf16_f32 v121,v170,v171\n"
"v_mfma_f32_32x32x16_bf16 a[96:111],a[204:207],v[64:67],a[96:111]\n"
"ds_read_b64_tr_b16 a[172:173],v233 offset:8960\n"
"ds_read_b64_tr_b16 a[174:175],v233 offset:9984\n"
"v_cvt_pk_bf16_f32 v122,v172,v173\n"
"v_cvt_pk_bf16_f32 v123,v174,v175\n"
"ds_read_b32 v160,v223 offset:1536\n"
"ds_read_b128 v[168:171],v242 offset:3840\n"
"v_mfma_f32_32x32x16_bf16 a[112:127],a[220:223],v[64:67],a[112:127]\n"
"ds_read_b64_tr_b16 a[188:189],v233 offset:25344\n"
"ds_read_b64_tr_b16 a[190:191],v233 offset:26368\n"
"ds_read_b128 v[172:175],v242 offset:3856\n"
"v_lshlrev_b32 v200,16,v124\n"
"v_and_b32 v201,s73,v124\n"
"v_lshlrev_b32 v202,16,v125\n"
"s_waitcnt lgkmcnt(15)\n"
"v_mfma_f32_32x32x16_bf16 a[0:15],a[128:131],v[68:71],a[0:15]\n"
"ds_read_b64_tr_b16 a[192:193],v233 offset:12288\n"
"ds_read_b64_tr_b16 a[194:195],v233 offset:13312\n"
"ds_read_b64_tr_b16 a[208:209],v233 offset:28672\n"
"buffer_load_dwordx4 v217,s[44:47],s86 offen offset:3072 lds\n"
"v_mfma_f32_32x32x16_bf16 a[16:31],a[144:147],v[68:71],a[16:31]\n"
"ds_read_b64_tr_b16 a[210:211],v233 offset:29696\n"
"ds_read_b64_tr_b16 a[196:197],v233 offset:12544\n"
"v_and_b32 v203,s73,v125\n"
"v_lshlrev_b32 v204,16,v126\n"
"v_and_b32 v205,s73,v126\n"
"v_lshlrev_b32 v206,16,v127\n"
"v_mfma_f32_32x32x16_bf16 a[32:47],a[132:135],v[68:71],a[32:47]\n"
"ds_read_b64_tr_b16 a[198:199],v233 offset:13568\n"
"ds_read_b64_tr_b16 a[212:213],v233 offset:28928\n"
"L_pgg48_%=:\n"
"s_cbranch_execz L_pgg112_%=\n"
"s_add_u32 m0,s66,0\n"
"s_add_u32 s78,s78,524288\n"
"s_cmp_eq_u32 s78,4718592\n"
"s_cselect_b32 s78,0,s78\n"
"v_mfma_f32_32x32x16_bf16 a[48:63],a[148:151],v[68:71],a[48:63]\n"
"ds_read_b64_tr_b16 a[214:215],v233 offset:29952\n"
"ds_read_b64_tr_b16 a[200:201],v233 offset:12800\n"
"ds_read_b64_tr_b16 a[202:203],v233 offset:13824\n"
"buffer_load_dwordx4 v230,s[44:47],s78 offen offset:0 lds\n"
"v_and_b32 v207,s73,v127\n"
"v_mfma_f32_32x32x16_bf16 a[64:79],a[136:139],v[68:71],a[64:79]\n"
"ds_read_b64_tr_b16 a[216:217],v233 offset:29184\n"
"ds_read_b64_tr_b16 a[218:219],v233 offset:30208\n"
"v_fma_f32 v200,v200,v222,v220\n"
"v_fma_f32 v201,v201,v222,v220\n"
"v_fma_f32 v202,v202,v222,v220\n"
"v_mfma_f32_32x32x16_bf16 a[80:95],a[152:155],v[68:71],a[80:95]\n"
"ds_read_b64_tr_b16 a[204:205],v233 offset:13056\n"
"ds_read_b64_tr_b16 a[206:207],v233 offset:14080\n"
"v_fma_f32 v203,v203,v222,v220\n"
"v_fma_f32 v204,v204,v222,v220\n"
"v_fma_f32 v205,v205,v222,v220\n"
"v_fma_f32 v206,v206,v222,v220\n"
"v_mfma_f32_32x32x16_bf16 a[96:111],a[140:143],v[68:71],a[96:111]\n"
"ds_read_b64_tr_b16 a[220:221],v233 offset:29440\n"
"ds_read_b64_tr_b16 a[222:223],v233 offset:30464\n"
"buffer_load_dwordx4 v231,s[44:47],s78 offen offset:1024 lds\n"
"v_fma_f32 v207,v207,v222,v220\n"
"v_mfma_f32_32x32x16_bf16 a[112:127],a[156:159],v[68:71],a[112:127]\n"
"v_fmac_f32_dpp v184,v161,v200 row_newbcast:8 row_mask:0xf bank_mask:0xf\n"
"v_fmac_f32_dpp v185,v161,v201 row_newbcast:9 row_mask:0xf bank_mask:0xf\n"
"v_fmac_f32_dpp v186,v161,v202 row_newbcast:10 row_mask:0xf bank_mask:0xf\n"
"v_fmac_f32_dpp v187,v161,v203 row_newbcast:11 row_mask:0xf bank_mask:0xf\n"
"v_fmac_f32_dpp v188,v161,v204 row_newbcast:12 row_mask:0xf bank_mask:0xf\n"
"v_fmac_f32_dpp v189,v161,v205 row_newbcast:13 row_mask:0xf bank_mask:0xf\n"
"s_waitcnt lgkmcnt(15)\n"
"v_mfma_f32_32x32x16_bf16 a[0:15],a[160:163],v[72:75],a[0:15]\n"
"v_fmac_f32_dpp v190,v161,v206 row_newbcast:14 row_mask:0xf bank_mask:0xf\n"
"v_fmac_f32_dpp v191,v161,v207 row_newbcast:15 row_mask:0xf bank_mask:0xf\n"
"v_cvt_pk_bf16_f32 v124,v184,v185\n"
"v_cvt_pk_bf16_f32 v125,v186,v187\n"
"v_cvt_pk_bf16_f32 v126,v188,v189\n"
"v_mfma_f32_32x32x16_bf16 a[16:31],a[176:179],v[72:75],a[16:31]\n"
"s_waitcnt vmcnt(16) lgkmcnt(0)\n"
"s_barrier\n"
"v_add_u32 v233,98304,v232\n"
"buffer_load_dwordx4 v216,s[44:47],s78 offen offset:2048 lds\n"
"v_mfma_f32_32x32x16_bf16 a[32:47],a[164:167],v[72:75],a[32:47]\n"
"ds_read_b64_tr_b16 a[128:129],v233 offset:0\n"
"ds_read_b64_tr_b16 a[130:131],v233 offset:1024\n"
"ds_read_b64_tr_b16 a[144:145],v233 offset:16384\n"
"v_cvt_pk_bf16_f32 v127,v190,v191\n"
"ds_read_b128 v[184:187],v242 offset:3904\n"
"ds_read_b128 v[188:191],v242 offset:3920\n"
"v_mfma_f32_32x32x16_bf16 a[48:63],a[180:183],v[72:75],a[48:63]\n"
"ds_read_b64_tr_b16 a[146:147],v233 offset:17408\n"
"ds_read_b64_tr_b16 a[132:133],v233 offset:256\n"
"ds_read_b64_tr_b16 a[134:135],v233 offset:1280\n"
"v_lshlrev_b32 v192,16,v128\n"
"v_and_b32 v193,s73,v128\n"
"v_mfma_f32_32x32x16_bf16 a[64:79],a[168:171],v[72:75],a[64:79]\n"
"ds_read_b64_tr_b16 a[148:149],v233 offset:16640\n"
"ds_read_b64_tr_b16 a[150:151],v233 offset:17664\n"
"buffer_load_dwordx4 v217,s[44:47],s78 offen offset:3072 lds\n"
"s_mov_b32 s61,442368\n"
"buffer_load_dwordx4 v[40:43],v218,s[80:83],s61 offen\n"
"v_mfma_f32_32x32x16_bf16 a[80:95],a[184:187],v[72:75],a[80:95]\n"
"ds_read_b64_tr_b16 a[136:137],v233 offset:512\n"
"ds_read_b64_tr_b16 a[138:139],v233 offset:1536\n"
"ds_read_b64_tr_b16 a[152:153],v233 offset:16896\n"
"v_lshlrev_b32 v194,16,v129\n"
"v_and_b32 v195,s73,v129\n"
"v_lshlrev_b32 v196,16,v130\n"
"v_mfma_f32_32x32x16_bf16 a[96:111],a[172:175],v[72:75],a[96:111]\n"
"ds_read_b64_tr_b16 a[154:155],v233 offset:17920\n"
"ds_read_b64_tr_b16 a[140:141],v233 offset:768\n"
"ds_read_b64_tr_b16 a[142:143],v233 offset:1792\n"
"s_add_u32 m0,s66,16384\n"
"s_add_u32 s86,s78,256\n"
"v_and_b32 v197,s73,v130\n"
"v_mfma_f32_32x32x16_bf16 a[112:127],a[188:191],v[72:75],a[112:127]\n"
"ds_read_b64_tr_b16 a[156:157],v233 offset:17152\n"
"ds_read_b64_tr_b16 a[158:159],v233 offset:18176\n"
"buffer_load_dwordx4 v230,s[44:47],s86 offen offset:0 lds\n"
"v_lshlrev_b32 v198,16,v131\n"
"v_and_b32 v199,s73,v131\n"
"v_mfma_f32_32x32x16_bf16 a[0:15],a[192:195],v[76:79],a[0:15]\n"
"ds_read_b64_tr_b16 a[160:161],v233 offset:4096\n"
"ds_read_b64_tr_b16 a[162:163],v233 offset:5120\n"
"v_fma_f32 v192,v192,v222,v220\n"
"v_fma_f32 v193,v193,v222,v220\n"
"v_fma_f32 v194,v194,v222,v220\n"
"v_mfma_f32_32x32x16_bf16 a[16:31],a[208:211],v[76:79],a[16:31]\n"
"ds_read_b64_tr_b16 a[176:177],v233 offset:20480\n"
"ds_read_b64_tr_b16 a[178:179],v233 offset:21504\n"
"v_fma_f32 v195,v195,v222,v220\n"
"v_fma_f32 v196,v196,v222,v220\n"
"v_fma_f32 v197,v197,v222,v220\n"
"v_fma_f32 v198,v198,v222,v220\n"
"v_mfma_f32_32x32x16_bf16 a[32:47],a[196:199],v[76:79],a[32:47]\n"
"ds_read_b64_tr_b16 a[164:165],v233 offset:4352\n"
"ds_read_b64_tr_b16 a[166:167],v233 offset:5376\n"
"buffer_load_dwordx4 v231,s[44:47],s86 offen offset:1024 lds\n"
"v_fma_f32 v199,v199,v222,v220\n"
"v_mfma_f32_32x32x16_bf16 a[48:63],a[212:215],v[76:79],a[48:63]\n"
"ds_read_b64_tr_b16 a[180:181],v233 offset:20736\n"
"ds_read_b64_tr_b16 a[182:183],v233 offset:21760\n"
"v_fmac_f32_dpp v168,v160,v192 row_newbcast:0 row_mask:0xf bank_mask:0xf\n"
"v_fmac_f32_dpp v169,v160,v193 row_newbcast:1 row_mask:0xf bank_mask:0xf\n"
"v_fmac_f32_dpp v170,v160,v194 row_newbcast:2 row_mask:0xf bank_mask:0xf\n"
"v_fmac_f32_dpp v171,v160,v195 row_newbcast:3 row_mask:0xf bank_mask:0xf\n"
"v_mfma_f32_32x32x16_bf16 a[64:79],a[200:203],v[76:79],a[64:79]\n"
"ds_read_b64_tr_b16 a[168:169],v233 offset:4608\n"
"ds_read_b64_tr_b16 a[170:171],v233 offset:5632\n"
"v_fmac_f32_dpp v172,v160,v196 row_newbcast:4 row_mask:0xf bank_mask:0xf\n"
"v_fmac_f32_dpp v173,v160,v197 row_newbcast:5 row_mask:0xf bank_mask:0xf\n"
"v_fmac_f32_dpp v174,v160,v198 row_newbcast:6 row_mask:0xf bank_mask:0xf\n"
"v_mfma_f32_32x32x16_bf16 a[80:95],a[216:219],v[76:79],a[80:95]\n"
"ds_read_b64_tr_b16 a[184:185],v233 offset:20992\n"
"ds_read_b64_tr_b16 a[186:187],v233 offset:22016\n"
"buffer_load_dwordx4 v216,s[44:47],s86 offen offset:2048 lds\n"
"v_fmac_f32_dpp v175,v160,v199 row_newbcast:7 row_mask:0xf bank_mask:0xf\n"
"v_cvt_pk_bf16_f32 v128,v168,v169\n"
"v_mfma_f32_32x32x16_bf16 a[96:111],a[204:207],v[76:79],a[96:111]\n"
"ds_read_b64_tr_b16 a[172:173],v233 offset:4864\n"
"ds_read_b64_tr_b16 a[174:175],v233 offset:5888\n"
"v_cvt_pk_bf16_f32 v129,v170,v171\n"
"v_cvt_pk_bf16_f32 v130,v172,v173\n"
"v_cvt_pk_bf16_f32 v131,v174,v175\n"
"ds_read_b32 v161,v223 offset:1664\n"
"v_mfma_f32_32x32x16_bf16 a[112:127],a[220:223],v[76:79],a[112:127]\n"
"ds_read_b64_tr_b16 a[188:189],v233 offset:21248\n"
"ds_read_b64_tr_b16 a[190:191],v233 offset:22272\n"
"ds_read_b128 v[168:171],v242 offset:3968\n"
"ds_read_b128 v[172:175],v242 offset:3984\n"
"v_lshlrev_b32 v200,16,v132\n"
"v_and_b32 v201,s73,v132\n"
"s_waitcnt lgkmcnt(15)\n"
"v_mfma_f32_32x32x16_bf16 a[0:15],a[128:131],v[80:83],a[0:15]\n"
"ds_read_b64_tr_b16 a[192:193],v233 offset:8192\n"
"ds_read_b64_tr_b16 a[194:195],v233 offset:9216\n"
"buffer_load_dwordx4 v217,s[44:47],s86 offen offset:3072 lds\n"
"v_lshlrev_b32 v202,16,v133\n"
"v_mfma_f32_32x32x16_bf16 a[16:31],a[144:147],v[80:83],a[16:31]\n"
"ds_read_b64_tr_b16 a[208:209],v233 offset:24576\n"
"ds_read_b64_tr_b16 a[210:211],v233 offset:25600\n"
"v_and_b32 v203,s73,v133\n"
"v_lshlrev_b32 v204,16,v134\n"
"v_and_b32 v205,s73,v134\n"
"v_lshlrev_b32 v206,16,v135\n"
"v_mfma_f32_32x32x16_bf16 a[32:47],a[132:135],v[80:83],a[32:47]\n"
"ds_read_b64_tr_b16 a[196:197],v233 offset:8448\n"
"ds_read_b64_tr_b16 a[198:199],v233 offset:9472\n"
"s_add_u32 m0,s66,65536\n"
"s_add_u32 s78,s78,524288\n"
"s_cmp_eq_u32 s78,4718592\n"
"s_cselect_b32 s78,0,s78\n"
"v_mfma_f32_32x32x16_bf16 a[48:63],a[148:151],v[80:83],a[48:63]\n"
"ds_read_b64_tr_b16 a[212:213],v233 offset:24832\n"
"ds_read_b64_tr_b16 a[214:215],v233 offset:25856\n"
"buffer_load_dwordx4 v230,s[44:47],s78 offen offset:0 lds\n"
"v_and_b32 v207,s73,v135\n"
"v_fma_f32 v200,v200,v222,v220\n"
"v_mfma_f32_32x32x16_bf16 a[64:79],a[136:139],v[80:83],a[64:79]\n"
"ds_read_b64_tr_b16 a[200:201],v233 offset:8704\n"
"ds_read_b64_tr_b16 a[202:203],v233 offset:9728\n"
"v_fma_f32 v201,v201,v222,v220\n"
"v_fma_f32 v202,v202,v222,v220\n"
"v_fma_f32 v203,v203,v222,v220\n"
"v_mfma_f32_32x32x16_bf16 a[80:95],a[152:155],v[80:83],a[80:95]\n"
"ds_read_b64_tr_b16 a[216:217],v233 offset:25088\n"
"ds_read_b64_tr_b16 a[218:219],v233 offset:26112\n"
"v_fma_f32 v204,v204,v222,v220\n"
"v_fma_f32 v205,v205,v222,v220\n"
"v_fma_f32 v206,v206,v222,v220\n"
"v_fma_f32 v207,v207,v222,v220\n"
"v_mfma_f32_32x32x16_bf16 a[96:111],a[140:143],v[80:83],a[96:111]\n"
"ds_read_b64_tr_b16 a[204:205],v233 offset:8960\n"
"ds_read_b64_tr_b16 a[206:207],v233 offset:9984\n"
"buffer_load_dwordx4 v231,s[44:47],s78 offen offset:1024 lds\n"
"v_fmac_f32_dpp v184,v160,v200 row_newbcast:8 row_mask:0xf bank_mask:0xf\n"
"v_mfma_f32_32x32x16_bf16 a[112:127],a[156:159],v[80:83],a[112:127]\n"
"ds_read_b64_tr_b16 a[220:221],v233 offset:25344\n"
"ds_read_b64_tr_b16 a[222:223],v233 offset:26368\n"
"v_fmac_f32_dpp v185,v160,v201 row_newbcast:9 row_mask:0xf bank_mask:0xf\n"
"v_fmac_f32_dpp v186,v160,v202 row_newbcast:10 row_mask:0xf bank_mask:0xf\n"
"v_fmac_f32_dpp v187,v160,v203 row_newbcast:11 row_mask:0xf bank_mask:0xf\n"
"v_fmac_f32_dpp v188,v160,v204 row_newbcast:12 row_mask:0xf bank_mask:0xf\n"
"s_waitcnt lgkmcnt(15)\n"
"v_mfma_f32_32x32x16_bf16 a[0:15],a[160:163],v[84:87],a[0:15]\n"
"ds_read_b64_tr_b16 a[128:129],v233 offset:12288\n"
"ds_read_b64_tr_b16 a[130:131],v233 offset:13312\n"
"ds_read_b64_tr_b16 a[144:145],v233 offset:28672\n"
"v_fmac_f32_dpp v189,v160,v205 row_newbcast:13 row_mask:0xf bank_mask:0xf\n"
"v_fmac_f32_dpp v190,v160,v206 row_newbcast:14 row_mask:0xf bank_mask:0xf\n"
"v_mfma_f32_32x32x16_bf16 a[16:31],a[176:179],v[84:87],a[16:31]\n"
"ds_read_b64_tr_b16 a[146:147],v233 offset:29696\n"
"ds_read_b64_tr_b16 a[132:133],v233 offset:12544\n"
"buffer_load_dwordx4 v216,s[44:47],s78 offen offset:2048 lds\n"
"s_mov_b32 s61,663552\n"
"buffer_load_dwordx4 v[44:47],v218,s[80:83],s61 offen\n"
"v_mfma_f32_32x32x16_bf16 a[32:47],a[164:167],v[84:87],a[32:47]\n"
"ds_read_b64_tr_b16 a[134:135],v233 offset:13568\n"
"ds_read_b64_tr_b16 a[148:149],v233 offset:28928\n"
"v_fmac_f32_dpp v191,v160,v207 row_newbcast:15 row_mask:0xf bank_mask:0xf\n"
"v_cvt_pk_bf16_f32 v132,v184,v185\n"
"v_cvt_pk_bf16_f32 v133,v186,v187\n"
"v_cvt_pk_bf16_f32 v134,v188,v189\n"
"v_mfma_f32_32x32x16_bf16 a[48:63],a[180:183],v[84:87],a[48:63]\n"
"ds_read_b64_tr_b16 a[150:151],v233 offset:29952\n"
"ds_read_b64_tr_b16 a[136:137],v233 offset:12800\n"
"ds_read_b64_tr_b16 a[138:139],v233 offset:13824\n"
"v_cvt_pk_bf16_f32 v135,v190,v191\n"
"ds_read_b128 v[184:187],v242 offset:4032\n"
"ds_read_b128 v[188:191],v242 offset:4048\n"
"v_mfma_f32_32x32x16_bf16 a[64:79],a[168:171],v[84:87],a[64:79]\n"
"ds_read_b64_tr_b16 a[152:153],v233 offset:29184\n"
"ds_read_b64_tr_b16 a[154:155],v233 offset:30208\n"
"buffer_load_dwordx4 v217,s[44:47],s78 offen offset:3072 lds\n"
"v_lshlrev_b32 v192,16,v136\n"
"v_mfma_f32_32x32x16_bf16 a[80:95],a[184:187],v[84:87],a[80:95]\n"
"ds_read_b64_tr_b16 a[140:141],v233 offset:13056\n"
"ds_read_b64_tr_b16 a[142:143],v233 offset:14080\n"
"v_and_b32 v193,s73,v136\n"
"v_lshlrev_b32 v194,16,v137\n"
"v_and_b32 v195,s73,v137\n"
"v_lshlrev_b32 v196,16,v138\n"
"v_mfma_f32_32x32x16_bf16 a[96:111],a[172:175],v[84:87],a[96:111]\n"
"ds_read_b64_tr_b16 a[156:157],v233 offset:29440\n"
"ds_read_b64_tr_b16 a[158:159],v233 offset:30464\n"
"s_add_u32 m0,s66,81920\n"
"s_add_u32 s86,s78,256\n"
"v_and_b32 v197,s73,v138\n"
"v_lshlrev_b32 v198,16,v139\n"
"v_mfma_f32_32x32x16_bf16 a[112:127],a[188:191],v[84:87],a[112:127]\n"
"buffer_load_dwordx4 v230,s[44:47],s86 offen offset:0 lds\n"
"v_and_b32 v199,s73,v139\n"
"v_fma_f32 v192,v192,v222,v220\n"
"v_fma_f32 v193,v193,v222,v220\n"
"v_fma_f32 v194,v194,v222,v220\n"
"s_waitcnt lgkmcnt(15)\n"
"v_mfma_f32_32x32x16_bf16 a[0:15],a[192:195],v[88:91],a[0:15]\n"
"v_fma_f32 v195,v195,v222,v220\n"
"v_fma_f32 v196,v196,v222,v220\n"
"v_fma_f32 v197,v197,v222,v220\n"
"v_fma_f32 v198,v198,v222,v220\n"
"v_fma_f32 v199,v199,v222,v220\n"
"v_mfma_f32_32x32x16_bf16 a[16:31],a[208:211],v[88:91],a[16:31]\n"
"s_waitcnt vmcnt(15) lgkmcnt(0)\n"
"s_barrier\n"
"v_add_u32 v233,32768,v232\n"
"v_fmac_f32_dpp v168,v161,v192 row_newbcast:0 row_mask:0xf bank_mask:0xf\n"
"v_mfma_f32_32x32x16_bf16 a[32:47],a[196:199],v[88:91],a[32:47]\n"
"ds_read_b64_tr_b16 a[160:161],v233 offset:0\n"
"ds_read_b64_tr_b16 a[162:163],v233 offset:1024\n"
"ds_read_b64_tr_b16 a[176:177],v233 offset:16384\n"
"buffer_load_dwordx4 v231,s[44:47],s86 offen offset:1024 lds\n"
"v_fmac_f32_dpp v169,v161,v193 row_newbcast:1 row_mask:0xf bank_mask:0xf\n"
"v_mfma_f32_32x32x16_bf16 a[48:63],a[212:215],v[88:91],a[48:63]\n"
"ds_read_b64_tr_b16 a[178:179],v233 offset:17408\n"
"ds_read_b64_tr_b16 a[164:165],v233 offset:256\n"
"ds_read_b64_tr_b16 a[166:167],v233 offset:1280\n"
"v_fmac_f32_dpp v170,v161,v194 row_newbcast:2 row_mask:0xf bank_mask:0xf\n"
"v_fmac_f32_dpp v171,v161,v195 row_newbcast:3 row_mask:0xf bank_mask:0xf\n"
"v_fmac_f32_dpp v172,v161,v196 row_newbcast:4 row_mask:0xf bank_mask:0xf\n"
"v_mfma_f32_32x32x16_bf16 a[64:79],a[200:203],v[88:91],a[64:79]\n"
"ds_read_b64_tr_b16 a[180:181],v233 offset:16640\n"
"ds_read_b64_tr_b16 a[182:183],v233 offset:17664\n"
"v_fmac_f32_dpp v173,v161,v197 row_newbcast:5 row_mask:0xf bank_mask:0xf\n"
"v_fmac_f32_dpp v174,v161,v198 row_newbcast:6 row_mask:0xf bank_mask:0xf\n"
"v_fmac_f32_dpp v175,v161,v199 row_newbcast:7 row_mask:0xf bank_mask:0xf\n"
"v_mfma_f32_32x32x16_bf16 a[80:95],a[216:219],v[88:91],a[80:95]\n"
"ds_read_b64_tr_b16 a[168:169],v233 offset:512\n"
"ds_read_b64_tr_b16 a[170:171],v233 offset:1536\n"
"ds_read_b64_tr_b16 a[184:185],v233 offset:16896\n"
"buffer_load_dwordx4 v216,s[44:47],s86 offen offset:2048 lds\n"
"v_cvt_pk_bf16_f32 v136,v168,v169\n"
"v_mfma_f32_32x32x16_bf16 a[96:111],a[204:207],v[88:91],a[96:111]\n"
"ds_read_b64_tr_b16 a[186:187],v233 offset:17920\n"
"ds_read_b64_tr_b16 a[172:173],v233 offset:768\n"
"ds_read_b64_tr_b16 a[174:175],v233 offset:1792\n"
"v_cvt_pk_bf16_f32 v137,v170,v171\n"
"v_cvt_pk_bf16_f32 v138,v172,v173\n"
"v_cvt_pk_bf16_f32 v139,v174,v175\n"
"v_mfma_f32_32x32x16_bf16 a[112:127],a[220:223],v[88:91],a[112:127]\n"
"ds_read_b64_tr_b16 a[188:189],v233 offset:17152\n"
"ds_read_b64_tr_b16 a[190:191],v233 offset:18176\n"
"ds_read_b32 v160,v223 offset:1792\n"
"ds_read_b128 v[168:171],v242 offset:4096\n"
"ds_read_b128 v[172:175],v242 offset:4112\n"
"v_lshlrev_b32 v200,16,v140\n"
"v_mfma_f32_32x32x16_bf16 a[0:15],a[128:131],v[92:95],a[0:15]\n"
"ds_read_b64_tr_b16 a[192:193],v233 offset:4096\n"
"ds_read_b64_tr_b16 a[194:195],v233 offset:5120\n"
"ds_read_b128 v[16:19],v212 offset:0\n"
"buffer_load_dwordx4 v217,s[44:47],s86 offen offset:3072 lds\n"
"v_mfma_f32_32x32x16_bf16 a[16:31],a[144:147],v[92:95],a[16:31]\n"
"ds_read_b64_tr_b16 a[208:209],v233 offset:20480\n"
"ds_read_b64_tr_b16 a[210:211],v233 offset:21504\n"
"ds_read_b128 v[20:23],v213 offset:0\n"
"ds_read_b128 v[24:27],v214 offset:0\n"
"v_and_b32 v201,s73,v140\n"
"v_lshlrev_b32 v202,16,v141\n"
"v_mfma_f32_32x32x16_bf16 a[32:47],a[132:135],v[92:95],a[32:47]\n"
"ds_read_b64_tr_b16 a[196:197],v233 offset:4352\n"
"ds_read_b64_tr_b16 a[198:199],v233 offset:5376\n"
"ds_read_b128 v[28:31],v215 offset:0\n"
"s_add_u32 m0,s66,98304\n"
"s_add_u32 s78,s78,524288\n"
"s_cmp_eq_u32 s78,4718592\n"
"s_cselect_b32 s78,0,s78\n"
"v_mfma_f32_32x32x16_bf16 a[48:63],a[148:151],v[92:95],a[48:63]\n"
"ds_read_b64_tr_b16 a[212:213],v233 offset:20736\n"
"ds_read_b64_tr_b16 a[214:215],v233 offset:21760\n"
"buffer_load_dwordx4 v230,s[44:47],s78 offen offset:0 lds\n"
"v_and_b32 v203,s73,v141\n"
"v_lshlrev_b32 v204,16,v142\n"
"v_mfma_f32_32x32x16_bf16 a[64:79],a[136:139],v[92:95],a[64:79]\n"
"ds_read_b64_tr_b16 a[200:201],v233 offset:4608\n"
"ds_read_b64_tr_b16 a[202:203],v233 offset:5632\n"
"v_and_b32 v205,s73,v142\n"
"v_lshlrev_b32 v206,16,v143\n"
"v_and_b32 v207,s73,v143\n"
"v_mfma_f32_32x32x16_bf16 a[80:95],a[152:155],v[92:95],a[80:95]\n"
"ds_read_b64_tr_b16 a[216:217],v233 offset:20992\n"
"ds_read_b64_tr_b16 a[218:219],v233 offset:22016\n"
"v_fma_f32 v200,v200,v222,v220\n"
"v_fma_f32 v201,v201,v222,v220\n"
"v_fma_f32 v202,v202,v222,v220\n"
"v_fma_f32 v203,v203,v222,v220\n"
"v_mfma_f32_32x32x16_bf16 a[96:111],a[140:143],v[92:95],a[96:111]\n"
"ds_read_b64_tr_b16 a[204:205],v233 offset:4864\n"
"ds_read_b64_tr_b16 a[206:207],v233 offset:5888\n"
"buffer_load_dwordx4 v231,s[44:47],s78 offen offset:1024 lds\n"
"s_mov_b32 s61,884736\n"
"buffer_load_dwordx4 v[48:51],v218,s[80:83],s61 offen\n"
"v_mfma_f32_32x32x16_bf16 a[112:127],a[156:159],v[92:95],a[112:127]\n"
"ds_read_b64_tr_b16 a[220:221],v233 offset:21248\n"
"ds_read_b64_tr_b16 a[222:223],v233 offset:22272\n"
"v_fma_f32 v204,v204,v222,v220\n"
"v_fma_f32 v205,v205,v222,v220\n"
"v_fma_f32 v206,v206,v222,v220\n"
"v_fma_f32 v207,v207,v222,v220\n"
"s_waitcnt lgkmcnt(15)\n"
"v_mfma_f32_32x32x16_bf16 a[0:15],a[160:163],v[96:99],a[0:15]\n"
"ds_read_b64_tr_b16 a[128:129],v233 offset:8192\n"
"ds_read_b64_tr_b16 a[130:131],v233 offset:9216\n"
"v_fmac_f32_dpp v184,v161,v200 row_newbcast:8 row_mask:0xf bank_mask:0xf\n"
"v_fmac_f32_dpp v185,v161,v201 row_newbcast:9 row_mask:0xf bank_mask:0xf\n"
"v_mfma_f32_32x32x16_bf16 a[16:31],a[176:179],v[96:99],a[16:31]\n"
"ds_read_b64_tr_b16 a[144:145],v233 offset:24576\n"
"ds_read_b64_tr_b16 a[146:147],v233 offset:25600\n"
"buffer_load_dwordx4 v216,s[44:47],s78 offen offset:2048 lds\n"
"v_fmac_f32_dpp v186,v161,v202 row_newbcast:10 row_mask:0xf bank_mask:0xf\n"
"v_fmac_f32_dpp v187,v161,v203 row_newbcast:11 row_mask:0xf bank_mask:0xf\n"
"v_mfma_f32_32x32x16_bf16 a[32:47],a[164:167],v[96:99],a[32:47]\n"
"ds_read_b64_tr_b16 a[132:133],v233 offset:8448\n"
"ds_read_b64_tr_b16 a[134:135],v233 offset:9472\n"
"v_fmac_f32_dpp v188,v161,v204 row_newbcast:12 row_mask:0xf bank_mask:0xf\n"
"v_fmac_f32_dpp v189,v161,v205 row_newbcast:13 row_mask:0xf bank_mask:0xf\n"
"v_fmac_f32_dpp v190,v161,v206 row_newbcast:14 row_mask:0xf bank_mask:0xf\n"
"v_fmac_f32_dpp v191,v161,v207 row_newbcast:15 row_mask:0xf bank_mask:0xf\n"
"v_mfma_f32_32x32x16_bf16 a[48:63],a[180:183],v[96:99],a[48:63]\n"
"ds_read_b64_tr_b16 a[148:149],v233 offset:24832\n"
"ds_read_b64_tr_b16 a[150:151],v233 offset:25856\n"
"v_cvt_pk_bf16_f32 v140,v184,v185\n"
"v_cvt_pk_bf16_f32 v141,v186,v187\n"
"v_cvt_pk_bf16_f32 v142,v188,v189\n"
"v_cvt_pk_bf16_f32 v143,v190,v191\n"
"v_mfma_f32_32x32x16_bf16 a[64:79],a[168:171],v[96:99],a[64:79]\n"
"ds_read_b64_tr_b16 a[136:137],v233 offset:8704\n"
"ds_read_b64_tr_b16 a[138:139],v233 offset:9728\n"
"buffer_load_dwordx4 v217,s[44:47],s78 offen offset:3072 lds\n"
"ds_read_b128 v[184:187],v242 offset:4160\n"
"v_mfma_f32_32x32x16_bf16 a[80:95],a[184:187],v[96:99],a[80:95]\n"
"ds_read_b64_tr_b16 a[152:153],v233 offset:25088\n"
"ds_read_b64_tr_b16 a[154:155],v233 offset:26112\n"
"ds_read_b128 v[188:191],v242 offset:4176\n"
"v_lshlrev_b32 v192,16,v144\n"
"v_and_b32 v193,s73,v144\n"
"v_mfma_f32_32x32x16_bf16 a[96:111],a[172:175],v[96:99],a[96:111]\n"
"ds_read_b64_tr_b16 a[140:141],v233 offset:8960\n"
"ds_read_b64_tr_b16 a[142:143],v233 offset:9984\n"
"s_add_u32 m0,s66,114688\n"
"s_add_u32 s86,s78,256\n"
"v_lshlrev_b32 v194,16,v145\n"
"v_and_b32 v195,s73,v145\n"
"v_mfma_f32_32x32x16_bf16 a[112:127],a[188:191],v[96:99],a[112:127]\n"
"ds_read_b64_tr_b16 a[156:157],v233 offset:25344\n"
"ds_read_b64_tr_b16 a[158:159],v233 offset:26368\n"
"buffer_load_dwordx4 v230,s[44:47],s86 offen offset:0 lds\n"
"v_lshlrev_b32 v196,16,v146\n"
"v_and_b32 v197,s73,v146\n"
"s_waitcnt lgkmcnt(15)\n"
"v_mfma_f32_32x32x16_bf16 a[0:15],a[192:195],v[100:103],a[0:15]\n"
"ds_read_b64_tr_b16 a[160:161],v233 offset:12288\n"
"ds_read_b64_tr_b16 a[162:163],v233 offset:13312\n"
"ds_read_b64_tr_b16 a[176:177],v233 offset:28672\n"
"v_lshlrev_b32 v198,16,v147\n"
"v_and_b32 v199,s73,v147\n"
"v_mfma_f32_32x32x16_bf16 a[16:31],a[208:211],v[100:103],a[16:31]\n"
"ds_read_b64_tr_b16 a[178:179],v233 offset:29696\n"
"ds_read_b64_tr_b16 a[164:165],v233 offset:12544\n"
"v_fma_f32 v192,v192,v222,v220\n"
"v_fma_f32 v193,v193,v222,v220\n"
"v_fma_f32 v194,v194,v222,v220\n"
"v_fma_f32 v195,v195,v222,v220\n"
"v_mfma_f32_32x32x16_bf16 a[32:47],a[196:199],v[100:103],a[32:47]\n"
"ds_read_b64_tr_b16 a[166:167],v233 offset:13568\n"
"ds_read_b64_tr_b16 a[180:181],v233 offset:28928\n"
"buffer_load_dwordx4 v231,s[44:47],s86 offen offset:1024 lds\n"
"v_fma_f32 v196,v196,v222,v220\n"
"v_fma_f32 v197,v197,v222,v220\n"
"v_mfma_f32_32x32x16_bf16 a[48:63],a[212:215],v[100:103],a[48:63]\n"
"ds_read_b64_tr_b16 a[182:183],v233 offset:29952\n"
"ds_read_b64_tr_b16 a[168:169],v233 offset:12800\n"
"ds_read_b64_tr_b16 a[170:171],v233 offset:13824\n"
"v_fma_f32 v198,v198,v222,v220\n"
"v_fma_f32 v199,v199,v222,v220\n"
"v_mfma_f32_32x32x16_bf16 a[64:79],a[200:203],v[100:103],a[64:79]\n"
"ds_read_b64_tr_b16 a[184:185],v233 offset:29184\n"
"ds_read_b64_tr_b16 a[186:187],v233 offset:30208\n"
"v_fmac_f32_dpp v168,v160,v192 row_newbcast:0 row_mask:0xf bank_mask:0xf\n"
"v_fmac_f32_dpp v169,v160,v193 row_newbcast:1 row_mask:0xf bank_mask:0xf\n"
"v_fmac_f32_dpp v170,v160,v194 row_newbcast:2 row_mask:0xf bank_mask:0xf\n"
"v_mfma_f32_32x32x16_bf16 a[80:95],a[216:219],v[100:103],a[80:95]\n"
"ds_read_b64_tr_b16 a[172:173],v233 offset:13056\n"
"ds_read_b64_tr_b16 a[174:175],v233 offset:14080\n"
"buffer_load_dwordx4 v216,s[44:47],s86 offen offset:2048 lds\n"
"v_fmac_f32_dpp v171,v160,v195 row_newbcast:3 row_mask:0xf bank_mask:0xf\n"
"v_fmac_f32_dpp v172,v160,v196 row_newbcast:4 row_mask:0xf bank_mask:0xf\n"
"v_mfma_f32_32x32x16_bf16 a[96:111],a[204:207],v[100:103],a[96:111]\n"
"ds_read_b64_tr_b16 a[188:189],v233 offset:29440\n"
"ds_read_b64_tr_b16 a[190:191],v233 offset:30464\n"
"v_fmac_f32_dpp v173,v160,v197 row_newbcast:5 row_mask:0xf bank_mask:0xf\n"
"v_fmac_f32_dpp v174,v160,v198 row_newbcast:6 row_mask:0xf bank_mask:0xf\n"
"v_fmac_f32_dpp v175,v160,v199 row_newbcast:7 row_mask:0xf bank_mask:0xf\n"
"v_cvt_pk_bf16_f32 v144,v168,v169\n"
"v_mfma_f32_32x32x16_bf16 a[112:127],a[220:223],v[100:103],a[112:127]\n"
"v_cvt_pk_bf16_f32 v145,v170,v171\n"
"v_cvt_pk_bf16_f32 v146,v172,v173\n"
"v_cvt_pk_bf16_f32 v147,v174,v175\n"
"ds_read_b32 v161,v223 offset:1920\n"
"ds_read_b128 v[168:171],v242 offset:4224\n"
"ds_read_b128 v[172:175],v242 offset:4240\n"
"s_waitcnt lgkmcnt(15)\n"
"v_mfma_f32_32x32x16_bf16 a[0:15],a[128:131],v[104:107],a[0:15]\n"
"buffer_load_dwordx4 v217,s[44:47],s86 offen offset:3072 lds\n"
"v_lshlrev_b32 v200,16,v148\n"
"v_and_b32 v201,s73,v148\n"
"v_lshlrev_b32 v202,16,v149\n"
"v_mfma_f32_32x32x16_bf16 a[16:31],a[144:147],v[104:107],a[16:31]\n"
"s_waitcnt vmcnt(18) lgkmcnt(0)\n"
"s_barrier\n"
"v_add_u32 v233,0,v232\n"
"v_and_b32 v203,s73,v149\n"
"v_lshlrev_b32 v204,16,v150\n"
"v_mfma_f32_32x32x16_bf16 a[32:47],a[132:135],v[104:107],a[32:47]\n"
"ds_read_b64_tr_b16 a[192:193],v233 offset:0\n"
"ds_read_b64_tr_b16 a[194:195],v233 offset:1024\n"
"ds_read_b64_tr_b16 a[208:209],v233 offset:16384\n"
"v_and_b32 v205,s73,v150\n"
"v_lshlrev_b32 v206,16,v151\n"
"v_and_b32 v207,s73,v151\n"
"v_mfma_f32_32x32x16_bf16 a[48:63],a[148:151],v[104:107],a[48:63]\n"
"ds_read_b64_tr_b16 a[210:211],v233 offset:17408\n"
"ds_read_b64_tr_b16 a[196:197],v233 offset:256\n"
"ds_read_b64_tr_b16 a[198:199],v233 offset:1280\n"
"v_fma_f32 v200,v200,v222,v220\n"
"v_fma_f32 v201,v201,v222,v220\n"
"v_fma_f32 v202,v202,v222,v220\n"
"v_mfma_f32_32x32x16_bf16 a[64:79],a[136:139],v[104:107],a[64:79]\n"
"ds_read_b64_tr_b16 a[212:213],v233 offset:16640\n"
"ds_read_b64_tr_b16 a[214:215],v233 offset:17664\n"
"v_fma_f32 v203,v203,v222,v220\n"
"v_fma_f32 v204,v204,v222,v220\n"
"v_fma_f32 v205,v205,v222,v220\n"
"v_mfma_f32_32x32x16_bf16 a[80:95],a[152:155],v[104:107],a[80:95]\n"
"ds_read_b64_tr_b16 a[200:201],v233 offset:512\n"
"ds_read_b64_tr_b16 a[202:203],v233 offset:1536\n"
"ds_read_b64_tr_b16 a[216:217],v233 offset:16896\n"
"v_fma_f32 v206,v206,v222,v220\n"
"v_fma_f32 v207,v207,v222,v220\n"
"v_mfma_f32_32x32x16_bf16 a[96:111],a[140:143],v[104:107],a[96:111]\n"
"ds_read_b64_tr_b16 a[218:219],v233 offset:17920\n"
"ds_read_b64_tr_b16 a[204:205],v233 offset:768\n"
"ds_read_b64_tr_b16 a[206:207],v233 offset:1792\n"
"v_fmac_f32_dpp v184,v160,v200 row_newbcast:8 row_mask:0xf bank_mask:0xf\n"
"v_fmac_f32_dpp v185,v160,v201 row_newbcast:9 row_mask:0xf bank_mask:0xf\n"
"v_fmac_f32_dpp v186,v160,v202 row_newbcast:10 row_mask:0xf bank_mask:0xf\n"
"v_mfma_f32_32x32x16_bf16 a[112:127],a[156:159],v[104:107],a[112:127]\n"
"ds_read_b64_tr_b16 a[220:221],v233 offset:17152\n"
"ds_read_b64_tr_b16 a[222:223],v233 offset:18176\n"
"v_fmac_f32_dpp v187,v160,v203 row_newbcast:11 row_mask:0xf bank_mask:0xf\n"
"v_fmac_f32_dpp v188,v160,v204 row_newbcast:12 row_mask:0xf bank_mask:0xf\n"
"v_fmac_f32_dpp v189,v160,v205 row_newbcast:13 row_mask:0xf bank_mask:0xf\n"
"v_fmac_f32_dpp v190,v160,v206 row_newbcast:14 row_mask:0xf bank_mask:0xf\n"
"v_mfma_f32_32x32x16_bf16 a[0:15],a[160:163],v[108:111],a[0:15]\n"
"ds_read_b64_tr_b16 a[128:129],v233 offset:4096\n"
"ds_read_b64_tr_b16 a[130:131],v233 offset:5120\n"
"s_add_u32 m0,s66,32768\n"
"s_add_u32 s78,s78,524288\n"
"s_cmp_eq_u32 s78,4718592\n"
"s_cselect_b32 s78,0,s78\n"
"v_mfma_f32_32x32x16_bf16 a[16:31],a[176:179],v[108:111],a[16:31]\n"
"ds_read_b64_tr_b16 a[144:145],v233 offset:20480\n"
"ds_read_b64_tr_b16 a[146:147],v233 offset:21504\n"
"buffer_load_dwordx4 v230,s[44:47],s78 offen offset:0 lds\n"
"v_fmac_f32_dpp v191,v160,v207 row_newbcast:15 row_mask:0xf bank_mask:0xf\n"
"v_cvt_pk_bf16_f32 v148,v184,v185\n"
"v_mfma_f32_32x32x16_bf16 a[32:47],a[164:167],v[108:111],a[32:47]\n"
"ds_read_b64_tr_b16 a[132:133],v233 offset:4352\n"
"ds_read_b64_tr_b16 a[134:135],v233 offset:5376\n"
"v_cvt_pk_bf16_f32 v149,v186,v187\n"
"v_cvt_pk_bf16_f32 v150,v188,v189\n"
"v_cvt_pk_bf16_f32 v151,v190,v191\n"
"ds_read_b128 v[184:187],v242 offset:4288\n"
"v_mfma_f32_32x32x16_bf16 a[48:63],a[180:183],v[108:111],a[48:63]\n"
"ds_read_b64_tr_b16 a[148:149],v233 offset:20736\n"
"ds_read_b64_tr_b16 a[150:151],v233 offset:21760\n"
"ds_read_b128 v[188:191],v242 offset:4304\n"
"v_lshlrev_b32 v192,16,v152\n"
"v_and_b32 v193,s73,v152\n"
"v_lshlrev_b32 v194,16,v153\n"
"v_mfma_f32_32x32x16_bf16 a[64:79],a[168:171],v[108:111],a[64:79]\n"
"ds_read_b64_tr_b16 a[136:137],v233 offset:4608\n"
"ds_read_b64_tr_b16 a[138:139],v233 offset:5632\n"
"buffer_load_dwordx4 v231,s[44:47],s78 offen offset:1024 lds\n"
"v_and_b32 v195,s73,v153\n"
"v_mfma_f32_32x32x16_bf16 a[80:95],a[184:187],v[108:111],a[80:95]\n"
"ds_read_b64_tr_b16 a[152:153],v233 offset:20992\n"
"ds_read_b64_tr_b16 a[154:155],v233 offset:22016\n"
"v_lshlrev_b32 v196,16,v154\n"
"v_and_b32 v197,s73,v154\n"
"v_lshlrev_b32 v198,16,v155\n"
"v_and_b32 v199,s73,v155\n"
"v_mfma_f32_32x32x16_bf16 a[96:111],a[172:175],v[108:111],a[96:111]\n"
"ds_read_b64_tr_b16 a[140:141],v233 offset:4864\n"
"ds_read_b64_tr_b16 a[142:143],v233 offset:5888\n"
"v_fma_f32 v192,v192,v222,v220\n"
"v_fma_f32 v193,v193,v222,v220\n"
"v_fma_f32 v194,v194,v222,v220\n"
"v_fma_f32 v195,v195,v222,v220\n"
"v_mfma_f32_32x32x16_bf16 a[112:127],a[188:191],v[108:111],a[112:127]\n"
"ds_read_b64_tr_b16 a[156:157],v233 offset:21248\n"
"ds_read_b64_tr_b16 a[158:159],v233 offset:22272\n"
"buffer_load_dwordx4 v216,s[44:47],s78 offen offset:2048 lds\n"
"v_fma_f32 v196,v196,v222,v220\n"
"v_fma_f32 v197,v197,v222,v220\n"
"s_waitcnt lgkmcnt(15)\n"
"L_pgg64_%=:\n"
"s_cbranch_execz L_pgg128_%=\n"
"v_mfma_f32_32x32x16_bf16 a[0:15],a[192:195],v[112:115],a[0:15]\n"
"ds_read_b64_tr_b16 a[160:161],v233 offset:8192\n"
"ds_read_b64_tr_b16 a[162:163],v233 offset:9216\n"
"v_fma_f32 v198,v198,v222,v220\n"
"v_fma_f32 v199,v199,v222,v220\n"
"v_mfma_f32_32x32x16_bf16 a[16:31],a[208:211],v[112:115],a[16:31]\n"
"ds_read_b64_tr_b16 a[176:177],v233 offset:24576\n"
"ds_read_b64_tr_b16 a[178:179],v233 offset:25600\n"
"v_fmac_f32_dpp v168,v161,v192 row_newbcast:0 row_mask:0xf bank_mask:0xf\n"
"v_fmac_f32_dpp v169,v161,v193 row_newbcast:1 row_mask:0xf bank_mask:0xf\n"
"v_fmac_f32_dpp v170,v161,v194 row_newbcast:2 row_mask:0xf bank_mask:0xf\n"
"v_fmac_f32_dpp v171,v161,v195 row_newbcast:3 row_mask:0xf bank_mask:0xf\n"
"v_mfma_f32_32x32x16_bf16 a[32:47],a[196:199],v[112:115],a[32:47]\n"
"ds_read_b64_tr_b16 a[164:165],v233 offset:8448\n"
"ds_read_b64_tr_b16 a[166:167],v233 offset:9472\n"
"buffer_load_dwordx4 v217,s[44:47],s78 offen offset:3072 lds\n"
"v_fmac_f32_dpp v172,v161,v196 row_newbcast:4 row_mask:0xf bank_mask:0xf\n"
"v_fmac_f32_dpp v173,v161,v197 row_newbcast:5 row_mask:0xf bank_mask:0xf\n"
"v_mfma_f32_32x32x16_bf16 a[48:63],a[212:215],v[112:115],a[48:63]\n"
"ds_read_b64_tr_b16 a[180:181],v233 offset:24832\n"
"ds_read_b64_tr_b16 a[182:183],v233 offset:25856\n"
"v_fmac_f32_dpp v174,v161,v198 row_newbcast:6 row_mask:0xf bank_mask:0xf\n"
"v_fmac_f32_dpp v175,v161,v199 row_newbcast:7 row_mask:0xf bank_mask:0xf\n"
"v_cvt_pk_bf16_f32 v152,v168,v169\n"
"v_cvt_pk_bf16_f32 v153,v170,v171\n"
"v_mfma_f32_32x32x16_bf16 a[64:79],a[200:203],v[112:115],a[64:79]\n"
"ds_read_b64_tr_b16 a[168:169],v233 offset:8704\n"
"ds_read_b64_tr_b16 a[170:171],v233 offset:9728\n"
"s_add_u32 m0,s66,49152\n"
"s_add_u32 s86,s78,256\n"
"v_cvt_pk_bf16_f32 v154,v172,v173\n"
"v_mfma_f32_32x32x16_bf16 a[80:95],a[216:219],v[112:115],a[80:95]\n"
"ds_read_b64_tr_b16 a[184:185],v233 offset:25088\n"
"ds_read_b64_tr_b16 a[186:187],v233 offset:26112\n"
"buffer_load_dwordx4 v230,s[44:47],s86 offen offset:0 lds\n"
"v_cvt_pk_bf16_f32 v155,v174,v175\n"
"ds_read_b32 v160,v223 offset:2048\n"
"v_mfma_f32_32x32x16_bf16 a[96:111],a[204:207],v[112:115],a[96:111]\n"
"ds_read_b64_tr_b16 a[172:173],v233 offset:8960\n"
"ds_read_b64_tr_b16 a[174:175],v233 offset:9984\n"
"ds_read_b128 v[168:171],v242 offset:4352\n"
"ds_read_b128 v[172:175],v242 offset:4368\n"
"v_lshlrev_b32 v200,16,v156\n"
"v_and_b32 v201,s73,v156\n"
"v_mfma_f32_32x32x16_bf16 a[112:127],a[220:223],v[112:115],a[112:127]\n"
"ds_read_b64_tr_b16 a[188:189],v233 offset:25344\n"
"ds_read_b64_tr_b16 a[190:191],v233 offset:26368\n"
"v_lshlrev_b32 v202,16,v157\n"
"v_and_b32 v203,s73,v157\n"
"v_lshlrev_b32 v204,16,v158\n"
"v_and_b32 v205,s73,v158\n"
"s_waitcnt lgkmcnt(15)\n"
"v_mfma_f32_32x32x16_bf16 a[0:15],a[128:131],v[116:119],a[0:15]\n"
"ds_read_b64_tr_b16 a[192:193],v233 offset:12288\n"
"ds_read_b64_tr_b16 a[194:195],v233 offset:13312\n"
"ds_read_b64_tr_b16 a[208:209],v233 offset:28672\n"
"buffer_load_dwordx4 v231,s[44:47],s86 offen offset:1024 lds\n"
"v_mfma_f32_32x32x16_bf16 a[16:31],a[144:147],v[116:119],a[16:31]\n"
"ds_read_b64_tr_b16 a[210:211],v233 offset:29696\n"
"ds_read_b64_tr_b16 a[196:197],v233 offset:12544\n"
"v_lshlrev_b32 v206,16,v159\n"
"v_and_b32 v207,s73,v159\n"
"v_fma_f32 v200,v200,v222,v220\n"
"v_fma_f32 v201,v201,v222,v220\n"
"v_mfma_f32_32x32x16_bf16 a[32:47],a[132:135],v[116:119],a[32:47]\n"
"ds_read_b64_tr_b16 a[198:199],v233 offset:13568\n"
"ds_read_b64_tr_b16 a[212:213],v233 offset:28928\n"
"v_fma_f32 v202,v202,v222,v220\n"
"v_fma_f32 v203,v203,v222,v220\n"
"v_fma_f32 v204,v204,v222,v220\n"
"v_fma_f32 v205,v205,v222,v220\n"
"v_mfma_f32_32x32x16_bf16 a[48:63],a[148:151],v[116:119],a[48:63]\n"
"ds_read_b64_tr_b16 a[214:215],v233 offset:29952\n"
"ds_read_b64_tr_b16 a[200:201],v233 offset:12800\n"
"ds_read_b64_tr_b16 a[202:203],v233 offset:13824\n"
"buffer_load_dwordx4 v216,s[44:47],s86 offen offset:2048 lds\n"
"v_fma_f32 v206,v206,v222,v220\n"
"v_mfma_f32_32x32x16_bf16 a[64:79],a[136:139],v[116:119],a[64:79]\n"
"ds_read_b64_tr_b16 a[216:217],v233 offset:29184\n"
"ds_read_b64_tr_b16 a[218:219],v233 offset:30208\n"
"v_fma_f32 v207,v207,v222,v220\n"
"v_fmac_f32_dpp v184,v161,v200 row_newbcast:8 row_mask:0xf bank_mask:0xf\n"
"v_mfma_f32_32x32x16_bf16 a[80:95],a[152:155],v[116:119],a[80:95]\n"
"ds_read_b64_tr_b16 a[204:205],v233 offset:13056\n"
"ds_read_b64_tr_b16 a[206:207],v233 offset:14080\n"
"v_fmac_f32_dpp v185,v161,v201 row_newbcast:9 row_mask:0xf bank_mask:0xf\n"
"v_fmac_f32_dpp v186,v161,v202 row_newbcast:10 row_mask:0xf bank_mask:0xf\n"
"v_fmac_f32_dpp v187,v161,v203 row_newbcast:11 row_mask:0xf bank_mask:0xf\n"
"v_fmac_f32_dpp v188,v161,v204 row_newbcast:12 row_mask:0xf bank_mask:0xf\n"
"v_mfma_f32_32x32x16_bf16 a[96:111],a[140:143],v[116:119],a[96:111]\n"
"ds_read_b64_tr_b16 a[220:221],v233 offset:29440\n"
"ds_read_b64_tr_b16 a[222:223],v233 offset:30464\n"
"buffer_load_dwordx4 v217,s[44:47],s86 offen offset:3072 lds\n"
"v_fmac_f32_dpp v189,v161,v205 row_newbcast:13 row_mask:0xf bank_mask:0xf\n"
"v_fmac_f32_dpp v190,v161,v206 row_newbcast:14 row_mask:0xf bank_mask:0xf\n"
"v_mfma_f32_32x32x16_bf16 a[112:127],a[156:159],v[116:119],a[112:127]\n"
"v_fmac_f32_dpp v191,v161,v207 row_newbcast:15 row_mask:0xf bank_mask:0xf\n"
"v_cvt_pk_bf16_f32 v156,v184,v185\n"
"v_cvt_pk_bf16_f32 v157,v186,v187\n"
"v_cvt_pk_bf16_f32 v158,v188,v189\n"
"v_cvt_pk_bf16_f32 v159,v190,v191\n"
"ds_read_b128 v[184:187],v242 offset:4416\n"
"s_waitcnt lgkmcnt(15)\n"
"v_mfma_f32_32x32x16_bf16 a[0:15],a[160:163],v[120:123],a[0:15]\n"
"ds_read_b128 v[188:191],v242 offset:4432\n"
"v_lshlrev_b32 v192,16,v16\n"
"v_and_b32 v193,s73,v16\n"
"v_lshlrev_b32 v194,16,v17\n"
"v_mfma_f32_32x32x16_bf16 a[16:31],a[176:179],v[120:123],a[16:31]\n"
"s_waitcnt vmcnt(17) lgkmcnt(0)\n"
"s_barrier\n"
"v_add_u32 v233,65536,v232\n"
"v_and_b32 v195,s73,v17\n"
"v_lshlrev_b32 v196,16,v18\n"
"v_mfma_f32_32x32x16_bf16 a[32:47],a[164:167],v[120:123],a[32:47]\n"
"ds_read_b64_tr_b16 a[128:129],v233 offset:0\n"
"ds_read_b64_tr_b16 a[130:131],v233 offset:1024\n"
"ds_read_b64_tr_b16 a[144:145],v233 offset:16384\n"
"v_and_b32 v197,s73,v18\n"
"v_lshlrev_b32 v198,16,v19\n"
"v_and_b32 v199,s73,v19\n"
"v_mfma_f32_32x32x16_bf16 a[48:63],a[180:183],v[120:123],a[48:63]\n"
"ds_read_b64_tr_b16 a[146:147],v233 offset:17408\n"
"ds_read_b64_tr_b16 a[132:133],v233 offset:256\n"
"ds_read_b64_tr_b16 a[134:135],v233 offset:1280\n"
"v_fma_f32 v192,v192,v222,v220\n"
"v_fma_f32 v193,v193,v222,v220\n"
"v_fma_f32 v194,v194,v222,v220\n"
"v_mfma_f32_32x32x16_bf16 a[64:79],a[168:171],v[120:123],a[64:79]\n"
"ds_read_b64_tr_b16 a[148:149],v233 offset:16640\n"
"ds_read_b64_tr_b16 a[150:151],v233 offset:17664\n"
"v_fma_f32 v195,v195,v222,v220\n"
"v_fma_f32 v196,v196,v222,v220\n"
"v_fma_f32 v197,v197,v222,v220\n"
"v_mfma_f32_32x32x16_bf16 a[80:95],a[184:187],v[120:123],a[80:95]\n"
"ds_read_b64_tr_b16 a[136:137],v233 offset:512\n"
"ds_read_b64_tr_b16 a[138:139],v233 offset:1536\n"
"ds_read_b64_tr_b16 a[152:153],v233 offset:16896\n"
"v_fma_f32 v198,v198,v222,v220\n"
"v_fma_f32 v199,v199,v222,v220\n"
"v_mfma_f32_32x32x16_bf16 a[96:111],a[172:175],v[120:123],a[96:111]\n"
"ds_read_b64_tr_b16 a[154:155],v233 offset:17920\n"
"ds_read_b64_tr_b16 a[140:141],v233 offset:768\n"
"ds_read_b64_tr_b16 a[142:143],v233 offset:1792\n"
"v_fmac_f32_dpp v168,v160,v192 row_newbcast:0 row_mask:0xf bank_mask:0xf\n"
"v_fmac_f32_dpp v169,v160,v193 row_newbcast:1 row_mask:0xf bank_mask:0xf\n"
"v_fmac_f32_dpp v170,v160,v194 row_newbcast:2 row_mask:0xf bank_mask:0xf\n"
"v_mfma_f32_32x32x16_bf16 a[112:127],a[188:191],v[120:123],a[112:127]\n"
"ds_read_b64_tr_b16 a[156:157],v233 offset:17152\n"
"ds_read_b64_tr_b16 a[158:159],v233 offset:18176\n"
"v_fmac_f32_dpp v171,v160,v195 row_newbcast:3 row_mask:0xf bank_mask:0xf\n"
"v_fmac_f32_dpp v172,v160,v196 row_newbcast:4 row_mask:0xf bank_mask:0xf\n"
"v_fmac_f32_dpp v173,v160,v197 row_newbcast:5 row_mask:0xf bank_mask:0xf\n"
"v_fmac_f32_dpp v174,v160,v198 row_newbcast:6 row_mask:0xf bank_mask:0xf\n"
"v_mfma_f32_32x32x16_bf16 a[0:15],a[192:195],v[124:127],a[0:15]\n"
"ds_read_b64_tr_b16 a[160:161],v233 offset:4096\n"
"ds_read_b64_tr_b16 a[162:163],v233 offset:5120\n"
"s_add_u32 m0,s65,0\n"
"v_subrev_u32 v239,8192,v228\n"
"v_fmac_f32_dpp v175,v160,v199 row_newbcast:7 row_mask:0xf bank_mask:0xf\n"
"v_mfma_f32_32x32x16_bf16 a[16:31],a[208:211],v[124:127],a[16:31]\n"
"ds_read_b64_tr_b16 a[176:177],v233 offset:20480\n"
"ds_read_b64_tr_b16 a[178:179],v233 offset:21504\n"
"buffer_load_dwordx4 v239,s[48:51],0 offen lds\n"
"v_cvt_pk_bf16_f32 v16,v168,v169\n"
"v_cvt_pk_bf16_f32 v17,v170,v171\n"
"v_mfma_f32_32x32x16_bf16 a[32:47],a[196:199],v[124:127],a[32:47]\n"
"ds_read_b64_tr_b16 a[164:165],v233 offset:4352\n"
"ds_read_b64_tr_b16 a[166:167],v233 offset:5376\n"
"v_cvt_pk_bf16_f32 v18,v172,v173\n"
"v_cvt_pk_bf16_f32 v19,v174,v175\n"
"ds_read_b32 v161,v223 offset:2176\n"
"ds_read_b128 v[168:171],v242 offset:4480\n"
"v_mfma_f32_32x32x16_bf16 a[48:63],a[212:215],v[124:127],a[48:63]\n"
"ds_read_b64_tr_b16 a[180:181],v233 offset:20736\n"
"ds_read_b64_tr_b16 a[182:183],v233 offset:21760\n"
"buffer_load_dwordx4 v239,s[48:51],0 offen offset:1024 lds\n"
"ds_read_b128 v[172:175],v242 offset:4496\n"
"v_lshlrev_b32 v200,16,v20\n"
"v_mfma_f32_32x32x16_bf16 a[64:79],a[200:203],v[124:127],a[64:79]\n"
"ds_read_b64_tr_b16 a[168:169],v233 offset:4608\n"
"ds_read_b64_tr_b16 a[170:171],v233 offset:5632\n"
"v_and_b32 v201,s73,v20\n"
"v_lshlrev_b32 v202,16,v21\n"
"v_and_b32 v203,s73,v21\n"
"v_mfma_f32_32x32x16_bf16 a[80:95],a[216:219],v[124:127],a[80:95]\n"
"ds_read_b64_tr_b16 a[184:185],v233 offset:20992\n"
"ds_read_b64_tr_b16 a[186:187],v233 offset:22016\n"
"s_add_u32 m0,s65,8192\n"
"s_mov_b32 s68,0\n"
"v_lshlrev_b32 v204,16,v22\n"
"v_and_b32 v205,s73,v22\n"
"v_mfma_f32_32x32x16_bf16 a[96:111],a[204:207],v[124:127],a[96:111]\n"
"ds_read_b64_tr_b16 a[172:173],v233 offset:4864\n"
"ds_read_b64_tr_b16 a[174:175],v233 offset:5888\n"
"buffer_load_dwordx4 v228,s[48:51],s68 offen lds\n"
"v_lshlrev_b32 v206,16,v23\n"
"v_and_b32 v207,s73,v23\n"
"v_mfma_f32_32x32x16_bf16 a[112:127],a[220:223],v[124:127],a[112:127]\n"
"ds_read_b64_tr_b16 a[188:189],v233 offset:21248\n"
"ds_read_b64_tr_b16 a[190:191],v233 offset:22272\n"
"v_fma_f32 v200,v200,v222,v220\n"
"v_fma_f32 v201,v201,v222,v220\n"
"v_fma_f32 v202,v202,v222,v220\n"
"v_fma_f32 v203,v203,v222,v220\n"
"s_waitcnt lgkmcnt(15)\n"
"v_mfma_f32_32x32x16_bf16 a[0:15],a[128:131],v[128:131],a[0:15]\n"
"ds_read_b64_tr_b16 a[192:193],v233 offset:8192\n"
"ds_read_b64_tr_b16 a[194:195],v233 offset:9216\n"
"buffer_load_dwordx4 v228,s[48:51],s68 offen offset:1024 lds\n"
"v_fma_f32 v204,v204,v222,v220\n"
"v_mfma_f32_32x32x16_bf16 a[16:31],a[144:147],v[128:131],a[16:31]\n"
"ds_read_b64_tr_b16 a[208:209],v233 offset:24576\n"
"ds_read_b64_tr_b16 a[210:211],v233 offset:25600\n"
"v_fma_f32 v205,v205,v222,v220\n"
"v_fma_f32 v206,v206,v222,v220\n"
"v_fma_f32 v207,v207,v222,v220\n"
"v_mfma_f32_32x32x16_bf16 a[32:47],a[132:135],v[128:131],a[32:47]\n"
"ds_read_b64_tr_b16 a[196:197],v233 offset:8448\n"
"ds_read_b64_tr_b16 a[198:199],v233 offset:9472\n"
"s_add_u32 m0,s65,16384\n"
"s_mov_b32 s68,8192\n"
"v_fmac_f32_dpp v184,v160,v200 row_newbcast:8 row_mask:0xf bank_mask:0xf\n"
"v_fmac_f32_dpp v185,v160,v201 row_newbcast:9 row_mask:0xf bank_mask:0xf\n"
"v_mfma_f32_32x32x16_bf16 a[48:63],a[148:151],v[128:131],a[48:63]\n"
"ds_read_b64_tr_b16 a[212:213],v233 offset:24832\n"
"ds_read_b64_tr_b16 a[214:215],v233 offset:25856\n"
"buffer_load_dwordx4 v228,s[48:51],s68 offen lds\n"
"v_fmac_f32_dpp v186,v160,v202 row_newbcast:10 row_mask:0xf bank_mask:0xf\n"
"v_fmac_f32_dpp v187,v160,v203 row_newbcast:11 row_mask:0xf bank_mask:0xf\n"
"v_mfma_f32_32x32x16_bf16 a[64:79],a[136:139],v[128:131],a[64:79]\n"
"ds_read_b64_tr_b16 a[200:201],v233 offset:8704\n"
"ds_read_b64_tr_b16 a[202:203],v233 offset:9728\n"
"v_fmac_f32_dpp v188,v160,v204 row_newbcast:12 row_mask:0xf bank_mask:0xf\n"
"v_fmac_f32_dpp v189,v160,v205 row_newbcast:13 row_mask:0xf bank_mask:0xf\n"
"v_fmac_f32_dpp v190,v160,v206 row_newbcast:14 row_mask:0xf bank_mask:0xf\n"
"v_mfma_f32_32x32x16_bf16 a[80:95],a[152:155],v[128:131],a[80:95]\n"
"ds_read_b64_tr_b16 a[216:217],v233 offset:25088\n"
"ds_read_b64_tr_b16 a[218:219],v233 offset:26112\n"
"buffer_load_dwordx4 v228,s[48:51],s68 offen offset:1024 lds\n"
"v_fmac_f32_dpp v191,v160,v207 row_newbcast:15 row_mask:0xf bank_mask:0xf\n"
"v_cvt_pk_bf16_f32 v20,v184,v185\n"
"v_mfma_f32_32x32x16_bf16 a[96:111],a[140:143],v[128:131],a[96:111]\n"
"ds_read_b64_tr_b16 a[204:205],v233 offset:8960\n"
"ds_read_b64_tr_b16 a[206:207],v233 offset:9984\n"
"v_cvt_pk_bf16_f32 v21,v186,v187\n"
"v_cvt_pk_bf16_f32 v22,v188,v189\n"
"v_cvt_pk_bf16_f32 v23,v190,v191\n"
"v_accvgpr_read_b32 v52,a224\n"
"v_mfma_f32_32x32x16_bf16 a[112:127],a[156:159],v[128:131],a[112:127]\n"
"ds_read_b64_tr_b16 a[220:221],v233 offset:25344\n"
"ds_read_b64_tr_b16 a[222:223],v233 offset:26368\n"
"s_add_u32 m0,s65,24576\n"
"s_mov_b32 s68,16384\n"
"v_accvgpr_read_b32 v53,a225\n"
"v_accvgpr_read_b32 v54,a226\n"
"s_waitcnt lgkmcnt(15)\n"
"v_mfma_f32_32x32x16_bf16 a[0:15],a[160:163],v[132:135],a[0:15]\n"
"ds_read_b64_tr_b16 a[128:129],v233 offset:12288\n"
"ds_read_b64_tr_b16 a[130:131],v233 offset:13312\n"
"ds_read_b64_tr_b16 a[144:145],v233 offset:28672\n"
"buffer_load_dwordx4 v228,s[48:51],s68 offen lds\n"
"v_mfma_f32_32x32x16_bf16 a[16:31],a[176:179],v[132:135],a[16:31]\n"
"ds_read_b64_tr_b16 a[146:147],v233 offset:29696\n"
"ds_read_b64_tr_b16 a[132:133],v233 offset:12544\n"
"v_accvgpr_read_b32 v55,a227\n"
"v_accvgpr_read_b32 v56,a229\n"
"s_waitcnt vmcnt(21)\n"
"v_lshlrev_b32 v60,16,v32\n"
"v_mfma_f32_32x32x16_bf16 a[32:47],a[164:167],v[132:135],a[32:47]\n"
"ds_read_b64_tr_b16 a[134:135],v233 offset:13568\n"
"ds_read_b64_tr_b16 a[148:149],v233 offset:28928\n"
"buffer_load_dwordx4 v228,s[48:51],s68 offen offset:1024 lds\n"
"v_and_b32 v61,s73,v32\n"
"v_lshlrev_b32 v62,16,v33\n"
"v_mfma_f32_32x32x16_bf16 a[48:63],a[180:183],v[132:135],a[48:63]\n"
"ds_read_b64_tr_b16 a[150:151],v233 offset:29952\n"
"ds_read_b64_tr_b16 a[136:137],v233 offset:12800\n"
"ds_read_b64_tr_b16 a[138:139],v233 offset:13824\n"
"v_and_b32 v63,s73,v33\n"
"v_lshlrev_b32 v64,16,v34\n"
"v_and_b32 v65,s73,v34\n"
"v_mfma_f32_32x32x16_bf16 a[64:79],a[168:171],v[132:135],a[64:79]\n"
"ds_read_b64_tr_b16 a[152:153],v233 offset:29184\n"
"ds_read_b64_tr_b16 a[154:155],v233 offset:30208\n"
"v_lshlrev_b32 v66,16,v35\n"
"v_and_b32 v67,s73,v35\n"
"v_mul_f32 v68,v52,v60\n"
"v_mfma_f32_32x32x16_bf16 a[80:95],a[184:187],v[132:135],a[80:95]\n"
"ds_read_b64_tr_b16 a[140:141],v233 offset:13056\n"
"ds_read_b64_tr_b16 a[142:143],v233 offset:14080\n"
"v_mul_f32 v69,v52,v61\n"
"v_mul_f32 v70,v52,v62\n"
"v_mul_f32 v71,v52,v63\n"
"v_mul_f32 v72,v52,v64\n"
"v_mfma_f32_32x32x16_bf16 a[96:111],a[172:175],v[132:135],a[96:111]\n"
"ds_read_b64_tr_b16 a[156:157],v233 offset:29440\n"
"ds_read_b64_tr_b16 a[158:159],v233 offset:30464\n"
"v_mul_f32 v73,v52,v65\n"
"v_mul_f32 v74,v52,v66\n"
"v_mul_f32 v75,v52,v67\n"
"v_lshlrev_b32 v60,16,v36\n"
"v_mfma_f32_32x32x16_bf16 a[112:127],a[188:191],v[132:135],a[112:127]\n"
"v_and_b32 v61,s73,v36\n"
"v_lshlrev_b32 v62,16,v37\n"
"v_and_b32 v63,s73,v37\n"
"v_lshlrev_b32 v64,16,v38\n"
"v_and_b32 v65,s73,v38\n"
"v_lshlrev_b32 v66,16,v39\n"
"s_waitcnt lgkmcnt(15)\n"
"v_mfma_f32_32x32x16_bf16 a[0:15],a[192:195],v[136:139],a[0:15]\n"
"v_and_b32 v67,s73,v39\n"
"v_fmac_f32 v68,v53,v60\n"
"v_fmac_f32 v69,v53,v61\n"
"v_fmac_f32 v70,v53,v62\n"
"v_fmac_f32 v71,v53,v63\n"
"v_mfma_f32_32x32x16_bf16 a[16:31],a[208:211],v[136:139],a[16:31]\n"
"s_waitcnt vmcnt(16) lgkmcnt(0)\n"
"s_barrier\n"
"v_add_u32 v233,98304,v232\n"
"v_fmac_f32 v72,v53,v64\n"
"v_fmac_f32 v73,v53,v65\n"
"v_mfma_f32_32x32x16_bf16 a[32:47],a[196:199],v[136:139],a[32:47]\n"
"ds_read_b64_tr_b16 a[160:161],v233 offset:0\n"
"ds_read_b64_tr_b16 a[162:163],v233 offset:1024\n"
"ds_read_b64_tr_b16 a[176:177],v233 offset:16384\n"
"v_fmac_f32 v74,v53,v66\n"
"v_fmac_f32 v75,v53,v67\n"
"v_lshlrev_b32 v60,16,v40\n"
"v_mfma_f32_32x32x16_bf16 a[48:63],a[212:215],v[136:139],a[48:63]\n"
"ds_read_b64_tr_b16 a[178:179],v233 offset:17408\n"
"ds_read_b64_tr_b16 a[164:165],v233 offset:256\n"
"ds_read_b64_tr_b16 a[166:167],v233 offset:1280\n"
"v_and_b32 v61,s73,v40\n"
"v_lshlrev_b32 v62,16,v41\n"
"v_and_b32 v63,s73,v41\n"
"v_mfma_f32_32x32x16_bf16 a[64:79],a[200:203],v[136:139],a[64:79]\n"
"ds_read_b64_tr_b16 a[180:181],v233 offset:16640\n"
"ds_read_b64_tr_b16 a[182:183],v233 offset:17664\n"
"v_lshlrev_b32 v64,16,v42\n"
"v_and_b32 v65,s73,v42\n"
"v_lshlrev_b32 v66,16,v43\n"
"v_mfma_f32_32x32x16_bf16 a[80:95],a[216:219],v[136:139],a[80:95]\n"
"ds_read_b64_tr_b16 a[168:169],v233 offset:512\n"
"ds_read_b64_tr_b16 a[170:171],v233 offset:1536\n"
"ds_read_b64_tr_b16 a[184:185],v233 offset:16896\n"
"v_and_b32 v67,s73,v43\n"
"v_fmac_f32 v68,v54,v60\n"
"v_fmac_f32 v69,v54,v61\n"
"v_mfma_f32_32x32x16_bf16 a[96:111],a[204:207],v[136:139],a[96:111]\n"
"ds_read_b64_tr_b16 a[186:187],v233 offset:17920\n"
"ds_read_b64_tr_b16 a[172:173],v233 offset:768\n"
"ds_read_b64_tr_b16 a[174:175],v233 offset:1792\n"
"v_fmac_f32 v70,v54,v62\n"
"v_fmac_f32 v71,v54,v63\n"
"v_fmac_f32 v72,v54,v64\n"
"v_mfma_f32_32x32x16_bf16 a[112:127],a[220:223],v[136:139],a[112:127]\n"
"ds_read_b64_tr_b16 a[188:189],v233 offset:17152\n"
"ds_read_b64_tr_b16 a[190:191],v233 offset:18176\n"
"v_fmac_f32 v73,v54,v65\n"
"v_fmac_f32 v74,v54,v66\n"
"v_fmac_f32 v75,v54,v67\n"
"v_lshlrev_b32 v60,16,v44\n"
"v_mfma_f32_32x32x16_bf16 a[0:15],a[128:131],v[140:143],a[0:15]\n"
"ds_read_b64_tr_b16 a[192:193],v233 offset:4096\n"
"ds_read_b64_tr_b16 a[194:195],v233 offset:5120\n"
"s_add_u32 m0,s65,73728\n"
"s_mov_b32 s68,0\n"
"v_and_b32 v61,s73,v44\n"
"v_mfma_f32_32x32x16_bf16 a[16:31],a[144:147],v[140:143],a[16:31]\n"
"ds_read_b64_tr_b16 a[208:209],v233 offset:20480\n"
"ds_read_b64_tr_b16 a[210:211],v233 offset:21504\n"
"buffer_load_dwordx4 v229,s[52:55],s68 offen lds\n"
"v_lshlrev_b32 v62,16,v45\n"
"v_and_b32 v63,s73,v45\n"
"v_mfma_f32_32x32x16_bf16 a[32:47],a[132:135],v[140:143],a[32:47]\n"
"ds_read_b64_tr_b16 a[196:197],v233 offset:4352\n"
"ds_read_b64_tr_b16 a[198:199],v233 offset:5376\n"
"v_lshlrev_b32 v64,16,v46\n"
"v_and_b32 v65,s73,v46\n"
"v_lshlrev_b32 v66,16,v47\n"
"v_and_b32 v67,s73,v47\n"
"v_mfma_f32_32x32x16_bf16 a[48:63],a[148:151],v[140:143],a[48:63]\n"
"ds_read_b64_tr_b16 a[212:213],v233 offset:20736\n"
"ds_read_b64_tr_b16 a[214:215],v233 offset:21760\n"
"buffer_load_dwordx4 v229,s[52:55],s68 offen offset:1024 lds\n"
"v_fmac_f32 v68,v55,v60\n"
"v_fmac_f32 v69,v55,v61\n"
"v_mfma_f32_32x32x16_bf16 a[64:79],a[136:139],v[140:143],a[64:79]\n"
"ds_read_b64_tr_b16 a[200:201],v233 offset:4608\n"
"ds_read_b64_tr_b16 a[202:203],v233 offset:5632\n"
"v_fmac_f32 v70,v55,v62\n"
"v_fmac_f32 v71,v55,v63\n"
"v_fmac_f32 v72,v55,v64\n"
"v_mfma_f32_32x32x16_bf16 a[80:95],a[152:155],v[140:143],a[80:95]\n"
"ds_read_b64_tr_b16 a[216:217],v233 offset:20992\n"
"ds_read_b64_tr_b16 a[218:219],v233 offset:22016\n"
"s_add_u32 m0,s65,81920\n"
"s_mov_b32 s68,8192\n"
"v_fmac_f32 v73,v55,v65\n"
"v_fmac_f32 v74,v55,v66\n"
"v_mfma_f32_32x32x16_bf16 a[96:111],a[140:143],v[140:143],a[96:111]\n"
"ds_read_b64_tr_b16 a[204:205],v233 offset:4864\n"
"ds_read_b64_tr_b16 a[206:207],v233 offset:5888\n"
"buffer_load_dwordx4 v229,s[52:55],s68 offen lds\n"
"v_fmac_f32 v75,v55,v67\n"
"v_lshlrev_b32 v60,16,v48\n"
"v_mfma_f32_32x32x16_bf16 a[112:127],a[156:159],v[140:143],a[112:127]\n"
"ds_read_b64_tr_b16 a[220:221],v233 offset:21248\n"
"ds_read_b64_tr_b16 a[222:223],v233 offset:22272\n"
"v_and_b32 v61,s73,v48\n"
"v_lshlrev_b32 v62,16,v49\n"
"v_and_b32 v63,s73,v49\n"
"v_lshlrev_b32 v64,16,v50\n"
"s_waitcnt lgkmcnt(15)\n"
"v_mfma_f32_32x32x16_bf16 a[0:15],a[160:163],v[144:147],a[0:15]\n"
"ds_read_b64_tr_b16 a[128:129],v233 offset:8192\n"
"ds_read_b64_tr_b16 a[130:131],v233 offset:9216\n"
"buffer_load_dwordx4 v229,s[52:55],s68 offen offset:1024 lds\n"
"v_and_b32 v65,s73,v50\n"
"v_mfma_f32_32x32x16_bf16 a[16:31],a[176:179],v[144:147],a[16:31]\n"
"ds_read_b64_tr_b16 a[144:145],v233 offset:24576\n"
"ds_read_b64_tr_b16 a[146:147],v233 offset:25600\n"
"v_lshlrev_b32 v66,16,v51\n"
"v_and_b32 v67,s73,v51\n"
"v_fmac_f32 v68,v56,v60\n"
"v_fmac_f32 v69,v56,v61\n"
"v_mfma_f32_32x32x16_bf16 a[32:47],a[164:167],v[144:147],a[32:47]\n"
"ds_read_b64_tr_b16 a[132:133],v233 offset:8448\n"
"ds_read_b64_tr_b16 a[134:135],v233 offset:9472\n"
"s_add_u32 m0,s65,90112\n"
"s_mov_b32 s68,16384\n"
"v_fmac_f32 v70,v56,v62\n"
"v_fmac_f32 v71,v56,v63\n"
"v_mfma_f32_32x32x16_bf16 a[48:63],a[180:183],v[144:147],a[48:63]\n"
"ds_read_b64_tr_b16 a[148:149],v233 offset:24832\n"
"ds_read_b64_tr_b16 a[150:151],v233 offset:25856\n"
"buffer_load_dwordx4 v229,s[52:55],s68 offen lds\n"
"v_fmac_f32 v72,v56,v64\n"
"v_fmac_f32 v73,v56,v65\n"
"v_mfma_f32_32x32x16_bf16 a[64:79],a[168:171],v[144:147],a[64:79]\n"
"ds_read_b64_tr_b16 a[136:137],v233 offset:8704\n"
"ds_read_b64_tr_b16 a[138:139],v233 offset:9728\n"
"v_fmac_f32 v74,v56,v66\n"
"v_fmac_f32 v75,v56,v67\n"
"v_permlane32_swap_b32 v68,v72\n"
"v_mfma_f32_32x32x16_bf16 a[80:95],a[184:187],v[144:147],a[80:95]\n"
"ds_read_b64_tr_b16 a[152:153],v233 offset:25088\n"
"ds_read_b64_tr_b16 a[154:155],v233 offset:26112\n"
"buffer_load_dwordx4 v229,s[52:55],s68 offen offset:1024 lds\n"
"v_permlane32_swap_b32 v69,v73\n"
"v_permlane32_swap_b32 v70,v74\n"
"v_mfma_f32_32x32x16_bf16 a[96:111],a[172:175],v[144:147],a[96:111]\n"
"ds_read_b64_tr_b16 a[140:141],v233 offset:8960\n"
"ds_read_b64_tr_b16 a[142:143],v233 offset:9984\n"
"v_permlane32_swap_b32 v71,v75\n"
"v_add_f32 v68,v68,v72\n"
"v_add_f32 v69,v69,v73\n"
"v_add_f32 v70,v70,v74\n"
"v_mfma_f32_32x32x16_bf16 a[112:127],a[188:191],v[144:147],a[112:127]\n"
"ds_read_b64_tr_b16 a[156:157],v233 offset:25344\n"
"ds_read_b64_tr_b16 a[158:159],v233 offset:26368\n"
"v_add_f32 v71,v71,v75\n"
"s_nop 0\n"
"v_permlane16_swap_b32 v68,v70\n"
"v_permlane16_swap_b32 v69,v71\n"
"s_waitcnt lgkmcnt(15)\n"
"v_mfma_f32_32x32x16_bf16 a[0:15],a[192:195],v[148:151],a[0:15]\n"
"ds_read_b64_tr_b16 a[160:161],v233 offset:12288\n"
"ds_read_b64_tr_b16 a[162:163],v233 offset:13312\n"
"ds_read_b64_tr_b16 a[176:177],v233 offset:28672\n"
"v_add_f32 v68,v68,v70\n"
"v_add_f32 v69,v69,v71\n"
"v_mfma_f32_32x32x16_bf16 a[16:31],a[208:211],v[148:151],a[16:31]\n"
"ds_read_b64_tr_b16 a[178:179],v233 offset:29696\n"
"ds_read_b64_tr_b16 a[164:165],v233 offset:12544\n"
"s_nop 0\n"
"v_add_f32_dpp v68,v68,v68 row_ror:8 row_mask:0xf bank_mask:0xf\n"
"v_add_f32_dpp v69,v69,v69 row_ror:8 row_mask:0xf bank_mask:0xf\n"
"s_nop 0\n"
"v_mfma_f32_32x32x16_bf16 a[32:47],a[196:199],v[148:151],a[32:47]\n"
"ds_read_b64_tr_b16 a[166:167],v233 offset:13568\n"
"ds_read_b64_tr_b16 a[180:181],v233 offset:28928\n"
"v_add_f32_dpp v68,v68,v68 row_ror:4 row_mask:0xf bank_mask:0xf\n"
"v_add_f32_dpp v69,v69,v69 row_ror:4 row_mask:0xf bank_mask:0xf\n"
"s_nop 0\n"
"v_add_f32_dpp v68,v68,v68 row_ror:2 row_mask:0xf bank_mask:0xf\n"
"v_mfma_f32_32x32x16_bf16 a[48:63],a[212:215],v[148:151],a[48:63]\n"
"ds_read_b64_tr_b16 a[182:183],v233 offset:29952\n"
"ds_read_b64_tr_b16 a[168:169],v233 offset:12800\n"
"ds_read_b64_tr_b16 a[170:171],v233 offset:13824\n"
"v_add_f32_dpp v69,v69,v69 row_ror:2 row_mask:0xf bank_mask:0xf\n"
"ds_write_b64 v224,v[68:69]\n"
"ds_read_b128 v[184:187],v242 offset:4544\n"
"v_mfma_f32_32x32x16_bf16 a[64:79],a[200:203],v[148:151],a[64:79]\n"
"ds_read_b64_tr_b16 a[184:185],v233 offset:29184\n"
"ds_read_b64_tr_b16 a[186:187],v233 offset:30208\n"
"ds_read_b128 v[188:191],v242 offset:4560\n"
"v_lshlrev_b32 v192,16,v24\n"
"v_and_b32 v193,s73,v24\n"
"v_mfma_f32_32x32x16_bf16 a[80:95],a[216:219],v[148:151],a[80:95]\n"
"ds_read_b64_tr_b16 a[172:173],v233 offset:13056\n"
"ds_read_b64_tr_b16 a[174:175],v233 offset:14080\n"
"v_lshlrev_b32 v194,16,v25\n"
"v_and_b32 v195,s73,v25\n"
"v_lshlrev_b32 v196,16,v26\n"
"v_and_b32 v197,s73,v26\n"
"v_mfma_f32_32x32x16_bf16 a[96:111],a[204:207],v[148:151],a[96:111]\n"
"ds_read_b64_tr_b16 a[188:189],v233 offset:29440\n"
"ds_read_b64_tr_b16 a[190:191],v233 offset:30464\n"
"v_lshlrev_b32 v198,16,v27\n"
"v_and_b32 v199,s73,v27\n"
"v_fma_f32 v192,v192,v222,v220\n"
"v_fma_f32 v193,v193,v222,v220\n"
"v_mfma_f32_32x32x16_bf16 a[112:127],a[220:223],v[148:151],a[112:127]\n"
"v_fma_f32 v194,v194,v222,v220\n"
"v_fma_f32 v195,v195,v222,v220\n"
"v_fma_f32 v196,v196,v222,v220\n"
"v_fma_f32 v197,v197,v222,v220\n"
"v_fma_f32 v198,v198,v222,v220\n"
"v_fma_f32 v199,v199,v222,v220\n"
"s_waitcnt lgkmcnt(15)\n"
"v_mfma_f32_32x32x16_bf16 a[0:15],a[128:131],v[152:155],a[0:15]\n"
"v_fmac_f32_dpp v168,v161,v192 row_newbcast:0 row_mask:0xf bank_mask:0xf\n"
"v_fmac_f32_dpp v169,v161,v193 row_newbcast:1 row_mask:0xf bank_mask:0xf\n"
"v_fmac_f32_dpp v170,v161,v194 row_newbcast:2 row_mask:0xf bank_mask:0xf\n"
"v_fmac_f32_dpp v171,v161,v195 row_newbcast:3 row_mask:0xf bank_mask:0xf\n"
"v_mfma_f32_32x32x16_bf16 a[16:31],a[144:147],v[152:155],a[16:31]\n"
"s_waitcnt vmcnt(14) lgkmcnt(0)\n"
"s_barrier\n"
"v_add_u32 v233,32768,v232\n"
"v_fmac_f32_dpp v172,v161,v196 row_newbcast:4 row_mask:0xf bank_mask:0xf\n"
"v_fmac_f32_dpp v173,v161,v197 row_newbcast:5 row_mask:0xf bank_mask:0xf\n"
"v_mfma_f32_32x32x16_bf16 a[32:47],a[132:135],v[152:155],a[32:47]\n"
"ds_read_b64_tr_b16 a[192:193],v233 offset:0\n"
"ds_read_b64_tr_b16 a[194:195],v233 offset:1024\n"
"ds_read_b64_tr_b16 a[208:209],v233 offset:4096\n"
"ds_read2_b32 v[68:69],v221 offset0:0 offset1:16\n"
"ds_read2_b32 v[70:71],v221 offset0:32 offset1:48\n"
"v_fmac_f32_dpp v174,v161,v198 row_newbcast:6 row_mask:0xf bank_mask:0xf\n"
"v_mfma_f32_32x32x16_bf16 a[48:63],a[148:151],v[152:155],a[48:63]\n"
"ds_read_b64_tr_b16 a[210:211],v233 offset:5120\n"
"ds_read_b64_tr_b16 a[196:197],v233 offset:256\n"
"ds_read_b64_tr_b16 a[198:199],v233 offset:1280\n"
"v_fmac_f32_dpp v175,v161,v199 row_newbcast:7 row_mask:0xf bank_mask:0xf\n"
"v_cvt_pk_bf16_f32 v24,v168,v169\n"
"v_cvt_pk_bf16_f32 v25,v170,v171\n"
"v_mfma_f32_32x32x16_bf16 a[64:79],a[136:139],v[152:155],a[64:79]\n"
"ds_read_b64_tr_b16 a[212:213],v233 offset:4352\n"
"ds_read_b64_tr_b16 a[214:215],v233 offset:5376\n"
"v_cvt_pk_bf16_f32 v26,v172,v173\n"
"v_cvt_pk_bf16_f32 v27,v174,v175\n"
"v_lshlrev_b32 v200,16,v28\n"
"v_mfma_f32_32x32x16_bf16 a[80:95],a[152:155],v[152:155],a[80:95]\n"
"ds_read_b64_tr_b16 a[200:201],v233 offset:512\n"
"ds_read_b64_tr_b16 a[202:203],v233 offset:1536\n"
"ds_read_b64_tr_b16 a[216:217],v233 offset:4608\n"
"v_and_b32 v201,s73,v28\n"
"v_lshlrev_b32 v202,16,v29\n"
"v_and_b32 v203,s73,v29\n"
"v_mfma_f32_32x32x16_bf16 a[96:111],a[140:143],v[152:155],a[96:111]\n"
"ds_read_b64_tr_b16 a[218:219],v233 offset:5632\n"
"ds_read_b64_tr_b16 a[204:205],v233 offset:768\n"
"ds_read_b64_tr_b16 a[206:207],v233 offset:1792\n"
"s_waitcnt lgkmcnt(11)\n"
"v_add_f32 v68,v68,v69\n"
"v_add_f32 v70,v70,v71\n"
"v_mfma_f32_32x32x16_bf16 a[112:127],a[156:159],v[152:155],a[112:127]\n"
"ds_read_b64_tr_b16 a[220:221],v233 offset:4864\n"
"ds_read_b64_tr_b16 a[222:223],v233 offset:5888\n"
"v_add_f32 v68,v68,v70\n"
"v_add_f32 v68,s71,v68\n"
"buffer_store_dword v68,v219,s[88:91],s67 offen sc1\n"
"v_lshlrev_b32 v204,16,v30\n"
"v_mfma_f32_32x32x16_bf16 a[0:15],a[160:163],v[156:159],a[0:15]\n"
"ds_read_b64_tr_b16 a[128:129],v233 offset:8192\n"
"ds_read_b64_tr_b16 a[130:131],v233 offset:9216\n"
"s_add_u32 m0,s65,98304\n"
"s_mov_b32 s68,24576\n"
"v_and_b32 v205,s73,v30\n"
"v_mfma_f32_32x32x16_bf16 a[16:31],a[176:179],v[156:159],a[16:31]\n"
"ds_read_b64_tr_b16 a[144:145],v233 offset:12288\n"
"ds_read_b64_tr_b16 a[146:147],v233 offset:13312\n"
"buffer_load_dwordx4 v229,s[52:55],s68 offen lds\n"
"v_lshlrev_b32 v206,16,v31\n"
"v_and_b32 v207,s73,v31\n"
"v_mfma_f32_32x32x16_bf16 a[32:47],a[164:167],v[156:159],a[32:47]\n"
"ds_read_b64_tr_b16 a[132:133],v233 offset:8448\n"
"ds_read_b64_tr_b16 a[134:135],v233 offset:9472\n"
"v_fma_f32 v200,v200,v222,v220\n"
"v_fma_f32 v201,v201,v222,v220\n"
"L_pgg80_%=:\n"
"s_cbranch_execz L_pgg144_%=\n"
"v_fma_f32 v202,v202,v222,v220\n"
"v_fma_f32 v203,v203,v222,v220\n"
"v_mfma_f32_32x32x16_bf16 a[48:63],a[180:183],v[156:159],a[48:63]\n"
"ds_read_b64_tr_b16 a[148:149],v233 offset:12544\n"
"ds_read_b64_tr_b16 a[150:151],v233 offset:13568\n"
"buffer_load_dwordx4 v229,s[52:55],s68 offen offset:1024 lds\n"
"v_fma_f32 v204,v204,v222,v220\n"
"v_fma_f32 v205,v205,v222,v220\n"
"v_mfma_f32_32x32x16_bf16 a[64:79],a[168:171],v[156:159],a[64:79]\n"
"ds_read_b64_tr_b16 a[136:137],v233 offset:8704\n"
"ds_read_b64_tr_b16 a[138:139],v233 offset:9728\n"
"v_fma_f32 v206,v206,v222,v220\n"
"v_fma_f32 v207,v207,v222,v220\n"
"v_mfma_f32_32x32x16_bf16 a[80:95],a[184:187],v[156:159],a[80:95]\n"
"ds_read_b64_tr_b16 a[152:153],v233 offset:12800\n"
"ds_read_b64_tr_b16 a[154:155],v233 offset:13824\n"
"s_add_u32 m0,s65,106496\n"
"s_mov_b32 s68,32768\n"
"v_fmac_f32_dpp v184,v161,v200 row_newbcast:8 row_mask:0xf bank_mask:0xf\n"
"v_fmac_f32_dpp v185,v161,v201 row_newbcast:9 row_mask:0xf bank_mask:0xf\n"
"v_mfma_f32_32x32x16_bf16 a[96:111],a[172:175],v[156:159],a[96:111]\n"
"ds_read_b64_tr_b16 a[140:141],v233 offset:8960\n"
"ds_read_b64_tr_b16 a[142:143],v233 offset:9984\n"
"buffer_load_dwordx4 v229,s[52:55],s68 offen lds\n"
"v_fmac_f32_dpp v186,v161,v202 row_newbcast:10 row_mask:0xf bank_mask:0xf\n"
"v_fmac_f32_dpp v187,v161,v203 row_newbcast:11 row_mask:0xf bank_mask:0xf\n"
"v_mfma_f32_32x32x16_bf16 a[112:127],a[188:191],v[156:159],a[112:127]\n"
"ds_read_b64_tr_b16 a[156:157],v233 offset:13056\n"
"ds_read_b64_tr_b16 a[158:159],v233 offset:14080\n"
"v_fmac_f32_dpp v188,v161,v204 row_newbcast:12 row_mask:0xf bank_mask:0xf\n"
"v_fmac_f32_dpp v189,v161,v205 row_newbcast:13 row_mask:0xf bank_mask:0xf\n"
"v_fmac_f32_dpp v190,v161,v206 row_newbcast:14 row_mask:0xf bank_mask:0xf\n"
"v_fmac_f32_dpp v191,v161,v207 row_newbcast:15 row_mask:0xf bank_mask:0xf\n"
"s_waitcnt lgkmcnt(15)\n"
"v_mfma_f32_32x32x16_bf16 a[0:15],a[192:195],v[16:19],a[0:15]\n"
"ds_read_b64_tr_b16 a[160:161],v233 offset:16384\n"
"ds_read_b64_tr_b16 a[162:163],v233 offset:17408\n"
"buffer_load_dwordx4 v229,s[52:55],s68 offen offset:1024 lds\n"
"v_cvt_pk_bf16_f32 v28,v184,v185\n"
"v_mfma_f32_32x32x16_bf16 a[0:15],a[208:211],v[20:23],a[0:15]\n"
"ds_read_b64_tr_b16 a[176:177],v233 offset:20480\n"
"ds_read_b64_tr_b16 a[178:179],v233 offset:21504\n"
"v_cvt_pk_bf16_f32 v29,v186,v187\n"
"v_cvt_pk_bf16_f32 v30,v188,v189\n"
"v_cvt_pk_bf16_f32 v31,v190,v191\n"
"v_mfma_f32_32x32x16_bf16 a[32:47],a[196:199],v[16:19],a[32:47]\n"
"ds_read_b64_tr_b16 a[164:165],v233 offset:16640\n"
"ds_read_b64_tr_b16 a[166:167],v233 offset:17664\n"
"v_mfma_f32_32x32x16_bf16 a[32:47],a[212:215],v[20:23],a[32:47]\n"
"ds_read_b64_tr_b16 a[180:181],v233 offset:20736\n"
"ds_read_b64_tr_b16 a[182:183],v233 offset:21760\n"
"v_mfma_f32_32x32x16_bf16 a[64:79],a[200:203],v[16:19],a[64:79]\n"
"ds_read_b64_tr_b16 a[168:169],v233 offset:16896\n"
"ds_read_b64_tr_b16 a[170:171],v233 offset:17920\n"
"s_lshr_b32 s68,s39,3\n"
"s_and_b32 s69,s39,7\n"
"s_lshl_b32 s69,s69,9\n"
"s_mul_i32 s74,s68,40960\n"
"v_mfma_f32_32x32x16_bf16 a[64:79],a[216:219],v[20:23],a[64:79]\n"
"ds_read_b64_tr_b16 a[184:185],v233 offset:20992\n"
"ds_read_b64_tr_b16 a[186:187],v233 offset:22016\n"
"s_add_u32 s74,s74,s69\n"
"s_add_u32 s86,s20,s74\n"
"s_addc_u32 s87,s21,0\n"
"s_sub_u32 s86,s86,24576\n"
"s_subb_u32 s87,s87,0\n"
"v_mfma_f32_32x32x16_bf16 a[96:111],a[204:207],v[16:19],a[96:111]\n"
"ds_read_b64_tr_b16 a[172:173],v233 offset:17152\n"
"ds_read_b64_tr_b16 a[174:175],v233 offset:18176\n"
"s_and_b32 s87,s87,0xffff\n"
"s_lshl_b32 s74,s68,4\n"
"s_mov_b64 s[40:41],s[18:19]\n"
"s_sub_u32 s75,s74,1152\n"
"s_min_u32 s75,s75,632\n"
"v_mfma_f32_32x32x16_bf16 a[96:111],a[220:223],v[20:23],a[96:111]\n"
"ds_read_b64_tr_b16 a[188:189],v233 offset:21248\n"
"ds_read_b64_tr_b16 a[190:191],v233 offset:22272\n"
"s_cmp_ge_u32 s68,72\n"
"s_cselect_b32 s74,s75,s74\n"
"s_cselect_b64 s[40:41],s[26:27],s[40:41]\n"
"s_cmp_ge_u32 s68,113\n"
"s_cselect_b32 s74,1280,s74\n"
"s_waitcnt lgkmcnt(15)\n"
"v_mfma_f32_32x32x16_bf16 a[0:15],a[128:131],v[24:27],a[0:15]\n"
"ds_read_b64_tr_b16 a[192:193],v233 offset:24576\n"
"ds_read_b64_tr_b16 a[194:195],v233 offset:25600\n"
"ds_read_b64_tr_b16 a[208:209],v233 offset:28672\n"
"s_cselect_b64 s[40:41],s[20:21],s[40:41]\n"
"s_lshl_b32 s74,s74,12\n"
"s_add_u32 s74,s74,s69\n"
"v_mfma_f32_32x32x16_bf16 a[0:15],a[144:147],v[28:31],a[0:15]\n"
"ds_read_b64_tr_b16 a[210:211],v233 offset:29696\n"
"ds_read_b64_tr_b16 a[196:197],v233 offset:24832\n"
"s_add_u32 s40,s40,s74\n"
"s_addc_u32 s41,s41,0\n"
"s_and_b32 s41,s41,0xffff\n"
"s_mov_b32 s42,61952\n"
"s_mov_b32 s43,0x20000\n"
"v_mfma_f32_32x32x16_bf16 a[32:47],a[132:135],v[24:27],a[32:47]\n"
"ds_read_b64_tr_b16 a[198:199],v233 offset:25856\n"
"ds_read_b64_tr_b16 a[212:213],v233 offset:28928\n"
"s_mov_b32 s68,0\n"
"s_mul_i32 s69,s35,16\n"
"s_add_u32 s68,s68,s69\n"
"s_mul_i32 s69,s36,4\n"
"s_add_u32 s68,s68,s69\n"
"v_mfma_f32_32x32x16_bf16 a[32:47],a[148:151],v[28:31],a[32:47]\n"
"ds_read_b64_tr_b16 a[214:215],v233 offset:29952\n"
"ds_read_b64_tr_b16 a[200:201],v233 offset:25088\n"
"ds_read_b64_tr_b16 a[202:203],v233 offset:26112\n"
"s_mul_i32 s69,s37,1\n"
"s_add_u32 s68,s68,s69\n"
"s_and_b32 s74,s68,7\n"
"s_lshr_b32 s68,s68,3\n"
"v_mfma_f32_32x32x16_bf16 a[64:79],a[136:139],v[24:27],a[64:79]\n"
"ds_read_b64_tr_b16 a[216:217],v233 offset:29184\n"
"ds_read_b64_tr_b16 a[218:219],v233 offset:30208\n"
"s_mov_b32 s75,0\n"
"s_mul_i32 s69,s34,524288\n"
"s_add_u32 s75,s75,s69\n"
"s_mul_i32 s69,s68,65536\n"
"v_mfma_f32_32x32x16_bf16 a[64:79],a[152:155],v[28:31],a[64:79]\n"
"ds_read_b64_tr_b16 a[204:205],v233 offset:25344\n"
"ds_read_b64_tr_b16 a[206:207],v233 offset:26368\n"
"s_add_u32 s75,s75,s69\n"
"s_mul_i32 s69,s74,512\n"
"s_add_u32 s75,s75,s69\n"
"s_add_u32 s84,s22,s75\n"
"s_addc_u32 s85,s23,0\n"
"v_mfma_f32_32x32x16_bf16 a[96:111],a[140:143],v[24:27],a[96:111]\n"
"ds_read_b64_tr_b16 a[220:221],v233 offset:29440\n"
"ds_read_b64_tr_b16 a[222:223],v233 offset:30464\n"
"s_and_b32 s85,s85,0xffff\n"
"s_mov_b32 s75,73400320\n"
"s_mul_i32 s69,s34,524288\n"
"s_add_u32 s75,s75,s69\n"
"s_mul_i32 s69,s68,1024\n"
"v_mfma_f32_32x32x16_bf16 a[96:111],a[156:159],v[28:31],a[96:111]\n"
"s_add_u32 s75,s75,s69\n"
"s_mul_i32 s69,s74,65536\n"
"s_add_u32 s75,s75,s69\n"
"s_add_u32 s80,s8,s75\n"
"s_addc_u32 s81,s9,0\n"
"s_and_b32 s81,s81,0xffff\n"
"s_mov_b32 s82,58368\n"
"s_mov_b32 s83,0x20000\n"
"s_lshl_b32 s66,s37,13\n"
"s_add_u32 s66,s66,131072\n"
"s_mov_b32 s68,25165824\n"
"s_mul_i32 s69,s39,1024\n"
"s_add_u32 s68,s68,s69\n"
"s_add_u32 s44,s8,s68\n"
"s_addc_u32 s45,s9,0\n"
"s_and_b32 s45,s45,0xffff\n"
"s_mov_b32 s46,30409728\n"
"s_mov_b32 s47,0x20000\n"
"s_mov_b32 s72,0\n"
"s_waitcnt lgkmcnt(15)\n"
"v_mfma_f32_32x32x16_bf16 a[16:31],a[160:163],v[16:19],a[16:31]\n"
"v_accvgpr_read_b32 v208,a0\n"
"v_accvgpr_read_b32 v209,a1\n"
"v_accvgpr_read_b32 v210,a2\n"
"v_accvgpr_read_b32 v211,a3\n"
"v_accvgpr_read_b32 v212,a4\n"
"v_accvgpr_read_b32 v213,a5\n"
"v_mfma_f32_32x32x16_bf16 a[16:31],a[176:179],v[20:23],a[16:31]\n"
"s_waitcnt vmcnt(9) lgkmcnt(0)\n"
"s_barrier\n"
"v_accvgpr_read_b32 v214,a6\n"
"v_accvgpr_read_b32 v215,a7\n"
"v_cvt_pk_bf16_f32 v144,v208,v209\n"
"v_cvt_pk_bf16_f32 v145,v210,v211\n"
"v_cvt_pk_bf16_f32 v146,v212,v213\n"
"v_mfma_f32_32x32x16_bf16 a[48:63],a[164:167],v[16:19],a[48:63]\n"
"ds_read_b128 a[128:131],v225 offset:0\n"
"v_cvt_pk_bf16_f32 v147,v214,v215\n"
"v_accvgpr_read_b32 v96,a8\n"
"v_accvgpr_read_b32 v97,a9\n"
"v_accvgpr_read_b32 v98,a10\n"
"v_accvgpr_read_b32 v99,a11\n"
"v_accvgpr_read_b32 v100,a12\n"
"v_mfma_f32_32x32x16_bf16 a[48:63],a[180:183],v[20:23],a[48:63]\n"
"ds_read_b128 a[132:135],v225 offset:1024\n"
"v_accvgpr_read_b32 v101,a13\n"
"v_accvgpr_read_b32 v102,a14\n"
"v_accvgpr_read_b32 v103,a15\n"
"v_cvt_pk_bf16_f32 v148,v96,v97\n"
"v_cvt_pk_bf16_f32 v149,v98,v99\n"
"v_cvt_pk_bf16_f32 v150,v100,v101\n"
"v_mfma_f32_32x32x16_bf16 a[80:95],a[168:171],v[16:19],a[80:95]\n"
"ds_read_b128 a[136:139],v225 offset:256\n"
"v_cvt_pk_bf16_f32 v151,v102,v103\n"
"v_permlane16_swap_b32 v144,v148\n"
"v_permlane16_swap_b32 v145,v149\n"
"v_permlane16_swap_b32 v146,v150\n"
"v_permlane16_swap_b32 v147,v151\n"
"v_mfma_f32_32x32x16_bf16 a[80:95],a[184:187],v[20:23],a[80:95]\n"
"ds_read_b128 a[140:143],v225 offset:1280\n"
"v_accvgpr_read_b32 v208,a32\n"
"v_accvgpr_read_b32 v209,a33\n"
"v_accvgpr_read_b32 v210,a34\n"
"v_accvgpr_read_b32 v211,a35\n"
"v_accvgpr_read_b32 v212,a36\n"
"v_accvgpr_read_b32 v213,a37\n"
"v_mfma_f32_32x32x16_bf16 a[112:127],a[172:175],v[16:19],a[112:127]\n"
"ds_read_b128 a[144:147],v225 offset:512\n"
"v_accvgpr_read_b32 v214,a38\n"
"v_accvgpr_read_b32 v215,a39\n"
"v_cvt_pk_bf16_f32 v152,v208,v209\n"
"v_cvt_pk_bf16_f32 v153,v210,v211\n"
"v_cvt_pk_bf16_f32 v154,v212,v213\n"
"v_cvt_pk_bf16_f32 v155,v214,v215\n"
"v_mfma_f32_32x32x16_bf16 a[112:127],a[188:191],v[20:23],a[112:127]\n"
"ds_read_b128 a[148:151],v225 offset:1536\n"
"v_accvgpr_read_b32 v96,a40\n"
"v_accvgpr_read_b32 v97,a41\n"
"v_accvgpr_read_b32 v98,a42\n"
"v_accvgpr_read_b32 v99,a43\n"
"v_accvgpr_read_b32 v100,a44\n"
"v_accvgpr_read_b32 v101,a45\n"
"v_mfma_f32_32x32x16_bf16 a[16:31],a[192:195],v[24:27],a[16:31]\n"
"ds_read_b128 a[152:155],v225 offset:768\n"
"v_accvgpr_read_b32 v102,a46\n"
"v_accvgpr_read_b32 v103,a47\n"
"v_cvt_pk_bf16_f32 v156,v96,v97\n"
"v_cvt_pk_bf16_f32 v157,v98,v99\n"
"v_cvt_pk_bf16_f32 v158,v100,v101\n"
"v_mfma_f32_32x32x16_bf16 a[16:31],a[208:211],v[28:31],a[16:31]\n"
"ds_read_b128 a[156:159],v225 offset:1792\n"
"v_cvt_pk_bf16_f32 v159,v102,v103\n"
"v_permlane16_swap_b32 v152,v156\n"
"v_permlane16_swap_b32 v153,v157\n"
"v_permlane16_swap_b32 v154,v158\n"
"v_permlane16_swap_b32 v155,v159\n"
"v_accvgpr_read_b32 v208,a64\n"
"v_mfma_f32_32x32x16_bf16 a[48:63],a[196:199],v[24:27],a[48:63]\n"
"v_accvgpr_read_b32 v209,a65\n"
"v_accvgpr_read_b32 v210,a66\n"
"v_accvgpr_read_b32 v211,a67\n"
"v_accvgpr_read_b32 v212,a68\n"
"v_accvgpr_read_b32 v213,a69\n"
"v_accvgpr_read_b32 v214,a70\n"
"v_accvgpr_read_b32 v215,a71\n"
"v_mfma_f32_32x32x16_bf16 a[48:63],a[212:215],v[28:31],a[48:63]\n"
"v_cvt_pk_bf16_f32 v160,v208,v209\n"
"v_cvt_pk_bf16_f32 v161,v210,v211\n"
"v_cvt_pk_bf16_f32 v162,v212,v213\n"
"v_cvt_pk_bf16_f32 v163,v214,v215\n"
"v_accvgpr_read_b32 v96,a72\n"
"v_accvgpr_read_b32 v97,a73\n"
"v_accvgpr_read_b32 v98,a74\n"
"v_mfma_f32_32x32x16_bf16 a[80:95],a[200:203],v[24:27],a[80:95]\n"
"v_accvgpr_read_b32 v99,a75\n"
"v_accvgpr_read_b32 v100,a76\n"
"v_accvgpr_read_b32 v101,a77\n"
"v_accvgpr_read_b32 v102,a78\n"
"v_accvgpr_read_b32 v103,a79\n"
"v_cvt_pk_bf16_f32 v164,v96,v97\n"
"v_mfma_f32_32x32x16_bf16 a[80:95],a[216:219],v[28:31],a[80:95]\n"
"v_cvt_pk_bf16_f32 v165,v98,v99\n"
"v_cvt_pk_bf16_f32 v166,v100,v101\n"
"v_cvt_pk_bf16_f32 v167,v102,v103\n"
"v_permlane16_swap_b32 v160,v164\n"
"v_permlane16_swap_b32 v161,v165\n"
"v_permlane16_swap_b32 v162,v166\n"
"v_permlane16_swap_b32 v163,v167\n"
"v_mfma_f32_32x32x16_bf16 a[112:127],a[204:207],v[24:27],a[112:127]\n"
"v_accvgpr_read_b32 v208,a96\n"
"v_accvgpr_read_b32 v209,a97\n"
"v_accvgpr_read_b32 v210,a98\n"
"v_accvgpr_read_b32 v211,a99\n"
"v_accvgpr_read_b32 v212,a100\n"
"v_accvgpr_read_b32 v213,a101\n"
"v_accvgpr_read_b32 v214,a102\n"
"v_mfma_f32_32x32x16_bf16 a[112:127],a[220:223],v[28:31],a[112:127]\n"
"v_accvgpr_read_b32 v215,a103\n"
"v_cvt_pk_bf16_f32 v168,v208,v209\n"
"v_cvt_pk_bf16_f32 v169,v210,v211\n"
"v_cvt_pk_bf16_f32 v170,v212,v213\n"
"v_cvt_pk_bf16_f32 v171,v214,v215\n"
"v_accvgpr_read_b32 v96,a104\n"
"v_accvgpr_read_b32 v97,a105\n"
"v_accvgpr_read_b32 v98,a106\n"
"v_accvgpr_read_b32 v99,a107\n"
"v_accvgpr_read_b32 v100,a108\n"
"v_accvgpr_read_b32 v101,a109\n"
"v_accvgpr_read_b32 v102,a110\n"
"v_accvgpr_read_b32 v103,a111\n"
"v_cvt_pk_bf16_f32 v172,v96,v97\n"
"v_cvt_pk_bf16_f32 v173,v98,v99\n"
"v_cvt_pk_bf16_f32 v174,v100,v101\n"
"v_cvt_pk_bf16_f32 v175,v102,v103\n"
"v_permlane16_swap_b32 v168,v172\n"
"v_permlane16_swap_b32 v169,v173\n"
"v_permlane16_swap_b32 v170,v174\n"
"v_permlane16_swap_b32 v171,v175\n"
"v_mov_b32 v128,0\n"
"v_mov_b32 v129,0\n"
"v_mov_b32 v130,0\n"
"v_mov_b32 v131,0\n"
"s_waitcnt lgkmcnt(0)\n"
"v_mfma_f32_16x16x32_bf16 v[16:19],a[128:131],v[144:147],0\n"
"s_add_u32 m0,s65,32768\n"
"s_mov_b32 s68,24576\n"
"v_accvgpr_read_b32 v208,a16\n"
"v_accvgpr_read_b32 v209,a17\n"
"v_accvgpr_read_b32 v210,a18\n"
"v_mfma_f32_16x16x32_bf16 v[24:27],a[128:131],v[148:151],0\n"
"buffer_load_dwordx4 v228,s[48:51],s68 offen lds\n"
"v_accvgpr_read_b32 v211,a19\n"
"v_accvgpr_read_b32 v212,a20\n"
"v_accvgpr_read_b32 v213,a21\n"
"v_mfma_f32_16x16x32_bf16 v[20:23],a[132:135],v[144:147],0\n"
"v_accvgpr_read_b32 v214,a22\n"
"v_accvgpr_read_b32 v215,a23\n"
"v_cvt_pk_bf16_f32 v176,v208,v209\n"
"v_cvt_pk_bf16_f32 v177,v210,v211\n"
"v_cvt_pk_bf16_f32 v178,v212,v213\n"
"v_mfma_f32_16x16x32_bf16 v[28:31],a[132:135],v[148:151],0\n"
"v_cvt_pk_bf16_f32 v179,v214,v215\n"
"v_accvgpr_read_b32 v208,a24\n"
"v_accvgpr_read_b32 v209,a25\n"
"v_accvgpr_read_b32 v210,a26\n"
"v_accvgpr_read_b32 v211,a27\n"
"v_mfma_f32_16x16x32_bf16 v[16:19],a[136:139],v[152:155],v[16:19]\n"
"v_accvgpr_read_b32 v212,a28\n"
"v_accvgpr_read_b32 v213,a29\n"
"v_accvgpr_read_b32 v214,a30\n"
"v_accvgpr_read_b32 v215,a31\n"
"v_cvt_pk_bf16_f32 v180,v208,v209\n"
"v_mfma_f32_16x16x32_bf16 v[24:27],a[136:139],v[156:159],v[24:27]\n"
"buffer_load_dwordx4 v228,s[48:51],s68 offen offset:1024 lds\n"
"v_cvt_pk_bf16_f32 v181,v210,v211\n"
"v_cvt_pk_bf16_f32 v182,v212,v213\n"
"v_cvt_pk_bf16_f32 v183,v214,v215\n"
"v_mfma_f32_16x16x32_bf16 v[20:23],a[140:143],v[152:155],v[20:23]\n"
"v_permlane16_swap_b32 v176,v180\n"
"v_permlane16_swap_b32 v177,v181\n"
"v_permlane16_swap_b32 v178,v182\n"
"v_permlane16_swap_b32 v179,v183\n"
"s_nop 0\n"
"v_mfma_f32_16x16x32_bf16 v[28:31],a[140:143],v[156:159],v[28:31]\n"
"v_accvgpr_read_b32 v208,a48\n"
"v_accvgpr_read_b32 v209,a49\n"
"v_accvgpr_read_b32 v210,a50\n"
"v_accvgpr_read_b32 v211,a51\n"
"v_accvgpr_read_b32 v212,a52\n"
"v_mfma_f32_16x16x32_bf16 v[16:19],a[144:147],v[160:163],v[16:19]\n"
"ds_read_b128 a[192:195],v225 offset:8192\n"
"v_accvgpr_read_b32 v213,a53\n"
"v_accvgpr_read_b32 v214,a54\n"
"v_accvgpr_read_b32 v215,a55\n"
"v_cvt_pk_bf16_f32 v184,v208,v209\n"
"v_mfma_f32_16x16x32_bf16 v[24:27],a[144:147],v[164:167],v[24:27]\n"
"ds_read_b128 a[196:199],v225 offset:9216\n"
"s_add_u32 m0,s65,40960\n"
"s_mov_b32 s68,32768\n"
"v_cvt_pk_bf16_f32 v185,v210,v211\n"
"v_cvt_pk_bf16_f32 v186,v212,v213\n"
"v_mfma_f32_16x16x32_bf16 v[20:23],a[148:151],v[160:163],v[20:23]\n"
"ds_read_b128 a[200:203],v225 offset:8448\n"
"buffer_load_dwordx4 v228,s[48:51],s68 offen lds\n"
"v_cvt_pk_bf16_f32 v187,v214,v215\n"
"v_accvgpr_read_b32 v208,a56\n"
"v_mfma_f32_16x16x32_bf16 v[28:31],a[148:151],v[164:167],v[28:31]\n"
"ds_read_b128 a[204:207],v225 offset:9472\n"
"v_accvgpr_read_b32 v209,a57\n"
"v_accvgpr_read_b32 v210,a58\n"
"v_accvgpr_read_b32 v211,a59\n"
"v_accvgpr_read_b32 v212,a60\n"
"v_mfma_f32_16x16x32_bf16 v[16:19],a[152:155],v[168:171],v[16:19]\n"
"ds_read_b128 a[208:211],v225 offset:8704\n"
"v_accvgpr_read_b32 v213,a61\n"
"v_accvgpr_read_b32 v214,a62\n"
"v_accvgpr_read_b32 v215,a63\n"
"v_cvt_pk_bf16_f32 v188,v208,v209\n"
"v_mfma_f32_16x16x32_bf16 v[24:27],a[152:155],v[172:175],v[24:27]\n"
"ds_read_b128 a[212:215],v225 offset:9728\n"
"v_cvt_pk_bf16_f32 v189,v210,v211\n"
"v_cvt_pk_bf16_f32 v190,v212,v213\n"
"v_cvt_pk_bf16_f32 v191,v214,v215\n"
"v_permlane16_swap_b32 v184,v188\n"
"v_mfma_f32_16x16x32_bf16 v[20:23],a[156:159],v[168:171],v[20:23]\n"
"ds_read_b128 a[216:219],v225 offset:8960\n"
"buffer_load_dwordx4 v228,s[48:51],s68 offen offset:1024 lds\n"
"v_permlane16_swap_b32 v185,v189\n"
"v_permlane16_swap_b32 v186,v190\n"
"v_mfma_f32_16x16x32_bf16 v[28:31],a[156:159],v[172:175],v[28:31]\n"
"ds_read_b128 a[220:223],v225 offset:9984\n"
"v_permlane16_swap_b32 v187,v191\n"
"s_nop 0\n"
"v_accvgpr_read_b32 v208,a80\n"
"v_accvgpr_read_b32 v209,a81\n"
"v_mfma_f32_32x32x16_bf16 a[0:15],v[128:131],v[128:131],0\n"
"v_accvgpr_read_b32 v210,a82\n"
"v_accvgpr_read_b32 v211,a83\n"
"v_accvgpr_read_b32 v212,a84\n"
"v_accvgpr_read_b32 v213,a85\n"
"v_accvgpr_read_b32 v214,a86\n"
"v_mfma_f32_32x32x16_bf16 a[32:47],v[128:131],v[128:131],0\n"
"v_accvgpr_read_b32 v215,a87\n"
"v_cvt_pk_bf16_f32 v192,v208,v209\n"
"v_cvt_pk_bf16_f32 v193,v210,v211\n"
"v_cvt_pk_bf16_f32 v194,v212,v213\n"
"v_cvt_pk_bf16_f32 v195,v214,v215\n"
"v_mfma_f32_32x32x16_bf16 a[64:79],v[128:131],v[128:131],0\n"
"s_add_u32 m0,s65,49152\n"
"s_mov_b32 s68,40960\n"
"v_accvgpr_read_b32 v208,a88\n"
"v_accvgpr_read_b32 v209,a89\n"
"v_accvgpr_read_b32 v210,a90\n"
"v_mfma_f32_32x32x16_bf16 a[96:111],v[128:131],v[128:131],0\n"
"buffer_load_dwordx4 v228,s[48:51],s68 offen lds\n"
"v_accvgpr_read_b32 v211,a91\n"
"v_accvgpr_read_b32 v212,a92\n"
"v_accvgpr_read_b32 v213,a93\n"
"v_mfma_f32_16x16x32_bf16 v[32:35],a[128:131],v[176:179],0\n"
"v_accvgpr_read_b32 v214,a94\n"
"v_accvgpr_read_b32 v215,a95\n"
"v_cvt_pk_bf16_f32 v196,v208,v209\n"
"v_cvt_pk_bf16_f32 v197,v210,v211\n"
"v_cvt_pk_bf16_f32 v198,v212,v213\n"
"v_mfma_f32_16x16x32_bf16 v[40:43],a[128:131],v[180:183],0\n"
"v_cvt_pk_bf16_f32 v199,v214,v215\n"
"v_permlane16_swap_b32 v192,v196\n"
"v_permlane16_swap_b32 v193,v197\n"
"v_permlane16_swap_b32 v194,v198\n"
"v_permlane16_swap_b32 v195,v199\n"
"v_mfma_f32_16x16x32_bf16 v[36:39],a[132:135],v[176:179],0\n"
"s_nop 0\n"
"v_accvgpr_read_b32 v208,a112\n"
"v_accvgpr_read_b32 v209,a113\n"
"v_accvgpr_read_b32 v210,a114\n"
"v_accvgpr_read_b32 v211,a115\n"
"v_mfma_f32_16x16x32_bf16 v[44:47],a[132:135],v[180:183],0\n"
"buffer_load_dwordx4 v228,s[48:51],s68 offen offset:1024 lds\n"
"v_accvgpr_read_b32 v212,a116\n"
"v_accvgpr_read_b32 v213,a117\n"
"v_accvgpr_read_b32 v214,a118\n"
"v_mfma_f32_16x16x32_bf16 v[32:35],a[136:139],v[184:187],v[32:35]\n"
"v_accvgpr_read_b32 v215,a119\n"
"v_cvt_pk_bf16_f32 v200,v208,v209\n"
"v_cvt_pk_bf16_f32 v201,v210,v211\n"
"v_cvt_pk_bf16_f32 v202,v212,v213\n"
"v_cvt_pk_bf16_f32 v203,v214,v215\n"
"v_mfma_f32_16x16x32_bf16 v[40:43],a[136:139],v[188:191],v[40:43]\n"
"v_accvgpr_read_b32 v208,a120\n"
"v_accvgpr_read_b32 v209,a121\n"
"v_accvgpr_read_b32 v210,a122\n"
"v_accvgpr_read_b32 v211,a123\n"
"v_accvgpr_read_b32 v212,a124\n"
"v_mfma_f32_16x16x32_bf16 v[36:39],a[140:143],v[184:187],v[36:39]\n"
"v_accvgpr_read_b32 v213,a125\n"
"v_accvgpr_read_b32 v214,a126\n"
"v_accvgpr_read_b32 v215,a127\n"
"v_cvt_pk_bf16_f32 v204,v208,v209\n"
"v_cvt_pk_bf16_f32 v205,v210,v211\n"
"v_mfma_f32_16x16x32_bf16 v[44:47],a[140:143],v[188:191],v[44:47]\n"
"s_add_u32 m0,s65,57344\n"
"s_mov_b32 s68,49152\n"
"v_cvt_pk_bf16_f32 v206,v212,v213\n"
"v_cvt_pk_bf16_f32 v207,v214,v215\n"
"v_permlane16_swap_b32 v200,v204\n"
"v_mfma_f32_16x16x32_bf16 v[32:35],a[144:147],v[192:195],v[32:35]\n"
"buffer_load_dwordx4 v228,s[48:51],s68 offen lds\n"
"v_permlane16_swap_b32 v201,v205\n"
"v_permlane16_swap_b32 v202,v206\n"
"v_permlane16_swap_b32 v203,v207\n"
"v_mfma_f32_16x16x32_bf16 v[40:43],a[144:147],v[196:199],v[40:43]\n"
"s_nop 0\n"
"v_max3_f32 v235,v16,v17,v18\n"
"v_max3_f32 v235,v235,v19,v20\n"
"v_max3_f32 v235,v235,v21,v22\n"
"v_max_f32 v235,v235,v23\n"
"v_mfma_f32_16x16x32_bf16 v[36:39],a[148:151],v[192:195],v[36:39]\n"
"v_max3_f32 v236,v24,v25,v26\n"
"v_max3_f32 v236,v236,v27,v28\n"
"v_max3_f32 v236,v236,v29,v30\n"
"v_max_f32 v236,v236,v31\n"
"v_mov_b32 v96,0\n"
"v_mfma_f32_16x16x32_bf16 v[44:47],a[148:151],v[196:199],v[44:47]\n"
"s_nop 1\n"
"v_permlane32_swap_b32 v235,v236\n"
"v_max_f32 v235,v235,v236\n"
"v_mov_b32 v97,0\n"
"v_mov_b32 v98,0\n"
"v_mfma_f32_16x16x32_bf16 v[32:35],a[152:155],v[200:203],v[32:35]\n"
"buffer_load_dwordx4 v228,s[48:51],s68 offen offset:1024 lds\n"
"v_mov_b32 v236,v235\n"
"s_nop 1\n"
"v_permlane16_swap_b32 v235,v236\n"
"v_mfma_f32_16x16x32_bf16 v[40:43],a[152:155],v[204:207],v[40:43]\n"
"v_max_f32 v235,v235,v236\n"
"v_add_f32 v236,s70,v235\n"
"v_add_f32 v235,s70,v235\n"
"s_nop 1\n"
"v_permlane32_swap_b32 v235,v236\n"
"v_mfma_f32_16x16x32_bf16 v[36:39],a[156:159],v[200:203],v[36:39]\n"
"v_sub_f32 v80,0,v235\n"
"v_sub_f32 v81,0,v235\n"
"v_sub_f32 v82,0,v235\n"
"v_sub_f32 v83,0,v235\n"
"v_sub_f32 v84,0,v236\n"
"v_mfma_f32_16x16x32_bf16 v[44:47],a[156:159],v[204:207],v[44:47]\n"
"v_sub_f32 v85,0,v236\n"
"v_sub_f32 v86,0,v236\n"
"v_sub_f32 v87,0,v236\n"
"s_nop 1\n"
"v_sub_f32 v16,v16,v235\n"
"s_waitcnt lgkmcnt(0)\n"
"v_mfma_f32_16x16x32_bf16 v[48:51],a[192:195],v[144:147],v[80:83]\n"
"ds_read_b64_tr_b16 a[160:161],v226 offset:8192\n"
"s_add_u32 m0,s65,114688\n"
"s_mov_b32 s68,40960\n"
"v_sub_f32 v17,v17,v235\n"
"v_sub_f32 v18,v18,v235\n"
"v_mfma_f32_16x16x32_bf16 v[56:59],a[192:195],v[148:151],v[84:87]\n"
"ds_read_b64_tr_b16 a[162:163],v226 offset:9216\n"
"buffer_load_dwordx4 v229,s[52:55],s68 offen lds\n"
"v_sub_f32 v19,v19,v235\n"
"v_sub_f32 v20,v20,v235\n"
"v_mfma_f32_16x16x32_bf16 v[52:55],a[196:199],v[144:147],v[80:83]\n"
"ds_read_b64_tr_b16 a[164:165],v227 offset:8192\n"
"v_sub_f32 v21,v21,v235\n"
"v_sub_f32 v22,v22,v235\n"
"v_sub_f32 v23,v23,v235\n"
"v_sub_f32 v24,v24,v236\n"
"v_mfma_f32_16x16x32_bf16 v[60:63],a[196:199],v[148:151],v[84:87]\n"
"ds_read_b64_tr_b16 a[166:167],v227 offset:9216\n"
"v_sub_f32 v25,v25,v236\n"
"v_sub_f32 v26,v26,v236\n"
"v_sub_f32 v27,v27,v236\n"
"v_sub_f32 v28,v28,v236\n"
"v_mfma_f32_16x16x32_bf16 v[48:51],a[200:203],v[152:155],v[48:51]\n"
"ds_read_b64_tr_b16 a[168:169],v226 offset:8448\n"
"v_sub_f32 v29,v29,v236\n"
"v_sub_f32 v30,v30,v236\n"
"v_sub_f32 v31,v31,v236\n"
"v_max3_f32 v237,v32,v33,v34\n"
"v_mfma_f32_16x16x32_bf16 v[56:59],a[200:203],v[156:159],v[56:59]\n"
"ds_read_b64_tr_b16 a[170:171],v226 offset:9472\n"
"buffer_load_dwordx4 v229,s[52:55],s68 offen offset:1024 lds\n"
"v_max3_f32 v237,v237,v35,v36\n"
"v_max3_f32 v237,v237,v37,v38\n"
"v_mfma_f32_16x16x32_bf16 v[52:55],a[204:207],v[152:155],v[52:55]\n"
"ds_read_b64_tr_b16 a[172:173],v227 offset:8448\n"
"v_max_f32 v237,v237,v39\n"
"v_max3_f32 v238,v40,v41,v42\n"
"v_max3_f32 v238,v238,v43,v44\n"
"v_max3_f32 v238,v238,v45,v46\n"
"v_mfma_f32_16x16x32_bf16 v[60:63],a[204:207],v[156:159],v[60:63]\n"
"ds_read_b64_tr_b16 a[174:175],v227 offset:9472\n"
"v_max_f32 v238,v238,v47\n"
"s_nop 1\n"
"v_permlane32_swap_b32 v237,v238\n"
"v_max_f32 v237,v237,v238\n"
"v_mfma_f32_16x16x32_bf16 v[48:51],a[208:211],v[160:163],v[48:51]\n"
"ds_read_b64_tr_b16 a[176:177],v226 offset:8704\n"
"v_mov_b32 v238,v237\n"
"s_nop 1\n"
"v_permlane16_swap_b32 v237,v238\n"
"v_max_f32 v237,v237,v238\n"
"v_mfma_f32_16x16x32_bf16 v[56:59],a[208:211],v[164:167],v[56:59]\n"
"ds_read_b64_tr_b16 a[178:179],v226 offset:9728\n"
"s_add_u32 m0,s65,122880\n"
"s_mov_b32 s68,49152\n"
"v_mov_b32 v99,0\n"
"v_accvgpr_read_b32 v104,a248\n"
"v_mfma_f32_16x16x32_bf16 v[52:55],a[212:215],v[160:163],v[52:55]\n"
"ds_read_b64_tr_b16 a[180:181],v227 offset:8704\n"
"buffer_load_dwordx4 v229,s[52:55],s68 offen lds\n"
"v_lshlrev_b32 v105,3,v8\n"
"v_mfma_f32_16x16x32_bf16 v[60:63],a[212:215],v[164:167],v[60:63]\n"
"ds_read_b64_tr_b16 a[182:183],v227 offset:9728\n"
"v_add_f32 v238,s70,v237\n"
"v_add_f32 v237,s70,v237\n"
"s_nop 1\n"
"v_permlane32_swap_b32 v237,v238\n"
"v_mfma_f32_16x16x32_bf16 v[48:51],a[216:219],v[168:171],v[48:51]\n"
"ds_read_b64_tr_b16 a[184:185],v226 offset:8960\n"
"v_sub_f32 v88,0,v237\n"
"v_sub_f32 v89,0,v237\n"
"v_sub_f32 v90,0,v237\n"
"v_sub_f32 v91,0,v237\n"
"v_mfma_f32_16x16x32_bf16 v[56:59],a[216:219],v[172:175],v[56:59]\n"
"ds_read_b64_tr_b16 a[186:187],v226 offset:9984\n"
"L_pgg96_%=:\n"
"s_cbranch_execz L_pgg160_%=\n"
"v_sub_f32 v92,0,v238\n"
"v_sub_f32 v93,0,v238\n"
"v_sub_f32 v94,0,v238\n"
"v_sub_f32 v95,0,v238\n"
"v_mfma_f32_16x16x32_bf16 v[52:55],a[220:223],v[168:171],v[52:55]\n"
"ds_read_b64_tr_b16 a[188:189],v227 offset:8960\n"
"buffer_load_dwordx4 v229,s[52:55],s68 offen offset:1024 lds\n"
"s_nop 1\n"
"v_mfma_f32_16x16x32_bf16 v[60:63],a[220:223],v[172:175],v[60:63]\n"
"ds_read_b64_tr_b16 a[190:191],v227 offset:9984\n"
"v_exp_f32 v208,v16\n"
"v_exp_f32 v209,v17\n"
"v_mfma_f32_16x16x32_bf16 v[64:67],a[192:195],v[176:179],v[88:91]\n"
"v_exp_f32 v210,v18\n"
"v_cvt_pk_bf16_f32 v112,v208,v209\n"
"v_exp_f32 v211,v19\n"
"v_mfma_f32_16x16x32_bf16 v[72:75],a[192:195],v[180:183],v[92:95]\n"
"v_exp_f32 v212,v20\n"
"v_cvt_pk_bf16_f32 v113,v210,v211\n"
"v_exp_f32 v213,v21\n"
"v_mfma_f32_16x16x32_bf16 v[68:71],a[196:199],v[176:179],v[88:91]\n"
"v_exp_f32 v214,v22\n"
"v_cvt_pk_bf16_f32 v114,v212,v213\n"
"v_exp_f32 v215,v23\n"
"v_mfma_f32_16x16x32_bf16 v[76:79],a[196:199],v[180:183],v[92:95]\n"
"v_exp_f32 v208,v24\n"
"v_cvt_pk_bf16_f32 v115,v214,v215\n"
"v_exp_f32 v209,v25\n"
"v_mfma_f32_16x16x32_bf16 v[64:67],a[200:203],v[184:187],v[64:67]\n"
"v_exp_f32 v210,v26\n"
"v_cvt_pk_bf16_f32 v116,v208,v209\n"
"v_exp_f32 v211,v27\n"
"v_mfma_f32_16x16x32_bf16 v[72:75],a[200:203],v[188:191],v[72:75]\n"
"v_exp_f32 v212,v28\n"
"v_cvt_pk_bf16_f32 v117,v210,v211\n"
"v_exp_f32 v213,v29\n"
"v_mfma_f32_16x16x32_bf16 v[68:71],a[204:207],v[184:187],v[68:71]\n"
"v_exp_f32 v214,v30\n"
"v_cvt_pk_bf16_f32 v118,v212,v213\n"
"v_exp_f32 v215,v31\n"
"v_mfma_f32_16x16x32_bf16 v[76:79],a[204:207],v[188:191],v[76:79]\n"
"v_sub_f32 v32,v32,v237\n"
"v_sub_f32 v33,v33,v237\n"
"v_sub_f32 v34,v34,v237\n"
"v_sub_f32 v35,v35,v237\n"
"v_sub_f32 v36,v36,v237\n"
"v_mfma_f32_16x16x32_bf16 v[64:67],a[208:211],v[192:195],v[64:67]\n"
"v_sub_f32 v37,v37,v237\n"
"v_sub_f32 v38,v38,v237\n"
"v_sub_f32 v39,v39,v237\n"
"v_sub_f32 v40,v40,v238\n"
"v_sub_f32 v41,v41,v238\n"
"v_mfma_f32_16x16x32_bf16 v[72:75],a[208:211],v[196:199],v[72:75]\n"
"v_sub_f32 v42,v42,v238\n"
"v_sub_f32 v43,v43,v238\n"
"v_sub_f32 v44,v44,v238\n"
"v_sub_f32 v45,v45,v238\n"
"v_sub_f32 v46,v46,v238\n"
"v_mfma_f32_16x16x32_bf16 v[68:71],a[212:215],v[192:195],v[68:71]\n"
"v_sub_f32 v47,v47,v238\n"
"v_exp_f32 v208,v32\n"
"v_cvt_pk_bf16_f32 v119,v214,v215\n"
"v_mfma_f32_16x16x32_bf16 v[76:79],a[212:215],v[196:199],v[76:79]\n"
"v_exp_f32 v209,v33\n"
"v_exp_f32 v210,v34\n"
"v_cvt_pk_bf16_f32 v120,v208,v209\n"
"v_mfma_f32_16x16x32_bf16 v[64:67],a[216:219],v[200:203],v[64:67]\n"
"v_exp_f32 v211,v35\n"
"v_exp_f32 v212,v36\n"
"v_cvt_pk_bf16_f32 v121,v210,v211\n"
"v_mfma_f32_16x16x32_bf16 v[72:75],a[216:219],v[204:207],v[72:75]\n"
"v_exp_f32 v213,v37\n"
"v_exp_f32 v214,v38\n"
"v_cvt_pk_bf16_f32 v122,v212,v213\n"
"v_mfma_f32_16x16x32_bf16 v[68:71],a[220:223],v[200:203],v[68:71]\n"
"v_exp_f32 v215,v39\n"
"v_exp_f32 v208,v40\n"
"v_cvt_pk_bf16_f32 v123,v214,v215\n"
"v_mfma_f32_16x16x32_bf16 v[76:79],a[220:223],v[204:207],v[76:79]\n"
"v_exp_f32 v209,v41\n"
"v_exp_f32 v210,v42\n"
"v_cvt_pk_bf16_f32 v124,v208,v209\n"
"v_mfma_f32_32x32x16_bf16 a[16:31],v[128:131],v[128:131],0\n"
"v_exp_f32 v211,v43\n"
"v_exp_f32 v212,v44\n"
"v_cvt_pk_bf16_f32 v125,v210,v211\n"
"v_mfma_f32_32x32x16_bf16 a[48:63],v[128:131],v[128:131],0\n"
"v_exp_f32 v213,v45\n"
"v_exp_f32 v214,v46\n"
"v_cvt_pk_bf16_f32 v126,v212,v213\n"
"v_mfma_f32_32x32x16_bf16 a[80:95],v[128:131],v[128:131],0\n"
"v_exp_f32 v215,v47\n"
"s_nop 0\n"
"v_cvt_pk_bf16_f32 v127,v214,v215\n"
"v_mfma_f32_32x32x16_bf16 a[112:127],v[128:131],v[128:131],0\n"
"s_mov_b32 s60,16384\n"
"s_mov_b32 s61,s65\n"
"s_nop 0\n"
"s_mov_b32 s62,57344\n"
"v_add_u32 v222,16384,v225\n"
"v_smfmac_f32_16x16x64_bf16 v[96:99],a[240:243],v[112:119],v104\n"
"s_mov_b32 s63,31\n"
"v_add_u32 v223,16384,v226\n"
"s_nop 0\n"
"v_add_u32 v224,16384,v227\n"
"v_smfmac_f32_16x16x64_bf16 v[96:99],a[244:247],v[120:127],v104\n"
"s_waitcnt vmcnt(17) lgkmcnt(0)\n"
"s_barrier\n"
"ds_read_b128 a[128:131],v225 offset:16384\n"
"ds_read_b128 a[132:135],v225 offset:17408\n"
"ds_read_b128 a[136:139],v225 offset:16640\n"
"ds_read_b128 a[140:143],v225 offset:17664\n"
"ds_read_b128 a[144:147],v225 offset:16896\n"
"ds_read_b128 a[148:151],v225 offset:17920\n"
"ds_read_b128 a[152:155],v225 offset:17152\n"
"ds_read_b128 a[156:159],v225 offset:18176\n"
"buffer_store_dwordx4 a[232:235],v9,s[44:47],s72 offen offset:0 sc1\n"
"s_mov_b32 s72,1048576\n"
"buffer_store_dwordx4 a[232:235],v9,s[44:47],s72 offen offset:0 sc1\n"
"s_branch L_loop_%=\n"
".p2align 6\n"
"s_nop 0\n"
"s_nop 0\n"
"s_nop 0\n"
"L_loop_%=:\n"
"v_mfma_f32_16x16x32_bf16 a[0:3],a[160:163],v[112:115],a[0:3]\n"
"s_add_u32 m0,s61,0\n"
"v_exp_f32 v208,v48\n"
"v_mfma_f32_16x16x32_bf16 a[8:11],a[160:163],v[116:119],a[8:11]\n"
"v_exp_f32 v209,v49\n"
"v_mfma_f32_16x16x32_bf16 a[16:19],a[160:163],v[120:123],a[16:19]\n"
"buffer_load_dwordx4 v228,s[48:51],s62 offen lds\n"
"v_mfma_f32_16x16x32_bf16 a[24:27],a[160:163],v[124:127],a[24:27]\n"
"s_lshl_b32 s71,s63,10\n"
"v_exp_f32 v210,v50\n"
"v_mfma_f32_16x16x32_bf16 a[4:7],a[164:167],v[112:115],a[4:7]\n"
"s_and_b32 s71,s71,0x1c00\n"
"ds_read_b64_tr_b16 a[160:161],v223 offset:0\n"
"v_cvt_pk_bf16_f32 v128,v208,v209\n"
"v_mfma_f32_16x16x32_bf16 a[12:15],a[164:167],v[116:119],a[12:15]\n"
"s_lshl_b32 s74,s71,3\n"
"v_exp_f32 v211,v51\n"
"v_mfma_f32_16x16x32_bf16 a[20:23],a[164:167],v[120:123],a[20:23]\n"
"s_cmp_eq_u32 s63,23\n"
"ds_read_b64_tr_b16 a[162:163],v223 offset:1024\n"
"v_cvt_pk_bf16_f32 v129,v210,v211\n"
"v_mfma_f32_16x16x32_bf16 a[28:31],a[164:167],v[124:127],a[28:31]\n"
"s_cselect_b64 s[40:41],s[86:87],s[40:41]\n"
"v_exp_f32 v212,v52\n"
"v_mfma_f32_16x16x32_bf16 a[32:35],a[168:171],v[112:115],a[32:35]\n"
"ds_read_b64_tr_b16 a[164:165],v224 offset:0\n"
"v_mov_b32 v100,v96\n"
"v_mfma_f32_16x16x32_bf16 a[40:43],a[168:171],v[116:119],a[40:43]\n"
"v_exp_f32 v213,v53\n"
"v_mfma_f32_16x16x32_bf16 a[48:51],a[168:171],v[120:123],a[48:51]\n"
"ds_read_b64_tr_b16 a[166:167],v224 offset:1024\n"
"v_cvt_pk_bf16_f32 v130,v212,v213\n"
"v_mfma_f32_16x16x32_bf16 a[56:59],a[168:171],v[124:127],a[56:59]\n"
"v_exp_f32 v214,v54\n"
"v_mfma_f32_16x16x32_bf16 a[36:39],a[172:175],v[112:115],a[36:39]\n"
"v_exp_f32 v215,v55\n"
"v_mfma_f32_16x16x32_bf16 a[44:47],a[172:175],v[116:119],a[44:47]\n"
"ds_read_b64_tr_b16 a[168:169],v223 offset:256\n"
"v_cvt_pk_bf16_f32 v131,v214,v215\n"
"v_mfma_f32_16x16x32_bf16 a[52:55],a[172:175],v[120:123],a[52:55]\n"
"buffer_load_dwordx4 v228,s[48:51],s62 offen offset:1024 lds\n"
"v_mfma_f32_16x16x32_bf16 a[60:63],a[172:175],v[124:127],a[60:63]\n"
"s_add_u32 m0,s66,s71\n"
"v_exp_f32 v208,v56\n"
"v_mfma_f32_16x16x32_bf16 a[64:67],a[176:179],v[112:115],a[64:67]\n"
"ds_read_b64_tr_b16 a[170:171],v223 offset:1280\n"
"v_mov_b32 v101,v97\n"
"v_mfma_f32_16x16x32_bf16 a[72:75],a[176:179],v[116:119],a[72:75]\n"
"v_exp_f32 v209,v57\n"
"v_mfma_f32_16x16x32_bf16 a[80:83],a[176:179],v[120:123],a[80:83]\n"
"v_exp_f32 v210,v58\n"
"v_mfma_f32_16x16x32_bf16 a[88:91],a[176:179],v[124:127],a[88:91]\n"
"ds_read_b64_tr_b16 a[172:173],v224 offset:256\n"
"v_cvt_pk_bf16_f32 v132,v208,v209\n"
"v_mfma_f32_16x16x32_bf16 a[68:71],a[180:183],v[112:115],a[68:71]\n"
"v_exp_f32 v211,v59\n"
"v_mfma_f32_16x16x32_bf16 a[76:79],a[180:183],v[116:119],a[76:79]\n"
"ds_read_b64_tr_b16 a[174:175],v224 offset:1280\n"
"v_cvt_pk_bf16_f32 v133,v210,v211\n"
"v_mfma_f32_16x16x32_bf16 a[84:87],a[180:183],v[120:123],a[84:87]\n"
"v_exp_f32 v212,v60\n"
"v_mfma_f32_16x16x32_bf16 a[92:95],a[180:183],v[124:127],a[92:95]\n"
"v_exp_f32 v213,v61\n"
"v_mfma_f32_16x16x32_bf16 a[96:99],a[184:187],v[112:115],a[96:99]\n"
"ds_read_b64_tr_b16 a[176:177],v223 offset:512\n"
"v_cvt_pk_bf16_f32 v134,v212,v213\n"
"v_mfma_f32_16x16x32_bf16 a[104:107],a[184:187],v[116:119],a[104:107]\n"
"v_exp_f32 v214,v62\n"
"v_mfma_f32_16x16x32_bf16 a[112:115],a[184:187],v[120:123],a[112:115]\n"
"buffer_load_dwordx4 v240,s[40:43],s74 offen lds\n"
"v_mfma_f32_16x16x32_bf16 a[120:123],a[184:187],v[124:127],a[120:123]\n"
"s_add_u32 m0,s61,65536\n"
"ds_read_b64_tr_b16 a[178:179],v223 offset:1536\n"
"v_mov_b32 v102,v98\n"
"v_mfma_f32_16x16x32_bf16 a[100:103],a[188:191],v[112:115],a[100:103]\n"
"s_sub_u32 s63,s63,1\n"
"v_exp_f32 v215,v63\n"
"v_mfma_f32_16x16x32_bf16 a[108:111],a[188:191],v[116:119],a[108:111]\n"
"v_exp_f32 v208,v64\n"
"v_mfma_f32_16x16x32_bf16 a[116:119],a[188:191],v[120:123],a[116:119]\n"
"ds_read_b64_tr_b16 a[180:181],v224 offset:512\n"
"v_cvt_pk_bf16_f32 v135,v214,v215\n"
"v_mfma_f32_16x16x32_bf16 a[124:127],a[188:191],v[124:127],a[124:127]\n"
"s_waitcnt lgkmcnt(11)\n"
"v_exp_f32 v209,v65\n"
"v_mfma_f32_16x16x32_bf16 v[16:19],a[128:131],v[144:147],v[80:83]\n"
"ds_read_b64_tr_b16 a[182:183],v224 offset:1536\n"
"v_cvt_pk_bf16_f32 v136,v208,v209\n"
"v_mfma_f32_16x16x32_bf16 v[16:19],a[136:139],v[152:155],v[16:19]\n"
"v_exp_f32 v210,v66\n"
"v_mfma_f32_16x16x32_bf16 v[16:19],a[144:147],v[160:163],v[16:19]\n"
"v_exp_f32 v211,v67\n"
"v_mfma_f32_16x16x32_bf16 v[16:19],a[152:155],v[168:171],v[16:19]\n"
"ds_read_b64_tr_b16 a[184:185],v223 offset:768\n"
"v_cvt_pk_bf16_f32 v137,v210,v211\n"
"v_mfma_f32_16x16x32_bf16 v[24:27],a[152:155],v[172:175],v[84:87]\n"
"v_exp_f32 v212,v68\n"
"v_mfma_f32_16x16x32_bf16 v[24:27],a[144:147],v[164:167],v[24:27]\n"
"ds_read_b64_tr_b16 a[186:187],v223 offset:1792\n"
"v_mov_b32 v103,v99\n"
"v_mfma_f32_16x16x32_bf16 v[24:27],a[136:139],v[156:159],v[24:27]\n"
"v_exp_f32 v213,v69\n"
"v_mfma_f32_16x16x32_bf16 v[24:27],a[128:131],v[148:151],v[24:27]\n"
"v_exp_f32 v214,v70\n"
"v_smfmac_f32_16x16x64_bf16 v[100:103],a[240:243],v[128:135],v104\n"
"ds_read_b64_tr_b16 a[188:189],v224 offset:768\n"
"v_cvt_pk_bf16_f32 v138,v212,v213\n"
"v_mfma_f32_16x16x32_bf16 v[32:35],a[128:131],v[176:179],v[88:91]\n"
"v_exp_f32 v215,v71\n"
"v_mfma_f32_16x16x32_bf16 v[32:35],a[136:139],v[184:187],v[32:35]\n"
"buffer_load_dwordx4 v229,s[52:55],s62 offen lds\n"
"v_mfma_f32_16x16x32_bf16 v[32:35],a[144:147],v[192:195],v[32:35]\n"
"ds_read_b64_tr_b16 a[190:191],v224 offset:1792\n"
"v_cvt_pk_bf16_f32 v139,v214,v215\n"
"v_mfma_f32_16x16x32_bf16 v[32:35],a[152:155],v[200:203],v[32:35]\n"
"v_exp_f32 v208,v72\n"
"v_mfma_f32_16x16x32_bf16 v[40:43],a[152:155],v[204:207],v[92:95]\n"
"v_exp_f32 v209,v73\n"
"v_mfma_f32_16x16x32_bf16 v[40:43],a[144:147],v[196:199],v[40:43]\n"
"v_cvt_pk_bf16_f32 v140,v208,v209\n"
"v_mfma_f32_16x16x32_bf16 v[40:43],a[136:139],v[188:191],v[40:43]\n"
"v_exp_f32 v210,v74\n"
"v_mfma_f32_16x16x32_bf16 v[40:43],a[128:131],v[180:183],v[40:43]\n"
"v_exp_f32 v211,v75\n"
"v_mfma_f32_16x16x32_bf16 v[20:23],a[132:135],v[144:147],v[80:83]\n"
"v_exp_f32 v212,v76\n"
"v_mfma_f32_16x16x32_bf16 v[20:23],a[140:143],v[152:155],v[20:23]\n"
"v_exp_f32 v213,v77\n"
"v_mfma_f32_16x16x32_bf16 v[20:23],a[148:151],v[160:163],v[20:23]\n"
"v_exp_f32 v214,v78\n"
"v_mfma_f32_16x16x32_bf16 v[20:23],a[156:159],v[168:171],v[20:23]\n"
"v_exp_f32 v215,v79\n"
"v_mfma_f32_16x16x32_bf16 v[28:31],a[156:159],v[172:175],v[84:87]\n"
"v_cvt_pk_bf16_f32 v141,v210,v211\n"
"v_cvt_pk_bf16_f32 v142,v212,v213\n"
"v_mfma_f32_16x16x32_bf16 v[28:31],a[148:151],v[164:167],v[28:31]\n"
"v_cvt_pk_bf16_f32 v143,v214,v215\n"
"v_mfma_f32_16x16x32_bf16 v[28:31],a[140:143],v[156:159],v[28:31]\n"
"buffer_load_dwordx4 v229,s[52:55],s62 offen offset:1024 lds\n"
"v_mfma_f32_16x16x32_bf16 v[28:31],a[132:135],v[148:151],v[28:31]\n"
"v_smfmac_f32_16x16x64_bf16 v[100:103],a[244:247],v[136:143],v104\n"
"s_add_u32 s62,s62,8192\n"
"ds_read_b128 a[192:195],v222 offset:8192\n"
"v_mfma_f32_16x16x32_bf16 v[36:39],a[132:135],v[176:179],v[88:91]\n"
"ds_read_b128 a[196:199],v222 offset:9216\n"
"v_mfma_f32_16x16x32_bf16 v[36:39],a[140:143],v[184:187],v[36:39]\n"
"ds_read_b128 a[200:203],v222 offset:8448\n"
"v_mfma_f32_16x16x32_bf16 v[36:39],a[148:151],v[192:195],v[36:39]\n"
"ds_read_b128 a[204:207],v222 offset:9472\n"
"v_maximum3_f32 v234,v100,v101,v102\n"
"v_mfma_f32_16x16x32_bf16 v[36:39],a[156:159],v[200:203],v[36:39]\n"
"ds_read_b128 a[208:211],v222 offset:8704\n"
"v_maximum3_f32 v234,v234,v103,v103\n"
"v_mfma_f32_16x16x32_bf16 v[44:47],a[156:159],v[204:207],v[92:95]\n"
"ds_read_b128 a[212:215],v222 offset:9728\n"
"v_cmp_nge_f32 vcc,s64,v234\n"
"v_mfma_f32_16x16x32_bf16 v[44:47],a[148:151],v[196:199],v[44:47]\n"
"ds_read_b128 a[216:219],v222 offset:8960\n"
"v_mfma_f32_16x16x32_bf16 v[44:47],a[140:143],v[188:191],v[44:47]\n"
"ds_read_b128 a[220:223],v222 offset:9984\n"
"v_mfma_f32_16x16x32_bf16 v[44:47],a[132:135],v[180:183],v[44:47]\n"
"s_waitcnt lgkmcnt(8)\n"
"s_cbranch_vccnz L_stub0_%=\n"
"L_ret0_%=:\n"
"v_mfma_f32_16x16x32_bf16 a[0:3],a[160:163],v[128:131],a[0:3]\n"
"s_add_u32 m0,s61,8192\n"
"v_exp_f32 v208,v16\n"
"v_mfma_f32_16x16x32_bf16 a[8:11],a[160:163],v[132:135],a[8:11]\n"
"v_exp_f32 v209,v17\n"
"v_mfma_f32_16x16x32_bf16 a[16:19],a[160:163],v[136:139],a[16:19]\n"
"buffer_load_dwordx4 v228,s[48:51],s62 offen lds\n"
"v_mfma_f32_16x16x32_bf16 a[24:27],a[160:163],v[140:143],a[24:27]\n"
"s_cmp_eq_u32 s63,18\n"
"v_exp_f32 v210,v18\n"
"v_mfma_f32_16x16x32_bf16 a[4:7],a[164:167],v[128:131],a[4:7]\n"
"s_cselect_b64 s[40:41],s[84:85],s[40:41]\n"
"ds_read_b64_tr_b16 a[160:161],v223 offset:8192\n"
"v_cvt_pk_bf16_f32 v112,v208,v209\n"
"v_mfma_f32_16x16x32_bf16 a[12:15],a[164:167],v[132:135],a[12:15]\n"
"s_add_u32 s72,s72,1048576\n"
"v_exp_f32 v211,v19\n"
"v_mfma_f32_16x16x32_bf16 a[20:23],a[164:167],v[136:139],a[20:23]\n"
"ds_read_b64_tr_b16 a[162:163],v223 offset:9216\n"
"v_cvt_pk_bf16_f32 v113,v210,v211\n"
"v_mfma_f32_16x16x32_bf16 a[28:31],a[164:167],v[140:143],a[28:31]\n"
"v_exp_f32 v212,v20\n"
"v_mfma_f32_16x16x32_bf16 a[32:35],a[168:171],v[128:131],a[32:35]\n"
"ds_read_b64_tr_b16 a[164:165],v224 offset:8192\n"
"v_mov_b32 v96,v100\n"
"v_mfma_f32_16x16x32_bf16 a[40:43],a[168:171],v[132:135],a[40:43]\n"
"v_exp_f32 v213,v21\n"
"v_mfma_f32_16x16x32_bf16 a[48:51],a[168:171],v[136:139],a[48:51]\n"
"ds_read_b64_tr_b16 a[166:167],v224 offset:9216\n"
"v_cvt_pk_bf16_f32 v114,v212,v213\n"
"v_mfma_f32_16x16x32_bf16 a[56:59],a[168:171],v[140:143],a[56:59]\n"
"v_exp_f32 v214,v22\n"
"v_mfma_f32_16x16x32_bf16 a[36:39],a[172:175],v[128:131],a[36:39]\n"
"v_exp_f32 v215,v23\n"
"v_mfma_f32_16x16x32_bf16 a[44:47],a[172:175],v[132:135],a[44:47]\n"
"ds_read_b64_tr_b16 a[168:169],v223 offset:8448\n"
"v_cvt_pk_bf16_f32 v115,v214,v215\n"
"v_mfma_f32_16x16x32_bf16 a[52:55],a[172:175],v[136:139],a[52:55]\n"
"buffer_load_dwordx4 v228,s[48:51],s62 offen offset:1024 lds\n"
"v_mfma_f32_16x16x32_bf16 a[60:63],a[172:175],v[140:143],a[60:63]\n"
"v_exp_f32 v208,v24\n"
"v_mfma_f32_16x16x32_bf16 a[64:67],a[176:179],v[128:131],a[64:67]\n"
"ds_read_b64_tr_b16 a[170:171],v223 offset:9472\n"
"v_mov_b32 v97,v101\n"
"v_mfma_f32_16x16x32_bf16 a[72:75],a[176:179],v[132:135],a[72:75]\n"
"v_exp_f32 v209,v25\n"
"v_mfma_f32_16x16x32_bf16 a[80:83],a[176:179],v[136:139],a[80:83]\n"
"v_exp_f32 v210,v26\n"
"v_mfma_f32_16x16x32_bf16 a[88:91],a[176:179],v[140:143],a[88:91]\n"
"ds_read_b64_tr_b16 a[172:173],v224 offset:8448\n"
"v_cvt_pk_bf16_f32 v116,v208,v209\n"
"v_mfma_f32_16x16x32_bf16 a[68:71],a[180:183],v[128:131],a[68:71]\n"
"v_exp_f32 v211,v27\n"
"v_mfma_f32_16x16x32_bf16 a[76:79],a[180:183],v[132:135],a[76:79]\n"
"ds_read_b64_tr_b16 a[174:175],v224 offset:9472\n"
"v_cvt_pk_bf16_f32 v117,v210,v211\n"
"v_mfma_f32_16x16x32_bf16 a[84:87],a[180:183],v[136:139],a[84:87]\n"
"v_exp_f32 v212,v28\n"
"v_mfma_f32_16x16x32_bf16 a[92:95],a[180:183],v[140:143],a[92:95]\n"
"v_exp_f32 v213,v29\n"
"v_mfma_f32_16x16x32_bf16 a[96:99],a[184:187],v[128:131],a[96:99]\n"
"ds_read_b64_tr_b16 a[176:177],v223 offset:8704\n"
"v_cvt_pk_bf16_f32 v118,v212,v213\n"
"v_mfma_f32_16x16x32_bf16 a[104:107],a[184:187],v[132:135],a[104:107]\n"
"v_exp_f32 v214,v30\n"
"v_mfma_f32_16x16x32_bf16 a[112:115],a[184:187],v[136:139],a[112:115]\n"
"buffer_store_dwordx2 a[232:233],v105,s[44:47],s72 offen offset:0 sc1\n"
"v_mfma_f32_16x16x32_bf16 a[120:123],a[184:187],v[140:143],a[120:123]\n"
"s_add_u32 m0,s61,73728\n"
"ds_read_b64_tr_b16 a[178:179],v223 offset:9728\n"
"v_mov_b32 v98,v102\n"
"v_mfma_f32_16x16x32_bf16 a[100:103],a[188:191],v[128:131],a[100:103]\n"
"s_add_u32 s61,s65,s60\n"
"v_exp_f32 v215,v31\n"
"v_mfma_f32_16x16x32_bf16 a[108:111],a[188:191],v[132:135],a[108:111]\n"
"s_add_u32 s60,s60,16384\n"
"v_exp_f32 v208,v32\n"
"v_mfma_f32_16x16x32_bf16 a[116:119],a[188:191],v[136:139],a[116:119]\n"
"s_and_b32 s60,s60,0xffff\n"
"ds_read_b64_tr_b16 a[180:181],v224 offset:8704\n"
"v_cvt_pk_bf16_f32 v119,v214,v215\n"
"v_mfma_f32_16x16x32_bf16 a[124:127],a[188:191],v[140:143],a[124:127]\n"
"s_waitcnt lgkmcnt(11)\n"
"v_exp_f32 v209,v33\n"
"v_mfma_f32_16x16x32_bf16 v[48:51],a[192:195],v[144:147],v[80:83]\n"
"ds_read_b64_tr_b16 a[182:183],v224 offset:9728\n"
"v_cvt_pk_bf16_f32 v120,v208,v209\n"
"v_mfma_f32_16x16x32_bf16 v[48:51],a[200:203],v[152:155],v[48:51]\n"
"v_exp_f32 v210,v34\n"
"v_mfma_f32_16x16x32_bf16 v[48:51],a[208:211],v[160:163],v[48:51]\n"
"v_exp_f32 v211,v35\n"
"v_mfma_f32_16x16x32_bf16 v[48:51],a[216:219],v[168:171],v[48:51]\n"
"ds_read_b64_tr_b16 a[184:185],v223 offset:8960\n"
"v_cvt_pk_bf16_f32 v121,v210,v211\n"
"v_mfma_f32_16x16x32_bf16 v[56:59],a[216:219],v[172:175],v[84:87]\n"
"v_exp_f32 v212,v36\n"
"v_mfma_f32_16x16x32_bf16 v[56:59],a[208:211],v[164:167],v[56:59]\n"
"ds_read_b64_tr_b16 a[186:187],v223 offset:9984\n"
"v_mov_b32 v99,v103\n"
"v_mfma_f32_16x16x32_bf16 v[56:59],a[200:203],v[156:159],v[56:59]\n"
"v_exp_f32 v213,v37\n"
"v_mfma_f32_16x16x32_bf16 v[56:59],a[192:195],v[148:151],v[56:59]\n"
"v_exp_f32 v214,v38\n"
"v_smfmac_f32_16x16x64_bf16 v[96:99],a[240:243],v[112:119],v104\n"
"ds_read_b64_tr_b16 a[188:189],v224 offset:8960\n"
"v_cvt_pk_bf16_f32 v122,v212,v213\n"
"v_mfma_f32_16x16x32_bf16 v[64:67],a[192:195],v[176:179],v[88:91]\n"
"v_exp_f32 v215,v39\n"
"v_mfma_f32_16x16x32_bf16 v[64:67],a[200:203],v[184:187],v[64:67]\n"
"buffer_load_dwordx4 v229,s[52:55],s62 offen lds\n"
"v_mfma_f32_16x16x32_bf16 v[64:67],a[208:211],v[192:195],v[64:67]\n"
"ds_read_b64_tr_b16 a[190:191],v224 offset:9984\n"
"v_cvt_pk_bf16_f32 v123,v214,v215\n"
"v_mfma_f32_16x16x32_bf16 v[64:67],a[216:219],v[200:203],v[64:67]\n"
"v_exp_f32 v208,v40\n"
"v_mfma_f32_16x16x32_bf16 v[72:75],a[216:219],v[204:207],v[92:95]\n"
"v_exp_f32 v209,v41\n"
"v_mfma_f32_16x16x32_bf16 v[72:75],a[208:211],v[196:199],v[72:75]\n"
"v_cvt_pk_bf16_f32 v124,v208,v209\n"
"v_add_u32 v222,s60,v225\n"
"v_mfma_f32_16x16x32_bf16 v[72:75],a[200:203],v[188:191],v[72:75]\n"
"v_exp_f32 v210,v42\n"
"v_mfma_f32_16x16x32_bf16 v[72:75],a[192:195],v[180:183],v[72:75]\n"
"v_exp_f32 v211,v43\n"
"v_mfma_f32_16x16x32_bf16 v[52:55],a[196:199],v[144:147],v[80:83]\n"
"v_exp_f32 v212,v44\n"
"v_mfma_f32_16x16x32_bf16 v[52:55],a[204:207],v[152:155],v[52:55]\n"
"v_exp_f32 v213,v45\n"
"v_mfma_f32_16x16x32_bf16 v[52:55],a[212:215],v[160:163],v[52:55]\n"
"v_exp_f32 v214,v46\n"
"v_mfma_f32_16x16x32_bf16 v[52:55],a[220:223],v[168:171],v[52:55]\n"
"v_exp_f32 v215,v47\n"
"v_mfma_f32_16x16x32_bf16 v[60:63],a[220:223],v[172:175],v[84:87]\n"
"v_cvt_pk_bf16_f32 v125,v210,v211\n"
"v_cvt_pk_bf16_f32 v126,v212,v213\n"
"v_mfma_f32_16x16x32_bf16 v[60:63],a[212:215],v[164:167],v[60:63]\n"
"v_cvt_pk_bf16_f32 v127,v214,v215\n"
"v_mfma_f32_16x16x32_bf16 v[60:63],a[204:207],v[156:159],v[60:63]\n"
"buffer_load_dwordx4 v229,s[52:55],s62 offen offset:1024 lds\n"
"v_mfma_f32_16x16x32_bf16 v[60:63],a[196:199],v[148:151],v[60:63]\n"
"s_waitcnt vmcnt(20) lgkmcnt(0)\n"
"s_barrier\n"
"v_add_u32 v223,s60,v226\n"
"v_smfmac_f32_16x16x64_bf16 v[96:99],a[244:247],v[120:127],v104\n"
"s_add_u32 s62,s62,8192\n"
"ds_read_b128 a[128:131],v222 offset:0\n"
"v_add_u32 v224,s60,v227\n"
"v_mfma_f32_16x16x32_bf16 v[68:71],a[196:199],v[176:179],v[88:91]\n"
"s_cmp_lg_u32 s63,8\n"
"ds_read_b128 a[132:135],v222 offset:1024\n"
"v_mfma_f32_16x16x32_bf16 v[68:71],a[204:207],v[184:187],v[68:71]\n"
"ds_read_b128 a[136:139],v222 offset:256\n"
"v_maximum3_f32 v234,v96,v97,v98\n"
"v_mfma_f32_16x16x32_bf16 v[68:71],a[212:215],v[192:195],v[68:71]\n"
"ds_read_b128 a[140:143],v222 offset:1280\n"
"v_maximum3_f32 v234,v234,v99,v99\n"
"v_mfma_f32_16x16x32_bf16 v[68:71],a[220:223],v[200:203],v[68:71]\n"
"ds_read_b128 a[144:147],v222 offset:512\n"
"buffer_store_dwordx2 a[232:233],v105,s[44:47],s72 offen offset:512 sc1\n"
"v_mfma_f32_16x16x32_bf16 v[76:79],a[220:223],v[204:207],v[92:95]\n"
"ds_read_b128 a[148:151],v222 offset:1536\n"
"v_cmp_nge_f32 vcc,s64,v234\n"
"v_mfma_f32_16x16x32_bf16 v[76:79],a[212:215],v[196:199],v[76:79]\n"
"ds_read_b128 a[152:155],v222 offset:768\n"
"v_mfma_f32_16x16x32_bf16 v[76:79],a[204:207],v[188:191],v[76:79]\n"
"ds_read_b128 a[156:159],v222 offset:1792\n"
"v_mfma_f32_16x16x32_bf16 v[76:79],a[196:199],v[180:183],v[76:79]\n"
"s_cbranch_vccnz L_stub1_%=\n"
"L_ret1_%=:\n"
"s_cbranch_scc1 L_loop_%=\n"
"s_and_b32 s68,s39,0xffc\n"
"s_add_u32 s68,s68,491520\n"
"v_mov_b32 v235,s38\n"
"buffer_store_dword v235,v11,s[88:91],s68 offen sc1\n"
".p2align 6\n"
"L_looq_%=:\n"
"v_mfma_f32_16x16x32_bf16 a[0:3],a[160:163],v[112:115],a[0:3]\n"
"s_add_u32 m0,s61,0\n"
"v_exp_f32 v208,v48\n"
"v_mfma_f32_16x16x32_bf16 a[8:11],a[160:163],v[116:119],a[8:11]\n"
"v_exp_f32 v209,v49\n"
"v_mfma_f32_16x16x32_bf16 a[16:19],a[160:163],v[120:123],a[16:19]\n"
"buffer_load_dwordx4 v228,s[48:51],s62 offen lds\n"
"v_mfma_f32_16x16x32_bf16 a[24:27],a[160:163],v[124:127],a[24:27]\n"
"s_lshl_b32 s71,s63,10\n"
"v_exp_f32 v210,v50\n"
"v_mfma_f32_16x16x32_bf16 a[4:7],a[164:167],v[112:115],a[4:7]\n"
"s_and_b32 s71,s71,0x1c00\n"
"ds_read_b64_tr_b16 a[160:161],v223 offset:0\n"
"v_cvt_pk_bf16_f32 v128,v208,v209\n"
"v_mfma_f32_16x16x32_bf16 a[12:15],a[164:167],v[116:119],a[12:15]\n"
"s_lshl_b32 s74,s71,3\n"
"v_exp_f32 v211,v51\n"
"v_mfma_f32_16x16x32_bf16 a[20:23],a[164:167],v[120:123],a[20:23]\n"
"s_add_u32 s72,s72,1048576\n"
"ds_read_b64_tr_b16 a[162:163],v223 offset:1024\n"
"v_cvt_pk_bf16_f32 v129,v210,v211\n"
"v_mfma_f32_16x16x32_bf16 a[28:31],a[164:167],v[124:127],a[28:31]\n"
"s_lshr_b32 s69,s71,4\n"
"v_exp_f32 v212,v52\n"
"v_mfma_f32_16x16x32_bf16 a[32:35],a[168:171],v[112:115],a[32:35]\n"
"ds_read_b64_tr_b16 a[164:165],v224 offset:0\n"
"v_mov_b32 v100,v96\n"
"v_mfma_f32_16x16x32_bf16 a[40:43],a[168:171],v[116:119],a[40:43]\n"
"v_exp_f32 v213,v53\n"
"v_mfma_f32_16x16x32_bf16 a[48:51],a[168:171],v[120:123],a[48:51]\n"
"ds_read_b64_tr_b16 a[166:167],v224 offset:1024\n"
"v_cvt_pk_bf16_f32 v130,v212,v213\n"
"v_mfma_f32_16x16x32_bf16 a[56:59],a[168:171],v[124:127],a[56:59]\n"
"v_exp_f32 v214,v54\n"
"v_mfma_f32_16x16x32_bf16 a[36:39],a[172:175],v[112:115],a[36:39]\n"
"v_exp_f32 v215,v55\n"
"v_mfma_f32_16x16x32_bf16 a[44:47],a[172:175],v[116:119],a[44:47]\n"
"ds_read_b64_tr_b16 a[168:169],v223 offset:256\n"
"v_cvt_pk_bf16_f32 v131,v214,v215\n"
"v_mfma_f32_16x16x32_bf16 a[52:55],a[172:175],v[120:123],a[52:55]\n"
"buffer_load_dwordx4 v228,s[48:51],s62 offen offset:1024 lds\n"
"v_mfma_f32_16x16x32_bf16 a[60:63],a[172:175],v[124:127],a[60:63]\n"
"v_exp_f32 v208,v56\n"
"v_mfma_f32_16x16x32_bf16 a[64:67],a[176:179],v[112:115],a[64:67]\n"
"ds_read_b64_tr_b16 a[170:171],v223 offset:1280\n"
"v_add_u32 v239,s69,v241\n"
"v_mfma_f32_16x16x32_bf16 a[72:75],a[176:179],v[116:119],a[72:75]\n"
"v_exp_f32 v209,v57\n"
"v_mfma_f32_16x16x32_bf16 a[80:83],a[176:179],v[120:123],a[80:83]\n"
"v_exp_f32 v210,v58\n"
"v_mfma_f32_16x16x32_bf16 a[88:91],a[176:179],v[124:127],a[88:91]\n"
"ds_read_b64_tr_b16 a[172:173],v224 offset:256\n"
"v_cvt_pk_bf16_f32 v132,v208,v209\n"
"v_mfma_f32_16x16x32_bf16 a[68:71],a[180:183],v[112:115],a[68:71]\n"
"v_exp_f32 v211,v59\n"
"v_mfma_f32_16x16x32_bf16 a[76:79],a[180:183],v[116:119],a[76:79]\n"
"ds_read_b64_tr_b16 a[174:175],v224 offset:1280\n"
"v_cvt_pk_bf16_f32 v133,v210,v211\n"
"v_mfma_f32_16x16x32_bf16 a[84:87],a[180:183],v[120:123],a[84:87]\n"
"v_exp_f32 v212,v60\n"
"v_mfma_f32_16x16x32_bf16 a[92:95],a[180:183],v[124:127],a[92:95]\n"
"v_exp_f32 v213,v61\n"
"v_mfma_f32_16x16x32_bf16 a[96:99],a[184:187],v[112:115],a[96:99]\n"
"ds_read_b64_tr_b16 a[176:177],v223 offset:512\n"
"v_cvt_pk_bf16_f32 v134,v212,v213\n"
"v_mfma_f32_16x16x32_bf16 a[104:107],a[184:187],v[116:119],a[104:107]\n"
"v_exp_f32 v214,v62\n"
"v_mfma_f32_16x16x32_bf16 a[112:115],a[184:187],v[120:123],a[112:115]\n"
"buffer_store_dwordx4 a[232:235],v9,s[44:47],s72 offen offset:0 sc1\n"
"v_mfma_f32_16x16x32_bf16 a[120:123],a[184:187],v[124:127],a[120:123]\n"
"s_add_u32 m0,s61,65536\n"
"ds_read_b64_tr_b16 a[178:179],v223 offset:1536\n"
"v_mov_b32 v101,v97\n"
"v_mfma_f32_16x16x32_bf16 a[100:103],a[188:191],v[112:115],a[100:103]\n"
"s_sub_u32 s63,s63,1\n"
"v_exp_f32 v215,v63\n"
"v_mfma_f32_16x16x32_bf16 a[108:111],a[188:191],v[116:119],a[108:111]\n"
"v_exp_f32 v208,v64\n"
"v_mfma_f32_16x16x32_bf16 a[116:119],a[188:191],v[120:123],a[116:119]\n"
"ds_read_b64_tr_b16 a[180:181],v224 offset:512\n"
"v_cvt_pk_bf16_f32 v135,v214,v215\n"
"v_mfma_f32_16x16x32_bf16 a[124:127],a[188:191],v[124:127],a[124:127]\n"
"s_waitcnt lgkmcnt(11)\n"
"v_exp_f32 v209,v65\n"
"v_mfma_f32_16x16x32_bf16 v[16:19],a[128:131],v[144:147],v[80:83]\n"
"ds_read_b64_tr_b16 a[182:183],v224 offset:1536\n"
"v_cvt_pk_bf16_f32 v136,v208,v209\n"
"v_mfma_f32_16x16x32_bf16 v[16:19],a[136:139],v[152:155],v[16:19]\n"
"v_exp_f32 v210,v66\n"
"v_mfma_f32_16x16x32_bf16 v[16:19],a[144:147],v[160:163],v[16:19]\n"
"v_exp_f32 v211,v67\n"
"v_mfma_f32_16x16x32_bf16 v[16:19],a[152:155],v[168:171],v[16:19]\n"
"ds_read_b64_tr_b16 a[184:185],v223 offset:768\n"
"v_cvt_pk_bf16_f32 v137,v210,v211\n"
"v_mfma_f32_16x16x32_bf16 v[24:27],a[152:155],v[172:175],v[84:87]\n"
"v_exp_f32 v212,v68\n"
"v_mfma_f32_16x16x32_bf16 v[24:27],a[144:147],v[164:167],v[24:27]\n"
"ds_read_b64_tr_b16 a[186:187],v223 offset:1792\n"
"v_mov_b32 v102,v98\n"
"v_mfma_f32_16x16x32_bf16 v[24:27],a[136:139],v[156:159],v[24:27]\n"
"v_exp_f32 v213,v69\n"
"v_mfma_f32_16x16x32_bf16 v[24:27],a[128:131],v[148:151],v[24:27]\n"
"v_exp_f32 v214,v70\n"
"v_mfma_f32_16x16x32_bf16 v[32:35],a[128:131],v[176:179],v[88:91]\n"
"ds_read_b64_tr_b16 a[188:189],v224 offset:768\n"
"v_cvt_pk_bf16_f32 v138,v212,v213\n"
"v_mfma_f32_16x16x32_bf16 v[32:35],a[136:139],v[184:187],v[32:35]\n"
"v_exp_f32 v215,v71\n"
"v_mfma_f32_16x16x32_bf16 v[32:35],a[144:147],v[192:195],v[32:35]\n"
"buffer_load_dwordx4 v229,s[52:55],s62 offen lds\n"
"v_mfma_f32_16x16x32_bf16 v[32:35],a[152:155],v[200:203],v[32:35]\n"
"ds_read_b64_tr_b16 a[190:191],v224 offset:1792\n"
"v_cvt_pk_bf16_f32 v139,v214,v215\n"
"v_mfma_f32_16x16x32_bf16 v[40:43],a[152:155],v[204:207],v[92:95]\n"
"v_exp_f32 v208,v72\n"
"v_mfma_f32_16x16x32_bf16 v[40:43],a[144:147],v[196:199],v[40:43]\n"
"v_exp_f32 v209,v73\n"
"v_mfma_f32_16x16x32_bf16 v[40:43],a[136:139],v[188:191],v[40:43]\n"
"ds_read_b128 a[224:227],v239\n"
"v_cvt_pk_bf16_f32 v140,v208,v209\n"
"v_mfma_f32_16x16x32_bf16 v[40:43],a[128:131],v[180:183],v[40:43]\n"
"v_exp_f32 v210,v74\n"
"v_mfma_f32_16x16x32_bf16 v[20:23],a[132:135],v[144:147],v[80:83]\n"
"v_exp_f32 v211,v75\n"
"v_mfma_f32_16x16x32_bf16 v[20:23],a[140:143],v[152:155],v[20:23]\n"
"v_exp_f32 v212,v76\n"
"v_mfma_f32_16x16x32_bf16 v[20:23],a[148:151],v[160:163],v[20:23]\n"
"v_exp_f32 v213,v77\n"
"v_mfma_f32_16x16x32_bf16 v[20:23],a[156:159],v[168:171],v[20:23]\n"
"v_exp_f32 v214,v78\n"
"v_mfma_f32_16x16x32_bf16 v[28:31],a[156:159],v[172:175],v[84:87]\n"
"v_exp_f32 v215,v79\n"
"v_mfma_f32_16x16x32_bf16 v[28:31],a[148:151],v[164:167],v[28:31]\n"
"v_cvt_pk_bf16_f32 v141,v210,v211\n"
"v_cvt_pk_bf16_f32 v142,v212,v213\n"
"v_mfma_f32_16x16x32_bf16 v[28:31],a[140:143],v[156:159],v[28:31]\n"
"v_cvt_pk_bf16_f32 v143,v214,v215\n"
"v_mov_b32 v103,v99\n"
"v_mfma_f32_16x16x32_bf16 v[28:31],a[132:135],v[148:151],v[28:31]\n"
"buffer_load_dwordx4 v229,s[52:55],s62 offen offset:1024 lds\n"
"v_smfmac_f32_16x16x64_bf16 v[100:103],a[240:243],v[128:135],v104\n"
"v_smfmac_f32_16x16x64_bf16 v[100:103],a[244:247],v[136:143],v104\n"
"L_pgg112_%=:\n"
"s_cbranch_execz L_pgg176_%=\n"
"s_add_u32 s62,s62,8192\n"
"ds_read_b128 a[192:195],v222 offset:8192\n"
"v_mfma_f32_16x16x32_bf16 v[36:39],a[132:135],v[176:179],v[88:91]\n"
"ds_read_b128 a[196:199],v222 offset:9216\n"
"v_mfma_f32_16x16x32_bf16 v[36:39],a[140:143],v[184:187],v[36:39]\n"
"ds_read_b128 a[200:203],v222 offset:8448\n"
"v_mfma_f32_16x16x32_bf16 v[36:39],a[148:151],v[192:195],v[36:39]\n"
"ds_read_b128 a[204:207],v222 offset:9472\n"
"v_maximum3_f32 v234,v100,v101,v102\n"
"v_mfma_f32_16x16x32_bf16 v[36:39],a[156:159],v[200:203],v[36:39]\n"
"ds_read_b128 a[208:211],v222 offset:8704\n"
"v_maximum3_f32 v234,v234,v103,v103\n"
"v_mfma_f32_16x16x32_bf16 v[44:47],a[156:159],v[204:207],v[92:95]\n"
"ds_read_b128 a[212:215],v222 offset:9728\n"
"v_cmp_nge_f32 vcc,s64,v234\n"
"v_mfma_f32_16x16x32_bf16 v[44:47],a[148:151],v[196:199],v[44:47]\n"
"ds_read_b128 a[216:219],v222 offset:8960\n"
"v_mfma_f32_16x16x32_bf16 v[44:47],a[140:143],v[188:191],v[44:47]\n"
"ds_read_b128 a[220:223],v222 offset:9984\n"
"v_mfma_f32_16x16x32_bf16 v[44:47],a[132:135],v[180:183],v[44:47]\n"
"s_waitcnt lgkmcnt(9)\n"
"s_cbranch_vccnz L_stub9_%=\n"
"L_ret9_%=:\n"
"v_mfma_f32_16x16x32_bf16 a[0:3],a[160:163],v[128:131],a[0:3]\n"
"s_add_u32 m0,s61,8192\n"
"v_exp_f32 v208,v16\n"
"v_mfma_f32_16x16x32_bf16 a[8:11],a[160:163],v[132:135],a[8:11]\n"
"v_exp_f32 v209,v17\n"
"v_mfma_f32_16x16x32_bf16 a[16:19],a[160:163],v[136:139],a[16:19]\n"
"buffer_load_dwordx4 v228,s[48:51],s62 offen lds\n"
"v_mfma_f32_16x16x32_bf16 a[24:27],a[160:163],v[140:143],a[24:27]\n"
"s_waitcnt lgkmcnt(8)\n"
"v_exp_f32 v210,v18\n"
"v_mfma_f32_16x16x32_bf16 a[4:7],a[164:167],v[128:131],a[4:7]\n"
"ds_read_b64_tr_b16 a[160:161],v223 offset:8192\n"
"v_cvt_pk_bf16_f32 v112,v208,v209\n"
"v_mfma_f32_16x16x32_bf16 a[12:15],a[164:167],v[132:135],a[12:15]\n"
"v_exp_f32 v211,v19\n"
"v_mfma_f32_16x16x32_bf16 a[20:23],a[164:167],v[136:139],a[20:23]\n"
"ds_read_b64_tr_b16 a[162:163],v223 offset:9216\n"
"v_cvt_pk_bf16_f32 v113,v210,v211\n"
"v_mfma_f32_16x16x32_bf16 a[28:31],a[164:167],v[140:143],a[28:31]\n"
"v_exp_f32 v212,v20\n"
"v_mfma_f32_16x16x32_bf16 a[32:35],a[168:171],v[128:131],a[32:35]\n"
"ds_read_b64_tr_b16 a[164:165],v224 offset:8192\n"
"v_mov_b32 v96,v100\n"
"v_mfma_f32_16x16x32_bf16 a[40:43],a[168:171],v[132:135],a[40:43]\n"
"v_exp_f32 v213,v21\n"
"v_mfma_f32_16x16x32_bf16 a[48:51],a[168:171],v[136:139],a[48:51]\n"
"ds_read_b64_tr_b16 a[166:167],v224 offset:9216\n"
"v_cvt_pk_bf16_f32 v114,v212,v213\n"
"v_mfma_f32_16x16x32_bf16 a[56:59],a[168:171],v[140:143],a[56:59]\n"
"v_exp_f32 v214,v22\n"
"v_mfma_f32_16x16x32_bf16 a[36:39],a[172:175],v[128:131],a[36:39]\n"
"v_exp_f32 v215,v23\n"
"v_mfma_f32_16x16x32_bf16 a[44:47],a[172:175],v[132:135],a[44:47]\n"
"ds_read_b64_tr_b16 a[168:169],v223 offset:8448\n"
"v_cvt_pk_bf16_f32 v115,v214,v215\n"
"v_mfma_f32_16x16x32_bf16 a[52:55],a[172:175],v[136:139],a[52:55]\n"
"buffer_load_dwordx4 v228,s[48:51],s62 offen offset:1024 lds\n"
"v_mfma_f32_16x16x32_bf16 a[60:63],a[172:175],v[140:143],a[60:63]\n"
"v_exp_f32 v208,v24\n"
"v_mfma_f32_16x16x32_bf16 a[64:67],a[176:179],v[128:131],a[64:67]\n"
"ds_read_b64_tr_b16 a[170:171],v223 offset:9472\n"
"v_mov_b32 v97,v101\n"
"v_mfma_f32_16x16x32_bf16 a[72:75],a[176:179],v[132:135],a[72:75]\n"
"v_exp_f32 v209,v25\n"
"v_mfma_f32_16x16x32_bf16 a[80:83],a[176:179],v[136:139],a[80:83]\n"
"v_exp_f32 v210,v26\n"
"v_mfma_f32_16x16x32_bf16 a[88:91],a[176:179],v[140:143],a[88:91]\n"
"ds_read_b64_tr_b16 a[172:173],v224 offset:8448\n"
"v_cvt_pk_bf16_f32 v116,v208,v209\n"
"v_mfma_f32_16x16x32_bf16 a[68:71],a[180:183],v[128:131],a[68:71]\n"
"v_exp_f32 v211,v27\n"
"v_mfma_f32_16x16x32_bf16 a[76:79],a[180:183],v[132:135],a[76:79]\n"
"ds_read_b64_tr_b16 a[174:175],v224 offset:9472\n"
"v_cvt_pk_bf16_f32 v117,v210,v211\n"
"v_mfma_f32_16x16x32_bf16 a[84:87],a[180:183],v[136:139],a[84:87]\n"
"v_exp_f32 v212,v28\n"
"v_mfma_f32_16x16x32_bf16 a[92:95],a[180:183],v[140:143],a[92:95]\n"
"v_exp_f32 v213,v29\n"
"v_mfma_f32_16x16x32_bf16 a[96:99],a[184:187],v[128:131],a[96:99]\n"
"ds_read_b64_tr_b16 a[176:177],v223 offset:8704\n"
"v_cvt_pk_bf16_f32 v118,v212,v213\n"
"v_mfma_f32_16x16x32_bf16 a[104:107],a[184:187],v[132:135],a[104:107]\n"
"v_exp_f32 v214,v30\n"
"v_mfma_f32_16x16x32_bf16 a[112:115],a[184:187],v[136:139],a[112:115]\n"
"buffer_store_dwordx4 a[224:227],v9,s[80:83],s74 offen sc1\n"
"v_mfma_f32_16x16x32_bf16 a[120:123],a[184:187],v[140:143],a[120:123]\n"
"s_add_u32 m0,s61,73728\n"
"ds_read_b64_tr_b16 a[178:179],v223 offset:9728\n"
"v_mov_b32 v98,v102\n"
"v_mfma_f32_16x16x32_bf16 a[100:103],a[188:191],v[128:131],a[100:103]\n"
"s_add_u32 s61,s65,s60\n"
"v_exp_f32 v215,v31\n"
"v_mfma_f32_16x16x32_bf16 a[108:111],a[188:191],v[132:135],a[108:111]\n"
"s_add_u32 s60,s60,16384\n"
"v_exp_f32 v208,v32\n"
"v_mfma_f32_16x16x32_bf16 a[116:119],a[188:191],v[136:139],a[116:119]\n"
"s_and_b32 s60,s60,0xffff\n"
"ds_read_b64_tr_b16 a[180:181],v224 offset:8704\n"
"v_cvt_pk_bf16_f32 v119,v214,v215\n"
"v_mfma_f32_16x16x32_bf16 a[124:127],a[188:191],v[140:143],a[124:127]\n"
"s_waitcnt lgkmcnt(11)\n"
"v_exp_f32 v209,v33\n"
"v_mfma_f32_16x16x32_bf16 v[48:51],a[192:195],v[144:147],v[80:83]\n"
"ds_read_b64_tr_b16 a[182:183],v224 offset:9728\n"
"v_cvt_pk_bf16_f32 v120,v208,v209\n"
"v_mfma_f32_16x16x32_bf16 v[48:51],a[200:203],v[152:155],v[48:51]\n"
"v_exp_f32 v210,v34\n"
"v_mfma_f32_16x16x32_bf16 v[48:51],a[208:211],v[160:163],v[48:51]\n"
"v_exp_f32 v211,v35\n"
"v_mfma_f32_16x16x32_bf16 v[48:51],a[216:219],v[168:171],v[48:51]\n"
"ds_read_b64_tr_b16 a[184:185],v223 offset:8960\n"
"v_cvt_pk_bf16_f32 v121,v210,v211\n"
"v_mfma_f32_16x16x32_bf16 v[56:59],a[216:219],v[172:175],v[84:87]\n"
"v_exp_f32 v212,v36\n"
"v_mfma_f32_16x16x32_bf16 v[56:59],a[208:211],v[164:167],v[56:59]\n"
"ds_read_b64_tr_b16 a[186:187],v223 offset:9984\n"
"v_mov_b32 v99,v103\n"
"v_mfma_f32_16x16x32_bf16 v[56:59],a[200:203],v[156:159],v[56:59]\n"
"v_exp_f32 v213,v37\n"
"v_mfma_f32_16x16x32_bf16 v[56:59],a[192:195],v[148:151],v[56:59]\n"
"v_exp_f32 v214,v38\n"
"v_smfmac_f32_16x16x64_bf16 v[96:99],a[240:243],v[112:119],v104\n"
"ds_read_b64_tr_b16 a[188:189],v224 offset:8960\n"
"v_cvt_pk_bf16_f32 v122,v212,v213\n"
"v_mfma_f32_16x16x32_bf16 v[64:67],a[192:195],v[176:179],v[88:91]\n"
"v_exp_f32 v215,v39\n"
"v_mfma_f32_16x16x32_bf16 v[64:67],a[200:203],v[184:187],v[64:67]\n"
"buffer_load_dwordx4 v229,s[52:55],s62 offen lds\n"
"v_mfma_f32_16x16x32_bf16 v[64:67],a[208:211],v[192:195],v[64:67]\n"
"ds_read_b64_tr_b16 a[190:191],v224 offset:9984\n"
"v_cvt_pk_bf16_f32 v123,v214,v215\n"
"v_mfma_f32_16x16x32_bf16 v[64:67],a[216:219],v[200:203],v[64:67]\n"
"v_exp_f32 v208,v40\n"
"v_mfma_f32_16x16x32_bf16 v[72:75],a[216:219],v[204:207],v[92:95]\n"
"v_exp_f32 v209,v41\n"
"v_mfma_f32_16x16x32_bf16 v[72:75],a[208:211],v[196:199],v[72:75]\n"
"v_cvt_pk_bf16_f32 v124,v208,v209\n"
"v_add_u32 v222,s60,v225\n"
"v_mfma_f32_16x16x32_bf16 v[72:75],a[200:203],v[188:191],v[72:75]\n"
"v_exp_f32 v210,v42\n"
"v_mfma_f32_16x16x32_bf16 v[72:75],a[192:195],v[180:183],v[72:75]\n"
"v_exp_f32 v211,v43\n"
"v_mfma_f32_16x16x32_bf16 v[52:55],a[196:199],v[144:147],v[80:83]\n"
"v_exp_f32 v212,v44\n"
"v_mfma_f32_16x16x32_bf16 v[52:55],a[204:207],v[152:155],v[52:55]\n"
"v_exp_f32 v213,v45\n"
"v_mfma_f32_16x16x32_bf16 v[52:55],a[212:215],v[160:163],v[52:55]\n"
"v_exp_f32 v214,v46\n"
"v_mfma_f32_16x16x32_bf16 v[52:55],a[220:223],v[168:171],v[52:55]\n"
"v_exp_f32 v215,v47\n"
"v_mfma_f32_16x16x32_bf16 v[60:63],a[220:223],v[172:175],v[84:87]\n"
"v_cvt_pk_bf16_f32 v125,v210,v211\n"
"v_cvt_pk_bf16_f32 v126,v212,v213\n"
"v_mfma_f32_16x16x32_bf16 v[60:63],a[212:215],v[164:167],v[60:63]\n"
"v_cvt_pk_bf16_f32 v127,v214,v215\n"
"v_mfma_f32_16x16x32_bf16 v[60:63],a[204:207],v[156:159],v[60:63]\n"
"buffer_load_dwordx4 v229,s[52:55],s62 offen offset:1024 lds\n"
"v_mfma_f32_16x16x32_bf16 v[60:63],a[196:199],v[148:151],v[60:63]\n"
"s_waitcnt vmcnt(20) lgkmcnt(0)\n"
"s_barrier\n"
"v_add_u32 v223,s60,v226\n"
"v_smfmac_f32_16x16x64_bf16 v[96:99],a[244:247],v[120:127],v104\n"
"s_add_u32 s62,s62,8192\n"
"ds_read_b128 a[128:131],v222 offset:0\n"
"v_add_u32 v224,s60,v227\n"
"v_mfma_f32_16x16x32_bf16 v[68:71],a[196:199],v[176:179],v[88:91]\n"
"s_cmp_lg_u32 s63,3\n"
"ds_read_b128 a[132:135],v222 offset:1024\n"
"v_mfma_f32_16x16x32_bf16 v[68:71],a[204:207],v[184:187],v[68:71]\n"
"ds_read_b128 a[136:139],v222 offset:256\n"
"v_maximum3_f32 v234,v96,v97,v98\n"
"v_mfma_f32_16x16x32_bf16 v[68:71],a[212:215],v[192:195],v[68:71]\n"
"ds_read_b128 a[140:143],v222 offset:1280\n"
"v_maximum3_f32 v234,v234,v99,v99\n"
"v_mfma_f32_16x16x32_bf16 v[68:71],a[220:223],v[200:203],v[68:71]\n"
"ds_read_b128 a[144:147],v222 offset:512\n"
"v_cmp_nge_f32 vcc,s64,v234\n"
"v_mfma_f32_16x16x32_bf16 v[76:79],a[220:223],v[204:207],v[92:95]\n"
"ds_read_b128 a[148:151],v222 offset:1536\n"
"v_mfma_f32_16x16x32_bf16 v[76:79],a[212:215],v[196:199],v[76:79]\n"
"ds_read_b128 a[152:155],v222 offset:768\n"
"v_mfma_f32_16x16x32_bf16 v[76:79],a[204:207],v[188:191],v[76:79]\n"
"ds_read_b128 a[156:159],v222 offset:1792\n"
"v_mfma_f32_16x16x32_bf16 v[76:79],a[196:199],v[180:183],v[76:79]\n"
"s_cbranch_vccnz L_stub10_%=\n"
"L_ret10_%=:\n"
"s_cbranch_scc1 L_looq_%=\n"
"v_add_u32 v230,65536,v226\n"
"v_add_u32 v231,65536,v227\n"
"v_mfma_f32_16x16x32_bf16 a[0:3],a[160:163],v[112:115],a[0:3]\n"
"s_add_u32 m0,s65,0\n"
"v_exp_f32 v208,v48\n"
"v_mfma_f32_16x16x32_bf16 a[8:11],a[160:163],v[116:119],a[8:11]\n"
"s_add_u32 s85,s99,147456\n"
"buffer_load_dwordx4 v246,s[56:59],s99 offen offset:0 lds\n"
"v_mfma_f32_16x16x32_bf16 a[16:19],a[160:163],v[120:123],a[16:19]\n"
"s_lshl_b32 s61,s37,10\n"
"v_exp_f32 v209,v49\n"
"v_mfma_f32_16x16x32_bf16 a[24:27],a[160:163],v[124:127],a[24:27]\n"
"s_lshl_b32 s72,s35,14\n"
"v_exp_f32 v210,v50\n"
"v_mfma_f32_16x16x32_bf16 a[4:7],a[164:167],v[112:115],a[4:7]\n"
"s_add_u32 s12,s72,16384\n"
"ds_read_b128 a[224:227],v241 offset:192\n"
"v_cvt_pk_bf16_f32 v128,v208,v209\n"
"v_mfma_f32_16x16x32_bf16 a[12:15],a[164:167],v[116:119],a[12:15]\n"
"s_and_b32 s12,s12,0xffff\n"
"v_exp_f32 v211,v51\n"
"v_mfma_f32_16x16x32_bf16 a[20:23],a[164:167],v[120:123],a[20:23]\n"
"s_add_u32 s13,s72,32768\n"
"buffer_load_dwordx4 v247,s[56:59],s99 offen offset:1024 lds\n"
"v_mfma_f32_16x16x32_bf16 a[28:31],a[164:167],v[124:127],a[28:31]\n"
"s_add_u32 m0,s65,8192\n"
"v_exp_f32 v212,v52\n"
"v_mfma_f32_16x16x32_bf16 a[32:35],a[168:171],v[112:115],a[32:35]\n"
"s_and_b32 s13,s13,0xffff\n"
"v_exp_f32 v213,v53\n"
"v_mfma_f32_16x16x32_bf16 a[40:43],a[168:171],v[116:119],a[40:43]\n"
"s_add_u32 s14,s72,49152\n"
"ds_read_b128 a[228:231],v241 offset:128\n"
"v_cvt_pk_bf16_f32 v129,v210,v211\n"
"v_mfma_f32_16x16x32_bf16 a[48:51],a[168:171],v[120:123],a[48:51]\n"
"s_and_b32 s14,s14,0xffff\n"
"v_exp_f32 v214,v54\n"
"v_mfma_f32_16x16x32_bf16 a[56:59],a[168:171],v[124:127],a[56:59]\n"
"s_mul_i32 s86,s12,18\n"
"buffer_load_dwordx4 v248,s[56:59],s99 offen offset:0 lds\n"
"v_mfma_f32_16x16x32_bf16 a[36:39],a[172:175],v[112:115],a[36:39]\n"
"s_mul_i32 s87,s13,18\n"
"v_exp_f32 v215,v55\n"
"v_mfma_f32_16x16x32_bf16 a[44:47],a[172:175],v[116:119],a[44:47]\n"
"s_mov_b32 s68,8388608\n"
"v_exp_f32 v208,v56\n"
"v_mfma_f32_16x16x32_bf16 a[52:55],a[172:175],v[120:123],a[52:55]\n"
"s_mul_i32 s69,s34,1048576\n"
"ds_read_b128 a[232:235],v241 offset:64\n"
"v_cvt_pk_bf16_f32 v130,v212,v213\n"
"v_mfma_f32_16x16x32_bf16 a[60:63],a[172:175],v[124:127],a[60:63]\n"
"s_add_u32 s68,s68,s69\n"
"v_exp_f32 v209,v57\n"
"v_mfma_f32_16x16x32_bf16 a[64:67],a[176:179],v[112:115],a[64:67]\n"
"s_mul_i32 s69,s37,262144\n"
"buffer_load_dwordx4 v249,s[56:59],s99 offen offset:1024 lds\n"
"v_mfma_f32_16x16x32_bf16 a[72:75],a[176:179],v[116:119],a[72:75]\n"
"s_add_u32 s68,s68,s69\n"
"v_exp_f32 v210,v58\n"
"v_mfma_f32_16x16x32_bf16 a[80:83],a[176:179],v[120:123],a[80:83]\n"
"s_mul_i32 s69,s36,65536\n"
"ds_read_b128 a[192:195],v225 offset:24576\n"
"v_cvt_pk_bf16_f32 v131,v214,v215\n"
"v_mfma_f32_16x16x32_bf16 a[88:91],a[176:179],v[124:127],a[88:91]\n"
"s_add_u32 s68,s68,s69\n"
"v_exp_f32 v211,v59\n"
"v_mfma_f32_16x16x32_bf16 a[68:71],a[180:183],v[112:115],a[68:71]\n"
"s_mul_i32 s69,s35,16384\n"
"v_exp_f32 v212,v60\n"
"v_mfma_f32_16x16x32_bf16 a[76:79],a[180:183],v[116:119],a[76:79]\n"
"s_add_u32 s68,s68,s69\n"
"ds_read_b128 a[196:199],v225 offset:24832\n"
"v_cvt_pk_bf16_f32 v132,v208,v209\n"
"v_mfma_f32_16x16x32_bf16 a[84:87],a[180:183],v[120:123],a[84:87]\n"
"s_add_u32 s48,s8,s68\n"
"s_addc_u32 s49,s9,0\n"
"v_exp_f32 v213,v61\n"
"v_mfma_f32_16x16x32_bf16 a[92:95],a[180:183],v[124:127],a[92:95]\n"
"s_and_b32 s49,s49,0xffff\n"
"ds_read_b128 a[200:203],v225 offset:25088\n"
"v_cvt_pk_bf16_f32 v133,v210,v211\n"
"v_mfma_f32_16x16x32_bf16 a[96:99],a[184:187],v[112:115],a[96:99]\n"
"s_waitcnt lgkmcnt(6)\n"
"v_exp_f32 v214,v62\n"
"v_mfma_f32_16x16x32_bf16 a[104:107],a[184:187],v[116:119],a[104:107]\n"
"s_mov_b32 s50,16384\n"
"ds_read_b128 a[204:207],v225 offset:25344\n"
"v_cvt_pk_bf16_f32 v134,v212,v213\n"
"v_mfma_f32_16x16x32_bf16 a[112:115],a[184:187],v[120:123],a[112:115]\n"
"s_mov_b32 s51,0x20000\n"
"v_exp_f32 v215,v63\n"
"v_mfma_f32_16x16x32_bf16 a[120:123],a[184:187],v[124:127],a[120:123]\n"
"s_mov_b32 s75,8388608\n"
"v_exp_f32 v208,v64\n"
"v_mfma_f32_16x16x32_bf16 a[100:103],a[188:191],v[112:115],a[100:103]\n"
"s_mul_i32 s78,s34,1048576\n"
"ds_read_b128 a[208:211],v225 offset:25600\n"
"v_cvt_pk_bf16_f32 v135,v214,v215\n"
"v_mfma_f32_16x16x32_bf16 a[108:111],a[188:191],v[116:119],a[108:111]\n"
"s_add_u32 s75,s75,s78\n"
"v_exp_f32 v209,v65\n"
"v_mfma_f32_16x16x32_bf16 a[116:119],a[188:191],v[120:123],a[116:119]\n"
"s_add_u32 m0,s61,69632\n"
"ds_read_b128 a[212:215],v225 offset:25856\n"
"v_cvt_pk_bf16_f32 v136,v208,v209\n"
"v_mfma_f32_16x16x32_bf16 a[124:127],a[188:191],v[124:127],a[124:127]\n"
"s_mul_i32 s78,s37,262144\n"
"buffer_load_dwordx4 v254,s[56:59],s85 offen offset:0 lds\n"
"v_mfma_f32_16x16x32_bf16 v[16:19],a[128:131],v[144:147],v[80:83]\n"
"v_exp_f32 v210,v66\n"
"v_mfma_f32_16x16x32_bf16 v[16:19],a[136:139],v[152:155],v[16:19]\n"
"s_add_u32 m0,s65,73728\n"
"v_exp_f32 v211,v67\n"
"v_mfma_f32_16x16x32_bf16 v[16:19],a[144:147],v[160:163],v[16:19]\n"
"s_add_u32 s75,s75,s78\n"
"v_exp_f32 v212,v68\n"
"v_mfma_f32_16x16x32_bf16 v[16:19],a[152:155],v[168:171],v[16:19]\n"
"s_mul_i32 s78,s36,65536\n"
"ds_read_b128 a[216:219],v225 offset:26112\n"
"v_cvt_pk_bf16_f32 v137,v210,v211\n"
"v_mfma_f32_16x16x32_bf16 v[24:27],a[152:155],v[172:175],v[84:87]\n"
"s_add_u32 s75,s75,s78\n"
"buffer_load_dwordx4 v246,s[56:59],s86 offen offset:0 lds\n"
"v_mfma_f32_16x16x32_bf16 v[24:27],a[144:147],v[164:167],v[24:27]\n"
"s_add_u32 s44,s8,s75\n"
"s_addc_u32 s45,s9,0\n"
"v_exp_f32 v213,v69\n"
"v_mfma_f32_16x16x32_bf16 v[24:27],a[136:139],v[156:159],v[24:27]\n"
"s_and_b32 s45,s45,0xffff\n"
"v_exp_f32 v214,v70\n"
"v_mfma_f32_16x16x32_bf16 v[24:27],a[128:131],v[148:151],v[24:27]\n"
"s_mov_b32 s46,65536\n"
"ds_read_b128 a[220:223],v225 offset:26368\n"
"v_cvt_pk_bf16_f32 v138,v212,v213\n"
"v_mfma_f32_16x16x32_bf16 v[20:23],a[132:135],v[144:147],v[80:83]\n"
"s_waitcnt lgkmcnt(7)\n"
"v_exp_f32 v215,v71\n"
"v_mfma_f32_16x16x32_bf16 v[20:23],a[140:143],v[152:155],v[20:23]\n"
"s_mov_b32 s47,0x20000\n"
"buffer_load_dwordx4 v247,s[56:59],s86 offen offset:1024 lds\n"
"v_mfma_f32_16x16x32_bf16 v[20:23],a[148:151],v[160:163],v[20:23]\n"
"s_mov_b32 s15,294912\n"
"v_exp_f32 v208,v72\n"
"v_mfma_f32_16x16x32_bf16 v[20:23],a[156:159],v[168:171],v[20:23]\n"
"s_mul_i32 s78,s34,2048\n"
"v_exp_f32 v209,v73\n"
"v_mfma_f32_16x16x32_bf16 v[28:31],a[156:159],v[172:175],v[84:87]\n"
"s_add_u32 s15,s15,s78\n"
"ds_read_b64_tr_b16 a[160:161],v226 offset:16384\n"
"v_cvt_pk_bf16_f32 v139,v214,v215\n"
"v_mfma_f32_16x16x32_bf16 v[28:31],a[148:151],v[164:167],v[28:31]\n"
"s_mul_i32 s78,s37,512\n"
"v_exp_f32 v210,v74\n"
"v_mfma_f32_16x16x32_bf16 v[28:31],a[140:143],v[156:159],v[28:31]\n"
"s_add_u32 s15,s15,s78\n"
"ds_read_b64_tr_b16 a[162:163],v226 offset:17408\n"
"v_cvt_pk_bf16_f32 v140,v208,v209\n"
"v_mfma_f32_16x16x32_bf16 v[28:31],a[132:135],v[148:151],v[28:31]\n"
"s_mul_i32 s78,s36,128\n"
"v_exp_f32 v211,v75\n"
"v_mfma_f32_16x16x32_bf16 v[32:35],a[192:195],v[144:147],v[80:83]\n"
"s_waitcnt lgkmcnt(5)\n"
"v_exp_f32 v212,v76\n"
"v_mfma_f32_16x16x32_bf16 v[32:35],a[196:199],v[152:155],v[32:35]\n"
"s_add_u32 s15,s15,s78\n"
"ds_read_b64_tr_b16 a[164:165],v227 offset:16384\n"
"v_cvt_pk_bf16_f32 v141,v210,v211\n"
"v_mfma_f32_16x16x32_bf16 v[32:35],a[200:203],v[160:163],v[32:35]\n"
"s_mov_b32 s16,s15\n"
"v_exp_f32 v213,v77\n"
"v_mfma_f32_16x16x32_bf16 v[32:35],a[204:207],v[168:171],v[32:35]\n"
"s_mul_i32 s78,s35,4\n"
"ds_read_b64_tr_b16 a[166:167],v227 offset:17408\n"
"v_cvt_pk_bf16_f32 v142,v212,v213\n"
"v_mfma_f32_16x16x32_bf16 v[40:43],a[204:207],v[172:175],v[84:87]\n"
"s_add_u32 s16,s16,s78\n"
"v_exp_f32 v214,v78\n"
"v_mfma_f32_16x16x32_bf16 v[40:43],a[200:203],v[164:167],v[40:43]\n"
"s_mul_i32 s60,s37,9216\n"
"ds_read_b64_tr_b16 a[168:169],v226 offset:16640\n"
"v_mov_b32 v100,v96\n"
"v_mfma_f32_16x16x32_bf16 v[40:43],a[196:199],v[156:159],v[40:43]\n"
"v_exp_f32 v215,v79\n"
"v_mfma_f32_16x16x32_bf16 v[40:43],a[192:195],v[148:151],v[40:43]\n"
"ds_read_b64_tr_b16 a[170:171],v226 offset:17664\n"
"v_cvt_pk_bf16_f32 v143,v214,v215\n"
"v_mfma_f32_16x16x32_bf16 v[36:39],a[208:211],v[144:147],v[80:83]\n"
"s_waitcnt lgkmcnt(4)\n"
"ds_read_b64_tr_b16 a[172:173],v227 offset:16640\n"
"v_mov_b32 v101,v97\n"
"v_mfma_f32_16x16x32_bf16 v[36:39],a[212:215],v[152:155],v[36:39]\n"
"ds_read_b64_tr_b16 a[174:175],v227 offset:17664\n"
"v_mov_b32 v102,v98\n"
"v_mfma_f32_16x16x32_bf16 v[36:39],a[216:219],v[160:163],v[36:39]\n"
"s_waitcnt vmcnt(17) lgkmcnt(4)\n"
"s_barrier\n"
"v_mov_b32 v103,v99\n"
"v_mfma_f32_16x16x32_bf16 v[36:39],a[220:223],v[168:171],v[36:39]\n"
"s_add_u32 m0,s65,147456\n"
"ds_read_b64_tr_b16 a[176:177],v226 offset:16896\n"
"ds_read_b64_tr_b16 a[178:179],v226 offset:17920\n"
"v_smfmac_f32_16x16x64_bf16 v[100:103],a[240:243],v[128:135],v104\n"
"buffer_load_dwordx4 v229,s[52:55],s62 offen offset:0 lds\n"
"v_smfmac_f32_16x16x64_bf16 v[100:103],a[244:247],v[136:143],v104\n"
"ds_read_b64_tr_b16 a[180:181],v227 offset:16896\n"
"ds_read_b64_tr_b16 a[182:183],v227 offset:17920\n"
"v_mfma_f32_16x16x32_bf16 v[44:47],a[220:223],v[172:175],v[84:87]\n"
"ds_read_b64_tr_b16 a[184:185],v226 offset:17152\n"
"ds_read_b64_tr_b16 a[186:187],v226 offset:18176\n"
"v_mfma_f32_16x16x32_bf16 v[44:47],a[216:219],v[164:167],v[44:47]\n"
"ds_read_b64_tr_b16 a[188:189],v227 offset:17152\n"
"ds_read_b64_tr_b16 a[190:191],v227 offset:18176\n"
"v_mfma_f32_16x16x32_bf16 v[44:47],a[212:215],v[156:159],v[44:47]\n"
"ds_read_b128 a[152:155],v225 offset:32768\n"
"ds_read_b128 a[144:147],v225 offset:33024\n"
"v_mfma_f32_16x16x32_bf16 v[44:47],a[208:211],v[148:151],v[44:47]\n"
"buffer_load_dwordx4 v229,s[52:55],s62 offen offset:1024 lds\n"
"s_nop 0\n"
"v_maximum3_f32 v234,v100,v101,v102\n"
"v_maximum3_f32 v234,v234,v103,v103\n"
"v_cmp_nge_f32 vcc,s64,v234\n"
"s_cbranch_vccnz L_stub3_%=\n"
"L_ret3_%=:\n"
"v_mfma_f32_16x16x32_bf16 a[0:3],a[160:163],v[128:131],a[0:3]\n"
"s_waitcnt lgkmcnt(12)\n"
"v_exp_f32 v208,v16\n"
"v_mfma_f32_16x16x32_bf16 a[8:11],a[160:163],v[132:135],a[8:11]\n"
"s_add_u32 m0,s65,135168\n"
"v_exp_f32 v209,v17\n"
"v_mfma_f32_16x16x32_bf16 a[16:19],a[160:163],v[136:139],a[16:19]\n"
"ds_read_b128 a[136:139],v225 offset:33280\n"
"v_cvt_pk_bf16_f32 v112,v208,v209\n"
"v_mfma_f32_16x16x32_bf16 a[24:27],a[160:163],v[140:143],a[24:27]\n"
"v_exp_f32 v210,v18\n"
"v_mfma_f32_16x16x32_bf16 a[4:7],a[164:167],v[128:131],a[4:7]\n"
"buffer_load_dwordx4 v252,s[56:59],s87 offen offset:0 lds\n"
"v_mfma_f32_16x16x32_bf16 a[12:15],a[164:167],v[132:135],a[12:15]\n"
"v_exp_f32 v211,v19\n"
"v_mfma_f32_16x16x32_bf16 a[20:23],a[164:167],v[136:139],a[20:23]\n"
"v_exp_f32 v212,v20\n"
"v_mfma_f32_16x16x32_bf16 a[28:31],a[164:167],v[140:143],a[28:31]\n"
"ds_read_b128 a[128:131],v225 offset:33536\n"
"v_cvt_pk_bf16_f32 v113,v210,v211\n"
"v_mfma_f32_16x16x32_bf16 a[32:35],a[168:171],v[128:131],a[32:35]\n"
"s_waitcnt lgkmcnt(10)\n"
"v_exp_f32 v213,v21\n"
"v_mfma_f32_16x16x32_bf16 a[40:43],a[168:171],v[132:135],a[40:43]\n"
"buffer_load_dwordx4 v253,s[56:59],s87 offen offset:1024 lds\n"
"v_mfma_f32_16x16x32_bf16 a[48:51],a[168:171],v[136:139],a[48:51]\n"
"s_add_u32 m0,s61,143360\n"
"v_exp_f32 v214,v22\n"
"v_mfma_f32_16x16x32_bf16 a[56:59],a[168:171],v[140:143],a[56:59]\n"
"ds_read_b128 a[156:159],v225 offset:33792\n"
"v_cvt_pk_bf16_f32 v114,v212,v213\n"
"v_mfma_f32_16x16x32_bf16 a[36:39],a[172:175],v[128:131],a[36:39]\n"
"v_exp_f32 v215,v23\n"
"v_mfma_f32_16x16x32_bf16 a[44:47],a[172:175],v[132:135],a[44:47]\n"
"v_exp_f32 v208,v24\n"
"v_mfma_f32_16x16x32_bf16 a[52:55],a[172:175],v[136:139],a[52:55]\n"
"buffer_load_dwordx4 v254,s[56:59],s87 offen offset:0 lds\n"
"v_mfma_f32_16x16x32_bf16 a[60:63],a[172:175],v[140:143],a[60:63]\n"
"v_exp_f32 v209,v25\n"
"v_mfma_f32_16x16x32_bf16 a[64:67],a[176:179],v[128:131],a[64:67]\n"
"s_waitcnt lgkmcnt(7)\n"
"ds_read_b128 a[148:151],v225 offset:34048\n"
"v_cvt_pk_bf16_f32 v115,v214,v215\n"
"v_mfma_f32_16x16x32_bf16 a[72:75],a[176:179],v[132:135],a[72:75]\n"
"v_exp_f32 v210,v26\n"
"v_mfma_f32_16x16x32_bf16 a[80:83],a[176:179],v[136:139],a[80:83]\n"
"ds_read_b128 a[140:143],v225 offset:34304\n"
"v_cvt_pk_bf16_f32 v116,v208,v209\n"
"v_mfma_f32_16x16x32_bf16 a[88:91],a[176:179],v[140:143],a[88:91]\n"
"v_exp_f32 v211,v27\n"
"v_mfma_f32_16x16x32_bf16 a[68:71],a[180:183],v[128:131],a[68:71]\n"
"v_exp_f32 v212,v28\n"
"v_mfma_f32_16x16x32_bf16 a[76:79],a[180:183],v[132:135],a[76:79]\n"
"ds_read_b128 a[132:135],v225 offset:34560\n"
"v_cvt_pk_bf16_f32 v117,v210,v211\n"
"v_mfma_f32_16x16x32_bf16 a[84:87],a[180:183],v[136:139],a[84:87]\n"
"v_exp_f32 v213,v29\n"
"v_mfma_f32_16x16x32_bf16 a[92:95],a[180:183],v[140:143],a[92:95]\n"
"ds_read_b128 a[204:207],v225 offset:40960\n"
"v_cvt_pk_bf16_f32 v118,v212,v213\n"
"v_mfma_f32_16x16x32_bf16 a[96:99],a[184:187],v[128:131],a[96:99]\n"
"s_waitcnt lgkmcnt(8)\n"
"v_exp_f32 v214,v30\n"
"v_mfma_f32_16x16x32_bf16 a[104:107],a[184:187],v[132:135],a[104:107]\n"
"ds_read_b128 a[200:203],v225 offset:41216\n"
"v_mov_b32 v96,v100\n"
"v_mfma_f32_16x16x32_bf16 a[112:115],a[184:187],v[136:139],a[112:115]\n"
"v_exp_f32 v215,v31\n"
"v_mfma_f32_16x16x32_bf16 a[120:123],a[184:187],v[140:143],a[120:123]\n"
"v_exp_f32 v208,v32\n"
"v_mfma_f32_16x16x32_bf16 a[100:103],a[188:191],v[128:131],a[100:103]\n"
"ds_read_b128 a[196:199],v225 offset:41472\n"
"v_cvt_pk_bf16_f32 v119,v214,v215\n"
"v_mfma_f32_16x16x32_bf16 a[108:111],a[188:191],v[132:135],a[108:111]\n"
"v_exp_f32 v209,v33\n"
"v_mfma_f32_16x16x32_bf16 a[116:119],a[188:191],v[136:139],a[116:119]\n"
"s_mov_b32 s63,491520\n"
"ds_read_b128 a[192:195],v225 offset:41728\n"
"v_cvt_pk_bf16_f32 v120,v208,v209\n"
"v_mfma_f32_16x16x32_bf16 a[124:127],a[188:191],v[140:143],a[124:127]\n"
"buffer_load_dwordx4 v[218:221],v9,s[88:91],s63 offen sc1\n"
"v_mfma_f32_16x16x32_bf16 v[48:51],a[152:155],v[144:147],v[80:83]\n"
"s_waitcnt lgkmcnt(7)\n"
"v_exp_f32 v210,v34\n"
"v_mfma_f32_16x16x32_bf16 v[48:51],a[144:147],v[152:155],v[48:51]\n"
"s_mov_b32 s74,24576\n"
"v_exp_f32 v211,v35\n"
"v_mfma_f32_16x16x32_bf16 v[48:51],a[136:139],v[160:163],v[48:51]\n"
"v_exp_f32 v212,v36\n"
"v_mfma_f32_16x16x32_bf16 v[48:51],a[128:131],v[168:171],v[48:51]\n"
"ds_read_b128 a[220:223],v225 offset:41984\n"
"v_cvt_pk_bf16_f32 v121,v210,v211\n"
"v_mfma_f32_16x16x32_bf16 v[56:59],a[128:131],v[172:175],v[84:87]\n"
"buffer_store_dwordx4 a[224:227],v9,s[80:83],s74 offen sc1\n"
"v_mfma_f32_16x16x32_bf16 v[56:59],a[136:139],v[164:167],v[56:59]\n"
"v_exp_f32 v213,v37\n"
"v_mfma_f32_16x16x32_bf16 v[56:59],a[144:147],v[156:159],v[56:59]\n"
"v_exp_f32 v214,v38\n"
"v_mfma_f32_16x16x32_bf16 v[56:59],a[152:155],v[148:151],v[56:59]\n"
"ds_read_b128 a[216:219],v225 offset:42240\n"
"v_cvt_pk_bf16_f32 v122,v212,v213\n"
"v_mfma_f32_16x16x32_bf16 v[52:55],a[156:159],v[144:147],v[80:83]\n"
"s_waitcnt lgkmcnt(5)\n"
"v_exp_f32 v215,v39\n"
"v_mfma_f32_16x16x32_bf16 v[52:55],a[148:151],v[152:155],v[52:55]\n"
"v_exp_f32 v208,v40\n"
"v_mfma_f32_16x16x32_bf16 v[52:55],a[140:143],v[160:163],v[52:55]\n"
"ds_read_b128 a[212:215],v225 offset:42496\n"
"v_cvt_pk_bf16_f32 v123,v214,v215\n"
"v_mfma_f32_16x16x32_bf16 v[52:55],a[132:135],v[168:171],v[52:55]\n"
"v_exp_f32 v209,v41\n"
"v_mfma_f32_16x16x32_bf16 v[60:63],a[132:135],v[172:175],v[84:87]\n"
"ds_read_b128 a[208:211],v225 offset:42752\n"
"v_cvt_pk_bf16_f32 v124,v208,v209\n"
"v_mfma_f32_16x16x32_bf16 v[60:63],a[140:143],v[164:167],v[60:63]\n"
"v_exp_f32 v210,v42\n"
"v_mfma_f32_16x16x32_bf16 v[60:63],a[148:151],v[156:159],v[60:63]\n"
"ds_read_b64_tr_b16 a[160:161],v226 offset:24576\n"
"v_mov_b32 v97,v101\n"
"v_mfma_f32_16x16x32_bf16 v[60:63],a[156:159],v[148:151],v[60:63]\n"
"v_exp_f32 v211,v43\n"
"v_mfma_f32_16x16x32_bf16 v[64:67],a[204:207],v[144:147],v[80:83]\n"
"s_waitcnt lgkmcnt(4)\n"
"v_exp_f32 v212,v44\n"
"v_mfma_f32_16x16x32_bf16 v[64:67],a[200:203],v[152:155],v[64:67]\n"
"ds_read_b64_tr_b16 a[162:163],v226 offset:25600\n"
"v_cvt_pk_bf16_f32 v125,v210,v211\n"
"v_mfma_f32_16x16x32_bf16 v[64:67],a[196:199],v[160:163],v[64:67]\n"
"v_exp_f32 v213,v45\n"
"v_mfma_f32_16x16x32_bf16 v[64:67],a[192:195],v[168:171],v[64:67]\n"
"ds_read_b64_tr_b16 a[164:165],v226 offset:32768\n"
"v_cvt_pk_bf16_f32 v126,v212,v213\n"
"v_mfma_f32_16x16x32_bf16 v[72:75],a[192:195],v[172:175],v[84:87]\n"
"v_exp_f32 v214,v46\n"
"v_mfma_f32_16x16x32_bf16 v[72:75],a[196:199],v[164:167],v[72:75]\n"
"ds_read_b64_tr_b16 a[166:167],v226 offset:33792\n"
"v_mov_b32 v98,v102\n"
"v_mfma_f32_16x16x32_bf16 v[72:75],a[200:203],v[156:159],v[72:75]\n"
"v_exp_f32 v215,v47\n"
"v_mfma_f32_16x16x32_bf16 v[72:75],a[204:207],v[148:151],v[72:75]\n"
"ds_read_b64_tr_b16 a[168:169],v227 offset:24576\n"
"v_cvt_pk_bf16_f32 v127,v214,v215\n"
"v_mfma_f32_16x16x32_bf16 v[68:71],a[220:223],v[144:147],v[80:83]\n"
"s_waitcnt lgkmcnt(3)\n"
"ds_read_b64_tr_b16 a[170:171],v227 offset:25600\n"
"v_mov_b32 v99,v103\n"
"v_mfma_f32_16x16x32_bf16 v[68:71],a[216:219],v[152:155],v[68:71]\n"
"ds_read_b64_tr_b16 a[172:173],v227 offset:32768\n"
"ds_read_b64_tr_b16 a[174:175],v227 offset:33792\n"
"v_mfma_f32_16x16x32_bf16 v[68:71],a[212:215],v[160:163],v[68:71]\n"
"s_waitcnt vmcnt(14)\n"
"s_barrier\n"
"ds_read_b64_tr_b16 a[176:177],v226 offset:24832\n"
"v_mfma_f32_16x16x32_bf16 v[68:71],a[208:211],v[168:171],v[68:71]\n"
"s_add_u32 m0,s65,81920\n"
"ds_read_b64_tr_b16 a[178:179],v226 offset:25856\n"
"ds_read_b64_tr_b16 a[180:181],v226 offset:33024\n"
"v_smfmac_f32_16x16x64_bf16 v[96:99],a[240:243],v[112:119],v104\n"
"buffer_load_dwordx4 v248,s[56:59],s86 offen offset:0 lds\n"
"v_smfmac_f32_16x16x64_bf16 v[96:99],a[240:243],v[120:127],v104\n"
"ds_read_b64_tr_b16 a[182:183],v226 offset:34048\n"
"ds_read_b64_tr_b16 a[184:185],v227 offset:24832\n"
"v_mfma_f32_16x16x32_bf16 v[76:79],a[208:211],v[172:175],v[84:87]\n"
"ds_read_b64_tr_b16 a[186:187],v227 offset:25856\n"
"ds_read_b64_tr_b16 a[188:189],v227 offset:33024\n"
"v_mfma_f32_16x16x32_bf16 v[76:79],a[212:215],v[164:167],v[76:79]\n"
"ds_read_b64_tr_b16 a[190:191],v227 offset:34048\n"
"ds_read_b64_tr_b16 a[128:129],v226 offset:25088\n"
"v_mfma_f32_16x16x32_bf16 v[76:79],a[216:219],v[156:159],v[76:79]\n"
"ds_read_b64_tr_b16 a[130:131],v226 offset:26112\n"
"ds_read_b64_tr_b16 a[136:137],v226 offset:33280\n"
"v_mfma_f32_16x16x32_bf16 v[76:79],a[220:223],v[148:151],v[76:79]\n"
"buffer_load_dwordx4 v249,s[56:59],s86 offen offset:1024 lds\n"
"s_nop 0\n"
"v_maximum3_f32 v234,v96,v97,v98\n"
"v_maximum3_f32 v234,v234,v99,v99\n"
"v_cmp_nge_f32 vcc,s64,v234\n"
"s_cbranch_vccnz L_stub4_%=\n"
"L_ret4_%=:\n"
"v_mfma_f32_16x16x32_bf16 a[0:3],a[160:163],v[112:115],a[0:3]\n"
"s_waitcnt lgkmcnt(9)\n"
"v_exp_f32 v208,v48\n"
"v_mfma_f32_16x16x32_bf16 a[0:3],a[164:167],v[120:123],a[0:3]\n"
"v_exp_f32 v209,v49\n"
"v_mfma_f32_16x16x32_bf16 a[8:11],a[164:167],v[124:127],a[8:11]\n"
"ds_read_b64_tr_b16 a[138:139],v226 offset:34304\n"
"v_cvt_pk_bf16_f32 v128,v208,v209\n"
"L_pgg128_%=:\n"
"s_cbranch_execz L_pgg192_%=\n"
"v_mfma_f32_16x16x32_bf16 a[8:11],a[160:163],v[116:119],a[8:11]\n"
"v_exp_f32 v210,v50\n"
"v_mfma_f32_16x16x32_bf16 a[4:7],a[168:171],v[112:115],a[4:7]\n"
"ds_read_b64_tr_b16 a[144:145],v227 offset:25088\n"
"v_mov_b32 v100,v96\n"
"v_mfma_f32_16x16x32_bf16 a[4:7],a[172:175],v[120:123],a[4:7]\n"
"v_exp_f32 v211,v51\n"
"v_mfma_f32_16x16x32_bf16 a[12:15],a[172:175],v[124:127],a[12:15]\n"
"v_exp_f32 v212,v52\n"
"v_mfma_f32_16x16x32_bf16 a[12:15],a[168:171],v[116:119],a[12:15]\n"
"ds_read_b64_tr_b16 a[146:147],v227 offset:26112\n"
"v_cvt_pk_bf16_f32 v129,v210,v211\n"
"v_mfma_f32_16x16x32_bf16 a[32:35],a[176:179],v[112:115],a[32:35]\n"
"s_waitcnt lgkmcnt(4)\n"
"v_exp_f32 v213,v53\n"
"v_mfma_f32_16x16x32_bf16 a[32:35],a[180:183],v[120:123],a[32:35]\n"
"s_mov_b32 s74,16384\n"
"ds_read_b64_tr_b16 a[152:153],v227 offset:33280\n"
"v_cvt_pk_bf16_f32 v130,v212,v213\n"
"v_mfma_f32_16x16x32_bf16 a[40:43],a[180:183],v[124:127],a[40:43]\n"
"ds_read_b64_tr_b16 a[154:155],v227 offset:34304\n"
"v_exp_f32 v214,v54\n"
"v_mfma_f32_16x16x32_bf16 a[40:43],a[176:179],v[116:119],a[40:43]\n"
"buffer_store_dwordx4 a[228:231],v9,s[80:83],s74 offen sc1\n"
"v_mfma_f32_16x16x32_bf16 a[36:39],a[184:187],v[112:115],a[36:39]\n"
"ds_read_b64_tr_b16 a[132:133],v226 offset:25344\n"
"v_exp_f32 v215,v55\n"
"v_mfma_f32_16x16x32_bf16 a[36:39],a[188:191],v[120:123],a[36:39]\n"
"ds_read_b64_tr_b16 a[134:135],v226 offset:26368\n"
"v_exp_f32 v208,v56\n"
"v_mfma_f32_16x16x32_bf16 a[44:47],a[188:191],v[124:127],a[44:47]\n"
"ds_read_b64_tr_b16 a[140:141],v226 offset:33536\n"
"v_cvt_pk_bf16_f32 v131,v214,v215\n"
"v_mfma_f32_16x16x32_bf16 a[44:47],a[184:187],v[116:119],a[44:47]\n"
"ds_read_b64_tr_b16 a[142:143],v226 offset:34560\n"
"v_exp_f32 v209,v57\n"
"v_mfma_f32_16x16x32_bf16 a[64:67],a[128:131],v[112:115],a[64:67]\n"
"s_waitcnt lgkmcnt(4)\n"
"ds_read_b64_tr_b16 a[148:149],v227 offset:25344\n"
"v_cvt_pk_bf16_f32 v132,v208,v209\n"
"v_mfma_f32_16x16x32_bf16 a[64:67],a[136:139],v[120:123],a[64:67]\n"
"ds_read_b64_tr_b16 a[150:151],v227 offset:26368\n"
"v_exp_f32 v210,v58\n"
"v_mfma_f32_16x16x32_bf16 a[72:75],a[136:139],v[124:127],a[72:75]\n"
"ds_read_b64_tr_b16 a[156:157],v227 offset:33536\n"
"v_mov_b32 v101,v97\n"
"v_mfma_f32_16x16x32_bf16 a[72:75],a[128:131],v[116:119],a[72:75]\n"
"ds_read_b64_tr_b16 a[158:159],v227 offset:34560\n"
"v_exp_f32 v211,v59\n"
"v_mfma_f32_16x16x32_bf16 a[68:71],a[144:147],v[112:115],a[68:71]\n"
"s_waitcnt vmcnt(4)\n"
"ds_read_b128 a[192:195],v225 offset:49152\n"
"v_exp_f32 v212,v60\n"
"v_mfma_f32_16x16x32_bf16 a[68:71],a[152:155],v[120:123],a[68:71]\n"
"ds_read_b128 a[196:199],v225 offset:49408\n"
"v_cvt_pk_bf16_f32 v133,v210,v211\n"
"v_mfma_f32_16x16x32_bf16 a[76:79],a[152:155],v[124:127],a[76:79]\n"
"ds_read_b128 a[200:203],v225 offset:49664\n"
"v_exp_f32 v213,v61\n"
"v_mfma_f32_16x16x32_bf16 a[76:79],a[144:147],v[116:119],a[76:79]\n"
"s_waitcnt lgkmcnt(5)\n"
"ds_read_b128 a[204:207],v225 offset:49920\n"
"v_cvt_pk_bf16_f32 v134,v212,v213\n"
"v_mfma_f32_16x16x32_bf16 a[96:99],a[132:135],v[112:115],a[96:99]\n"
"v_min3_u32 v216,v218,v219,s38\n"
"v_max3_u32 v217,v218,v219,s38\n"
"v_min3_u32 v216,v220,v221,v216\n"
"v_max3_u32 v217,v220,v221,v217\n"
"v_cmp_ne_u32 vcc,v216,v217\n"
"s_cbranch_vccnz L_cl_%=\n"
"L_co_%=:\n"
"s_movk_i32 s63,0x4000\n"
"v_mfma_f32_16x16x32_bf16 a[96:99],a[140:143],v[120:123],a[96:99]\n"
"s_add_u32 m0,s65,155648\n"
"v_exp_f32 v214,v62\n"
"v_mfma_f32_16x16x32_bf16 a[104:107],a[140:143],v[124:127],a[104:107]\n"
"s_add_u32 s76,s65,7344640\n"
"v_exp_f32 v215,v63\n"
"v_mfma_f32_16x16x32_bf16 a[104:107],a[132:135],v[116:119],a[104:107]\n"
"v_exp_f32 v208,v64\n"
"v_mfma_f32_16x16x32_bf16 a[100:103],a[148:151],v[112:115],a[100:103]\n"
"s_waitcnt lgkmcnt(1)\n"
"ds_read_b128 a[208:211],v225 offset:50176\n"
"v_cvt_pk_bf16_f32 v135,v214,v215\n"
"v_mfma_f32_16x16x32_bf16 a[100:103],a[156:159],v[120:123],a[100:103]\n"
"ds_read_b128 a[212:215],v225 offset:50432\n"
"v_exp_f32 v209,v65\n"
"v_mfma_f32_16x16x32_bf16 a[108:111],a[156:159],v[124:127],a[108:111]\n"
"ds_read_b128 a[216:219],v225 offset:50688\n"
"v_cvt_pk_bf16_f32 v136,v208,v209\n"
"v_mfma_f32_16x16x32_bf16 a[108:111],a[148:151],v[116:119],a[108:111]\n"
"ds_read_b128 a[220:223],v225 offset:50944\n"
"v_exp_f32 v210,v66\n"
"v_mfma_f32_16x16x32_bf16 v[16:19],a[192:195],v[144:147],v[80:83]\n"
"buffer_load_dwordx4 v9,s[88:91],s76 offen offset:0 sc1 lds\n"
"v_mfma_f32_16x16x32_bf16 v[16:19],a[196:199],v[152:155],v[16:19]\n"
"v_exp_f32 v211,v67\n"
"v_mfma_f32_16x16x32_bf16 v[16:19],a[200:203],v[160:163],v[16:19]\n"
"s_waitcnt lgkmcnt(3)\n"
"v_exp_f32 v212,v68\n"
"v_mfma_f32_16x16x32_bf16 v[16:19],a[204:207],v[168:171],v[16:19]\n"
"ds_read_b128 a[164:167],v225 offset:57344\n"
"v_cvt_pk_bf16_f32 v137,v210,v211\n"
"v_mfma_f32_16x16x32_bf16 v[24:27],a[204:207],v[172:175],v[84:87]\n"
"v_exp_f32 v213,v69\n"
"v_mfma_f32_16x16x32_bf16 v[24:27],a[200:203],v[164:167],v[24:27]\n"
"ds_read_b128 a[160:163],v225 offset:57600\n"
"v_cvt_pk_bf16_f32 v138,v212,v213\n"
"v_mfma_f32_16x16x32_bf16 v[24:27],a[196:199],v[156:159],v[24:27]\n"
"ds_read_b128 a[172:175],v225 offset:57856\n"
"v_exp_f32 v214,v70\n"
"v_mfma_f32_16x16x32_bf16 v[24:27],a[192:195],v[148:151],v[24:27]\n"
"ds_read_b128 a[168:171],v225 offset:58112\n"
"v_mov_b32 v102,v98\n"
"v_mfma_f32_16x16x32_bf16 v[20:23],a[208:211],v[144:147],v[80:83]\n"
"s_waitcnt lgkmcnt(4)\n"
"buffer_load_dwordx4 v9,s[88:91],s76 offen offset:1024 sc1 lds\n"
"v_mfma_f32_16x16x32_bf16 v[20:23],a[212:215],v[152:155],v[20:23]\n"
"s_mov_b32 s74,8192\n"
"v_exp_f32 v215,v71\n"
"v_mfma_f32_16x16x32_bf16 v[20:23],a[216:219],v[160:163],v[20:23]\n"
"v_exp_f32 v208,v72\n"
"v_mfma_f32_16x16x32_bf16 v[20:23],a[220:223],v[168:171],v[20:23]\n"
"v_exp_f32 v209,v73\n"
"v_mfma_f32_16x16x32_bf16 v[28:31],a[220:223],v[172:175],v[84:87]\n"
"ds_read_b128 a[180:183],v225 offset:58368\n"
"v_cvt_pk_bf16_f32 v139,v214,v215\n"
"v_mfma_f32_16x16x32_bf16 v[28:31],a[216:219],v[164:167],v[28:31]\n"
"ds_read_b128 a[176:179],v225 offset:58624\n"
"v_exp_f32 v210,v74\n"
"v_mfma_f32_16x16x32_bf16 v[28:31],a[212:215],v[156:159],v[28:31]\n"
"ds_read_b128 a[188:191],v225 offset:58880\n"
"v_cvt_pk_bf16_f32 v140,v208,v209\n"
"v_mfma_f32_16x16x32_bf16 v[28:31],a[208:211],v[148:151],v[28:31]\n"
"s_waitcnt lgkmcnt(3)\n"
"ds_read_b128 a[184:187],v225 offset:59136\n"
"v_exp_f32 v211,v75\n"
"v_mfma_f32_16x16x32_bf16 v[32:35],a[164:167],v[144:147],v[80:83]\n"
"buffer_store_dwordx4 a[232:235],v9,s[80:83],s74 offen sc1\n"
"v_mfma_f32_16x16x32_bf16 v[32:35],a[160:163],v[152:155],v[32:35]\n"
"v_exp_f32 v212,v76\n"
"v_mfma_f32_16x16x32_bf16 v[32:35],a[172:175],v[160:163],v[32:35]\n"
"v_exp_f32 v213,v77\n"
"v_mfma_f32_16x16x32_bf16 v[32:35],a[168:171],v[168:171],v[32:35]\n"
"ds_read_b64_tr_b16 a[136:137],v226 offset:40960\n"
"v_cvt_pk_bf16_f32 v141,v210,v211\n"
"v_mfma_f32_16x16x32_bf16 v[40:43],a[168:171],v[172:175],v[84:87]\n"
"ds_read_b64_tr_b16 a[138:139],v226 offset:41984\n"
"v_exp_f32 v214,v78\n"
"v_mfma_f32_16x16x32_bf16 v[40:43],a[172:175],v[164:167],v[40:43]\n"
"ds_read_b64_tr_b16 a[128:129],v226 offset:49152\n"
"v_cvt_pk_bf16_f32 v142,v212,v213\n"
"v_mfma_f32_16x16x32_bf16 v[40:43],a[160:163],v[156:159],v[40:43]\n"
"ds_read_b64_tr_b16 a[130:131],v226 offset:50176\n"
"v_exp_f32 v215,v79\n"
"v_mfma_f32_16x16x32_bf16 v[40:43],a[164:167],v[148:151],v[40:43]\n"
"s_waitcnt lgkmcnt(4)\n"
"ds_read_b64_tr_b16 a[152:153],v227 offset:40960\n"
"v_cvt_pk_bf16_f32 v143,v214,v215\n"
"v_mfma_f32_16x16x32_bf16 v[36:39],a[180:183],v[144:147],v[80:83]\n"
"ds_read_b64_tr_b16 a[154:155],v227 offset:41984\n"
"v_mov_b32 v103,v99\n"
"v_mfma_f32_16x16x32_bf16 v[36:39],a[176:179],v[152:155],v[36:39]\n"
"ds_read_b64_tr_b16 a[144:145],v227 offset:49152\n"
"ds_read_b64_tr_b16 a[146:147],v227 offset:50176\n"
"v_mfma_f32_16x16x32_bf16 v[36:39],a[188:191],v[160:163],v[36:39]\n"
"s_waitcnt lgkmcnt(6)\n"
"s_barrier\n"
"ds_read_b64_tr_b16 a[140:141],v226 offset:41216\n"
"v_mfma_f32_16x16x32_bf16 v[36:39],a[184:187],v[168:171],v[36:39]\n"
"ds_read_b64_tr_b16 a[142:143],v226 offset:42240\n"
"ds_read_b64_tr_b16 a[132:133],v226 offset:49408\n"
"v_smfmac_f32_16x16x64_bf16 v[100:103],a[240:243],v[128:135],v104\n"
"ds_read_b64_tr_b16 a[134:135],v226 offset:50432\n"
"ds_read_b64_tr_b16 a[156:157],v227 offset:41216\n"
"v_smfmac_f32_16x16x64_bf16 v[100:103],a[240:243],v[136:143],v104\n"
"ds_read_b64_tr_b16 a[158:159],v227 offset:42240\n"
"ds_read_b64_tr_b16 a[148:149],v227 offset:49408\n"
"v_mfma_f32_16x16x32_bf16 v[44:47],a[184:187],v[172:175],v[84:87]\n"
"ds_read_b64_tr_b16 a[150:151],v227 offset:50432\n"
"ds_read_b64_tr_b16 a[204:205],v226 offset:41472\n"
"v_mfma_f32_16x16x32_bf16 v[44:47],a[188:191],v[164:167],v[44:47]\n"
"ds_read_b64_tr_b16 a[206:207],v226 offset:42496\n"
"ds_read_b64_tr_b16 a[200:201],v226 offset:49664\n"
"v_mfma_f32_16x16x32_bf16 v[44:47],a[176:179],v[156:159],v[44:47]\n"
"ds_read_b64_tr_b16 a[202:203],v226 offset:50688\n"
"ds_read_b64_tr_b16 a[196:197],v227 offset:41472\n"
"v_mfma_f32_16x16x32_bf16 v[44:47],a[180:183],v[148:151],v[44:47]\n"
"s_waitcnt lgkmcnt(13)\n"
"ds_read_b64_tr_b16 a[198:199],v227 offset:42496\n"
"ds_read_b64_tr_b16 a[192:193],v227 offset:49664\n"
"s_nop 0\n"
"v_maximum3_f32 v234,v100,v101,v102\n"
"v_maximum3_f32 v234,v234,v103,v103\n"
"v_cmp_nge_f32 vcc,s64,v234\n"
"s_cbranch_vccnz L_stub5_%=\n"
"L_ret5_%=:\n"
"v_mfma_f32_16x16x32_bf16 a[0:3],a[136:139],v[128:131],a[0:3]\n"
"v_exp_f32 v208,v16\n"
"v_mfma_f32_16x16x32_bf16 a[0:3],a[128:131],v[136:139],a[0:3]\n"
"v_exp_f32 v209,v17\n"
"v_mfma_f32_16x16x32_bf16 a[8:11],a[128:131],v[140:143],a[8:11]\n"
"ds_read_b64_tr_b16 a[194:195],v227 offset:50688\n"
"v_cvt_pk_bf16_f32 v112,v208,v209\n"
"v_mfma_f32_16x16x32_bf16 a[8:11],a[136:139],v[132:135],a[8:11]\n"
"v_exp_f32 v210,v18\n"
"v_mfma_f32_16x16x32_bf16 a[4:7],a[152:155],v[128:131],a[4:7]\n"
"ds_read_b64_tr_b16 a[220:221],v226 offset:41728\n"
"v_mov_b32 v96,v100\n"
"v_mfma_f32_16x16x32_bf16 a[4:7],a[144:147],v[136:139],a[4:7]\n"
"v_exp_f32 v211,v19\n"
"v_mfma_f32_16x16x32_bf16 a[12:15],a[144:147],v[140:143],a[12:15]\n"
"v_exp_f32 v212,v20\n"
"v_mfma_f32_16x16x32_bf16 a[12:15],a[152:155],v[132:135],a[12:15]\n"
"s_waitcnt lgkmcnt(9)\n"
"ds_read_b64_tr_b16 a[222:223],v226 offset:42752\n"
"v_cvt_pk_bf16_f32 v113,v210,v211\n"
"v_mfma_f32_16x16x32_bf16 a[32:35],a[140:143],v[128:131],a[32:35]\n"
"v_exp_f32 v213,v21\n"
"v_mfma_f32_16x16x32_bf16 a[32:35],a[132:135],v[136:139],a[32:35]\n"
"ds_read_b64_tr_b16 a[216:217],v226 offset:49920\n"
"v_cvt_pk_bf16_f32 v114,v212,v213\n"
"v_mfma_f32_16x16x32_bf16 a[40:43],a[132:135],v[140:143],a[40:43]\n"
"v_exp_f32 v214,v22\n"
"v_mfma_f32_16x16x32_bf16 a[40:43],a[140:143],v[132:135],a[40:43]\n"
"ds_read_b64_tr_b16 a[218:219],v226 offset:50944\n"
"v_mov_b32 v97,v101\n"
"v_mfma_f32_16x16x32_bf16 a[36:39],a[156:159],v[128:131],a[36:39]\n"
"v_exp_f32 v215,v23\n"
"v_mfma_f32_16x16x32_bf16 a[36:39],a[148:151],v[136:139],a[36:39]\n"
"v_exp_f32 v208,v24\n"
"v_mfma_f32_16x16x32_bf16 a[44:47],a[148:151],v[140:143],a[44:47]\n"
"ds_read_b64_tr_b16 a[212:213],v227 offset:41728\n"
"v_cvt_pk_bf16_f32 v115,v214,v215\n"
"v_mfma_f32_16x16x32_bf16 a[44:47],a[156:159],v[132:135],a[44:47]\n"
"s_waitcnt lgkmcnt(5)\n"
"v_exp_f32 v209,v25\n"
"v_mfma_f32_16x16x32_bf16 a[64:67],a[204:207],v[128:131],a[64:67]\n"
"ds_read_b64_tr_b16 a[214:215],v227 offset:42752\n"
"v_cvt_pk_bf16_f32 v116,v208,v209\n"
"v_mfma_f32_16x16x32_bf16 a[64:67],a[200:203],v[136:139],a[64:67]\n"
"ds_read_b64_tr_b16 a[208:209],v227 offset:49920\n"
"v_exp_f32 v210,v26\n"
"v_mfma_f32_16x16x32_bf16 a[72:75],a[200:203],v[140:143],a[72:75]\n"
"ds_read_b64_tr_b16 a[210:211],v227 offset:50944\n"
"v_mov_b32 v98,v102\n"
"v_mfma_f32_16x16x32_bf16 a[72:75],a[204:207],v[132:135],a[72:75]\n"
"v_exp_f32 v211,v27\n"
"v_mfma_f32_16x16x32_bf16 a[68:71],a[196:199],v[128:131],a[68:71]\n"
"ds_read_b128 a[168:171],v225 offset:16384\n"
"v_exp_f32 v212,v28\n"
"v_mfma_f32_16x16x32_bf16 a[68:71],a[192:195],v[136:139],a[68:71]\n"
"ds_read_b128 a[172:175],v225 offset:16640\n"
"v_cvt_pk_bf16_f32 v117,v210,v211\n"
"v_mfma_f32_16x16x32_bf16 a[76:79],a[192:195],v[140:143],a[76:79]\n"
"ds_read_b128 a[160:163],v225 offset:16896\n"
"v_exp_f32 v213,v29\n"
"v_mfma_f32_16x16x32_bf16 a[76:79],a[196:199],v[132:135],a[76:79]\n"
"s_waitcnt lgkmcnt(5)\n"
"ds_read_b128 a[164:167],v225 offset:17152\n"
"v_cvt_pk_bf16_f32 v118,v212,v213\n"
"v_mfma_f32_16x16x32_bf16 a[96:99],a[220:223],v[128:131],a[96:99]\n"
"v_exp_f32 v214,v30\n"
"v_mfma_f32_16x16x32_bf16 a[96:99],a[216:219],v[136:139],a[96:99]\n"
"ds_read_b128 a[184:187],v225 offset:17408\n"
"v_mov_b32 v99,v103\n"
"v_mfma_f32_16x16x32_bf16 a[104:107],a[216:219],v[140:143],a[104:107]\n"
"v_exp_f32 v215,v31\n"
"v_mfma_f32_16x16x32_bf16 a[104:107],a[220:223],v[132:135],a[104:107]\n"
"v_exp_f32 v208,v32\n"
"v_mfma_f32_16x16x32_bf16 a[100:103],a[212:215],v[128:131],a[100:103]\n"
"s_waitcnt lgkmcnt(2)\n"
"ds_read_b128 a[188:191],v225 offset:17664\n"
"v_cvt_pk_bf16_f32 v119,v214,v215\n"
"v_mfma_f32_16x16x32_bf16 a[100:103],a[208:211],v[136:139],a[100:103]\n"
"v_exp_f32 v209,v33\n"
"v_mfma_f32_16x16x32_bf16 a[108:111],a[208:211],v[140:143],a[108:111]\n"
"ds_read_b128 a[176:179],v225 offset:17920\n"
"v_cvt_pk_bf16_f32 v120,v208,v209\n"
"v_mfma_f32_16x16x32_bf16 a[108:111],a[212:215],v[132:135],a[108:111]\n"
"ds_read_b128 a[180:183],v225 offset:18176\n"
"v_exp_f32 v210,v34\n"
"v_smfmac_f32_16x16x64_bf16 v[96:99],a[240:243],v[112:119],v104\n"
"ds_read_b128 a[128:131],v225 offset:24576\n"
"ds_read_b128 a[136:139],v225 offset:24832\n"
"v_mfma_f32_16x16x32_bf16 v[48:51],a[168:171],v[176:179],v[88:91]\n"
"v_exp_f32 v211,v35\n"
"v_mfma_f32_16x16x32_bf16 v[48:51],a[172:175],v[184:187],v[48:51]\n"
"v_exp_f32 v212,v36\n"
"v_mfma_f32_16x16x32_bf16 v[48:51],a[160:163],v[192:195],v[48:51]\n"
"s_waitcnt lgkmcnt(4)\n"
"ds_read_b128 a[144:147],v225 offset:25088\n"
"v_cvt_pk_bf16_f32 v121,v210,v211\n"
"v_mfma_f32_16x16x32_bf16 v[48:51],a[164:167],v[200:203],v[48:51]\n"
"v_exp_f32 v213,v37\n"
"v_mfma_f32_16x16x32_bf16 v[56:59],a[164:167],v[204:207],v[92:95]\n"
"ds_read_b128 a[152:155],v225 offset:25344\n"
"v_cvt_pk_bf16_f32 v122,v212,v213\n"
"v_mfma_f32_16x16x32_bf16 v[56:59],a[160:163],v[196:199],v[56:59]\n"
"v_exp_f32 v214,v38\n"
"v_mfma_f32_16x16x32_bf16 v[56:59],a[172:175],v[188:191],v[56:59]\n"
"ds_read_b128 a[132:135],v225 offset:25600\n"
"ds_read_b128 a[140:143],v225 offset:25856\n"
"v_mfma_f32_16x16x32_bf16 v[56:59],a[168:171],v[180:183],v[56:59]\n"
"v_exp_f32 v215,v39\n"
"v_mfma_f32_16x16x32_bf16 v[52:55],a[184:187],v[176:179],v[88:91]\n"
"v_exp_f32 v208,v40\n"
"v_mfma_f32_16x16x32_bf16 v[52:55],a[188:191],v[184:187],v[52:55]\n"
"s_waitcnt lgkmcnt(4)\n"
"ds_read_b128 a[148:151],v225 offset:26112\n"
"v_cvt_pk_bf16_f32 v123,v214,v215\n"
"v_mfma_f32_16x16x32_bf16 v[52:55],a[176:179],v[192:195],v[52:55]\n"
"v_exp_f32 v209,v41\n"
"v_mfma_f32_16x16x32_bf16 v[52:55],a[180:183],v[200:203],v[52:55]\n"
"ds_read_b128 a[156:159],v225 offset:26368\n"
"v_cvt_pk_bf16_f32 v124,v208,v209\n"
"v_mfma_f32_16x16x32_bf16 v[60:63],a[180:183],v[204:207],v[92:95]\n"
"v_exp_f32 v210,v42\n"
"v_mfma_f32_16x16x32_bf16 v[60:63],a[176:179],v[196:199],v[60:63]\n"
"ds_read_b64_tr_b16 a[200:201],v226 offset:57344\n"
"ds_read_b64_tr_b16 a[202:203],v226 offset:58368\n"
"v_mfma_f32_16x16x32_bf16 v[60:63],a[188:191],v[188:191],v[60:63]\n"
"v_exp_f32 v211,v43\n"
"v_mfma_f32_16x16x32_bf16 v[60:63],a[184:187],v[180:183],v[60:63]\n"
"v_exp_f32 v212,v44\n"
"v_mfma_f32_16x16x32_bf16 v[64:67],a[128:131],v[176:179],v[88:91]\n"
"ds_read_b64_tr_b16 a[204:205],v230 offset:16384\n"
"v_cvt_pk_bf16_f32 v125,v210,v211\n"
"v_mfma_f32_16x16x32_bf16 v[64:67],a[136:139],v[184:187],v[64:67]\n"
"s_waitcnt lgkmcnt(5)\n"
"s_waitcnt lgkmcnt(3)\n"
"s_barrier\n"
"ds_read_b64_tr_b16 a[206:207],v230 offset:17408\n"
"v_mfma_f32_16x16x32_bf16 v[64:67],a[144:147],v[192:195],v[64:67]\n"
"s_add_u32 m0,s65,16384\n"
"v_exp_f32 v213,v45\n"
"v_mfma_f32_16x16x32_bf16 v[64:67],a[152:155],v[200:203],v[64:67]\n"
"buffer_load_dwordx4 v250,s[56:59],s99 offen offset:0 lds\n"
"v_mfma_f32_16x16x32_bf16 v[72:75],a[152:155],v[204:207],v[92:95]\n"
"v_exp_f32 v214,v46\n"
"v_mfma_f32_16x16x32_bf16 v[72:75],a[144:147],v[196:199],v[72:75]\n"
"v_exp_f32 v215,v47\n"
"v_mfma_f32_16x16x32_bf16 v[72:75],a[136:139],v[188:191],v[72:75]\n"
"ds_read_b64_tr_b16 a[192:193],v227 offset:57344\n"
"v_cvt_pk_bf16_f32 v126,v212,v213\n"
"v_mfma_f32_16x16x32_bf16 v[72:75],a[128:131],v[180:183],v[72:75]\n"
"ds_read_b64_tr_b16 a[194:195],v227 offset:58368\n"
"v_cvt_pk_bf16_f32 v127,v214,v215\n"
"v_mfma_f32_16x16x32_bf16 v[68:71],a[132:135],v[176:179],v[88:91]\n"
"buffer_load_dwordx4 v251,s[56:59],s99 offen offset:1024 lds\n"
"v_mfma_f32_16x16x32_bf16 v[68:71],a[140:143],v[184:187],v[68:71]\n"
"s_waitcnt lgkmcnt(2)\n"
"ds_read_b64_tr_b16 a[196:197],v231 offset:16384\n"
"ds_read_b64_tr_b16 a[198:199],v231 offset:17408\n"
"v_mfma_f32_16x16x32_bf16 v[68:71],a[148:151],v[192:195],v[68:71]\n"
"s_add_u32 m0,s65,24576\n"
"ds_read_b64_tr_b16 a[216:217],v226 offset:57600\n"
"ds_read_b64_tr_b16 a[218:219],v226 offset:58624\n"
"v_mfma_f32_16x16x32_bf16 v[68:71],a[156:159],v[200:203],v[68:71]\n"
"ds_read_b64_tr_b16 a[220:221],v230 offset:16640\n"
"ds_read_b64_tr_b16 a[222:223],v230 offset:17664\n"
"v_smfmac_f32_16x16x64_bf16 v[96:99],a[240:243],v[120:127],v104\n"
"ds_read_b64_tr_b16 a[208:209],v227 offset:57600\n"
"ds_read_b64_tr_b16 a[210:211],v227 offset:58624\n"
"v_mfma_f32_16x16x32_bf16 v[76:79],a[156:159],v[204:207],v[92:95]\n"
"buffer_load_dwordx4 v252,s[56:59],s99 offen offset:0 lds\n"
"v_mfma_f32_16x16x32_bf16 v[76:79],a[148:151],v[196:199],v[76:79]\n"
"ds_read_b64_tr_b16 a[212:213],v231 offset:16640\n"
"ds_read_b64_tr_b16 a[214:215],v231 offset:17664\n"
"v_mfma_f32_16x16x32_bf16 v[76:79],a[140:143],v[188:191],v[76:79]\n"
"ds_read_b64_tr_b16 a[164:165],v226 offset:57856\n"
"ds_read_b64_tr_b16 a[166:167],v226 offset:58880\n"
"v_mfma_f32_16x16x32_bf16 v[76:79],a[132:135],v[180:183],v[76:79]\n"
"ds_read_b64_tr_b16 a[160:161],v230 offset:16896\n"
"ds_read_b64_tr_b16 a[162:163],v230 offset:17920\n"
"s_nop 0\n"
"v_maximum3_f32 v234,v96,v97,v98\n"
"v_maximum3_f32 v234,v234,v99,v99\n"
"v_cmp_nge_f32 vcc,s64,v234\n"
"s_cbranch_vccnz L_stub6_%=\n"
"L_ret6_%=:\n"
"v_mfma_f32_16x16x32_bf16 a[0:3],a[200:203],v[112:115],a[0:3]\n"
"v_exp_f32 v208,v48\n"
"v_mfma_f32_16x16x32_bf16 a[0:3],a[204:207],v[120:123],a[0:3]\n"
"s_waitcnt lgkmcnt(8)\n"
"buffer_load_dwordx4 v253,s[56:59],s99 offen offset:1024 lds\n"
"v_mfma_f32_16x16x32_bf16 a[8:11],a[204:207],v[124:127],a[8:11]\n"
"v_exp_f32 v209,v49\n"
"v_mfma_f32_16x16x32_bf16 a[8:11],a[200:203],v[116:119],a[8:11]\n"
"v_exp_f32 v210,v50\n"
"v_mfma_f32_16x16x32_bf16 a[4:7],a[192:195],v[112:115],a[4:7]\n"
"ds_read_b64_tr_b16 a[172:173],v227 offset:57856\n"
"v_cvt_pk_bf16_f32 v128,v208,v209\n"
"v_mfma_f32_16x16x32_bf16 a[4:7],a[196:199],v[120:123],a[4:7]\n"
"v_exp_f32 v211,v51\n"
"v_mfma_f32_16x16x32_bf16 a[12:15],a[196:199],v[124:127],a[12:15]\n"
"v_exp_f32 v212,v52\n"
"v_mfma_f32_16x16x32_bf16 a[12:15],a[192:195],v[116:119],a[12:15]\n"
"ds_read_b64_tr_b16 a[174:175],v227 offset:58880\n"
"v_cvt_pk_bf16_f32 v129,v210,v211\n"
"v_mfma_f32_16x16x32_bf16 a[32:35],a[216:219],v[112:115],a[32:35]\n"
"v_exp_f32 v213,v53\n"
"v_mfma_f32_16x16x32_bf16 a[32:35],a[220:223],v[120:123],a[32:35]\n"
"s_waitcnt lgkmcnt(2)\n"
"ds_read_b64_tr_b16 a[168:169],v231 offset:16896\n"
"v_cvt_pk_bf16_f32 v130,v212,v213\n"
"v_mfma_f32_16x16x32_bf16 a[40:43],a[220:223],v[124:127],a[40:43]\n"
"ds_read_b64_tr_b16 a[170:171],v231 offset:17920\n"
"v_exp_f32 v214,v54\n"
"v_mfma_f32_16x16x32_bf16 a[40:43],a[216:219],v[116:119],a[40:43]\n"
"ds_read_b64_tr_b16 a[180:181],v226 offset:58112\n"
"v_mov_b32 v100,v96\n"
"v_mfma_f32_16x16x32_bf16 a[36:39],a[208:211],v[112:115],a[36:39]\n"
"ds_read_b64_tr_b16 a[182:183],v226 offset:59136\n"
"v_exp_f32 v215,v55\n"
"v_mfma_f32_16x16x32_bf16 a[36:39],a[212:215],v[120:123],a[36:39]\n"
"ds_read_b64_tr_b16 a[176:177],v230 offset:17152\n"
"v_exp_f32 v208,v56\n"
"v_mfma_f32_16x16x32_bf16 a[44:47],a[212:215],v[124:127],a[44:47]\n"
"ds_read_b64_tr_b16 a[178:179],v230 offset:18176\n"
"v_cvt_pk_bf16_f32 v131,v214,v215\n"
"v_mfma_f32_16x16x32_bf16 a[44:47],a[208:211],v[116:119],a[44:47]\n"
"v_exp_f32 v209,v57\n"
"v_mfma_f32_16x16x32_bf16 a[64:67],a[164:167],v[112:115],a[64:67]\n"
"ds_read_b64_tr_b16 a[188:189],v227 offset:58112\n"
"v_cvt_pk_bf16_f32 v132,v208,v209\n"
"v_mfma_f32_16x16x32_bf16 a[64:67],a[160:163],v[120:123],a[64:67]\n"
"s_waitcnt lgkmcnt(5)\n"
"ds_read_b64_tr_b16 a[190:191],v227 offset:59136\n"
"v_exp_f32 v210,v58\n"
"v_mfma_f32_16x16x32_bf16 a[72:75],a[160:163],v[124:127],a[72:75]\n"
"ds_read_b64_tr_b16 a[184:185],v231 offset:17152\n"
"v_mov_b32 v101,v97\n"
"v_mfma_f32_16x16x32_bf16 a[72:75],a[164:167],v[116:119],a[72:75]\n"
"ds_read_b64_tr_b16 a[186:187],v231 offset:18176\n"
"v_exp_f32 v211,v59\n"
"v_mfma_f32_16x16x32_bf16 a[68:71],a[172:175],v[112:115],a[68:71]\n"
"ds_read_b128 a[152:155],v225 offset:32768\n"
"v_exp_f32 v212,v60\n"
"v_mfma_f32_16x16x32_bf16 a[68:71],a[168:171],v[120:123],a[68:71]\n"
"ds_read_b128 a[144:147],v225 offset:33024\n"
"v_cvt_pk_bf16_f32 v133,v210,v211\n"
"v_mfma_f32_16x16x32_bf16 a[76:79],a[168:171],v[124:127],a[76:79]\n"
"ds_read_b128 a[136:139],v225 offset:33280\n"
"v_exp_f32 v213,v61\n"
"v_mfma_f32_16x16x32_bf16 a[76:79],a[172:175],v[116:119],a[76:79]\n"
"s_waitcnt lgkmcnt(5)\n"
"ds_read_b128 a[128:131],v225 offset:33536\n"
"v_cvt_pk_bf16_f32 v134,v212,v213\n"
"v_mfma_f32_16x16x32_bf16 a[96:99],a[180:183],v[112:115],a[96:99]\n"
"v_exp_f32 v214,v62\n"
"v_mfma_f32_16x16x32_bf16 a[96:99],a[176:179],v[120:123],a[96:99]\n"
"ds_read_b128 a[156:159],v225 offset:33792\n"
"v_mov_b32 v102,v98\n"
"v_mfma_f32_16x16x32_bf16 a[104:107],a[176:179],v[124:127],a[104:107]\n"
"v_exp_f32 v215,v63\n"
"v_mfma_f32_16x16x32_bf16 a[104:107],a[180:183],v[116:119],a[104:107]\n"
"v_exp_f32 v208,v64\n"
"v_mfma_f32_16x16x32_bf16 a[100:103],a[188:191],v[112:115],a[100:103]\n"
"s_waitcnt lgkmcnt(2)\n"
"ds_read_b128 a[148:151],v225 offset:34048\n"
"v_cvt_pk_bf16_f32 v135,v214,v215\n"
"v_mfma_f32_16x16x32_bf16 a[100:103],a[184:187],v[120:123],a[100:103]\n"
"v_exp_f32 v209,v65\n"
"v_mfma_f32_16x16x32_bf16 a[108:111],a[184:187],v[124:127],a[108:111]\n"
"ds_read_b128 a[140:143],v225 offset:34304\n"
"v_cvt_pk_bf16_f32 v136,v208,v209\n"
"v_mfma_f32_16x16x32_bf16 a[108:111],a[188:191],v[116:119],a[108:111]\n"
"ds_read_b128 a[132:135],v225 offset:34560\n"
"v_exp_f32 v210,v66\n"
"s_nop 3\n"
"v_add_f32 v218,v96,v97\n"
"v_add_f32 v220,v96,v97\n"
"v_add_f32 v219,v98,v99\n"
"v_add_f32 v221,v98,v99\n"
"s_nop 1\n"
"v_permlane16_swap_b32 v218,v220\n"
"v_permlane16_swap_b32 v219,v221\n"
"v_rcp_f32 v218,v218\n"
"v_rcp_f32 v219,v219\n"
"v_accvgpr_read_b32 v16,a0\n"
"v_accvgpr_read_b32 v17,a1\n"
"v_accvgpr_read_b32 v18,a2\n"
"v_accvgpr_read_b32 v19,a3\n"
"v_accvgpr_read_b32 v20,a4\n"
"v_accvgpr_read_b32 v21,a5\n"
"v_accvgpr_read_b32 v22,a6\n"
"v_accvgpr_read_b32 v23,a7\n"
"v_accvgpr_read_b32 v24,a8\n"
"v_accvgpr_read_b32 v25,a9\n"
"v_accvgpr_read_b32 v26,a10\n"
"v_accvgpr_read_b32 v27,a11\n"
"v_accvgpr_read_b32 v28,a12\n"
"v_accvgpr_read_b32 v29,a13\n"
"v_accvgpr_read_b32 v30,a14\n"
"v_accvgpr_read_b32 v31,a15\n"
"v_pk_mul_f32 v[16:17],v[16:17],v[218:219] op_sel_hi:[1,0]\n"
"v_pk_mul_f32 v[18:19],v[18:19],v[218:219] op_sel_hi:[1,0]\n"
"v_pk_mul_f32 v[20:21],v[20:21],v[218:219] op_sel_hi:[1,0]\n"
"v_pk_mul_f32 v[22:23],v[22:23],v[218:219] op_sel_hi:[1,0]\n"
"v_pk_mul_f32 v[24:25],v[24:25],v[218:219] op_sel:[0,1]\n"
"v_pk_mul_f32 v[26:27],v[26:27],v[218:219] op_sel:[0,1]\n"
"v_pk_mul_f32 v[28:29],v[28:29],v[218:219] op_sel:[0,1]\n"
"v_pk_mul_f32 v[30:31],v[30:31],v[218:219] op_sel:[0,1]\n"
"v_cvt_pk_bf16_f32 v144,v16,v17\n"
"v_cvt_pk_bf16_f32 v145,v18,v19\n"
"v_cvt_pk_bf16_f32 v146,v20,v21\n"
"v_cvt_pk_bf16_f32 v147,v22,v23\n"
"v_cvt_pk_bf16_f32 v148,v24,v25\n"
"v_cvt_pk_bf16_f32 v149,v26,v27\n"
"v_cvt_pk_bf16_f32 v150,v28,v29\n"
"v_cvt_pk_bf16_f32 v151,v30,v31\n"
"s_movk_i32 s68,0\n"
"buffer_store_dwordx4 v[144:147],v9,s[48:51],s68 offen offset:0 sc1\n"
"buffer_store_dwordx4 v[148:151],v9,s[48:51],s68 offen offset:1024 sc1\n"
"v_accvgpr_read_b32 v32,a32\n"
"v_accvgpr_read_b32 v33,a33\n"
"v_accvgpr_read_b32 v34,a34\n"
"v_accvgpr_read_b32 v35,a35\n"
"v_accvgpr_read_b32 v36,a36\n"
"v_accvgpr_read_b32 v37,a37\n"
"v_accvgpr_read_b32 v38,a38\n"
"v_accvgpr_read_b32 v39,a39\n"
"v_accvgpr_read_b32 v40,a40\n"
"v_accvgpr_read_b32 v41,a41\n"
"v_accvgpr_read_b32 v42,a42\n"
"v_accvgpr_read_b32 v43,a43\n"
"v_accvgpr_read_b32 v44,a44\n"
"v_accvgpr_read_b32 v45,a45\n"
"v_accvgpr_read_b32 v46,a46\n"
"v_accvgpr_read_b32 v47,a47\n"
"v_pk_mul_f32 v[32:33],v[32:33],v[218:219] op_sel_hi:[1,0]\n"
"v_pk_mul_f32 v[34:35],v[34:35],v[218:219] op_sel_hi:[1,0]\n"
"v_pk_mul_f32 v[36:37],v[36:37],v[218:219] op_sel_hi:[1,0]\n"
"v_pk_mul_f32 v[38:39],v[38:39],v[218:219] op_sel_hi:[1,0]\n"
"v_pk_mul_f32 v[40:41],v[40:41],v[218:219] op_sel:[0,1]\n"
"v_pk_mul_f32 v[42:43],v[42:43],v[218:219] op_sel:[0,1]\n"
"v_pk_mul_f32 v[44:45],v[44:45],v[218:219] op_sel:[0,1]\n"
"v_pk_mul_f32 v[46:47],v[46:47],v[218:219] op_sel:[0,1]\n"
"v_cvt_pk_bf16_f32 v152,v32,v33\n"
"v_cvt_pk_bf16_f32 v153,v34,v35\n"
"v_cvt_pk_bf16_f32 v154,v36,v37\n"
"v_cvt_pk_bf16_f32 v155,v38,v39\n"
"v_cvt_pk_bf16_f32 v156,v40,v41\n"
"v_cvt_pk_bf16_f32 v157,v42,v43\n"
"v_cvt_pk_bf16_f32 v158,v44,v45\n"
"v_cvt_pk_bf16_f32 v159,v46,v47\n"
"s_movk_i32 s68,2048\n"
"buffer_store_dwordx4 v[152:155],v9,s[48:51],s68 offen offset:0 sc1\n"
"buffer_store_dwordx4 v[156:159],v9,s[48:51],s68 offen offset:1024 sc1\n"
"v_accvgpr_read_b32 v16,a64\n"
"v_accvgpr_read_b32 v17,a65\n"
"v_accvgpr_read_b32 v18,a66\n"
"v_accvgpr_read_b32 v19,a67\n"
"v_accvgpr_read_b32 v20,a68\n"
"v_accvgpr_read_b32 v21,a69\n"
"v_accvgpr_read_b32 v22,a70\n"
"v_accvgpr_read_b32 v23,a71\n"
"v_accvgpr_read_b32 v24,a72\n"
"v_accvgpr_read_b32 v25,a73\n"
"v_accvgpr_read_b32 v26,a74\n"
"v_accvgpr_read_b32 v27,a75\n"
"v_accvgpr_read_b32 v28,a76\n"
"v_accvgpr_read_b32 v29,a77\n"
"v_accvgpr_read_b32 v30,a78\n"
"v_accvgpr_read_b32 v31,a79\n"
"v_pk_mul_f32 v[16:17],v[16:17],v[218:219] op_sel_hi:[1,0]\n"
"v_pk_mul_f32 v[18:19],v[18:19],v[218:219] op_sel_hi:[1,0]\n"
"v_pk_mul_f32 v[20:21],v[20:21],v[218:219] op_sel_hi:[1,0]\n"
"v_pk_mul_f32 v[22:23],v[22:23],v[218:219] op_sel_hi:[1,0]\n"
"v_pk_mul_f32 v[24:25],v[24:25],v[218:219] op_sel:[0,1]\n"
"v_pk_mul_f32 v[26:27],v[26:27],v[218:219] op_sel:[0,1]\n"
"v_pk_mul_f32 v[28:29],v[28:29],v[218:219] op_sel:[0,1]\n"
"v_pk_mul_f32 v[30:31],v[30:31],v[218:219] op_sel:[0,1]\n"
"v_cvt_pk_bf16_f32 v160,v16,v17\n"
"v_cvt_pk_bf16_f32 v161,v18,v19\n"
"v_cvt_pk_bf16_f32 v162,v20,v21\n"
"v_cvt_pk_bf16_f32 v163,v22,v23\n"
"v_cvt_pk_bf16_f32 v164,v24,v25\n"
"v_cvt_pk_bf16_f32 v165,v26,v27\n"
"v_cvt_pk_bf16_f32 v166,v28,v29\n"
"v_cvt_pk_bf16_f32 v167,v30,v31\n"
"s_movk_i32 s68,4096\n"
"buffer_store_dwordx4 v[160:163],v9,s[48:51],s68 offen offset:0 sc1\n"
"buffer_store_dwordx4 v[164:167],v9,s[48:51],s68 offen offset:1024 sc1\n"
"L_pgg144_%=:\n"
"s_cbranch_execz L_pgg208_%=\n"
"v_accvgpr_read_b32 v32,a96\n"
"v_accvgpr_read_b32 v33,a97\n"
"v_accvgpr_read_b32 v34,a98\n"
"v_accvgpr_read_b32 v35,a99\n"
"v_accvgpr_read_b32 v36,a100\n"
"v_accvgpr_read_b32 v37,a101\n"
"v_accvgpr_read_b32 v38,a102\n"
"v_accvgpr_read_b32 v39,a103\n"
"v_accvgpr_read_b32 v40,a104\n"
"v_accvgpr_read_b32 v41,a105\n"
"v_accvgpr_read_b32 v42,a106\n"
"v_accvgpr_read_b32 v43,a107\n"
"v_accvgpr_read_b32 v44,a108\n"
"v_accvgpr_read_b32 v45,a109\n"
"v_accvgpr_read_b32 v46,a110\n"
"v_accvgpr_read_b32 v47,a111\n"
"v_pk_mul_f32 v[32:33],v[32:33],v[218:219] op_sel_hi:[1,0]\n"
"v_pk_mul_f32 v[34:35],v[34:35],v[218:219] op_sel_hi:[1,0]\n"
"v_pk_mul_f32 v[36:37],v[36:37],v[218:219] op_sel_hi:[1,0]\n"
"v_pk_mul_f32 v[38:39],v[38:39],v[218:219] op_sel_hi:[1,0]\n"
"v_pk_mul_f32 v[40:41],v[40:41],v[218:219] op_sel:[0,1]\n"
"v_pk_mul_f32 v[42:43],v[42:43],v[218:219] op_sel:[0,1]\n"
"v_pk_mul_f32 v[44:45],v[44:45],v[218:219] op_sel:[0,1]\n"
"v_pk_mul_f32 v[46:47],v[46:47],v[218:219] op_sel:[0,1]\n"
"v_cvt_pk_bf16_f32 v168,v32,v33\n"
"v_cvt_pk_bf16_f32 v169,v34,v35\n"
"v_cvt_pk_bf16_f32 v170,v36,v37\n"
"v_cvt_pk_bf16_f32 v171,v38,v39\n"
"v_cvt_pk_bf16_f32 v172,v40,v41\n"
"v_cvt_pk_bf16_f32 v173,v42,v43\n"
"v_cvt_pk_bf16_f32 v174,v44,v45\n"
"v_cvt_pk_bf16_f32 v175,v46,v47\n"
"s_movk_i32 s68,6144\n"
"buffer_store_dwordx4 v[168:171],v9,s[48:51],s68 offen offset:0 sc1\n"
"buffer_store_dwordx4 v[172:175],v9,s[48:51],s68 offen offset:1024 sc1\n"
"v_mfma_f32_16x16x32_bf16 v[16:19],a[152:155],v[176:179],v[88:91]\n"
"ds_read_b128 a[204:207],v225 offset:40960\n"
"v_mov_b32 v103,v99\n"
"v_mfma_f32_16x16x32_bf16 v[16:19],a[144:147],v[184:187],v[16:19]\n"
"v_exp_f32 v211,v67\n"
"v_mfma_f32_16x16x32_bf16 v[16:19],a[136:139],v[192:195],v[16:19]\n"
"s_waitcnt lgkmcnt(2)\n"
"v_exp_f32 v212,v68\n"
"v_mfma_f32_16x16x32_bf16 v[16:19],a[128:131],v[200:203],v[16:19]\n"
"ds_read_b128 a[200:203],v225 offset:41216\n"
"v_cvt_pk_bf16_f32 v137,v210,v211\n"
"v_smfmac_f32_16x16x64_bf16 v[100:103],a[244:247],v[128:135],v104\n"
"v_exp_f32 v213,v69\n"
"v_mfma_f32_16x16x32_bf16 v[24:27],a[128:131],v[204:207],v[92:95]\n"
"ds_read_b128 a[196:199],v225 offset:41472\n"
"v_cvt_pk_bf16_f32 v138,v212,v213\n"
"v_mfma_f32_16x16x32_bf16 v[24:27],a[136:139],v[196:199],v[24:27]\n"
"v_exp_f32 v214,v70\n"
"v_mfma_f32_16x16x32_bf16 v[24:27],a[144:147],v[188:191],v[24:27]\n"
"ds_read_b128 a[192:195],v225 offset:41728\n"
"ds_read_b128 a[220:223],v225 offset:41984\n"
"v_mfma_f32_16x16x32_bf16 v[24:27],a[152:155],v[180:183],v[24:27]\n"
"v_exp_f32 v215,v71\n"
"v_mfma_f32_16x16x32_bf16 v[20:23],a[156:159],v[176:179],v[88:91]\n"
"v_exp_f32 v208,v72\n"
"v_mfma_f32_16x16x32_bf16 v[20:23],a[148:151],v[184:187],v[20:23]\n"
"ds_read_b128 a[216:219],v225 offset:42240\n"
"v_cvt_pk_bf16_f32 v139,v214,v215\n"
"v_mfma_f32_16x16x32_bf16 v[20:23],a[140:143],v[192:195],v[20:23]\n"
"s_waitcnt lgkmcnt(3)\n"
"v_exp_f32 v209,v73\n"
"v_mfma_f32_16x16x32_bf16 v[20:23],a[132:135],v[200:203],v[20:23]\n"
"ds_read_b128 a[212:215],v225 offset:42496\n"
"v_cvt_pk_bf16_f32 v140,v208,v209\n"
"v_mfma_f32_16x16x32_bf16 v[28:31],a[132:135],v[204:207],v[92:95]\n"
"v_exp_f32 v210,v74\n"
"v_mfma_f32_16x16x32_bf16 v[28:31],a[140:143],v[196:199],v[28:31]\n"
"ds_read_b128 a[208:211],v225 offset:42752\n"
"ds_read_b64_tr_b16 a[160:161],v226 offset:24576\n"
"v_mfma_f32_16x16x32_bf16 v[28:31],a[148:151],v[188:191],v[28:31]\n"
"v_exp_f32 v211,v75\n"
"v_mfma_f32_16x16x32_bf16 v[28:31],a[156:159],v[180:183],v[28:31]\n"
"v_exp_f32 v212,v76\n"
"v_mfma_f32_16x16x32_bf16 v[32:35],a[204:207],v[176:179],v[88:91]\n"
"ds_read_b64_tr_b16 a[162:163],v226 offset:25600\n"
"v_cvt_pk_bf16_f32 v141,v210,v211\n"
"v_mfma_f32_16x16x32_bf16 v[32:35],a[200:203],v[184:187],v[32:35]\n"
"v_exp_f32 v213,v77\n"
"v_mfma_f32_16x16x32_bf16 v[32:35],a[196:199],v[192:195],v[32:35]\n"
"s_waitcnt lgkmcnt(3)\n"
"ds_read_b64_tr_b16 a[164:165],v226 offset:32768\n"
"v_cvt_pk_bf16_f32 v142,v212,v213\n"
"v_mfma_f32_16x16x32_bf16 v[32:35],a[192:195],v[200:203],v[32:35]\n"
"s_waitcnt lgkmcnt(3)\n"
"s_barrier\n"
"ds_read_b64_tr_b16 a[166:167],v226 offset:33792\n"
"v_mfma_f32_16x16x32_bf16 v[40:43],a[192:195],v[204:207],v[92:95]\n"
"s_add_u32 m0,s61,32768\n"
"v_exp_f32 v214,v78\n"
"v_mfma_f32_16x16x32_bf16 v[40:43],a[196:199],v[196:199],v[40:43]\n"
"buffer_load_dwordx4 v254,s[56:59],s99 offen offset:0 lds\n"
"v_mfma_f32_16x16x32_bf16 v[40:43],a[200:203],v[188:191],v[40:43]\n"
"s_add_u32 m0,s65,36864\n"
"v_exp_f32 v215,v79\n"
"v_mfma_f32_16x16x32_bf16 v[40:43],a[204:207],v[180:183],v[40:43]\n"
"ds_read_b64_tr_b16 a[168:169],v227 offset:24576\n"
"v_cvt_pk_bf16_f32 v143,v214,v215\n"
"v_mfma_f32_16x16x32_bf16 v[36:39],a[220:223],v[176:179],v[88:91]\n"
"ds_read_b64_tr_b16 a[170:171],v227 offset:25600\n"
"ds_read_b64_tr_b16 a[172:173],v227 offset:32768\n"
"v_mfma_f32_16x16x32_bf16 v[36:39],a[216:219],v[184:187],v[36:39]\n"
"ds_read_b64_tr_b16 a[174:175],v227 offset:33792\n"
"ds_read_b64_tr_b16 a[176:177],v226 offset:24832\n"
"v_mfma_f32_16x16x32_bf16 v[36:39],a[212:215],v[192:195],v[36:39]\n"
"s_waitcnt lgkmcnt(5)\n"
"buffer_load_dwordx4 v246,s[56:59],s85 offen offset:0 lds\n"
"v_mfma_f32_16x16x32_bf16 v[36:39],a[208:211],v[200:203],v[36:39]\n"
"ds_read_b64_tr_b16 a[178:179],v226 offset:25856\n"
"ds_read_b64_tr_b16 a[180:181],v226 offset:33024\n"
"v_smfmac_f32_16x16x64_bf16 v[100:103],a[244:247],v[136:143],v104\n"
"ds_read_b64_tr_b16 a[182:183],v226 offset:34048\n"
"ds_read_b64_tr_b16 a[184:185],v227 offset:24832\n"
"v_mfma_f32_16x16x32_bf16 v[44:47],a[208:211],v[204:207],v[92:95]\n"
"ds_read_b64_tr_b16 a[186:187],v227 offset:25856\n"
"ds_read_b64_tr_b16 a[188:189],v227 offset:33024\n"
"v_mfma_f32_16x16x32_bf16 v[44:47],a[212:215],v[196:199],v[44:47]\n"
"ds_read_b64_tr_b16 a[190:191],v227 offset:34048\n"
"ds_read_b64_tr_b16 a[128:129],v226 offset:25088\n"
"v_mfma_f32_16x16x32_bf16 v[44:47],a[216:219],v[188:191],v[44:47]\n"
"buffer_load_dwordx4 v247,s[56:59],s85 offen offset:1024 lds\n"
"v_mfma_f32_16x16x32_bf16 v[44:47],a[220:223],v[180:183],v[44:47]\n"
"ds_read_b64_tr_b16 a[130:131],v226 offset:26112\n"
"ds_read_b64_tr_b16 a[136:137],v226 offset:33280\n"
"s_nop 0\n"
"v_maximum3_f32 v234,v100,v101,v102\n"
"v_maximum3_f32 v234,v234,v103,v103\n"
"v_cmp_nge_f32 vcc,s64,v234\n"
"s_cbranch_vccnz L_stub7_%=\n"
"L_ret7_%=:\n"
"v_mfma_f32_16x16x32_bf16 a[16:19],a[160:163],v[128:131],a[16:19]\n"
"v_exp_f32 v208,v16\n"
"v_mfma_f32_16x16x32_bf16 a[16:19],a[164:167],v[136:139],a[16:19]\n"
"v_exp_f32 v209,v17\n"
"v_mfma_f32_16x16x32_bf16 a[24:27],a[164:167],v[140:143],a[24:27]\n"
"s_waitcnt lgkmcnt(7)\n"
"ds_read_b64_tr_b16 a[138:139],v226 offset:34304\n"
"v_cvt_pk_bf16_f32 v112,v208,v209\n"
"v_mfma_f32_16x16x32_bf16 a[24:27],a[160:163],v[132:135],a[24:27]\n"
"v_exp_f32 v210,v18\n"
"v_mfma_f32_16x16x32_bf16 a[20:23],a[168:171],v[128:131],a[20:23]\n"
"ds_read_b64_tr_b16 a[144:145],v227 offset:25088\n"
"v_mov_b32 v96,v100\n"
"v_mfma_f32_16x16x32_bf16 a[20:23],a[172:175],v[136:139],a[20:23]\n"
"v_exp_f32 v211,v19\n"
"v_mfma_f32_16x16x32_bf16 a[28:31],a[172:175],v[140:143],a[28:31]\n"
"v_exp_f32 v212,v20\n"
"v_mfma_f32_16x16x32_bf16 a[28:31],a[168:171],v[132:135],a[28:31]\n"
"ds_read_b64_tr_b16 a[146:147],v227 offset:26112\n"
"v_cvt_pk_bf16_f32 v113,v210,v211\n"
"v_mfma_f32_16x16x32_bf16 a[48:51],a[176:179],v[128:131],a[48:51]\n"
"v_exp_f32 v213,v21\n"
"v_mfma_f32_16x16x32_bf16 a[48:51],a[180:183],v[136:139],a[48:51]\n"
"ds_read_b64_tr_b16 a[152:153],v227 offset:33280\n"
"v_cvt_pk_bf16_f32 v114,v212,v213\n"
"v_mfma_f32_16x16x32_bf16 a[56:59],a[180:183],v[140:143],a[56:59]\n"
"s_waitcnt lgkmcnt(3)\n"
"ds_read_b64_tr_b16 a[154:155],v227 offset:34304\n"
"v_exp_f32 v214,v22\n"
"v_mfma_f32_16x16x32_bf16 a[56:59],a[176:179],v[132:135],a[56:59]\n"
"s_waitcnt vmcnt(3)\n"
"ds_read_b64_tr_b16 a[132:133],v226 offset:25344\n"
"v_mov_b32 v97,v101\n"
"v_mfma_f32_16x16x32_bf16 a[52:55],a[184:187],v[128:131],a[52:55]\n"
"ds_read_b64_tr_b16 a[134:135],v226 offset:26368\n"
"v_exp_f32 v215,v23\n"
"v_mfma_f32_16x16x32_bf16 a[52:55],a[188:191],v[136:139],a[52:55]\n"
"ds_read_b64_tr_b16 a[140:141],v226 offset:33536\n"
"v_exp_f32 v208,v24\n"
"v_mfma_f32_16x16x32_bf16 a[60:63],a[188:191],v[140:143],a[60:63]\n"
"ds_read_b64_tr_b16 a[142:143],v226 offset:34560\n"
"v_cvt_pk_bf16_f32 v115,v214,v215\n"
"v_mfma_f32_16x16x32_bf16 a[60:63],a[184:187],v[132:135],a[60:63]\n"
"v_mov_b32 v222,s38\n"
"buffer_store_dword v222,v11,s[88:91],s16 offen sc1\n"
"s_add_u32 s16,s16,16\n"
"v_mfma_f32_16x16x32_bf16 a[80:83],a[128:131],v[128:131],a[80:83]\n"
"ds_read_b64_tr_b16 a[148:149],v227 offset:25344\n"
"v_exp_f32 v209,v25\n"
"v_mfma_f32_16x16x32_bf16 a[80:83],a[136:139],v[136:139],a[80:83]\n"
"ds_read_b64_tr_b16 a[150:151],v227 offset:26368\n"
"v_exp_f32 v210,v26\n"
"v_mfma_f32_16x16x32_bf16 a[88:91],a[136:139],v[140:143],a[88:91]\n"
"s_waitcnt lgkmcnt(4)\n"
"ds_read_b64_tr_b16 a[156:157],v227 offset:33536\n"
"v_cvt_pk_bf16_f32 v116,v208,v209\n"
"v_mfma_f32_16x16x32_bf16 a[88:91],a[128:131],v[132:135],a[88:91]\n"
"ds_read_b64_tr_b16 a[158:159],v227 offset:34560\n"
"v_exp_f32 v211,v27\n"
"v_mfma_f32_16x16x32_bf16 a[84:87],a[144:147],v[128:131],a[84:87]\n"
"ds_read_b128 a[192:195],v225 offset:49152\n"
"v_exp_f32 v212,v28\n"
"v_mfma_f32_16x16x32_bf16 a[84:87],a[152:155],v[136:139],a[84:87]\n"
"ds_read_b128 a[196:199],v225 offset:49408\n"
"v_cvt_pk_bf16_f32 v117,v210,v211\n"
"v_mfma_f32_16x16x32_bf16 a[92:95],a[152:155],v[140:143],a[92:95]\n"
"ds_read_b128 a[200:203],v225 offset:49664\n"
"v_exp_f32 v213,v29\n"
"v_mfma_f32_16x16x32_bf16 a[92:95],a[144:147],v[132:135],a[92:95]\n"
"ds_read_b128 a[204:207],v225 offset:49920\n"
"v_cvt_pk_bf16_f32 v118,v212,v213\n"
"v_mfma_f32_16x16x32_bf16 a[112:115],a[132:135],v[128:131],a[112:115]\n"
"s_waitcnt lgkmcnt(6)\n"
"v_exp_f32 v214,v30\n"
"v_mfma_f32_16x16x32_bf16 a[112:115],a[140:143],v[136:139],a[112:115]\n"
"s_waitcnt lgkmcnt(4)\n"
"s_barrier\n"
"v_mov_b32 v98,v102\n"
"v_mfma_f32_16x16x32_bf16 a[120:123],a[140:143],v[140:143],a[120:123]\n"
"s_add_u32 m0,s65,90112\n"
"v_exp_f32 v215,v31\n"
"v_mfma_f32_16x16x32_bf16 a[120:123],a[132:135],v[132:135],a[120:123]\n"
"buffer_load_dwordx4 v250,s[56:59],s86 offen offset:0 lds\n"
"v_mfma_f32_16x16x32_bf16 a[116:119],a[148:151],v[128:131],a[116:119]\n"
"s_waitcnt lgkmcnt(1)\n"
"ds_read_b128 a[208:211],v225 offset:50176\n"
"v_exp_f32 v208,v32\n"
"v_mfma_f32_16x16x32_bf16 a[116:119],a[156:159],v[136:139],a[116:119]\n"
"ds_read_b128 a[212:215],v225 offset:50432\n"
"v_exp_f32 v209,v33\n"
"v_mfma_f32_16x16x32_bf16 a[124:127],a[156:159],v[140:143],a[124:127]\n"
"ds_read_b128 a[216:219],v225 offset:50688\n"
"v_cvt_pk_bf16_f32 v119,v214,v215\n"
"v_mfma_f32_16x16x32_bf16 a[124:127],a[148:151],v[132:135],a[124:127]\n"
"ds_read_b128 a[220:223],v225 offset:50944\n"
"v_exp_f32 v210,v34\n"
"v_mfma_f32_16x16x32_bf16 v[48:51],a[192:195],v[176:179],v[88:91]\n"
"buffer_load_dwordx4 v251,s[56:59],s86 offen offset:1024 lds\n"
"v_mfma_f32_16x16x32_bf16 v[48:51],a[196:199],v[184:187],v[48:51]\n"
"s_add_u32 m0,s65,98304\n"
"v_exp_f32 v211,v35\n"
"v_mfma_f32_16x16x32_bf16 v[48:51],a[200:203],v[192:195],v[48:51]\n"
"s_waitcnt lgkmcnt(3)\n"
"v_exp_f32 v212,v36\n"
"v_mfma_f32_16x16x32_bf16 v[48:51],a[204:207],v[200:203],v[48:51]\n"
"ds_read_b128 a[164:167],v225 offset:57344\n"
"v_cvt_pk_bf16_f32 v120,v208,v209\n"
"v_mfma_f32_16x16x32_bf16 v[56:59],a[204:207],v[204:207],v[92:95]\n"
"v_exp_f32 v213,v37\n"
"v_mfma_f32_16x16x32_bf16 v[56:59],a[200:203],v[196:199],v[56:59]\n"
"ds_read_b128 a[160:163],v225 offset:57600\n"
"v_cvt_pk_bf16_f32 v121,v210,v211\n"
"v_mfma_f32_16x16x32_bf16 v[56:59],a[196:199],v[188:191],v[56:59]\n"
"ds_read_b128 a[172:175],v225 offset:57856\n"
"v_exp_f32 v214,v38\n"
"v_mfma_f32_16x16x32_bf16 v[56:59],a[192:195],v[180:183],v[56:59]\n"
"ds_read_b128 a[168:171],v225 offset:58112\n"
"v_cvt_pk_bf16_f32 v122,v212,v213\n"
"v_mfma_f32_16x16x32_bf16 v[52:55],a[208:211],v[176:179],v[88:91]\n"
"s_waitcnt lgkmcnt(4)\n"
"buffer_load_dwordx4 v252,s[56:59],s86 offen offset:0 lds\n"
"v_mfma_f32_16x16x32_bf16 v[52:55],a[212:215],v[184:187],v[52:55]\n"
"v_exp_f32 v215,v39\n"
"v_mfma_f32_16x16x32_bf16 v[52:55],a[216:219],v[192:195],v[52:55]\n"
"v_exp_f32 v208,v40\n"
"v_mfma_f32_16x16x32_bf16 v[52:55],a[220:223],v[200:203],v[52:55]\n"
"v_exp_f32 v209,v41\n"
"v_mfma_f32_16x16x32_bf16 v[60:63],a[220:223],v[204:207],v[92:95]\n"
"ds_read_b128 a[180:183],v225 offset:58368\n"
"v_cvt_pk_bf16_f32 v123,v214,v215\n"
"v_mfma_f32_16x16x32_bf16 v[60:63],a[216:219],v[196:199],v[60:63]\n"
"ds_read_b128 a[176:179],v225 offset:58624\n"
"v_exp_f32 v210,v42\n"
"v_mfma_f32_16x16x32_bf16 v[60:63],a[212:215],v[188:191],v[60:63]\n"
"ds_read_b128 a[188:191],v225 offset:58880\n"
"v_cvt_pk_bf16_f32 v124,v208,v209\n"
"v_mfma_f32_16x16x32_bf16 v[60:63],a[208:211],v[180:183],v[60:63]\n"
"s_waitcnt lgkmcnt(3)\n"
"ds_read_b128 a[184:187],v225 offset:59136\n"
"v_exp_f32 v211,v43\n"
"v_mfma_f32_16x16x32_bf16 v[64:67],a[164:167],v[176:179],v[88:91]\n"
"buffer_load_dwordx4 v253,s[56:59],s86 offen offset:1024 lds\n"
"v_mfma_f32_16x16x32_bf16 v[64:67],a[160:163],v[184:187],v[64:67]\n"
"v_exp_f32 v212,v44\n"
"v_mfma_f32_16x16x32_bf16 v[64:67],a[172:175],v[192:195],v[64:67]\n"
"v_exp_f32 v213,v45\n"
"v_mfma_f32_16x16x32_bf16 v[64:67],a[168:171],v[200:203],v[64:67]\n"
"ds_read_b64_tr_b16 a[136:137],v226 offset:40960\n"
"v_cvt_pk_bf16_f32 v125,v210,v211\n"
"v_mfma_f32_16x16x32_bf16 v[72:75],a[168:171],v[204:207],v[92:95]\n"
"ds_read_b64_tr_b16 a[138:139],v226 offset:41984\n"
"v_exp_f32 v214,v46\n"
"v_mfma_f32_16x16x32_bf16 v[72:75],a[172:175],v[196:199],v[72:75]\n"
"s_waitcnt lgkmcnt(2)\n"
"s_barrier\n"
"v_cvt_pk_bf16_f32 v126,v212,v213\n"
"v_mfma_f32_16x16x32_bf16 v[72:75],a[160:163],v[188:191],v[72:75]\n"
"s_add_u32 m0,s65,45056\n"
"ds_read_b64_tr_b16 a[128:129],v226 offset:49152\n"
"v_exp_f32 v215,v47\n"
"v_mfma_f32_16x16x32_bf16 v[72:75],a[164:167],v[180:183],v[72:75]\n"
"ds_read_b64_tr_b16 a[130:131],v226 offset:50176\n"
"v_cvt_pk_bf16_f32 v127,v214,v215\n"
"v_mfma_f32_16x16x32_bf16 v[68:71],a[180:183],v[176:179],v[88:91]\n"
"ds_read_b64_tr_b16 a[152:153],v227 offset:40960\n"
"v_mov_b32 v99,v103\n"
"v_mfma_f32_16x16x32_bf16 v[68:71],a[176:179],v[184:187],v[68:71]\n"
"ds_read_b64_tr_b16 a[154:155],v227 offset:41984\n"
"ds_read_b64_tr_b16 a[144:145],v227 offset:49152\n"
"v_mfma_f32_16x16x32_bf16 v[68:71],a[188:191],v[192:195],v[68:71]\n"
"ds_read_b64_tr_b16 a[146:147],v227 offset:50176\n"
"ds_read_b64_tr_b16 a[140:141],v226 offset:41216\n"
"v_mfma_f32_16x16x32_bf16 v[68:71],a[184:187],v[200:203],v[68:71]\n"
"buffer_load_dwordx4 v248,s[56:59],s85 offen offset:0 lds\n"
"v_smfmac_f32_16x16x64_bf16 v[96:99],a[244:247],v[112:119],v104\n"
"ds_read_b64_tr_b16 a[142:143],v226 offset:42240\n"
"ds_read_b64_tr_b16 a[132:133],v226 offset:49408\n"
"v_smfmac_f32_16x16x64_bf16 v[96:99],a[244:247],v[120:127],v104\n"
"ds_read_b64_tr_b16 a[134:135],v226 offset:50432\n"
"ds_read_b64_tr_b16 a[156:157],v227 offset:41216\n"
"v_mfma_f32_16x16x32_bf16 v[76:79],a[184:187],v[204:207],v[92:95]\n"
"ds_read_b64_tr_b16 a[158:159],v227 offset:42240\n"
"ds_read_b64_tr_b16 a[148:149],v227 offset:49408\n"
"v_mfma_f32_16x16x32_bf16 v[76:79],a[188:191],v[196:199],v[76:79]\n"
"ds_read_b64_tr_b16 a[150:151],v227 offset:50432\n"
"ds_read_b64_tr_b16 a[204:205],v226 offset:41472\n"
"v_mfma_f32_16x16x32_bf16 v[76:79],a[176:179],v[188:191],v[76:79]\n"
"buffer_load_dwordx4 v249,s[56:59],s85 offen offset:1024 lds\n"
"v_mfma_f32_16x16x32_bf16 v[76:79],a[180:183],v[180:183],v[76:79]\n"
"s_waitcnt lgkmcnt(9)\n"
"ds_read_b64_tr_b16 a[206:207],v226 offset:42496\n"
"ds_read_b64_tr_b16 a[200:201],v226 offset:49664\n"
"s_nop 0\n"
"v_maximum3_f32 v234,v96,v97,v98\n"
"v_maximum3_f32 v234,v234,v99,v99\n"
"v_cmp_nge_f32 vcc,s64,v234\n"
"s_cbranch_vccnz L_stub8_%=\n"
"L_ret8_%=:\n"
"v_mfma_f32_16x16x32_bf16 a[16:19],a[136:139],v[112:115],a[16:19]\n"
"s_add_u32 m0,s65,53248\n"
"v_exp_f32 v208,v48\n"
"v_mfma_f32_16x16x32_bf16 a[16:19],a[128:131],v[120:123],a[16:19]\n"
"v_exp_f32 v209,v49\n"
"v_mfma_f32_16x16x32_bf16 a[24:27],a[128:131],v[124:127],a[24:27]\n"
"v_exp_f32 v210,v50\n"
"v_cvt_pk_bf16_f32 v128,v208,v209\n"
"v_mfma_f32_16x16x32_bf16 a[24:27],a[136:139],v[116:119],a[24:27]\n"
"buffer_load_dwordx4 v250,s[56:59],s85 offen offset:0 lds\n"
"v_mfma_f32_16x16x32_bf16 a[20:23],a[152:155],v[112:115],a[20:23]\n"
"v_exp_f32 v211,v51\n"
"v_mfma_f32_16x16x32_bf16 a[20:23],a[144:147],v[120:123],a[20:23]\n"
"ds_read_b64_tr_b16 a[202:203],v226 offset:50688\n"
"v_exp_f32 v212,v52\n"
"v_cvt_pk_bf16_f32 v129,v210,v211\n"
"v_mfma_f32_16x16x32_bf16 a[28:31],a[144:147],v[124:127],a[28:31]\n"
"v_exp_f32 v213,v53\n"
"v_mfma_f32_16x16x32_bf16 a[28:31],a[152:155],v[116:119],a[28:31]\n"
"s_waitcnt lgkmcnt(4)\n"
"v_exp_f32 v214,v54\n"
"v_cvt_pk_bf16_f32 v130,v212,v213\n"
"v_mfma_f32_16x16x32_bf16 a[48:51],a[140:143],v[112:115],a[48:51]\n"
"ds_read_b64_tr_b16 a[196:197],v227 offset:41472\n"
"v_exp_f32 v215,v55\n"
"v_mfma_f32_16x16x32_bf16 a[48:51],a[132:135],v[120:123],a[48:51]\n"
"ds_read_b64_tr_b16 a[198:199],v227 offset:42496\n"
"v_exp_f32 v208,v56\n"
"v_cvt_pk_bf16_f32 v131,v214,v215\n"
"v_mfma_f32_16x16x32_bf16 a[56:59],a[132:135],v[124:127],a[56:59]\n"
"ds_read_b64_tr_b16 a[192:193],v227 offset:49664\n"
"v_exp_f32 v209,v57\n"
"v_mfma_f32_16x16x32_bf16 a[56:59],a[140:143],v[116:119],a[56:59]\n"
"ds_read_b64_tr_b16 a[194:195],v227 offset:50688\n"
"v_exp_f32 v210,v58\n"
"v_cvt_pk_bf16_f32 v132,v208,v209\n"
"v_mfma_f32_16x16x32_bf16 a[52:55],a[156:159],v[112:115],a[52:55]\n"
"ds_read_b64_tr_b16 a[220:221],v226 offset:41728\n"
"v_exp_f32 v211,v59\n"
"v_mfma_f32_16x16x32_bf16 a[52:55],a[148:151],v[120:123],a[52:55]\n"
"ds_read_b64_tr_b16 a[222:223],v226 offset:42752\n"
"v_exp_f32 v212,v60\n"
"v_cvt_pk_bf16_f32 v133,v210,v211\n"
"v_mfma_f32_16x16x32_bf16 a[60:63],a[148:151],v[124:127],a[60:63]\n"
"ds_read_b64_tr_b16 a[216:217],v226 offset:49920\n"
"v_exp_f32 v213,v61\n"
"v_mfma_f32_16x16x32_bf16 a[60:63],a[156:159],v[116:119],a[60:63]\n"
"s_waitcnt lgkmcnt(5)\n"
"ds_read_b64_tr_b16 a[218:219],v226 offset:50944\n"
"v_exp_f32 v214,v62\n"
"v_cvt_pk_bf16_f32 v134,v212,v213\n"
"v_mfma_f32_16x16x32_bf16 a[80:83],a[204:207],v[112:115],a[80:83]\n"
"ds_read_b64_tr_b16 a[212:213],v227 offset:41728\n"
"v_exp_f32 v215,v63\n"
"v_mfma_f32_16x16x32_bf16 a[80:83],a[200:203],v[120:123],a[80:83]\n"
"ds_read_b64_tr_b16 a[214:215],v227 offset:42752\n"
"v_exp_f32 v208,v64\n"
"v_cvt_pk_bf16_f32 v135,v214,v215\n"
"v_mfma_f32_16x16x32_bf16 a[88:91],a[200:203],v[124:127],a[88:91]\n"
"ds_read_b64_tr_b16 a[208:209],v227 offset:49920\n"
"v_exp_f32 v209,v65\n"
"v_mfma_f32_16x16x32_bf16 a[88:91],a[204:207],v[116:119],a[88:91]\n"
"ds_read_b64_tr_b16 a[210:211],v227 offset:50944\n"
"v_exp_f32 v210,v66\n"
"v_cvt_pk_bf16_f32 v136,v208,v209\n"
"v_mfma_f32_16x16x32_bf16 a[84:87],a[196:199],v[112:115],a[84:87]\n"
"s_waitcnt lgkmcnt(6)\n"
"ds_read_b64_tr_b16 a[168:169],v226 offset:57344\n"
"v_exp_f32 v211,v67\n"
"v_mfma_f32_16x16x32_bf16 a[84:87],a[192:195],v[120:123],a[84:87]\n"
"ds_read_b64_tr_b16 a[170:171],v226 offset:58368\n"
"v_exp_f32 v212,v68\n"
"v_cvt_pk_bf16_f32 v137,v210,v211\n"
"v_mfma_f32_16x16x32_bf16 a[92:95],a[192:195],v[124:127],a[92:95]\n"
"ds_read_b64_tr_b16 a[172:173],v230 offset:16384\n"
"v_exp_f32 v213,v69\n"
"v_mfma_f32_16x16x32_bf16 a[92:95],a[196:199],v[116:119],a[92:95]\n"
"ds_read_b64_tr_b16 a[174:175],v230 offset:17408\n"
"v_exp_f32 v214,v70\n"
"v_cvt_pk_bf16_f32 v138,v212,v213\n"
"v_mfma_f32_16x16x32_bf16 a[112:115],a[220:223],v[112:115],a[112:115]\n"
"s_waitcnt lgkmcnt(6)\n"
"ds_read_b64_tr_b16 a[160:161],v227 offset:57344\n"
"v_exp_f32 v215,v71\n"
"v_mfma_f32_16x16x32_bf16 a[112:115],a[216:219],v[120:123],a[112:115]\n"
"s_waitcnt lgkmcnt(5)\n"
"s_barrier\n"
"v_cvt_pk_bf16_f32 v139,v214,v215\n"
"v_mfma_f32_16x16x32_bf16 a[120:123],a[216:219],v[124:127],a[120:123]\n"
"ds_read_b64_tr_b16 a[162:163],v227 offset:58368\n"
"v_exp_f32 v208,v72\n"
"v_mfma_f32_16x16x32_bf16 a[120:123],a[220:223],v[116:119],a[120:123]\n"
"ds_read_b64_tr_b16 a[164:165],v231 offset:16384\n"
"v_exp_f32 v209,v73\n"
"v_mfma_f32_16x16x32_bf16 a[116:119],a[212:215],v[112:115],a[116:119]\n"
"s_waitcnt lgkmcnt(5)\n"
"ds_read_b64_tr_b16 a[166:167],v231 offset:17408\n"
"v_exp_f32 v210,v74\n"
"v_cvt_pk_bf16_f32 v140,v208,v209\n"
"v_mfma_f32_16x16x32_bf16 a[116:119],a[208:211],v[120:123],a[116:119]\n"
"ds_read_b64_tr_b16 a[184:185],v226 offset:57600\n"
"v_exp_f32 v211,v75\n"
"v_mfma_f32_16x16x32_bf16 a[124:127],a[208:211],v[124:127],a[124:127]\n"
"ds_read_b64_tr_b16 a[186:187],v226 offset:58624\n"
"v_exp_f32 v212,v76\n"
"v_cvt_pk_bf16_f32 v141,v210,v211\n"
"v_mfma_f32_16x16x32_bf16 a[124:127],a[212:215],v[116:119],a[124:127]\n"
"ds_read_b64_tr_b16 a[188:189],v230 offset:16640\n"
"v_exp_f32 v213,v77\n"
"s_nop 0\n"
"ds_read_b64_tr_b16 a[190:191],v230 offset:17664\n"
"v_exp_f32 v214,v78\n"
"v_cvt_pk_bf16_f32 v142,v212,v213\n"
"s_nop 0\n"
"buffer_load_dwordx4 v251,s[56:59],s85 offen offset:1024 lds\n"
"s_nop 0\n"
"s_add_u32 m0,s65,61440\n"
"v_exp_f32 v215,v79\n"
"s_nop 0\n"
"ds_read_b64_tr_b16 a[176:177],v227 offset:57600\n"
"v_cvt_pk_bf16_f32 v143,v214,v215\n"
"s_nop 0\n"
"ds_read_b64_tr_b16 a[178:179],v227 offset:58624\n"
"v_mov_b32 v100,v96\n"
"s_nop 0\n"
"ds_read_b64_tr_b16 a[180:181],v231 offset:16640\n"
"v_mov_b32 v101,v97\n"
"s_nop 0\n"
"buffer_load_dwordx4 v252,s[56:59],s85 offen offset:0 lds\n"
"s_nop 0\n"
"ds_read_b64_tr_b16 a[182:183],v231 offset:17664\n"
"v_mov_b32 v102,v98\n"
"s_nop 0\n"
"ds_read_b64_tr_b16 a[128:129],v226 offset:57856\n"
"v_mov_b32 v103,v99\n"
"s_nop 0\n"
"ds_read_b64_tr_b16 a[130:131],v226 offset:58880\n"
"ds_read_b64_tr_b16 a[136:137],v230 offset:16896\n"
"v_smfmac_f32_16x16x64_bf16 v[100:103],a[244:247],v[128:135],v104\n"
"ds_read_b64_tr_b16 a[138:139],v230 offset:17920\n"
"ds_read_b64_tr_b16 a[144:145],v227 offset:57856\n"
"v_smfmac_f32_16x16x64_bf16 v[100:103],a[244:247],v[136:143],v104\n"
"buffer_load_dwordx4 v253,s[56:59],s85 offen offset:1024 lds\n"
"s_nop 7\n"
"v_maximum3_f32 v234,v100,v101,v102\n"
"v_maximum3_f32 v234,v234,v103,v103\n"
"v_cmp_nge_f32 vcc,s64,v234\n"
"s_cbranch_vccnz L_stub2_%=\n"
"L_ret2_%=:\n"
"v_mfma_f32_16x16x32_bf16 a[16:19],a[168:171],v[128:131],a[16:19]\n"
"s_waitcnt lgkmcnt(11)\n"
"ds_read_b64_tr_b16 a[146:147],v227 offset:58880\n"
"ds_read_b64_tr_b16 a[152:153],v231 offset:16896\n"
"v_mfma_f32_16x16x32_bf16 a[16:19],a[172:175],v[136:139],a[16:19]\n"
"s_add_u32 m0,s61,106496\n"
"ds_read_b64_tr_b16 a[154:155],v231 offset:17920\n"
"ds_read_b64_tr_b16 a[132:133],v226 offset:58112\n"
"v_mfma_f32_16x16x32_bf16 a[24:27],a[172:175],v[140:143],a[24:27]\n"
"ds_read_b64_tr_b16 a[134:135],v226 offset:59136\n"
"ds_read_b64_tr_b16 a[140:141],v230 offset:17152\n"
"v_mfma_f32_16x16x32_bf16 a[24:27],a[168:171],v[132:135],a[24:27]\n"
"ds_read_b64_tr_b16 a[142:143],v230 offset:18176\n"
"ds_read_b64_tr_b16 a[148:149],v227 offset:58112\n"
"v_mfma_f32_16x16x32_bf16 a[20:23],a[160:163],v[128:131],a[20:23]\n"
"buffer_load_dwordx4 v254,s[56:59],s86 offen offset:0 lds\n"
"v_mfma_f32_16x16x32_bf16 a[20:23],a[164:167],v[136:139],a[20:23]\n"
"s_add_u32 m0,s65,110592\n"
"ds_read_b64_tr_b16 a[150:151],v227 offset:59136\n"
"ds_read_b64_tr_b16 a[156:157],v231 offset:17152\n"
"v_mfma_f32_16x16x32_bf16 a[28:31],a[164:167],v[140:143],a[28:31]\n"
"ds_read_b64_tr_b16 a[158:159],v231 offset:18176\n"
"v_lshrrev_b32 v241,2,v8\n"
"v_mfma_f32_16x16x32_bf16 a[28:31],a[160:163],v[132:135],a[28:31]\n"
"v_mul_u32_u24 v241,288,v241\n"
"v_and_b32 v242,3,v8\n"
"v_mfma_f32_16x16x32_bf16 a[48:51],a[184:187],v[128:131],a[48:51]\n"
"s_waitcnt lgkmcnt(14)\n"
"v_lshl_add_u32 v240,v242,3,v241\n"
"v_add_u32 v241,55296,v240\n"
"v_mfma_f32_16x16x32_bf16 a[48:51],a[188:191],v[136:139],a[48:51]\n"
"buffer_load_dwordx4 v246,s[56:59],s87 offen offset:0 lds\n"
"v_mfma_f32_16x16x32_bf16 a[56:59],a[188:191],v[140:143],a[56:59]\n"
"v_add_u32 v242,110592,v240\n"
"v_add_f32 v218,v100,v101\n"
"v_mfma_f32_16x16x32_bf16 a[56:59],a[184:187],v[132:135],a[56:59]\n"
"v_add_f32 v220,v100,v101\n"
"v_add_f32 v219,v102,v103\n"
"v_mfma_f32_16x16x32_bf16 a[52:55],a[176:179],v[128:131],a[52:55]\n"
"s_waitcnt lgkmcnt(0)\n"
"s_barrier\n"
"v_add_f32 v221,v102,v103\n"
"v_mfma_f32_16x16x32_bf16 a[52:55],a[180:183],v[136:139],a[52:55]\n"
"s_nop 1\n"
"v_permlane16_swap_b32 v218,v220\n"
"v_mfma_f32_16x16x32_bf16 a[60:63],a[180:183],v[140:143],a[60:63]\n"
"buffer_load_dwordx4 v247,s[56:59],s87 offen offset:1024 lds\n"
"v_mfma_f32_16x16x32_bf16 a[60:63],a[176:179],v[132:135],a[60:63]\n"
"s_add_u32 m0,s65,118784\n"
"v_permlane16_swap_b32 v219,v221\n"
"v_rcp_f32 v220,v220\n"
"v_mfma_f32_16x16x32_bf16 a[80:83],a[128:131],v[128:131],a[80:83]\n"
"v_rcp_f32 v221,v221\n"
"v_accvgpr_read_b32 v16,a16\n"
"v_mfma_f32_16x16x32_bf16 a[80:83],a[136:139],v[136:139],a[80:83]\n"
"v_accvgpr_read_b32 v17,a17\n"
"v_accvgpr_read_b32 v18,a18\n"
"v_mfma_f32_16x16x32_bf16 a[88:91],a[136:139],v[140:143],a[88:91]\n"
"v_accvgpr_read_b32 v19,a19\n"
"v_accvgpr_read_b32 v20,a20\n"
"v_mfma_f32_16x16x32_bf16 a[88:91],a[128:131],v[132:135],a[88:91]\n"
"buffer_load_dwordx4 v248,s[56:59],s87 offen offset:0 lds\n"
"v_mfma_f32_16x16x32_bf16 a[84:87],a[144:147],v[128:131],a[84:87]\n"
"s_waitcnt vmcnt(17)\n"
"s_barrier\n"
"v_accvgpr_read_b32 v21,a21\n"
"v_mfma_f32_16x16x32_bf16 a[84:87],a[152:155],v[136:139],a[84:87]\n"
"v_accvgpr_read_b32 v22,a22\n"
"v_accvgpr_read_b32 v23,a23\n"
"v_mfma_f32_16x16x32_bf16 a[92:95],a[152:155],v[140:143],a[92:95]\n"
"v_accvgpr_read_b32 v24,a24\n"
"v_accvgpr_read_b32 v25,a25\n"
"v_mfma_f32_16x16x32_bf16 a[92:95],a[144:147],v[132:135],a[92:95]\n"
"v_accvgpr_read_b32 v26,a26\n"
"v_accvgpr_read_b32 v27,a27\n"
"v_mfma_f32_16x16x32_bf16 a[112:115],a[132:135],v[128:131],a[112:115]\n"
"buffer_load_dwordx4 v249,s[56:59],s87 offen offset:1024 lds\n"
"v_mfma_f32_16x16x32_bf16 a[112:115],a[140:143],v[136:139],a[112:115]\n"
"v_accvgpr_read_b32 v28,a28\n"
"v_accvgpr_read_b32 v29,a29\n"
"v_mfma_f32_16x16x32_bf16 a[120:123],a[140:143],v[140:143],a[120:123]\n"
"v_accvgpr_read_b32 v30,a30\n"
"v_accvgpr_read_b32 v31,a31\n"
"v_mfma_f32_16x16x32_bf16 a[120:123],a[132:135],v[132:135],a[120:123]\n"
"v_mul_f32 v16,v16,v220\n"
"v_mul_f32 v17,v17,v220\n"
"v_mfma_f32_16x16x32_bf16 a[116:119],a[148:151],v[128:131],a[116:119]\n"
"v_mul_f32 v18,v18,v220\n"
"v_mul_f32 v19,v19,v220\n"
"v_mfma_f32_16x16x32_bf16 a[116:119],a[156:159],v[136:139],a[116:119]\n"
"v_min_u32 v223,12,v10\n"
"buffer_load_dword v222,v223,s[88:91],s15 offen sc1\n"
"v_mfma_f32_16x16x32_bf16 a[124:127],a[156:159],v[140:143],a[124:127]\n"
"s_add_u32 m0,s65,126976\n"
"v_mul_f32 v20,v20,v220\n"
"v_mul_f32 v21,v21,v220\n"
"v_mfma_f32_16x16x32_bf16 a[124:127],a[148:151],v[132:135],a[124:127]\n"
"v_mul_f32 v22,v22,v220\n"
"v_mul_f32 v23,v23,v220\n"
"ds_read_b64_tr_b16 a[192:193],v240 offset:0\n"
"ds_read_b64_tr_b16 a[194:195],v240 offset:4608\n"
"ds_read_b64_tr_b16 a[196:197],v240 offset:32\n"
"ds_read_b64_tr_b16 a[198:199],v240 offset:4640\n"
"ds_read_b64_tr_b16 a[200:201],v240 offset:64\n"
"ds_read_b64_tr_b16 a[202:203],v240 offset:4672\n"
"ds_read_b64_tr_b16 a[204:205],v240 offset:96\n"
"ds_read_b64_tr_b16 a[206:207],v240 offset:4704\n"
"ds_read_b64_tr_b16 a[208:209],v240 offset:128\n"
"ds_read_b64_tr_b16 a[210:211],v240 offset:4736\n"
"ds_read_b64_tr_b16 a[212:213],v240 offset:160\n"
"ds_read_b64_tr_b16 a[214:215],v240 offset:4768\n"
"ds_read_b64_tr_b16 a[216:217],v240 offset:192\n"
"ds_read_b64_tr_b16 a[218:219],v240 offset:4800\n"
"ds_read_b64_tr_b16 a[220:221],v240 offset:224\n"
"L_pgg160_%=:\n"
"s_cbranch_execz L_hrg_%=\n"
"ds_read_b64_tr_b16 a[222:223],v240 offset:4832\n"
"ds_read_b64_tr_b16 a[224:225],v240 offset:256\n"
"ds_read_b64_tr_b16 a[226:227],v240 offset:4864\n"
"v_pk_mul_f32 v[24:25],v[24:25],v[220:221] op_sel:[0,1]\n"
"v_pk_mul_f32 v[26:27],v[26:27],v[220:221] op_sel:[0,1]\n"
"v_pk_mul_f32 v[28:29],v[28:29],v[220:221] op_sel:[0,1]\n"
"v_pk_mul_f32 v[30:31],v[30:31],v[220:221] op_sel:[0,1]\n"
"v_cvt_pk_bf16_f32 v176,v16,v17\n"
"v_cvt_pk_bf16_f32 v177,v18,v19\n"
"v_cvt_pk_bf16_f32 v178,v20,v21\n"
"v_cvt_pk_bf16_f32 v179,v22,v23\n"
"v_cvt_pk_bf16_f32 v180,v24,v25\n"
"v_cvt_pk_bf16_f32 v181,v26,v27\n"
"v_cvt_pk_bf16_f32 v182,v28,v29\n"
"v_cvt_pk_bf16_f32 v183,v30,v31\n"
"s_movk_i32 s68,8192\n"
"buffer_store_dwordx4 v[176:179],v9,s[48:51],s68 offen offset:0 sc1\n"
"buffer_store_dwordx4 v[180:183],v9,s[48:51],s68 offen offset:1024 sc1\n"
"v_accvgpr_read_b32 v32,a48\n"
"v_accvgpr_read_b32 v33,a49\n"
"v_accvgpr_read_b32 v34,a50\n"
"v_accvgpr_read_b32 v35,a51\n"
"v_accvgpr_read_b32 v36,a52\n"
"v_accvgpr_read_b32 v37,a53\n"
"v_accvgpr_read_b32 v38,a54\n"
"v_accvgpr_read_b32 v39,a55\n"
"v_accvgpr_read_b32 v40,a56\n"
"v_accvgpr_read_b32 v41,a57\n"
"v_accvgpr_read_b32 v42,a58\n"
"v_accvgpr_read_b32 v43,a59\n"
"v_accvgpr_read_b32 v44,a60\n"
"v_accvgpr_read_b32 v45,a61\n"
"v_accvgpr_read_b32 v46,a62\n"
"v_accvgpr_read_b32 v47,a63\n"
"v_pk_mul_f32 v[32:33],v[32:33],v[220:221] op_sel_hi:[1,0]\n"
"v_pk_mul_f32 v[34:35],v[34:35],v[220:221] op_sel_hi:[1,0]\n"
"v_pk_mul_f32 v[36:37],v[36:37],v[220:221] op_sel_hi:[1,0]\n"
"v_pk_mul_f32 v[38:39],v[38:39],v[220:221] op_sel_hi:[1,0]\n"
"v_pk_mul_f32 v[40:41],v[40:41],v[220:221] op_sel:[0,1]\n"
"v_pk_mul_f32 v[42:43],v[42:43],v[220:221] op_sel:[0,1]\n"
"v_pk_mul_f32 v[44:45],v[44:45],v[220:221] op_sel:[0,1]\n"
"v_pk_mul_f32 v[46:47],v[46:47],v[220:221] op_sel:[0,1]\n"
"v_cvt_pk_bf16_f32 v184,v32,v33\n"
"v_cvt_pk_bf16_f32 v185,v34,v35\n"
"v_cvt_pk_bf16_f32 v186,v36,v37\n"
"v_cvt_pk_bf16_f32 v187,v38,v39\n"
"v_cvt_pk_bf16_f32 v188,v40,v41\n"
"v_cvt_pk_bf16_f32 v189,v42,v43\n"
"v_cvt_pk_bf16_f32 v190,v44,v45\n"
"v_cvt_pk_bf16_f32 v191,v46,v47\n"
"s_movk_i32 s68,10240\n"
"buffer_store_dwordx4 v[184:187],v9,s[48:51],s68 offen offset:0 sc1\n"
"buffer_store_dwordx4 v[188:191],v9,s[48:51],s68 offen offset:1024 sc1\n"
"v_accvgpr_read_b32 v48,a80\n"
"v_accvgpr_read_b32 v49,a81\n"
"v_accvgpr_read_b32 v50,a82\n"
"v_accvgpr_read_b32 v51,a83\n"
"v_accvgpr_read_b32 v52,a84\n"
"v_accvgpr_read_b32 v53,a85\n"
"v_accvgpr_read_b32 v54,a86\n"
"v_accvgpr_read_b32 v55,a87\n"
"v_accvgpr_read_b32 v56,a88\n"
"v_accvgpr_read_b32 v57,a89\n"
"v_accvgpr_read_b32 v58,a90\n"
"v_accvgpr_read_b32 v59,a91\n"
"v_accvgpr_read_b32 v60,a92\n"
"v_accvgpr_read_b32 v61,a93\n"
"v_accvgpr_read_b32 v62,a94\n"
"v_accvgpr_read_b32 v63,a95\n"
"v_pk_mul_f32 v[48:49],v[48:49],v[220:221] op_sel_hi:[1,0]\n"
"v_pk_mul_f32 v[50:51],v[50:51],v[220:221] op_sel_hi:[1,0]\n"
"v_pk_mul_f32 v[52:53],v[52:53],v[220:221] op_sel_hi:[1,0]\n"
"v_pk_mul_f32 v[54:55],v[54:55],v[220:221] op_sel_hi:[1,0]\n"
"v_pk_mul_f32 v[56:57],v[56:57],v[220:221] op_sel:[0,1]\n"
"v_pk_mul_f32 v[58:59],v[58:59],v[220:221] op_sel:[0,1]\n"
"v_pk_mul_f32 v[60:61],v[60:61],v[220:221] op_sel:[0,1]\n"
"v_pk_mul_f32 v[62:63],v[62:63],v[220:221] op_sel:[0,1]\n"
"v_cvt_pk_bf16_f32 v192,v48,v49\n"
"v_cvt_pk_bf16_f32 v193,v50,v51\n"
"v_cvt_pk_bf16_f32 v194,v52,v53\n"
"v_cvt_pk_bf16_f32 v195,v54,v55\n"
"v_cvt_pk_bf16_f32 v196,v56,v57\n"
"v_cvt_pk_bf16_f32 v197,v58,v59\n"
"v_cvt_pk_bf16_f32 v198,v60,v61\n"
"v_cvt_pk_bf16_f32 v199,v62,v63\n"
"s_movk_i32 s68,12288\n"
"buffer_store_dwordx4 v[192:195],v9,s[48:51],s68 offen offset:0 sc1\n"
"buffer_store_dwordx4 v[196:199],v9,s[48:51],s68 offen offset:1024 sc1\n"
"v_accvgpr_read_b32 v16,a112\n"
"v_accvgpr_read_b32 v17,a113\n"
"v_accvgpr_read_b32 v18,a114\n"
"v_accvgpr_read_b32 v19,a115\n"
"v_accvgpr_read_b32 v20,a116\n"
"v_accvgpr_read_b32 v21,a117\n"
"v_accvgpr_read_b32 v22,a118\n"
"v_accvgpr_read_b32 v23,a119\n"
"v_accvgpr_read_b32 v24,a120\n"
"v_accvgpr_read_b32 v25,a121\n"
"v_accvgpr_read_b32 v26,a122\n"
"v_accvgpr_read_b32 v27,a123\n"
"v_accvgpr_read_b32 v28,a124\n"
"v_accvgpr_read_b32 v29,a125\n"
"v_accvgpr_read_b32 v30,a126\n"
"v_accvgpr_read_b32 v31,a127\n"
"v_pk_mul_f32 v[16:17],v[16:17],v[220:221] op_sel_hi:[1,0]\n"
"v_pk_mul_f32 v[18:19],v[18:19],v[220:221] op_sel_hi:[1,0]\n"
"v_pk_mul_f32 v[20:21],v[20:21],v[220:221] op_sel_hi:[1,0]\n"
"v_pk_mul_f32 v[22:23],v[22:23],v[220:221] op_sel_hi:[1,0]\n"
"v_pk_mul_f32 v[24:25],v[24:25],v[220:221] op_sel:[0,1]\n"
"v_pk_mul_f32 v[26:27],v[26:27],v[220:221] op_sel:[0,1]\n"
"v_pk_mul_f32 v[28:29],v[28:29],v[220:221] op_sel:[0,1]\n"
"v_pk_mul_f32 v[30:31],v[30:31],v[220:221] op_sel:[0,1]\n"
"v_cvt_pk_bf16_f32 v200,v16,v17\n"
"v_cvt_pk_bf16_f32 v201,v18,v19\n"
"v_cvt_pk_bf16_f32 v202,v20,v21\n"
"v_cvt_pk_bf16_f32 v203,v22,v23\n"
"v_cvt_pk_bf16_f32 v204,v24,v25\n"
"v_cvt_pk_bf16_f32 v205,v26,v27\n"
"v_cvt_pk_bf16_f32 v206,v28,v29\n"
"v_cvt_pk_bf16_f32 v207,v30,v31\n"
"s_movk_i32 s68,14336\n"
"buffer_store_dwordx4 v[200:203],v9,s[48:51],s68 offen offset:0 sc1\n"
"buffer_store_dwordx4 v[204:207],v9,s[48:51],s68 offen offset:1024 sc1\n"
"s_waitcnt lgkmcnt(12)\n"
"v_mfma_f32_16x16x32_bf16 v[64:67],a[192:195],v[144:147],0\n"
"ds_read_b64_tr_b16 a[128:129],v240 offset:9216\n"
"ds_read_b64_tr_b16 a[130:131],v240 offset:13824\n"
"v_mfma_f32_16x16x32_bf16 v[68:71],a[192:195],v[148:151],0\n"
"buffer_load_dwordx4 v250,s[56:59],s87 offen offset:0 lds\n"
"v_mfma_f32_16x16x32_bf16 v[72:75],a[196:199],v[144:147],0\n"
"ds_read_b64_tr_b16 a[132:133],v240 offset:9248\n"
"ds_read_b64_tr_b16 a[134:135],v240 offset:13856\n"
"v_mfma_f32_16x16x32_bf16 v[76:79],a[196:199],v[148:151],0\n"
"v_add_u32 v236,0,v8\n"
"v_mul_u32_u24 v237,3641,v236\n"
"v_mfma_f32_16x16x32_bf16 v[80:83],a[200:203],v[144:147],0\n"
"ds_read_b64_tr_b16 a[136:137],v240 offset:9280\n"
"ds_read_b64_tr_b16 a[138:139],v240 offset:13888\n"
"v_mfma_f32_16x16x32_bf16 v[84:87],a[200:203],v[148:151],0\n"
"v_lshrrev_b32 v237,16,v237\n"
"v_mul_u32_u24 v243,18,v237\n"
"s_waitcnt lgkmcnt(12)\n"
"v_mfma_f32_16x16x32_bf16 v[88:91],a[204:207],v[144:147],0\n"
"ds_read_b64_tr_b16 a[140:141],v240 offset:9312\n"
"ds_read_b64_tr_b16 a[142:143],v240 offset:13920\n"
"v_mfma_f32_16x16x32_bf16 v[92:95],a[204:207],v[148:151],0\n"
"v_sub_u32 v236,v236,v243\n"
"v_add_u32 v236,s98,v236\n"
"v_mfma_f32_16x16x32_bf16 v[96:99],a[208:211],v[144:147],0\n"
"ds_read_b64_tr_b16 a[144:145],v240 offset:9344\n"
"ds_read_b64_tr_b16 a[146:147],v240 offset:13952\n"
"v_mfma_f32_16x16x32_bf16 v[100:103],a[208:211],v[148:151],0\n"
"buffer_load_dwordx4 v251,s[56:59],s87 offen offset:1024 lds\n"
"v_mfma_f32_16x16x32_bf16 v[104:107],a[212:215],v[144:147],0\n"
"ds_read_b64_tr_b16 a[148:149],v240 offset:9376\n"
"ds_read_b64_tr_b16 a[150:151],v240 offset:13984\n"
"v_mfma_f32_16x16x32_bf16 v[108:111],a[212:215],v[148:151],0\n"
"v_subrev_u32 v243,18,v236\n"
"v_min_u32 v236,v236,v243\n"
"s_waitcnt lgkmcnt(12)\n"
"v_mfma_f32_16x16x32_bf16 v[112:115],a[216:219],v[144:147],0\n"
"ds_read_b64_tr_b16 a[152:153],v240 offset:9408\n"
"ds_read_b64_tr_b16 a[154:155],v240 offset:14016\n"
"v_mfma_f32_16x16x32_bf16 v[116:119],a[216:219],v[148:151],0\n"
"v_mul_u32_u24 v237,9216,v237\n"
"v_lshl_add_u32 v208,v236,4,v237\n"
"v_mfma_f32_16x16x32_bf16 v[120:123],a[220:223],v[144:147],0\n"
"ds_read_b64_tr_b16 a[156:157],v240 offset:9440\n"
"ds_read_b64_tr_b16 a[158:159],v240 offset:14048\n"
"v_mfma_f32_16x16x32_bf16 v[124:127],a[220:223],v[148:151],0\n"
"v_add_u32 v236,64,v8\n"
"v_mul_u32_u24 v237,3641,v236\n"
"v_mfma_f32_16x16x32_bf16 v[128:131],a[224:227],v[144:147],0\n"
"ds_read_b64_tr_b16 a[160:161],v240 offset:9472\n"
"ds_read_b64_tr_b16 a[162:163],v240 offset:14080\n"
"v_mfma_f32_16x16x32_bf16 v[132:135],a[224:227],v[148:151],0\n"
"v_lshrrev_b32 v237,16,v237\n"
"v_mul_u32_u24 v243,18,v237\n"
"s_waitcnt lgkmcnt(12)\n"
"v_mfma_f32_16x16x32_bf16 v[64:67],a[128:131],v[152:155],v[64:67]\n"
"ds_read_b64_tr_b16 a[192:193],v240 offset:18432\n"
"ds_read_b64_tr_b16 a[194:195],v240 offset:23040\n"
"v_mfma_f32_16x16x32_bf16 v[68:71],a[128:131],v[156:159],v[68:71]\n"
"s_add_u32 s73,s12,0\n"
"v_sub_u32 v236,v236,v243\n"
"s_waitcnt vmcnt(10)\n"
"v_cmp_ne_u32 vcc,s38,v222\n"
"s_cbranch_vccnz L_ap1_%=\n"
"L_aq1_%=:\n"
"s_add_u32 s15,s15,16\n"
"v_mfma_f32_16x16x32_bf16 v[72:75],a[132:135],v[152:155],v[72:75]\n"
"ds_read_b64_tr_b16 a[196:197],v240 offset:18464\n"
"ds_read_b64_tr_b16 a[198:199],v240 offset:23072\n"
"v_mfma_f32_16x16x32_bf16 v[76:79],a[132:135],v[156:159],v[76:79]\n"
"buffer_load_dwordx4 a[0:3],v9,s[44:47],s73 offen offset:0 sc1\n"
"v_add_u32 v236,s98,v236\n"
"v_mfma_f32_16x16x32_bf16 v[80:83],a[136:139],v[152:155],v[80:83]\n"
"ds_read_b64_tr_b16 a[200:201],v240 offset:18496\n"
"ds_read_b64_tr_b16 a[202:203],v240 offset:23104\n"
"v_mfma_f32_16x16x32_bf16 v[84:87],a[136:139],v[156:159],v[84:87]\n"
"v_subrev_u32 v243,18,v236\n"
"v_min_u32 v236,v236,v243\n"
"s_waitcnt lgkmcnt(12)\n"
"v_mfma_f32_16x16x32_bf16 v[88:91],a[140:143],v[152:155],v[88:91]\n"
"ds_read_b64_tr_b16 a[204:205],v240 offset:18528\n"
"ds_read_b64_tr_b16 a[206:207],v240 offset:23136\n"
"v_mfma_f32_16x16x32_bf16 v[92:95],a[140:143],v[156:159],v[92:95]\n"
"buffer_load_dwordx4 a[4:7],v9,s[44:47],s73 offen offset:1024 sc1\n"
"v_mul_u32_u24 v237,9216,v237\n"
"v_mfma_f32_16x16x32_bf16 v[96:99],a[144:147],v[152:155],v[96:99]\n"
"ds_read_b64_tr_b16 a[208:209],v240 offset:18560\n"
"ds_read_b64_tr_b16 a[210:211],v240 offset:23168\n"
"v_mfma_f32_16x16x32_bf16 v[100:103],a[144:147],v[156:159],v[100:103]\n"
"v_lshl_add_u32 v209,v236,4,v237\n"
"v_subrev_u32 v209,1024,v209\n"
"v_mfma_f32_16x16x32_bf16 v[104:107],a[148:151],v[152:155],v[104:107]\n"
"ds_read_b64_tr_b16 a[212:213],v240 offset:18592\n"
"ds_read_b64_tr_b16 a[214:215],v240 offset:23200\n"
"v_mfma_f32_16x16x32_bf16 v[108:111],a[148:151],v[156:159],v[108:111]\n"
"v_add_u32 v236,128,v8\n"
"v_mul_u32_u24 v237,3641,v236\n"
"s_waitcnt lgkmcnt(12)\n"
"v_mfma_f32_16x16x32_bf16 v[112:115],a[152:155],v[152:155],v[112:115]\n"
"ds_read_b64_tr_b16 a[216:217],v240 offset:18624\n"
"ds_read_b64_tr_b16 a[218:219],v240 offset:23232\n"
"v_mfma_f32_16x16x32_bf16 v[116:119],a[152:155],v[156:159],v[116:119]\n"
"buffer_load_dwordx4 a[8:11],v9,s[44:47],s73 offen offset:2048 sc1\n"
"v_lshrrev_b32 v237,16,v237\n"
"v_mfma_f32_16x16x32_bf16 v[120:123],a[156:159],v[152:155],v[120:123]\n"
"ds_read_b64_tr_b16 a[220:221],v240 offset:18656\n"
"ds_read_b64_tr_b16 a[222:223],v240 offset:23264\n"
"v_mfma_f32_16x16x32_bf16 v[124:127],a[156:159],v[156:159],v[124:127]\n"
"v_mul_u32_u24 v243,18,v237\n"
"v_sub_u32 v236,v236,v243\n"
"v_mfma_f32_16x16x32_bf16 v[128:131],a[160:163],v[152:155],v[128:131]\n"
"ds_read_b64_tr_b16 a[224:225],v240 offset:18688\n"
"ds_read_b64_tr_b16 a[226:227],v240 offset:23296\n"
"v_mfma_f32_16x16x32_bf16 v[132:135],a[160:163],v[156:159],v[132:135]\n"
"buffer_load_dwordx4 a[12:15],v9,s[44:47],s73 offen offset:3072 sc1\n"
"v_add_u32 v236,s98,v236\n"
"s_waitcnt lgkmcnt(12)\n"
"v_mfma_f32_16x16x32_bf16 v[64:67],a[192:195],v[160:163],v[64:67]\n"
"ds_read_b64_tr_b16 a[128:129],v240 offset:27648\n"
"ds_read_b64_tr_b16 a[130:131],v240 offset:32256\n"
"v_mfma_f32_16x16x32_bf16 v[68:71],a[192:195],v[164:167],v[68:71]\n"
"s_add_u32 s73,s12,4096\n"
"v_subrev_u32 v243,18,v236\n"
"v_mfma_f32_16x16x32_bf16 v[72:75],a[196:199],v[160:163],v[72:75]\n"
"ds_read_b64_tr_b16 a[132:133],v240 offset:27680\n"
"ds_read_b64_tr_b16 a[134:135],v240 offset:32288\n"
"v_mfma_f32_16x16x32_bf16 v[76:79],a[196:199],v[164:167],v[76:79]\n"
"buffer_load_dwordx4 a[32:35],v9,s[44:47],s73 offen offset:0 sc1\n"
"v_min_u32 v236,v236,v243\n"
"v_mfma_f32_16x16x32_bf16 v[80:83],a[200:203],v[160:163],v[80:83]\n"
"ds_read_b64_tr_b16 a[136:137],v240 offset:27712\n"
"ds_read_b64_tr_b16 a[138:139],v240 offset:32320\n"
"v_mfma_f32_16x16x32_bf16 v[84:87],a[200:203],v[164:167],v[84:87]\n"
"v_mul_u32_u24 v237,9216,v237\n"
"v_lshl_add_u32 v210,v236,4,v237\n"
"s_waitcnt lgkmcnt(12)\n"
"v_mfma_f32_16x16x32_bf16 v[88:91],a[204:207],v[160:163],v[88:91]\n"
"ds_read_b64_tr_b16 a[140:141],v240 offset:27744\n"
"ds_read_b64_tr_b16 a[142:143],v240 offset:32352\n"
"v_mfma_f32_16x16x32_bf16 v[92:95],a[204:207],v[164:167],v[92:95]\n"
"v_subrev_u32 v210,2048,v210\n"
"v_add_u32 v236,192,v8\n"
"v_mfma_f32_16x16x32_bf16 v[96:99],a[208:211],v[160:163],v[96:99]\n"
"ds_read_b64_tr_b16 a[144:145],v240 offset:27776\n"
"ds_read_b64_tr_b16 a[146:147],v240 offset:32384\n"
"v_mfma_f32_16x16x32_bf16 v[100:103],a[208:211],v[164:167],v[100:103]\n"
"buffer_load_dwordx4 a[36:39],v9,s[44:47],s73 offen offset:1024 sc1\n"
"v_mul_u32_u24 v237,3641,v236\n"
"v_mfma_f32_16x16x32_bf16 v[104:107],a[212:215],v[160:163],v[104:107]\n"
"ds_read_b64_tr_b16 a[148:149],v240 offset:27808\n"
"ds_read_b64_tr_b16 a[150:151],v240 offset:32416\n"
"v_mfma_f32_16x16x32_bf16 v[108:111],a[212:215],v[164:167],v[108:111]\n"
"v_lshrrev_b32 v237,16,v237\n"
"v_mul_u32_u24 v243,18,v237\n"
"s_waitcnt lgkmcnt(12)\n"
"v_mfma_f32_16x16x32_bf16 v[112:115],a[216:219],v[160:163],v[112:115]\n"
"ds_read_b64_tr_b16 a[152:153],v240 offset:27840\n"
"ds_read_b64_tr_b16 a[154:155],v240 offset:32448\n"
"v_mfma_f32_16x16x32_bf16 v[116:119],a[216:219],v[164:167],v[116:119]\n"
"buffer_load_dwordx4 a[40:43],v9,s[44:47],s73 offen offset:2048 sc1\n"
"v_sub_u32 v236,v236,v243\n"
"v_mfma_f32_16x16x32_bf16 v[120:123],a[220:223],v[160:163],v[120:123]\n"
"ds_read_b64_tr_b16 a[156:157],v240 offset:27872\n"
"ds_read_b64_tr_b16 a[158:159],v240 offset:32480\n"
"v_mfma_f32_16x16x32_bf16 v[124:127],a[220:223],v[164:167],v[124:127]\n"
"v_add_u32 v236,s98,v236\n"
"v_subrev_u32 v243,18,v236\n"
"v_mfma_f32_16x16x32_bf16 v[128:131],a[224:227],v[160:163],v[128:131]\n"
"ds_read_b64_tr_b16 a[160:161],v240 offset:27904\n"
"ds_read_b64_tr_b16 a[162:163],v240 offset:32512\n"
"v_mfma_f32_16x16x32_bf16 v[132:135],a[224:227],v[164:167],v[132:135]\n"
"buffer_load_dwordx4 a[44:47],v9,s[44:47],s73 offen offset:3072 sc1\n"
"v_min_u32 v236,v236,v243\n"
"s_waitcnt lgkmcnt(12)\n"
"v_mfma_f32_16x16x32_bf16 v[64:67],a[128:131],v[168:171],v[64:67]\n"
"v_mul_u32_u24 v237,9216,v237\n"
"v_lshl_add_u32 v211,v236,4,v237\n"
"v_mfma_f32_16x16x32_bf16 v[68:71],a[128:131],v[172:175],v[68:71]\n"
"v_subrev_u32 v211,3072,v211\n"
"v_add_u32 v236,256,v8\n"
"v_mfma_f32_16x16x32_bf16 v[72:75],a[132:135],v[168:171],v[72:75]\n"
"v_mul_u32_u24 v237,3641,v236\n"
"v_lshrrev_b32 v237,16,v237\n"
"v_mfma_f32_16x16x32_bf16 v[76:79],a[132:135],v[172:175],v[76:79]\n"
"s_waitcnt lgkmcnt(0)\n"
"s_barrier\n"
"v_mul_u32_u24 v243,18,v237\n"
"v_mfma_f32_16x16x32_bf16 v[80:83],a[136:139],v[168:171],v[80:83]\n"
"ds_read_b64_tr_b16 a[192:193],v240 offset:36864\n"
"ds_read_b64_tr_b16 a[194:195],v240 offset:41472\n"
"v_mfma_f32_16x16x32_bf16 v[84:87],a[136:139],v[172:175],v[84:87]\n"
"ds_read_b64_tr_b16 a[196:197],v240 offset:36896\n"
"ds_read_b64_tr_b16 a[198:199],v240 offset:41504\n"
"s_add_u32 s73,s13,0\n"
"v_mfma_f32_16x16x32_bf16 v[88:91],a[140:143],v[168:171],v[88:91]\n"
"ds_read_b64_tr_b16 a[200:201],v240 offset:36928\n"
"ds_read_b64_tr_b16 a[202:203],v240 offset:41536\n"
"v_mfma_f32_16x16x32_bf16 v[92:95],a[140:143],v[172:175],v[92:95]\n"
"buffer_load_dwordx4 a[64:67],v9,s[44:47],s73 offen offset:0 sc1\n"
"v_sub_u32 v236,v236,v243\n"
"v_mfma_f32_16x16x32_bf16 v[96:99],a[144:147],v[168:171],v[96:99]\n"
"ds_read_b64_tr_b16 a[204:205],v240 offset:36960\n"
"ds_read_b64_tr_b16 a[206:207],v240 offset:41568\n"
"v_mfma_f32_16x16x32_bf16 v[100:103],a[144:147],v[172:175],v[100:103]\n"
"buffer_load_dwordx4 a[68:71],v9,s[44:47],s73 offen offset:1024 sc1\n"
"v_add_u32 v236,s98,v236\n"
"v_mfma_f32_16x16x32_bf16 v[104:107],a[148:151],v[168:171],v[104:107]\n"
"ds_read_b64_tr_b16 a[208:209],v240 offset:36992\n"
"ds_read_b64_tr_b16 a[210:211],v240 offset:41600\n"
"v_mfma_f32_16x16x32_bf16 v[108:111],a[148:151],v[172:175],v[108:111]\n"
"v_subrev_u32 v243,18,v236\n"
"v_min_u32 v236,v236,v243\n"
"v_mfma_f32_16x16x32_bf16 v[112:115],a[152:155],v[168:171],v[112:115]\n"
"ds_read_b64_tr_b16 a[212:213],v240 offset:37024\n"
"ds_read_b64_tr_b16 a[214:215],v240 offset:41632\n"
"v_mfma_f32_16x16x32_bf16 v[116:119],a[152:155],v[172:175],v[116:119]\n"
"ds_read_b64_tr_b16 a[216:217],v240 offset:37056\n"
"ds_read_b64_tr_b16 a[218:219],v240 offset:41664\n"
"v_mfma_f32_16x16x32_bf16 v[120:123],a[156:159],v[168:171],v[120:123]\n"
"ds_read_b64_tr_b16 a[220:221],v240 offset:37088\n"
"ds_read_b64_tr_b16 a[222:223],v240 offset:41696\n"
"v_mfma_f32_16x16x32_bf16 v[124:127],a[156:159],v[172:175],v[124:127]\n"
"buffer_load_dwordx4 a[72:75],v9,s[44:47],s73 offen offset:2048 sc1\n"
"v_mul_u32_u24 v237,9216,v237\n"
"v_mfma_f32_16x16x32_bf16 v[128:131],a[160:163],v[168:171],v[128:131]\n"
"ds_read_b64_tr_b16 a[224:225],v240 offset:37120\n"
"ds_read_b64_tr_b16 a[226:227],v240 offset:41728\n"
"v_mfma_f32_16x16x32_bf16 v[132:135],a[160:163],v[172:175],v[132:135]\n"
"v_lshl_add_u32 v212,v236,4,v237\n"
"v_add_u32 v236,320,v8\n"
"s_waitcnt lgkmcnt(12)\n"
"v_mfma_f32_16x16x32_bf16 v[64:67],a[192:195],v[176:179],v[64:67]\n"
"ds_read_b64_tr_b16 a[128:129],v240 offset:46080\n"
"ds_read_b64_tr_b16 a[130:131],v240 offset:50688\n"
"v_mfma_f32_16x16x32_bf16 v[68:71],a[192:195],v[180:183],v[68:71]\n"
"buffer_load_dwordx4 a[76:79],v9,s[44:47],s73 offen offset:3072 sc1\n"
"v_mul_u32_u24 v237,3641,v236\n"
"v_mfma_f32_16x16x32_bf16 v[72:75],a[196:199],v[176:179],v[72:75]\n"
"ds_read_b64_tr_b16 a[132:133],v240 offset:46112\n"
"ds_read_b64_tr_b16 a[134:135],v240 offset:50720\n"
"v_mfma_f32_16x16x32_bf16 v[76:79],a[196:199],v[180:183],v[76:79]\n"
"s_add_u32 s73,s13,4096\n"
"v_lshrrev_b32 v237,16,v237\n"
"v_mfma_f32_16x16x32_bf16 v[80:83],a[200:203],v[176:179],v[80:83]\n"
"ds_read_b64_tr_b16 a[136:137],v240 offset:46144\n"
"ds_read_b64_tr_b16 a[138:139],v240 offset:50752\n"
"v_mfma_f32_16x16x32_bf16 v[84:87],a[200:203],v[180:183],v[84:87]\n"
"buffer_load_dwordx4 a[96:99],v9,s[44:47],s73 offen offset:0 sc1\n"
"v_mul_u32_u24 v243,18,v237\n"
"s_waitcnt lgkmcnt(12)\n"
"v_mfma_f32_16x16x32_bf16 v[88:91],a[204:207],v[176:179],v[88:91]\n"
"ds_read_b64_tr_b16 a[140:141],v240 offset:46176\n"
"ds_read_b64_tr_b16 a[142:143],v240 offset:50784\n"
"v_mfma_f32_16x16x32_bf16 v[92:95],a[204:207],v[180:183],v[92:95]\n"
"v_sub_u32 v236,v236,v243\n"
"v_add_u32 v236,s98,v236\n"
"v_mfma_f32_16x16x32_bf16 v[96:99],a[208:211],v[176:179],v[96:99]\n"
"ds_read_b64_tr_b16 a[144:145],v240 offset:46208\n"
"ds_read_b64_tr_b16 a[146:147],v240 offset:50816\n"
"v_mfma_f32_16x16x32_bf16 v[100:103],a[208:211],v[180:183],v[100:103]\n"
"v_subrev_u32 v243,18,v236\n"
"v_min_u32 v236,v236,v243\n"
"v_mfma_f32_16x16x32_bf16 v[104:107],a[212:215],v[176:179],v[104:107]\n"
"ds_read_b64_tr_b16 a[148:149],v240 offset:46240\n"
"ds_read_b64_tr_b16 a[150:151],v240 offset:50848\n"
"v_mfma_f32_16x16x32_bf16 v[108:111],a[212:215],v[180:183],v[108:111]\n"
"buffer_load_dwordx4 a[100:103],v9,s[44:47],s73 offen offset:1024 sc1\n"
"v_mul_u32_u24 v237,9216,v237\n"
"s_waitcnt lgkmcnt(12)\n"
"v_mfma_f32_16x16x32_bf16 v[112:115],a[216:219],v[176:179],v[112:115]\n"
"ds_read_b64_tr_b16 a[152:153],v240 offset:46272\n"
"ds_read_b64_tr_b16 a[154:155],v240 offset:50880\n"
"v_mfma_f32_16x16x32_bf16 v[116:119],a[216:219],v[180:183],v[116:119]\n"
"v_lshl_add_u32 v213,v236,4,v237\n"
"v_subrev_u32 v213,1024,v213\n"
"v_mfma_f32_16x16x32_bf16 v[120:123],a[220:223],v[176:179],v[120:123]\n"
"ds_read_b64_tr_b16 a[156:157],v240 offset:46304\n"
"ds_read_b64_tr_b16 a[158:159],v240 offset:50912\n"
"v_mfma_f32_16x16x32_bf16 v[124:127],a[220:223],v[180:183],v[124:127]\n"
"buffer_load_dwordx4 a[104:107],v9,s[44:47],s73 offen offset:2048 sc1\n"
"v_add_u32 v236,384,v8\n"
"v_mfma_f32_16x16x32_bf16 v[128:131],a[224:227],v[176:179],v[128:131]\n"
"ds_read_b64_tr_b16 a[160:161],v240 offset:46336\n"
"ds_read_b64_tr_b16 a[162:163],v240 offset:50944\n"
"v_mfma_f32_16x16x32_bf16 v[132:135],a[224:227],v[180:183],v[132:135]\n"
"v_mul_u32_u24 v237,3641,v236\n"
"v_lshrrev_b32 v237,16,v237\n"
"s_waitcnt lgkmcnt(12)\n"
"v_mfma_f32_16x16x32_bf16 v[64:67],a[128:131],v[184:187],v[64:67]\n"
"ds_read_b64_tr_b16 a[192:193],v241 offset:0\n"
"ds_read_b64_tr_b16 a[194:195],v241 offset:4608\n"
"v_mfma_f32_16x16x32_bf16 v[68:71],a[128:131],v[188:191],v[68:71]\n"
"buffer_load_dwordx4 a[108:111],v9,s[44:47],s73 offen offset:3072 sc1\n"
"v_mul_u32_u24 v243,18,v237\n"
"v_mfma_f32_16x16x32_bf16 v[72:75],a[132:135],v[184:187],v[72:75]\n"
"ds_read_b64_tr_b16 a[196:197],v241 offset:32\n"
"ds_read_b64_tr_b16 a[198:199],v241 offset:4640\n"
"v_mfma_f32_16x16x32_bf16 v[76:79],a[132:135],v[188:191],v[76:79]\n"
"v_sub_u32 v236,v236,v243\n"
"v_add_u32 v236,s98,v236\n"
"v_mfma_f32_16x16x32_bf16 v[80:83],a[136:139],v[184:187],v[80:83]\n"
"ds_read_b64_tr_b16 a[200:201],v241 offset:64\n"
"ds_read_b64_tr_b16 a[202:203],v241 offset:4672\n"
"v_mfma_f32_16x16x32_bf16 v[84:87],a[136:139],v[188:191],v[84:87]\n"
"s_add_u32 m0,s65,0\n"
"s_mul_i32 s62,s14,18\n"
"s_waitcnt lgkmcnt(12)\n"
"v_mfma_f32_16x16x32_bf16 v[88:91],a[140:143],v[184:187],v[88:91]\n"
"ds_read_b64_tr_b16 a[204:205],v241 offset:96\n"
"ds_read_b64_tr_b16 a[206:207],v241 offset:4704\n"
"v_mfma_f32_16x16x32_bf16 v[92:95],a[140:143],v[188:191],v[92:95]\n"
"buffer_load_dwordx4 v246,s[56:59],s62 offen offset:0 lds\n"
"v_mfma_f32_16x16x32_bf16 v[96:99],a[144:147],v[184:187],v[96:99]\n"
"ds_read_b64_tr_b16 a[208:209],v241 offset:128\n"
"ds_read_b64_tr_b16 a[210:211],v241 offset:4736\n"
"v_mfma_f32_16x16x32_bf16 v[100:103],a[144:147],v[188:191],v[100:103]\n"
"v_subrev_u32 v243,18,v236\n"
"v_min_u32 v236,v236,v243\n"
"v_mfma_f32_16x16x32_bf16 v[104:107],a[148:151],v[184:187],v[104:107]\n"
"ds_read_b64_tr_b16 a[212:213],v241 offset:160\n"
"ds_read_b64_tr_b16 a[214:215],v241 offset:4768\n"
"v_mfma_f32_16x16x32_bf16 v[108:111],a[148:151],v[188:191],v[108:111]\n"
"v_mul_u32_u24 v237,9216,v237\n"
"v_lshl_add_u32 v214,v236,4,v237\n"
"s_waitcnt lgkmcnt(12)\n"
"v_mfma_f32_16x16x32_bf16 v[112:115],a[152:155],v[184:187],v[112:115]\n"
"ds_read_b64_tr_b16 a[216:217],v241 offset:192\n"
"ds_read_b64_tr_b16 a[218:219],v241 offset:4800\n"
"v_mfma_f32_16x16x32_bf16 v[116:119],a[152:155],v[188:191],v[116:119]\n"
"buffer_load_dwordx4 v247,s[56:59],s62 offen offset:1024 lds\n"
"v_mfma_f32_16x16x32_bf16 v[120:123],a[156:159],v[184:187],v[120:123]\n"
"ds_read_b64_tr_b16 a[220:221],v241 offset:224\n"
"ds_read_b64_tr_b16 a[222:223],v241 offset:4832\n"
"v_mfma_f32_16x16x32_bf16 v[124:127],a[156:159],v[188:191],v[124:127]\n"
"v_subrev_u32 v214,2048,v214\n"
"v_add_u32 v236,448,v8\n"
"v_mfma_f32_16x16x32_bf16 v[128:131],a[160:163],v[184:187],v[128:131]\n"
"ds_read_b64_tr_b16 a[224:225],v241 offset:256\n"
"ds_read_b64_tr_b16 a[226:227],v241 offset:4864\n"
"v_mfma_f32_16x16x32_bf16 v[132:135],a[160:163],v[188:191],v[132:135]\n"
"v_mul_u32_u24 v237,3641,v236\n"
"v_lshrrev_b32 v237,16,v237\n"
"s_waitcnt lgkmcnt(12)\n"
"v_mfma_f32_16x16x32_bf16 v[64:67],a[192:195],v[192:195],v[64:67]\n"
"ds_read_b64_tr_b16 a[128:129],v241 offset:9216\n"
"ds_read_b64_tr_b16 a[130:131],v241 offset:13824\n"
"v_mfma_f32_16x16x32_bf16 v[68:71],a[192:195],v[196:199],v[68:71]\n"
"s_add_u32 m0,s65,8192\n"
"v_mul_u32_u24 v243,18,v237\n"
"v_mfma_f32_16x16x32_bf16 v[72:75],a[196:199],v[192:195],v[72:75]\n"
"ds_read_b64_tr_b16 a[132:133],v241 offset:9248\n"
"ds_read_b64_tr_b16 a[134:135],v241 offset:13856\n"
"v_mfma_f32_16x16x32_bf16 v[76:79],a[196:199],v[196:199],v[76:79]\n"
"buffer_load_dwordx4 v248,s[56:59],s62 offen offset:0 lds\n"
"v_mfma_f32_16x16x32_bf16 v[80:83],a[200:203],v[192:195],v[80:83]\n"
"ds_read_b64_tr_b16 a[136:137],v241 offset:9280\n"
"ds_read_b64_tr_b16 a[138:139],v241 offset:13888\n"
"v_mfma_f32_16x16x32_bf16 v[84:87],a[200:203],v[196:199],v[84:87]\n"
"v_sub_u32 v236,v236,v243\n"
"v_add_u32 v236,s98,v236\n"
"s_waitcnt lgkmcnt(12)\n"
"v_mfma_f32_16x16x32_bf16 v[88:91],a[204:207],v[192:195],v[88:91]\n"
"ds_read_b64_tr_b16 a[140:141],v241 offset:9312\n"
"ds_read_b64_tr_b16 a[142:143],v241 offset:13920\n"
"v_mfma_f32_16x16x32_bf16 v[92:95],a[204:207],v[196:199],v[92:95]\n"
"v_subrev_u32 v243,18,v236\n"
"v_min_u32 v236,v236,v243\n"
"v_mfma_f32_16x16x32_bf16 v[96:99],a[208:211],v[192:195],v[96:99]\n"
"ds_read_b64_tr_b16 a[144:145],v241 offset:9344\n"
"ds_read_b64_tr_b16 a[146:147],v241 offset:13952\n"
"v_mfma_f32_16x16x32_bf16 v[100:103],a[208:211],v[196:199],v[100:103]\n"
"buffer_load_dwordx4 v249,s[56:59],s62 offen offset:1024 lds\n"
"v_mfma_f32_16x16x32_bf16 v[104:107],a[212:215],v[192:195],v[104:107]\n"
"ds_read_b64_tr_b16 a[148:149],v241 offset:9376\n"
"ds_read_b64_tr_b16 a[150:151],v241 offset:13984\n"
"v_mfma_f32_16x16x32_bf16 v[108:111],a[212:215],v[196:199],v[108:111]\n"
"v_mul_u32_u24 v237,9216,v237\n"
"v_lshl_add_u32 v215,v236,4,v237\n"
"s_waitcnt lgkmcnt(12)\n"
"v_mfma_f32_16x16x32_bf16 v[112:115],a[216:219],v[192:195],v[112:115]\n"
"ds_read_b64_tr_b16 a[152:153],v241 offset:9408\n"
"ds_read_b64_tr_b16 a[154:155],v241 offset:14016\n"
"v_mfma_f32_16x16x32_bf16 v[116:119],a[216:219],v[196:199],v[116:119]\n"
"s_waitcnt vmcnt(22)\n"
"v_mov_b32 v222,s38\n"
"buffer_store_dword v222,v11,s[88:91],s16 offen sc1\n"
"v_mfma_f32_16x16x32_bf16 v[120:123],a[220:223],v[192:195],v[120:123]\n"
"ds_read_b64_tr_b16 a[156:157],v241 offset:9440\n"
"ds_read_b64_tr_b16 a[158:159],v241 offset:14048\n"
"v_mfma_f32_16x16x32_bf16 v[124:127],a[220:223],v[196:199],v[124:127]\n"
"s_add_u32 m0,s65,16384\n"
"v_subrev_u32 v215,3072,v215\n"
"v_mfma_f32_16x16x32_bf16 v[128:131],a[224:227],v[192:195],v[128:131]\n"
"ds_read_b64_tr_b16 a[160:161],v241 offset:9472\n"
"ds_read_b64_tr_b16 a[162:163],v241 offset:14080\n"
"v_mfma_f32_16x16x32_bf16 v[132:135],a[224:227],v[196:199],v[132:135]\n"
"buffer_load_dwordx4 v250,s[56:59],s62 offen offset:0 lds\n"
"s_waitcnt lgkmcnt(12)\n"
"v_mfma_f32_16x16x32_bf16 v[64:67],a[128:131],v[200:203],v[64:67]\n"
"v_add_u32 v236,512,v8\n"
"v_mul_u32_u24 v237,3641,v236\n"
"v_mfma_f32_16x16x32_bf16 v[68:71],a[128:131],v[204:207],v[68:71]\n"
"v_lshrrev_b32 v237,16,v237\n"
"v_mul_u32_u24 v243,18,v237\n"
"v_mfma_f32_16x16x32_bf16 v[72:75],a[132:135],v[200:203],v[72:75]\n"
"v_sub_u32 v236,v236,v243\n"
"v_add_u32 v236,s98,v236\n"
"v_mfma_f32_16x16x32_bf16 v[76:79],a[132:135],v[204:207],v[76:79]\n"
"s_waitcnt lgkmcnt(0)\n"
"s_barrier\n"
"v_subrev_u32 v243,18,v236\n"
"v_mfma_f32_16x16x32_bf16 v[80:83],a[136:139],v[200:203],v[80:83]\n"
"ds_read_b64_tr_b16 a[192:193],v241 offset:18432\n"
"ds_read_b64_tr_b16 a[194:195],v241 offset:23040\n"
"v_mfma_f32_16x16x32_bf16 v[84:87],a[136:139],v[204:207],v[84:87]\n"
"ds_read_b64_tr_b16 a[196:197],v241 offset:18464\n"
"ds_read_b64_tr_b16 a[198:199],v241 offset:23072\n"
"v_mfma_f32_16x16x32_bf16 v[88:91],a[140:143],v[200:203],v[88:91]\n"
"ds_read_b64_tr_b16 a[200:201],v241 offset:18496\n"
"ds_read_b64_tr_b16 a[202:203],v241 offset:23104\n"
"v_mfma_f32_16x16x32_bf16 v[92:95],a[140:143],v[204:207],v[92:95]\n"
"buffer_load_dwordx4 v251,s[56:59],s62 offen offset:1024 lds\n"
"v_mfma_f32_16x16x32_bf16 v[96:99],a[144:147],v[200:203],v[96:99]\n"
"ds_read_b64_tr_b16 a[204:205],v241 offset:18528\n"
"ds_read_b64_tr_b16 a[206:207],v241 offset:23136\n"
"v_mfma_f32_16x16x32_bf16 v[100:103],a[144:147],v[204:207],v[100:103]\n"
"v_min_u32 v236,v236,v243\n"
"v_mul_u32_u24 v237,9216,v237\n"
"v_mfma_f32_16x16x32_bf16 v[104:107],a[148:151],v[200:203],v[104:107]\n"
"ds_read_b64_tr_b16 a[208:209],v241 offset:18560\n"
"ds_read_b64_tr_b16 a[210:211],v241 offset:23168\n"
"v_mfma_f32_16x16x32_bf16 v[108:111],a[148:151],v[204:207],v[108:111]\n"
"v_lshl_add_u32 v216,v236,4,v237\n"
"s_mov_b32 s77,0\n"
"v_mfma_f32_16x16x32_bf16 v[112:115],a[152:155],v[200:203],v[112:115]\n"
"ds_read_b64_tr_b16 a[212:213],v241 offset:18592\n"
"L_pgg176_%=:\n"
"s_cbranch_execz L_hrg_%=\n"
"ds_read_b64_tr_b16 a[214:215],v241 offset:23200\n"
"v_mfma_f32_16x16x32_bf16 v[116:119],a[152:155],v[204:207],v[116:119]\n"
"ds_read_b64_tr_b16 a[216:217],v241 offset:18624\n"
"ds_read_b64_tr_b16 a[218:219],v241 offset:23232\n"
"s_add_u32 m0,s65,24576\n"
"v_mfma_f32_16x16x32_bf16 v[120:123],a[156:159],v[200:203],v[120:123]\n"
"ds_read_b64_tr_b16 a[220:221],v241 offset:18656\n"
"ds_read_b64_tr_b16 a[222:223],v241 offset:23264\n"
"v_mfma_f32_16x16x32_bf16 v[124:127],a[156:159],v[204:207],v[124:127]\n"
"buffer_load_dwordx4 v252,s[56:59],s62 offen offset:0 lds\n"
"v_mfma_f32_16x16x32_bf16 v[128:131],a[160:163],v[200:203],v[128:131]\n"
"ds_read_b64_tr_b16 a[224:225],v241 offset:18688\n"
"ds_read_b64_tr_b16 a[226:227],v241 offset:23296\n"
"v_mfma_f32_16x16x32_bf16 v[132:135],a[160:163],v[204:207],v[132:135]\n"
"s_mul_i32 s78,s36,1152\n"
"s_add_u32 s77,s77,s78\n"
"s_waitcnt lgkmcnt(12) vmcnt(22)\n"
"v_mfma_f32_16x16x32_bf16 v[64:67],a[192:195],a[0:3],v[64:67]\n"
"ds_read_b64_tr_b16 a[128:129],v241 offset:27648\n"
"ds_read_b64_tr_b16 a[130:131],v241 offset:32256\n"
"v_mfma_f32_16x16x32_bf16 v[68:71],a[192:195],a[4:7],v[68:71]\n"
"buffer_load_dwordx4 v253,s[56:59],s62 offen offset:1024 lds\n"
"v_mfma_f32_16x16x32_bf16 v[72:75],a[196:199],a[0:3],v[72:75]\n"
"ds_read_b64_tr_b16 a[132:133],v241 offset:27680\n"
"ds_read_b64_tr_b16 a[134:135],v241 offset:32288\n"
"v_mfma_f32_16x16x32_bf16 v[76:79],a[196:199],a[4:7],v[76:79]\n"
"s_mul_i32 s78,s35,288\n"
"s_add_u32 s77,s77,s78\n"
"v_mfma_f32_16x16x32_bf16 v[80:83],a[200:203],a[0:3],v[80:83]\n"
"ds_read_b64_tr_b16 a[136:137],v241 offset:27712\n"
"ds_read_b64_tr_b16 a[138:139],v241 offset:32320\n"
"v_mfma_f32_16x16x32_bf16 v[84:87],a[200:203],a[4:7],v[84:87]\n"
"s_add_u32 s40,s20,s77\n"
"s_addc_u32 s41,s21,0\n"
"s_waitcnt lgkmcnt(12)\n"
"v_mfma_f32_16x16x32_bf16 v[88:91],a[204:207],a[0:3],v[88:91]\n"
"ds_read_b64_tr_b16 a[140:141],v241 offset:27744\n"
"ds_read_b64_tr_b16 a[142:143],v241 offset:32352\n"
"v_mfma_f32_16x16x32_bf16 v[92:95],a[204:207],a[4:7],v[92:95]\n"
"s_add_u32 m0,s61,32768\n"
"s_and_b32 s41,s41,0xffff\n"
"v_mfma_f32_16x16x32_bf16 v[96:99],a[208:211],a[0:3],v[96:99]\n"
"ds_read_b64_tr_b16 a[144:145],v241 offset:27776\n"
"ds_read_b64_tr_b16 a[146:147],v241 offset:32384\n"
"v_mfma_f32_16x16x32_bf16 v[100:103],a[208:211],a[4:7],v[100:103]\n"
"buffer_load_dwordx4 v254,s[56:59],s62 offen offset:0 lds\n"
"v_mfma_f32_16x16x32_bf16 v[104:107],a[212:215],a[0:3],v[104:107]\n"
"ds_read_b64_tr_b16 a[148:149],v241 offset:27808\n"
"ds_read_b64_tr_b16 a[150:151],v241 offset:32416\n"
"v_mfma_f32_16x16x32_bf16 v[108:111],a[212:215],a[4:7],v[108:111]\n"
"s_mov_b32 s42,5304096\n"
"s_mov_b32 s43,0x20000\n"
"s_waitcnt lgkmcnt(12)\n"
"v_mfma_f32_16x16x32_bf16 v[112:115],a[216:219],a[0:3],v[112:115]\n"
"ds_read_b64_tr_b16 a[152:153],v241 offset:27840\n"
"ds_read_b64_tr_b16 a[154:155],v241 offset:32448\n"
"v_mfma_f32_16x16x32_bf16 v[116:119],a[216:219],a[4:7],v[116:119]\n"
"s_add_u32 s73,s14,0\n"
"s_mov_b32 s22,4608\n"
"v_mfma_f32_16x16x32_bf16 v[120:123],a[220:223],a[0:3],v[120:123]\n"
"ds_read_b64_tr_b16 a[156:157],v241 offset:27872\n"
"ds_read_b64_tr_b16 a[158:159],v241 offset:32480\n"
"v_mfma_f32_16x16x32_bf16 v[124:127],a[220:223],a[4:7],v[124:127]\n"
"buffer_load_dwordx4 a[16:19],v9,s[44:47],s73 offen offset:0 sc1\n"
"s_mul_i32 s78,s37,294912\n"
"v_mfma_f32_16x16x32_bf16 v[128:131],a[224:227],a[0:3],v[128:131]\n"
"ds_read_b64_tr_b16 a[160:161],v241 offset:27904\n"
"ds_read_b64_tr_b16 a[162:163],v241 offset:32512\n"
"v_mfma_f32_16x16x32_bf16 v[132:135],a[224:227],a[4:7],v[132:135]\n"
"s_add_u32 s22,s22,s78\n"
"s_mov_b32 s30,589824\n"
"s_waitcnt lgkmcnt(12) vmcnt(23)\n"
"v_mfma_f32_16x16x32_bf16 v[64:67],a[128:131],a[8:11],v[64:67]\n"
"ds_read_b64_tr_b16 a[192:193],v241 offset:36864\n"
"ds_read_b64_tr_b16 a[194:195],v241 offset:41472\n"
"v_mfma_f32_16x16x32_bf16 v[68:71],a[128:131],a[12:15],v[68:71]\n"
"buffer_load_dwordx4 a[20:23],v9,s[44:47],s73 offen offset:1024 sc1\n"
"s_mul_i32 s78,s37,294912\n"
"v_mfma_f32_16x16x32_bf16 v[72:75],a[132:135],a[8:11],v[72:75]\n"
"ds_read_b64_tr_b16 a[196:197],v241 offset:36896\n"
"ds_read_b64_tr_b16 a[198:199],v241 offset:41504\n"
"v_mfma_f32_16x16x32_bf16 v[76:79],a[132:135],a[12:15],v[76:79]\n"
"s_add_u32 s30,s30,s78\n"
"s_add_u32 s23,s22,4718592\n"
"v_mfma_f32_16x16x32_bf16 v[80:83],a[136:139],a[8:11],v[80:83]\n"
"ds_read_b64_tr_b16 a[200:201],v241 offset:36928\n"
"ds_read_b64_tr_b16 a[202:203],v241 offset:41536\n"
"v_mfma_f32_16x16x32_bf16 v[84:87],a[136:139],a[12:15],v[84:87]\n"
"s_sub_u32 s77,s30,1179648\n"
"s_waitcnt lgkmcnt(12)\n"
"v_mfma_f32_16x16x32_bf16 v[88:91],a[140:143],a[8:11],v[88:91]\n"
"ds_read_b64_tr_b16 a[204:205],v241 offset:36960\n"
"ds_read_b64_tr_b16 a[206:207],v241 offset:41568\n"
"v_mfma_f32_16x16x32_bf16 v[92:95],a[140:143],a[12:15],v[92:95]\n"
"buffer_load_dwordx4 a[24:27],v9,s[44:47],s73 offen offset:2048 sc1\n"
"v_mfma_f32_16x16x32_bf16 v[96:99],a[144:147],a[8:11],v[96:99]\n"
"ds_read_b64_tr_b16 a[208:209],v241 offset:36992\n"
"ds_read_b64_tr_b16 a[210:211],v241 offset:41600\n"
"v_mfma_f32_16x16x32_bf16 v[100:103],a[144:147],a[12:15],v[100:103]\n"
"s_cmp_lt_u32 s37,2\n"
"s_cselect_b32 s23,s23,s77\n"
"v_mfma_f32_16x16x32_bf16 v[104:107],a[148:151],a[8:11],v[104:107]\n"
"ds_read_b64_tr_b16 a[212:213],v241 offset:37024\n"
"ds_read_b64_tr_b16 a[214:215],v241 offset:41632\n"
"v_mfma_f32_16x16x32_bf16 v[108:111],a[148:151],a[12:15],v[108:111]\n"
"buffer_load_dwordx4 a[28:31],v9,s[44:47],s73 offen offset:3072 sc1\n"
"s_mov_b32 s77,34603008\n"
"s_waitcnt lgkmcnt(12)\n"
"v_mfma_f32_16x16x32_bf16 v[112:115],a[152:155],a[8:11],v[112:115]\n"
"ds_read_b64_tr_b16 a[216:217],v241 offset:37056\n"
"ds_read_b64_tr_b16 a[218:219],v241 offset:41664\n"
"v_mfma_f32_16x16x32_bf16 v[116:119],a[152:155],a[12:15],v[116:119]\n"
"s_mul_i32 s78,s34,147456\n"
"s_add_u32 s77,s77,s78\n"
"v_mfma_f32_16x16x32_bf16 v[120:123],a[156:159],a[8:11],v[120:123]\n"
"ds_read_b64_tr_b16 a[220:221],v241 offset:37088\n"
"ds_read_b64_tr_b16 a[222:223],v241 offset:41696\n"
"v_mfma_f32_16x16x32_bf16 v[124:127],a[156:159],a[12:15],v[124:127]\n"
"s_add_u32 s73,s14,4096\n"
"s_mul_i32 s78,s37,36864\n"
"v_mfma_f32_16x16x32_bf16 v[128:131],a[160:163],a[8:11],v[128:131]\n"
"ds_read_b64_tr_b16 a[224:225],v241 offset:37120\n"
"ds_read_b64_tr_b16 a[226:227],v241 offset:41728\n"
"v_mfma_f32_16x16x32_bf16 v[132:135],a[160:163],a[12:15],v[132:135]\n"
"buffer_load_dwordx4 a[48:51],v9,s[44:47],s73 offen offset:0 sc1\n"
"s_add_u32 s77,s77,s78\n"
"s_waitcnt lgkmcnt(12) vmcnt(25)\n"
"v_mfma_f32_16x16x32_bf16 v[64:67],a[192:195],a[32:35],v[64:67]\n"
"ds_read_b64_tr_b16 a[128:129],v241 offset:46080\n"
"ds_read_b64_tr_b16 a[130:131],v241 offset:50688\n"
"v_mfma_f32_16x16x32_bf16 v[68:71],a[192:195],a[36:39],v[68:71]\n"
"s_add_u32 s52,s8,s77\n"
"s_addc_u32 s53,s9,0\n"
"v_mfma_f32_16x16x32_bf16 v[72:75],a[196:199],a[32:35],v[72:75]\n"
"ds_read_b64_tr_b16 a[132:133],v241 offset:46112\n"
"ds_read_b64_tr_b16 a[134:135],v241 offset:50720\n"
"v_mfma_f32_16x16x32_bf16 v[76:79],a[196:199],a[36:39],v[76:79]\n"
"buffer_load_dwordx4 a[52:55],v9,s[44:47],s73 offen offset:1024 sc1\n"
"s_and_b32 s53,s53,0xffff\n"
"v_mfma_f32_16x16x32_bf16 v[80:83],a[200:203],a[32:35],v[80:83]\n"
"ds_read_b64_tr_b16 a[136:137],v241 offset:46144\n"
"ds_read_b64_tr_b16 a[138:139],v241 offset:50752\n"
"v_mfma_f32_16x16x32_bf16 v[84:87],a[200:203],a[36:39],v[84:87]\n"
"s_mov_b32 s54,36864\n"
"s_mov_b32 s55,0x20000\n"
"s_waitcnt lgkmcnt(12)\n"
"v_mfma_f32_16x16x32_bf16 v[88:91],a[204:207],a[32:35],v[88:91]\n"
"ds_read_b64_tr_b16 a[140:141],v241 offset:46176\n"
"ds_read_b64_tr_b16 a[142:143],v241 offset:50784\n"
"v_mfma_f32_16x16x32_bf16 v[92:95],a[204:207],a[36:39],v[92:95]\n"
"buffer_load_dwordx4 a[56:59],v9,s[44:47],s73 offen offset:2048 sc1\n"
"s_and_b32 s75,s37,1\n"
"v_mfma_f32_16x16x32_bf16 v[96:99],a[208:211],a[32:35],v[96:99]\n"
"ds_read_b64_tr_b16 a[144:145],v241 offset:46208\n"
"ds_read_b64_tr_b16 a[146:147],v241 offset:50816\n"
"v_mfma_f32_16x16x32_bf16 v[100:103],a[208:211],a[36:39],v[100:103]\n"
"s_lshl_b32 s76,s75,6\n"
"s_xor_b32 s75,s75,1\n"
"v_mfma_f32_16x16x32_bf16 v[104:107],a[212:215],a[32:35],v[104:107]\n"
"ds_read_b64_tr_b16 a[148:149],v241 offset:46240\n"
"ds_read_b64_tr_b16 a[150:151],v241 offset:50848\n"
"v_min_u32 v223,12,v10\n"
"v_mfma_f32_16x16x32_bf16 v[108:111],a[212:215],a[36:39],v[108:111]\n"
"buffer_load_dword v222,v223,s[88:91],s15 offen sc1\n"
"s_lshl_b32 s75,s75,9\n"
"s_waitcnt lgkmcnt(12)\n"
"v_mfma_f32_16x16x32_bf16 v[112:115],a[216:219],a[32:35],v[112:115]\n"
"ds_read_b64_tr_b16 a[152:153],v241 offset:46272\n"
"ds_read_b64_tr_b16 a[154:155],v241 offset:50880\n"
"v_mfma_f32_16x16x32_bf16 v[116:119],a[216:219],a[36:39],v[116:119]\n"
"buffer_load_dwordx4 a[60:63],v9,s[44:47],s73 offen offset:3072 sc1\n"
"v_lshrrev_b32 v234,5,v8\n"
"v_mfma_f32_16x16x32_bf16 v[120:123],a[220:223],a[32:35],v[120:123]\n"
"ds_read_b64_tr_b16 a[156:157],v241 offset:46304\n"
"ds_read_b64_tr_b16 a[158:159],v241 offset:50912\n"
"v_mfma_f32_16x16x32_bf16 v[124:127],a[220:223],a[36:39],v[124:127]\n"
"s_add_u32 m0,s65,36864\n"
"s_mul_i32 s62,s12,18\n"
"s_add_u32 s62,s62,147456\n"
"v_mfma_f32_16x16x32_bf16 v[128:131],a[224:227],a[32:35],v[128:131]\n"
"ds_read_b64_tr_b16 a[160:161],v241 offset:46336\n"
"ds_read_b64_tr_b16 a[162:163],v241 offset:50944\n"
"v_mfma_f32_16x16x32_bf16 v[132:135],a[224:227],a[36:39],v[132:135]\n"
"buffer_load_dwordx4 v246,s[56:59],s62 offen offset:0 lds\n"
"s_waitcnt lgkmcnt(12) vmcnt(28)\n"
"v_mfma_f32_16x16x32_bf16 v[64:67],a[128:131],a[40:43],v[64:67]\n"
"v_bfe_u32 v235,v8,3,2\n"
"v_lshlrev_b32 v235,4,v235\n"
"v_mfma_f32_16x16x32_bf16 v[68:71],a[128:131],a[44:47],v[68:71]\n"
"v_lshl_add_u32 v235,v234,7,v235\n"
"v_add_u32 v244,s76,v235\n"
"v_mfma_f32_16x16x32_bf16 v[72:75],a[132:135],a[40:43],v[72:75]\n"
"v_bfe_u32 v235,v8,3,1\n"
"v_lshl_add_u32 v235,v234,1,v235\n"
"v_mfma_f32_16x16x32_bf16 v[76:79],a[132:135],a[44:47],v[76:79]\n"
"s_waitcnt lgkmcnt(0)\n"
"s_barrier\n"
"v_lshlrev_b32 v235,4,v235\n"
"v_mfma_f32_16x16x32_bf16 v[80:83],a[136:139],a[40:43],v[80:83]\n"
"ds_read_b64_tr_b16 a[192:193],v242 offset:0\n"
"ds_read_b64_tr_b16 a[194:195],v242 offset:4608\n"
"v_mfma_f32_16x16x32_bf16 v[84:87],a[136:139],a[44:47],v[84:87]\n"
"ds_read_b64_tr_b16 a[196:197],v242 offset:32\n"
"ds_read_b64_tr_b16 a[198:199],v242 offset:4640\n"
"v_mfma_f32_16x16x32_bf16 v[88:91],a[140:143],a[40:43],v[88:91]\n"
"ds_read_b64_tr_b16 a[200:201],v242 offset:64\n"
"ds_read_b64_tr_b16 a[202:203],v242 offset:4672\n"
"v_mfma_f32_16x16x32_bf16 v[92:95],a[140:143],a[44:47],v[92:95]\n"
"buffer_load_dwordx4 v247,s[56:59],s62 offen offset:1024 lds\n"
"v_mfma_f32_16x16x32_bf16 v[96:99],a[144:147],a[40:43],v[96:99]\n"
"ds_read_b64_tr_b16 a[204:205],v242 offset:96\n"
"ds_read_b64_tr_b16 a[206:207],v242 offset:4704\n"
"v_mfma_f32_16x16x32_bf16 v[100:103],a[144:147],a[44:47],v[100:103]\n"
"v_add_u32 v235,s75,v235\n"
"v_bfe_u32 v234,v8,4,1\n"
"v_mfma_f32_16x16x32_bf16 v[104:107],a[148:151],a[40:43],v[104:107]\n"
"ds_read_b64_tr_b16 a[208:209],v242 offset:128\n"
"ds_read_b64_tr_b16 a[210:211],v242 offset:4736\n"
"v_mfma_f32_16x16x32_bf16 v[108:111],a[148:151],a[44:47],v[108:111]\n"
"v_lshl_or_b32 v245,v234,31,v235\n"
"s_mov_b32 s75,0\n"
"v_mfma_f32_16x16x32_bf16 v[112:115],a[152:155],a[40:43],v[112:115]\n"
"ds_read_b64_tr_b16 a[212:213],v242 offset:160\n"
"ds_read_b64_tr_b16 a[214:215],v242 offset:4768\n"
"v_mfma_f32_16x16x32_bf16 v[116:119],a[152:155],a[44:47],v[116:119]\n"
"ds_read_b64_tr_b16 a[216:217],v242 offset:192\n"
"ds_read_b64_tr_b16 a[218:219],v242 offset:4800\n"
"s_add_u32 m0,s65,45056\n"
"v_mfma_f32_16x16x32_bf16 v[120:123],a[156:159],a[40:43],v[120:123]\n"
"ds_read_b64_tr_b16 a[220:221],v242 offset:224\n"
"ds_read_b64_tr_b16 a[222:223],v242 offset:4832\n"
"v_mfma_f32_16x16x32_bf16 v[124:127],a[156:159],a[44:47],v[124:127]\n"
"buffer_load_dwordx4 v248,s[56:59],s62 offen offset:0 lds\n"
"v_mfma_f32_16x16x32_bf16 v[128:131],a[160:163],a[40:43],v[128:131]\n"
"ds_read_b64_tr_b16 a[224:225],v242 offset:256\n"
"ds_read_b64_tr_b16 a[226:227],v242 offset:4864\n"
"v_mfma_f32_16x16x32_bf16 v[132:135],a[160:163],a[44:47],v[132:135]\n"
"s_mul_i32 s78,s34,147456\n"
"s_add_u32 s75,s75,s78\n"
"s_waitcnt lgkmcnt(12) vmcnt(28)\n"
"v_mfma_f32_16x16x32_bf16 v[64:67],a[192:195],a[64:67],v[64:67]\n"
"ds_read_b64_tr_b16 a[128:129],v242 offset:9216\n"
"ds_read_b64_tr_b16 a[130:131],v242 offset:13824\n"
"v_mfma_f32_16x16x32_bf16 v[68:71],a[192:195],a[68:71],v[68:71]\n"
"buffer_load_dwordx4 v249,s[56:59],s62 offen offset:1024 lds\n"
"v_mfma_f32_16x16x32_bf16 v[72:75],a[196:199],a[64:67],v[72:75]\n"
"ds_read_b64_tr_b16 a[132:133],v242 offset:9248\n"
"ds_read_b64_tr_b16 a[134:135],v242 offset:13856\n"
"v_mfma_f32_16x16x32_bf16 v[76:79],a[196:199],a[68:71],v[76:79]\n"
"s_mul_i32 s78,s36,36864\n"
"s_add_u32 s75,s75,s78\n"
"v_mfma_f32_16x16x32_bf16 v[80:83],a[200:203],a[64:67],v[80:83]\n"
"ds_read_b64_tr_b16 a[136:137],v242 offset:9280\n"
"ds_read_b64_tr_b16 a[138:139],v242 offset:13888\n"
"v_mfma_f32_16x16x32_bf16 v[84:87],a[200:203],a[68:71],v[84:87]\n"
"s_mul_i32 s78,s35,9216\n"
"s_add_u32 s75,s75,s78\n"
"s_waitcnt lgkmcnt(12)\n"
"v_mfma_f32_16x16x32_bf16 v[88:91],a[204:207],a[64:67],v[88:91]\n"
"ds_read_b64_tr_b16 a[140:141],v242 offset:9312\n"
"ds_read_b64_tr_b16 a[142:143],v242 offset:13920\n"
"v_mfma_f32_16x16x32_bf16 v[92:95],a[204:207],a[68:71],v[92:95]\n"
"s_add_u32 m0,s65,53248\n"
"s_mul_i32 s78,s37,288\n"
"v_mfma_f32_16x16x32_bf16 v[96:99],a[208:211],a[64:67],v[96:99]\n"
"ds_read_b64_tr_b16 a[144:145],v242 offset:9344\n"
"ds_read_b64_tr_b16 a[146:147],v242 offset:13952\n"
"v_mfma_f32_16x16x32_bf16 v[100:103],a[208:211],a[68:71],v[100:103]\n"
"buffer_load_dwordx4 v250,s[56:59],s62 offen offset:0 lds\n"
"v_mfma_f32_16x16x32_bf16 v[104:107],a[212:215],a[64:67],v[104:107]\n"
"ds_read_b64_tr_b16 a[148:149],v242 offset:9376\n"
"ds_read_b64_tr_b16 a[150:151],v242 offset:13984\n"
"v_mfma_f32_16x16x32_bf16 v[108:111],a[212:215],a[68:71],v[108:111]\n"
"s_add_u32 s75,s75,s78\n"
"s_waitcnt lgkmcnt(12)\n"
"v_mfma_f32_16x16x32_bf16 v[112:115],a[216:219],a[64:67],v[112:115]\n"
"ds_read_b64_tr_b16 a[152:153],v242 offset:9408\n"
"ds_read_b64_tr_b16 a[154:155],v242 offset:14016\n"
"v_mfma_f32_16x16x32_bf16 v[116:119],a[216:219],a[68:71],v[116:119]\n"
"s_add_u32 s48,s10,s75\n"
"s_addc_u32 s49,s11,0\n"
"v_mfma_f32_16x16x32_bf16 v[120:123],a[220:223],a[64:67],v[120:123]\n"
"ds_read_b64_tr_b16 a[156:157],v242 offset:9440\n"
"ds_read_b64_tr_b16 a[158:159],v242 offset:14048\n"
"v_mfma_f32_16x16x32_bf16 v[124:127],a[220:223],a[68:71],v[124:127]\n"
"buffer_load_dwordx4 v251,s[56:59],s62 offen offset:1024 lds\n"
"v_mfma_f32_16x16x32_bf16 v[128:131],a[224:227],a[64:67],v[128:131]\n"
"ds_read_b64_tr_b16 a[160:161],v242 offset:9472\n"
"ds_read_b64_tr_b16 a[162:163],v242 offset:14080\n"
"v_mfma_f32_16x16x32_bf16 v[132:135],a[224:227],a[68:71],v[132:135]\n"
"s_and_b32 s49,s49,0xffff\n"
"s_mov_b32 s50,8352\n"
"s_waitcnt lgkmcnt(12) vmcnt(29)\n"
"v_mfma_f32_16x16x32_bf16 v[64:67],a[128:131],a[72:75],v[64:67]\n"
"ds_read_b64_tr_b16 a[192:193],v242 offset:18432\n"
"ds_read_b64_tr_b16 a[194:195],v242 offset:23040\n"
"v_mfma_f32_16x16x32_bf16 v[68:71],a[128:131],a[76:79],v[68:71]\n"
"s_mov_b32 s51,0x20000\n"
"v_and_b32 v234,7,v8\n"
"v_mfma_f32_16x16x32_bf16 v[72:75],a[132:135],a[72:75],v[72:75]\n"
"ds_read_b64_tr_b16 a[196:197],v242 offset:18464\n"
"ds_read_b64_tr_b16 a[198:199],v242 offset:23072\n"
"v_mfma_f32_16x16x32_bf16 v[76:79],a[132:135],a[76:79],v[76:79]\n"
"s_add_u32 m0,s65,61440\n"
"v_mul_u32_u24 v234,1152,v234\n"
"v_mfma_f32_16x16x32_bf16 v[80:83],a[136:139],a[72:75],v[80:83]\n"
"ds_read_b64_tr_b16 a[200:201],v242 offset:18496\n"
"ds_read_b64_tr_b16 a[202:203],v242 offset:23104\n"
"v_mfma_f32_16x16x32_bf16 v[84:87],a[136:139],a[76:79],v[84:87]\n"
"buffer_load_dwordx4 v252,s[56:59],s62 offen offset:0 lds\n"
"s_waitcnt lgkmcnt(12)\n"
"v_mfma_f32_16x16x32_bf16 v[88:91],a[140:143],a[72:75],v[88:91]\n"
"ds_read_b64_tr_b16 a[204:205],v242 offset:18528\n"
"ds_read_b64_tr_b16 a[206:207],v242 offset:23136\n"
"v_mfma_f32_16x16x32_bf16 v[92:95],a[140:143],a[76:79],v[92:95]\n"
"v_lshrrev_b32 v235,1,v244\n"
"v_add_u32 v238,v234,v235\n"
"v_mfma_f32_16x16x32_bf16 v[96:99],a[144:147],a[72:75],v[96:99]\n"
"ds_read_b64_tr_b16 a[208:209],v242 offset:18560\n"
"ds_read_b64_tr_b16 a[210:211],v242 offset:23168\n"
"v_mfma_f32_16x16x32_bf16 v[100:103],a[144:147],a[76:79],v[100:103]\n"
"v_lshrrev_b32 v235,1,v245\n"
"v_add_u32 v239,v234,v235\n"
"v_mfma_f32_16x16x32_bf16 v[104:107],a[148:151],a[72:75],v[104:107]\n"
"ds_read_b64_tr_b16 a[212:213],v242 offset:18592\n"
"ds_read_b64_tr_b16 a[214:215],v242 offset:23200\n"
"v_mfma_f32_16x16x32_bf16 v[108:111],a[148:151],a[76:79],v[108:111]\n"
"buffer_load_dwordx4 v253,s[56:59],s62 offen offset:1024 lds\n"
"s_waitcnt lgkmcnt(12)\n"
"v_mfma_f32_16x16x32_bf16 v[112:115],a[152:155],a[72:75],v[112:115]\n"
"ds_read_b64_tr_b16 a[216:217],v242 offset:18624\n"
"ds_read_b64_tr_b16 a[218:219],v242 offset:23232\n"
"v_mfma_f32_16x16x32_bf16 v[116:119],a[152:155],a[76:79],v[116:119]\n"
"buffer_load_dwordx2 v[52:53],v238,s[48:51],0 offen offset:0\n"
"buffer_load_dwordx2 v[54:55],v238,s[48:51],0 offen offset:32\n"
"v_mfma_f32_16x16x32_bf16 v[120:123],a[156:159],a[72:75],v[120:123]\n"
"ds_read_b64_tr_b16 a[220:221],v242 offset:18656\n"
"ds_read_b64_tr_b16 a[222:223],v242 offset:23264\n"
"v_mfma_f32_16x16x32_bf16 v[124:127],a[156:159],a[76:79],v[124:127]\n"
"buffer_load_dwordx2 v[56:57],v238,s[48:51],0 offen offset:128\n"
"buffer_load_dwordx2 v[58:59],v238,s[48:51],0 offen offset:160\n"
"v_mfma_f32_16x16x32_bf16 v[128:131],a[160:163],a[72:75],v[128:131]\n"
"ds_read_b64_tr_b16 a[224:225],v242 offset:18688\n"
"ds_read_b64_tr_b16 a[226:227],v242 offset:23296\n"
"v_mfma_f32_16x16x32_bf16 v[132:135],a[160:163],a[76:79],v[132:135]\n"
"s_waitcnt vmcnt(13)\n"
"v_cmp_ne_u32 vcc,s38,v222\n"
"s_cbranch_vccnz L_ap2_%=\n"
"L_aq2_%=:\n"
"s_add_u32 m0,s61,69632\n"
"s_waitcnt lgkmcnt(12)\n"
"v_mfma_f32_16x16x32_bf16 v[64:67],a[192:195],a[96:99],v[64:67]\n"
"ds_read_b64_tr_b16 a[128:129],v242 offset:27648\n"
"ds_read_b64_tr_b16 a[130:131],v242 offset:32256\n"
"v_mfma_f32_16x16x32_bf16 v[68:71],a[192:195],a[100:103],v[68:71]\n"
"buffer_load_dwordx4 v254,s[56:59],s62 offen offset:0 lds\n"
"v_mfma_f32_16x16x32_bf16 v[72:75],a[196:199],a[96:99],v[72:75]\n"
"ds_read_b64_tr_b16 a[132:133],v242 offset:27680\n"
"ds_read_b64_tr_b16 a[134:135],v242 offset:32288\n"
"v_mfma_f32_16x16x32_bf16 v[76:79],a[196:199],a[100:103],v[76:79]\n"
"buffer_load_dwordx2 v[60:61],v239,s[48:51],0 offen offset:0\n"
"v_and_b32 v234,7,v8\n"
"v_mfma_f32_16x16x32_bf16 v[80:83],a[200:203],a[96:99],v[80:83]\n"
"ds_read_b64_tr_b16 a[136:137],v242 offset:27712\n"
"ds_read_b64_tr_b16 a[138:139],v242 offset:32320\n"
"v_mfma_f32_16x16x32_bf16 v[84:87],a[200:203],a[100:103],v[84:87]\n"
"s_add_u32 s73,s12,8192\n"
"v_lshlrev_b32 v219,5,v234\n"
"s_waitcnt lgkmcnt(12)\n"
"v_mfma_f32_16x16x32_bf16 v[88:91],a[204:207],a[96:99],v[88:91]\n"
"ds_read_b64_tr_b16 a[140:141],v242 offset:27744\n"
"ds_read_b64_tr_b16 a[142:143],v242 offset:32352\n"
"v_mfma_f32_16x16x32_bf16 v[92:95],a[204:207],a[100:103],v[92:95]\n"
"buffer_load_dwordx4 a[80:83],v9,s[44:47],s73 offen offset:0 sc1\n"
"v_add_u32 v219,147456,v219\n"
"v_mfma_f32_16x16x32_bf16 v[96:99],a[208:211],a[96:99],v[96:99]\n"
"ds_read_b64_tr_b16 a[144:145],v242 offset:27776\n"
"ds_read_b64_tr_b16 a[146:147],v242 offset:32384\n"
"v_mfma_f32_16x16x32_bf16 v[100:103],a[208:211],a[100:103],v[100:103]\n"
"v_lshrrev_b32 v235,5,v8\n"
"v_lshl_add_u32 v235,v235,4,v219\n"
"v_mfma_f32_16x16x32_bf16 v[104:107],a[212:215],a[96:99],v[104:107]\n"
"ds_read_b64_tr_b16 a[148:149],v242 offset:27808\n"
"ds_read_b64_tr_b16 a[150:151],v242 offset:32416\n"
"v_mfma_f32_16x16x32_bf16 v[108:111],a[212:215],a[100:103],v[108:111]\n"
"s_lshl_b32 s75,s37,2\n"
"v_add_u32 v218,s75,v235\n"
"s_waitcnt lgkmcnt(12)\n"
"v_mfma_f32_16x16x32_bf16 v[112:115],a[216:219],a[96:99],v[112:115]\n"
"ds_read_b64_tr_b16 a[152:153],v242 offset:27840\n"
"ds_read_b64_tr_b16 a[154:155],v242 offset:32448\n"
"v_mfma_f32_16x16x32_bf16 v[116:119],a[216:219],a[100:103],v[116:119]\n"
"buffer_load_dwordx4 a[84:87],v9,s[44:47],s73 offen offset:1024 sc1\n"
"s_mul_i32 s70,s39,5120\n"
"v_mfma_f32_16x16x32_bf16 v[120:123],a[220:223],a[96:99],v[120:123]\n"
"ds_read_b64_tr_b16 a[156:157],v242 offset:27872\n"
"ds_read_b64_tr_b16 a[158:159],v242 offset:32480\n"
"v_mfma_f32_16x16x32_bf16 v[124:127],a[220:223],a[100:103],v[124:127]\n"
"s_add_u32 s70,s70,1048576\n"
"s_mul_i32 s75,s37,9\n"
"v_mfma_f32_16x16x32_bf16 v[128:131],a[224:227],a[96:99],v[128:131]\n"
"ds_read_b64_tr_b16 a[160:161],v242 offset:27904\n"
"ds_read_b64_tr_b16 a[162:163],v242 offset:32512\n"
"v_mfma_f32_16x16x32_bf16 v[132:135],a[224:227],a[100:103],v[132:135]\n"
"buffer_load_dwordx4 a[88:91],v9,s[44:47],s73 offen offset:2048 sc1\n"
"s_add_u32 s76,s75,1\n"
"s_waitcnt lgkmcnt(12)\n"
"v_mfma_f32_16x16x32_bf16 v[64:67],a[128:131],a[104:107],v[64:67]\n"
"s_lshr_b32 s76,s76,1\n"
"s_lshr_b32 s75,s35,1\n"
"v_mfma_f32_16x16x32_bf16 v[68:71],a[128:131],a[108:111],v[68:71]\n"
"s_mov_b32 s64,34603008\n"
"s_mul_i32 s78,s34,147456\n"
"v_mfma_f32_16x16x32_bf16 v[72:75],a[132:135],a[104:107],v[72:75]\n"
"s_add_u32 s64,s64,s78\n"
"s_mul_i32 s78,s36,36864\n"
"v_mfma_f32_16x16x32_bf16 v[76:79],a[132:135],a[108:111],v[76:79]\n"
"s_waitcnt lgkmcnt(0)\n"
"s_barrier\n"
"s_add_u32 s64,s64,s78\n"
"v_mfma_f32_16x16x32_bf16 v[80:83],a[136:139],a[104:107],v[80:83]\n"
"ds_read_b64_tr_b16 a[192:193],v240 offset:0\n"
"ds_read_b64_tr_b16 a[194:195],v240 offset:4608\n"
"v_mfma_f32_16x16x32_bf16 v[84:87],a[136:139],a[108:111],v[84:87]\n"
"ds_read_b64_tr_b16 a[196:197],v240 offset:32\n"
"ds_read_b64_tr_b16 a[198:199],v240 offset:4640\n"
"v_mfma_f32_16x16x32_bf16 v[88:91],a[140:143],a[104:107],v[88:91]\n"
"ds_read_b64_tr_b16 a[200:201],v240 offset:64\n"
"ds_read_b64_tr_b16 a[202:203],v240 offset:4672\n"
"v_mfma_f32_16x16x32_bf16 v[92:95],a[140:143],a[108:111],v[92:95]\n"
"buffer_load_dwordx4 a[92:95],v9,s[44:47],s73 offen offset:3072 sc1\n"
"s_mul_i32 s78,s75,18432\n"
"v_mfma_f32_16x16x32_bf16 v[96:99],a[144:147],a[104:107],v[96:99]\n"
"ds_read_b64_tr_b16 a[204:205],v240 offset:96\n"
"ds_read_b64_tr_b16 a[206:207],v240 offset:4704\n"
"s_add_u32 s73,s12,12288\n"
"v_mfma_f32_16x16x32_bf16 v[100:103],a[144:147],a[108:111],v[100:103]\n"
"buffer_load_dwordx4 a[112:115],v9,s[44:47],s73 offen offset:0 sc1\n"
"s_add_u32 s64,s64,s78\n"
"v_mfma_f32_16x16x32_bf16 v[104:107],a[148:151],a[104:107],v[104:107]\n"
"ds_read_b64_tr_b16 a[208:209],v240 offset:128\n"
"ds_read_b64_tr_b16 a[210:211],v240 offset:4736\n"
"v_mfma_f32_16x16x32_bf16 v[108:111],a[148:151],a[108:111],v[108:111]\n"
"s_mul_i32 s78,s98,64\n"
"s_add_u32 s64,s64,s78\n"
"v_mfma_f32_16x16x32_bf16 v[112:115],a[152:155],a[104:107],v[112:115]\n"
"ds_read_b64_tr_b16 a[212:213],v240 offset:160\n"
"ds_read_b64_tr_b16 a[214:215],v240 offset:4768\n"
"v_mfma_f32_16x16x32_bf16 v[116:119],a[152:155],a[108:111],v[116:119]\n"
"ds_read_b64_tr_b16 a[216:217],v240 offset:192\n"
"ds_read_b64_tr_b16 a[218:219],v240 offset:4800\n"
"v_mfma_f32_16x16x32_bf16 v[120:123],a[156:159],a[104:107],v[120:123]\n"
"ds_read_b64_tr_b16 a[220:221],v240 offset:224\n"
"ds_read_b64_tr_b16 a[222:223],v240 offset:4832\n"
"v_mfma_f32_16x16x32_bf16 v[124:127],a[156:159],a[108:111],v[124:127]\n"
"buffer_load_dwordx4 a[116:119],v9,s[44:47],s73 offen offset:1024 sc1\n"
"s_mul_i32 s78,s76,1024\n"
"v_mfma_f32_16x16x32_bf16 v[128:131],a[160:163],a[104:107],v[128:131]\n"
"ds_read_b64_tr_b16 a[224:225],v240 offset:256\n"
"ds_read_b64_tr_b16 a[226:227],v240 offset:4864\n"
"v_mfma_f32_16x16x32_bf16 v[132:135],a[160:163],a[108:111],v[132:135]\n"
"s_add_u32 s64,s64,s78\n"
"v_lshlrev_b32 v234,4,v234\n"
"s_waitcnt lgkmcnt(12)\n"
"v_mfma_f32_16x16x32_bf16 v[64:67],a[192:195],a[16:19],v[64:67]\n"
"ds_read_b64_tr_b16 a[128:129],v240 offset:9216\n"
"ds_read_b64_tr_b16 a[130:131],v240 offset:13824\n"
"v_mfma_f32_16x16x32_bf16 v[68:71],a[192:195],a[20:23],v[68:71]\n"
"buffer_load_dwordx4 a[120:123],v9,s[44:47],s73 offen offset:2048 sc1\n"
"v_lshrrev_b32 v235,3,v8\n"
"v_mfma_f32_16x16x32_bf16 v[72:75],a[196:199],a[16:19],v[72:75]\n"
"ds_read_b64_tr_b16 a[132:133],v240 offset:9248\n"
"ds_read_b64_tr_b16 a[134:135],v240 offset:13856\n"
"v_mfma_f32_16x16x32_bf16 v[76:79],a[196:199],a[20:23],v[76:79]\n"
"v_lshl_add_u32 v220,v235,8,v234\n"
"v_bfe_u32 v235,v245,4,2\n"
"v_mfma_f32_16x16x32_bf16 v[80:83],a[200:203],a[16:19],v[80:83]\n"
"ds_read_b64_tr_b16 a[136:137],v240 offset:9280\n"
"ds_read_b64_tr_b16 a[138:139],v240 offset:13888\n"
"v_mfma_f32_16x16x32_bf16 v[84:87],a[200:203],a[20:23],v[84:87]\n"
"buffer_load_dwordx4 a[124:127],v9,s[44:47],s73 offen offset:3072 sc1\n"
"v_lshl_add_u32 v221,v235,8,v234\n"
"s_waitcnt lgkmcnt(12)\n"
"v_mfma_f32_16x16x32_bf16 v[88:91],a[204:207],a[16:19],v[88:91]\n"
"ds_read_b64_tr_b16 a[140:141],v240 offset:9312\n"
"ds_read_b64_tr_b16 a[142:143],v240 offset:13920\n"
"v_mfma_f32_16x16x32_bf16 v[92:95],a[204:207],a[20:23],v[92:95]\n"
"s_add_u32 m0,s65,73728\n"
"s_mul_i32 s62,s13,18\n"
"s_add_u32 s62,s62,147456\n"
"v_mfma_f32_16x16x32_bf16 v[96:99],a[208:211],a[16:19],v[96:99]\n"
"ds_read_b64_tr_b16 a[144:145],v240 offset:9344\n"
"ds_read_b64_tr_b16 a[146:147],v240 offset:13952\n"
"v_mfma_f32_16x16x32_bf16 v[100:103],a[208:211],a[20:23],v[100:103]\n"
"buffer_load_dwordx4 v246,s[56:59],s62 offen offset:0 lds\n"
"v_mfma_f32_16x16x32_bf16 v[104:107],a[212:215],a[16:19],v[104:107]\n"
"ds_read_b64_tr_b16 a[148:149],v240 offset:9376\n"
"ds_read_b64_tr_b16 a[150:151],v240 offset:13984\n"
"v_mfma_f32_16x16x32_bf16 v[108:111],a[212:215],a[20:23],v[108:111]\n"
"v_and_b32 v235,0x80000000,v245\n"
"v_or_b32 v221,v221,v235\n"
"s_waitcnt lgkmcnt(12)\n"
"v_mfma_f32_16x16x32_bf16 v[112:115],a[216:219],a[16:19],v[112:115]\n"
"ds_read_b64_tr_b16 a[152:153],v240 offset:9408\n"
"ds_read_b64_tr_b16 a[154:155],v240 offset:14016\n"
"v_mfma_f32_16x16x32_bf16 v[116:119],a[216:219],a[20:23],v[116:119]\n"
"s_and_b32 s75,s37,1\n"
"s_mul_i32 s75,s75,-5112\n"
"v_mfma_f32_16x16x32_bf16 v[120:123],a[220:223],a[16:19],v[120:123]\n"
"ds_read_b64_tr_b16 a[156:157],v240 offset:9440\n"
"ds_read_b64_tr_b16 a[158:159],v240 offset:14048\n"
"v_mfma_f32_16x16x32_bf16 v[124:127],a[220:223],a[20:23],v[124:127]\n"
"s_add_u32 s75,s75,4096\n"
"s_add_u32 s66,s75,s64\n"
"v_mfma_f32_16x16x32_bf16 v[128:131],a[224:227],a[16:19],v[128:131]\n"
"ds_read_b64_tr_b16 a[160:161],v240 offset:9472\n"
"ds_read_b64_tr_b16 a[162:163],v240 offset:14080\n"
"v_mfma_f32_16x16x32_bf16 v[132:135],a[224:227],a[20:23],v[132:135]\n"
"buffer_load_dwordx4 v247,s[56:59],s62 offen offset:1024 lds\n"
"s_waitcnt lgkmcnt(12)\n"
"v_mfma_f32_16x16x32_bf16 v[64:67],a[128:131],a[24:27],v[64:67]\n"
"ds_read_b64_tr_b16 a[192:193],v240 offset:18432\n"
"ds_read_b64_tr_b16 a[194:195],v240 offset:23040\n"
"v_mfma_f32_16x16x32_bf16 v[68:71],a[128:131],a[28:31],v[68:71]\n"
"s_mov_b32 s75,155648\n"
"s_mul_i32 s78,s37,576\n"
"v_mfma_f32_16x16x32_bf16 v[72:75],a[132:135],a[24:27],v[72:75]\n"
"ds_read_b64_tr_b16 a[196:197],v240 offset:18464\n"
"ds_read_b64_tr_b16 a[198:199],v240 offset:23072\n"
"v_mfma_f32_16x16x32_bf16 v[76:79],a[132:135],a[28:31],v[76:79]\n"
"s_add_u32 m0,s65,81920\n"
"s_add_u32 s75,s75,s78\n"
"v_mfma_f32_16x16x32_bf16 v[80:83],a[136:139],a[24:27],v[80:83]\n"
"ds_read_b64_tr_b16 a[200:201],v240 offset:18496\n"
"ds_read_b64_tr_b16 a[202:203],v240 offset:23104\n"
"v_mfma_f32_16x16x32_bf16 v[84:87],a[136:139],a[28:31],v[84:87]\n"
"buffer_load_dwordx4 v248,s[56:59],s62 offen offset:0 lds\n"
"s_waitcnt lgkmcnt(12)\n"
"v_mfma_f32_16x16x32_bf16 v[88:91],a[140:143],a[24:27],v[88:91]\n"
"ds_read_b64_tr_b16 a[204:205],v240 offset:18528\n"
"ds_read_b64_tr_b16 a[206:207],v240 offset:23136\n"
"v_mfma_f32_16x16x32_bf16 v[92:95],a[140:143],a[28:31],v[92:95]\n"
"v_add_u32 v224,s75,v244\n"
"v_add_u32 v225,s75,v245\n"
"v_mfma_f32_16x16x32_bf16 v[96:99],a[144:147],a[24:27],v[96:99]\n"
"ds_read_b64_tr_b16 a[208:209],v240 offset:18560\n"
"ds_read_b64_tr_b16 a[210:211],v240 offset:23168\n"
"v_mfma_f32_16x16x32_bf16 v[100:103],a[144:147],a[28:31],v[100:103]\n"
"v_and_b32 v234,7,v8\n"
"v_bfe_u32 v235,v8,3,1\n"
"v_mfma_f32_16x16x32_bf16 v[104:107],a[148:151],a[24:27],v[104:107]\n"
"ds_read_b64_tr_b16 a[212:213],v240 offset:18592\n"
"ds_read_b64_tr_b16 a[214:215],v240 offset:23200\n"
"v_mfma_f32_16x16x32_bf16 v[108:111],a[148:151],a[28:31],v[108:111]\n"
"v_mul_u32_u24 v235,36864,v235\n"
"v_lshl_add_u32 v234,v234,4,v235\n"
"s_waitcnt lgkmcnt(12)\n"
"v_mfma_f32_16x16x32_bf16 v[112:115],a[152:155],a[24:27],v[112:115]\n"
"ds_read_b64_tr_b16 a[216:217],v240 offset:18624\n"
"ds_read_b64_tr_b16 a[218:219],v240 offset:23232\n"
"L_pgg192_%=:\n"
"s_cbranch_execz L_hrg_%=\n"
"v_mfma_f32_16x16x32_bf16 v[116:119],a[152:155],a[28:31],v[116:119]\n"
"buffer_load_dwordx4 v249,s[56:59],s62 offen offset:1024 lds\n"
"v_mfma_f32_16x16x32_bf16 v[120:123],a[156:159],a[24:27],v[120:123]\n"
"ds_read_b64_tr_b16 a[220:221],v240 offset:18656\n"
"ds_read_b64_tr_b16 a[222:223],v240 offset:23264\n"
"v_mfma_f32_16x16x32_bf16 v[124:127],a[156:159],a[28:31],v[124:127]\n"
"v_lshrrev_b32 v235,4,v8\n"
"v_lshl_add_u32 v238,v235,7,v234\n"
"v_mfma_f32_16x16x32_bf16 v[128:131],a[160:163],a[24:27],v[128:131]\n"
"ds_read_b64_tr_b16 a[224:225],v240 offset:18688\n"
"ds_read_b64_tr_b16 a[226:227],v240 offset:23296\n"
"v_mfma_f32_16x16x32_bf16 v[132:135],a[160:163],a[28:31],v[132:135]\n"
"s_add_u32 m0,s65,90112\n"
"v_and_b32 v235,1,v235\n"
"s_waitcnt lgkmcnt(12)\n"
"v_mfma_f32_16x16x32_bf16 v[64:67],a[192:195],a[48:51],v[64:67]\n"
"ds_read_b64_tr_b16 a[128:129],v240 offset:27648\n"
"ds_read_b64_tr_b16 a[130:131],v240 offset:32256\n"
"v_mfma_f32_16x16x32_bf16 v[68:71],a[192:195],a[52:55],v[68:71]\n"
"buffer_load_dwordx4 v250,s[56:59],s62 offen offset:0 lds\n"
"v_mfma_f32_16x16x32_bf16 v[72:75],a[196:199],a[48:51],v[72:75]\n"
"ds_read_b64_tr_b16 a[132:133],v240 offset:27680\n"
"ds_read_b64_tr_b16 a[134:135],v240 offset:32288\n"
"v_mfma_f32_16x16x32_bf16 v[76:79],a[196:199],a[52:55],v[76:79]\n"
"v_lshl_add_u32 v234,v235,7,v234\n"
"v_lshrrev_b32 v235,5,v8\n"
"v_mfma_f32_16x16x32_bf16 v[80:83],a[200:203],a[48:51],v[80:83]\n"
"ds_read_b64_tr_b16 a[136:137],v240 offset:27712\n"
"ds_read_b64_tr_b16 a[138:139],v240 offset:32320\n"
"v_mfma_f32_16x16x32_bf16 v[84:87],a[200:203],a[52:55],v[84:87]\n"
"v_mul_u32_u24 v235,73728,v235\n"
"v_add_u32 v239,v234,v235\n"
"s_waitcnt lgkmcnt(12)\n"
"v_mfma_f32_16x16x32_bf16 v[88:91],a[204:207],a[48:51],v[88:91]\n"
"ds_read_b64_tr_b16 a[140:141],v240 offset:27744\n"
"ds_read_b64_tr_b16 a[142:143],v240 offset:32352\n"
"v_mfma_f32_16x16x32_bf16 v[92:95],a[204:207],a[52:55],v[92:95]\n"
"s_mov_b32 s77,25165824\n"
"s_mul_i32 s78,s34,589824\n"
"v_mfma_f32_16x16x32_bf16 v[96:99],a[208:211],a[48:51],v[96:99]\n"
"ds_read_b64_tr_b16 a[144:145],v240 offset:27776\n"
"ds_read_b64_tr_b16 a[146:147],v240 offset:32384\n"
"v_mfma_f32_16x16x32_bf16 v[100:103],a[208:211],a[52:55],v[100:103]\n"
"buffer_load_dwordx4 v251,s[56:59],s62 offen offset:1024 lds\n"
"v_mfma_f32_16x16x32_bf16 v[104:107],a[212:215],a[48:51],v[104:107]\n"
"ds_read_b64_tr_b16 a[148:149],v240 offset:27808\n"
"ds_read_b64_tr_b16 a[150:151],v240 offset:32416\n"
"v_mfma_f32_16x16x32_bf16 v[108:111],a[212:215],a[52:55],v[108:111]\n"
"s_add_u32 s77,s77,s78\n"
"s_mul_i32 s78,s37,147456\n"
"s_waitcnt lgkmcnt(12)\n"
"v_mfma_f32_16x16x32_bf16 v[112:115],a[216:219],a[48:51],v[112:115]\n"
"ds_read_b64_tr_b16 a[152:153],v240 offset:27840\n"
"ds_read_b64_tr_b16 a[154:155],v240 offset:32448\n"
"v_mfma_f32_16x16x32_bf16 v[116:119],a[216:219],a[52:55],v[116:119]\n"
"s_add_u32 m0,s65,98304\n"
"s_add_u32 s77,s77,s78\n"
"v_mfma_f32_16x16x32_bf16 v[120:123],a[220:223],a[48:51],v[120:123]\n"
"ds_read_b64_tr_b16 a[156:157],v240 offset:27872\n"
"ds_read_b64_tr_b16 a[158:159],v240 offset:32480\n"
"v_mfma_f32_16x16x32_bf16 v[124:127],a[220:223],a[52:55],v[124:127]\n"
"buffer_load_dwordx4 v252,s[56:59],s62 offen offset:0 lds\n"
"v_mfma_f32_16x16x32_bf16 v[128:131],a[224:227],a[48:51],v[128:131]\n"
"ds_read_b64_tr_b16 a[160:161],v240 offset:27904\n"
"ds_read_b64_tr_b16 a[162:163],v240 offset:32512\n"
"v_mfma_f32_16x16x32_bf16 v[132:135],a[224:227],a[52:55],v[132:135]\n"
"s_mul_i32 s78,s35,9216\n"
"s_add_u32 s77,s77,s78\n"
"s_waitcnt lgkmcnt(12) vmcnt(29)\n"
"v_mfma_f32_16x16x32_bf16 v[64:67],a[128:131],a[56:59],v[64:67]\n"
"s_mul_i32 s78,s36,1024\n"
"s_add_u32 s77,s77,s78\n"
"v_mfma_f32_16x16x32_bf16 v[68:71],a[128:131],a[60:63],v[68:71]\n"
"s_mov_b32 s75,25174016\n"
"s_mul_i32 s78,s34,589824\n"
"v_mfma_f32_16x16x32_bf16 v[72:75],a[132:135],a[56:59],v[72:75]\n"
"s_add_u32 s75,s75,s78\n"
"s_mul_i32 s78,s37,147456\n"
"v_mfma_f32_16x16x32_bf16 v[76:79],a[132:135],a[60:63],v[76:79]\n"
"s_waitcnt vmcnt(16) lgkmcnt(0)\n"
"s_barrier\n"
"s_add_u32 s75,s75,s78\n"
"v_mfma_f32_16x16x32_bf16 v[80:83],a[136:139],a[56:59],v[80:83]\n"
"ds_read_b64_tr_b16 a[192:193],v240 offset:36864\n"
"ds_read_b64_tr_b16 a[194:195],v240 offset:41472\n"
"v_mfma_f32_16x16x32_bf16 v[84:87],a[136:139],a[60:63],v[84:87]\n"
"ds_read_b64_tr_b16 a[196:197],v240 offset:36896\n"
"ds_read_b64_tr_b16 a[198:199],v240 offset:41504\n"
"v_mfma_f32_16x16x32_bf16 v[88:91],a[140:143],a[56:59],v[88:91]\n"
"ds_read_b64_tr_b16 a[200:201],v240 offset:36928\n"
"ds_read_b64_tr_b16 a[202:203],v240 offset:41536\n"
"v_mfma_f32_16x16x32_bf16 v[92:95],a[140:143],a[60:63],v[92:95]\n"
"buffer_load_dwordx4 v253,s[56:59],s62 offen offset:1024 lds\n"
"v_mfma_f32_16x16x32_bf16 v[96:99],a[144:147],a[56:59],v[96:99]\n"
"ds_read_b64_tr_b16 a[204:205],v240 offset:36960\n"
"ds_read_b64_tr_b16 a[206:207],v240 offset:41568\n"
"v_mfma_f32_16x16x32_bf16 v[100:103],a[144:147],a[60:63],v[100:103]\n"
"s_add_u32 m0,s61,106496\n"
"s_mul_i32 s78,s35,9216\n"
"v_mfma_f32_16x16x32_bf16 v[104:107],a[148:151],a[56:59],v[104:107]\n"
"ds_read_b64_tr_b16 a[208:209],v240 offset:36992\n"
"ds_read_b64_tr_b16 a[210:211],v240 offset:41600\n"
"v_mfma_f32_16x16x32_bf16 v[108:111],a[148:151],a[60:63],v[108:111]\n"
"buffer_load_dwordx4 v254,s[56:59],s62 offen offset:0 lds\n"
"v_mfma_f32_16x16x32_bf16 v[112:115],a[152:155],a[56:59],v[112:115]\n"
"ds_read_b64_tr_b16 a[212:213],v240 offset:37024\n"
"ds_read_b64_tr_b16 a[214:215],v240 offset:41632\n"
"v_mfma_f32_16x16x32_bf16 v[116:119],a[152:155],a[60:63],v[116:119]\n"
"ds_read_b64_tr_b16 a[216:217],v240 offset:37056\n"
"ds_read_b64_tr_b16 a[218:219],v240 offset:41664\n"
"v_mfma_f32_16x16x32_bf16 v[120:123],a[156:159],a[56:59],v[120:123]\n"
"ds_read_b64_tr_b16 a[220:221],v240 offset:37088\n"
"ds_read_b64_tr_b16 a[222:223],v240 offset:41696\n"
"v_mfma_f32_16x16x32_bf16 v[124:127],a[156:159],a[60:63],v[124:127]\n"
"s_add_u32 s75,s75,s78\n"
"s_mul_i32 s78,s36,256\n"
"v_mfma_f32_16x16x32_bf16 v[128:131],a[160:163],a[56:59],v[128:131]\n"
"ds_read_b64_tr_b16 a[224:225],v240 offset:37120\n"
"ds_read_b64_tr_b16 a[226:227],v240 offset:41728\n"
"v_mfma_f32_16x16x32_bf16 v[132:135],a[160:163],a[60:63],v[132:135]\n"
"s_add_u32 s73,s13,8192\n"
"s_add_u32 s75,s75,s78\n"
"s_waitcnt lgkmcnt(12) vmcnt(15)\n"
"v_mfma_f32_16x16x32_bf16 v[64:67],a[192:195],a[80:83],v[64:67]\n"
"ds_read_b64_tr_b16 a[128:129],v240 offset:46080\n"
"ds_read_b64_tr_b16 a[130:131],v240 offset:50688\n"
"v_mfma_f32_16x16x32_bf16 v[68:71],a[192:195],a[84:87],v[68:71]\n"
"buffer_load_dwordx4 a[0:3],v9,s[44:47],s73 offen offset:0 sc1\n"
"v_mfma_f32_16x16x32_bf16 v[72:75],a[196:199],a[80:83],v[72:75]\n"
"ds_read_b64_tr_b16 a[132:133],v240 offset:46112\n"
"ds_read_b64_tr_b16 a[134:135],v240 offset:50720\n"
"v_mfma_f32_16x16x32_bf16 v[76:79],a[196:199],a[84:87],v[76:79]\n"
"v_mfma_f32_16x16x32_bf16 v[80:83],a[200:203],a[80:83],v[80:83]\n"
"ds_read_b64_tr_b16 a[136:137],v240 offset:46144\n"
"ds_read_b64_tr_b16 a[138:139],v240 offset:50752\n"
"v_mfma_f32_16x16x32_bf16 v[84:87],a[200:203],a[84:87],v[84:87]\n"
"buffer_load_dwordx4 a[4:7],v9,s[44:47],s73 offen offset:1024 sc1\n"
"s_waitcnt lgkmcnt(12)\n"
"v_mfma_f32_16x16x32_bf16 v[88:91],a[204:207],a[80:83],v[88:91]\n"
"ds_read_b64_tr_b16 a[140:141],v240 offset:46176\n"
"ds_read_b64_tr_b16 a[142:143],v240 offset:50784\n"
"v_mfma_f32_16x16x32_bf16 v[92:95],a[204:207],a[84:87],v[92:95]\n"
"v_mfma_f32_16x16x32_bf16 v[96:99],a[208:211],a[80:83],v[96:99]\n"
"ds_read_b64_tr_b16 a[144:145],v240 offset:46208\n"
"ds_read_b64_tr_b16 a[146:147],v240 offset:50816\n"
"v_mfma_f32_16x16x32_bf16 v[100:103],a[208:211],a[84:87],v[100:103]\n"
"v_mfma_f32_16x16x32_bf16 v[104:107],a[212:215],a[80:83],v[104:107]\n"
"ds_read_b64_tr_b16 a[148:149],v240 offset:46240\n"
"ds_read_b64_tr_b16 a[150:151],v240 offset:50848\n"
"v_mfma_f32_16x16x32_bf16 v[108:111],a[212:215],a[84:87],v[108:111]\n"
"buffer_load_dwordx4 a[8:11],v9,s[44:47],s73 offen offset:2048 sc1\n"
"s_waitcnt lgkmcnt(12)\n"
"v_mfma_f32_16x16x32_bf16 v[112:115],a[216:219],a[80:83],v[112:115]\n"
"ds_read_b64_tr_b16 a[152:153],v240 offset:46272\n"
"ds_read_b64_tr_b16 a[154:155],v240 offset:50880\n"
"v_mfma_f32_16x16x32_bf16 v[116:119],a[216:219],a[84:87],v[116:119]\n"
"v_mfma_f32_16x16x32_bf16 v[120:123],a[220:223],a[80:83],v[120:123]\n"
"ds_read_b64_tr_b16 a[156:157],v240 offset:46304\n"
"ds_read_b64_tr_b16 a[158:159],v240 offset:50912\n"
"v_mfma_f32_16x16x32_bf16 v[124:127],a[220:223],a[84:87],v[124:127]\n"
"buffer_load_dwordx4 a[12:15],v9,s[44:47],s73 offen offset:3072 sc1\n"
"v_mfma_f32_16x16x32_bf16 v[128:131],a[224:227],a[80:83],v[128:131]\n"
"ds_read_b64_tr_b16 a[160:161],v240 offset:46336\n"
"ds_read_b64_tr_b16 a[162:163],v240 offset:50944\n"
"v_mfma_f32_16x16x32_bf16 v[132:135],a[224:227],a[84:87],v[132:135]\n"
"s_add_u32 s73,s13,12288\n"
"s_waitcnt lgkmcnt(12) vmcnt(17)\n"
"v_mfma_f32_16x16x32_bf16 v[64:67],a[128:131],a[88:91],v[64:67]\n"
"ds_read_b64_tr_b16 a[192:193],v241 offset:0\n"
"ds_read_b64_tr_b16 a[194:195],v241 offset:4608\n"
"v_mfma_f32_16x16x32_bf16 v[68:71],a[128:131],a[92:95],v[68:71]\n"
"buffer_load_dwordx4 a[32:35],v9,s[44:47],s73 offen offset:0 sc1\n"
"v_mfma_f32_16x16x32_bf16 v[72:75],a[132:135],a[88:91],v[72:75]\n"
"ds_read_b64_tr_b16 a[196:197],v241 offset:32\n"
"ds_read_b64_tr_b16 a[198:199],v241 offset:4640\n"
"v_mfma_f32_16x16x32_bf16 v[76:79],a[132:135],a[92:95],v[76:79]\n"
"v_mfma_f32_16x16x32_bf16 v[80:83],a[136:139],a[88:91],v[80:83]\n"
"ds_read_b64_tr_b16 a[200:201],v241 offset:64\n"
"ds_read_b64_tr_b16 a[202:203],v241 offset:4672\n"
"v_mfma_f32_16x16x32_bf16 v[84:87],a[136:139],a[92:95],v[84:87]\n"
"s_waitcnt lgkmcnt(12)\n"
"v_mfma_f32_16x16x32_bf16 v[88:91],a[140:143],a[88:91],v[88:91]\n"
"ds_read_b64_tr_b16 a[204:205],v241 offset:96\n"
"ds_read_b64_tr_b16 a[206:207],v241 offset:4704\n"
"v_mfma_f32_16x16x32_bf16 v[92:95],a[140:143],a[92:95],v[92:95]\n"
"buffer_load_dwordx4 a[36:39],v9,s[44:47],s73 offen offset:1024 sc1\n"
"v_mfma_f32_16x16x32_bf16 v[96:99],a[144:147],a[88:91],v[96:99]\n"
"ds_read_b64_tr_b16 a[208:209],v241 offset:128\n"
"ds_read_b64_tr_b16 a[210:211],v241 offset:4736\n"
"v_mfma_f32_16x16x32_bf16 v[100:103],a[144:147],a[92:95],v[100:103]\n"
"v_mfma_f32_16x16x32_bf16 v[104:107],a[148:151],a[88:91],v[104:107]\n"
"ds_read_b64_tr_b16 a[212:213],v241 offset:160\n"
"ds_read_b64_tr_b16 a[214:215],v241 offset:4768\n"
"v_mfma_f32_16x16x32_bf16 v[108:111],a[148:151],a[92:95],v[108:111]\n"
"buffer_load_dwordx4 a[40:43],v9,s[44:47],s73 offen offset:2048 sc1\n"
"s_waitcnt lgkmcnt(12)\n"
"v_mfma_f32_16x16x32_bf16 v[112:115],a[152:155],a[88:91],v[112:115]\n"
"ds_read_b64_tr_b16 a[216:217],v241 offset:192\n"
"ds_read_b64_tr_b16 a[218:219],v241 offset:4800\n"
"v_mfma_f32_16x16x32_bf16 v[116:119],a[152:155],a[92:95],v[116:119]\n"
"v_mfma_f32_16x16x32_bf16 v[120:123],a[156:159],a[88:91],v[120:123]\n"
"ds_read_b64_tr_b16 a[220:221],v241 offset:224\n"
"ds_read_b64_tr_b16 a[222:223],v241 offset:4832\n"
"v_mfma_f32_16x16x32_bf16 v[124:127],a[156:159],a[92:95],v[124:127]\n"
"buffer_load_dwordx4 a[44:47],v9,s[44:47],s73 offen offset:3072 sc1\n"
"v_mfma_f32_16x16x32_bf16 v[128:131],a[160:163],a[88:91],v[128:131]\n"
"ds_read_b64_tr_b16 a[224:225],v241 offset:256\n"
"ds_read_b64_tr_b16 a[226:227],v241 offset:4864\n"
"v_mfma_f32_16x16x32_bf16 v[132:135],a[160:163],a[92:95],v[132:135]\n"
"s_waitcnt lgkmcnt(12) vmcnt(19)\n"
"v_mfma_f32_16x16x32_bf16 v[64:67],a[192:195],a[112:115],v[64:67]\n"
"ds_read_b64_tr_b16 a[128:129],v241 offset:9216\n"
"ds_read_b64_tr_b16 a[130:131],v241 offset:13824\n"
"v_mfma_f32_16x16x32_bf16 v[68:71],a[192:195],a[116:119],v[68:71]\n"
"s_add_u32 m0,s65,110592\n"
"s_mul_i32 s62,s14,18\n"
"s_add_u32 s62,s62,147456\n"
"v_mfma_f32_16x16x32_bf16 v[72:75],a[196:199],a[112:115],v[72:75]\n"
"ds_read_b64_tr_b16 a[132:133],v241 offset:9248\n"
"ds_read_b64_tr_b16 a[134:135],v241 offset:13856\n"
"v_mfma_f32_16x16x32_bf16 v[76:79],a[196:199],a[116:119],v[76:79]\n"
"buffer_load_dwordx4 v246,s[56:59],s62 offen offset:0 lds\n"
"v_mfma_f32_16x16x32_bf16 v[80:83],a[200:203],a[112:115],v[80:83]\n"
"ds_read_b64_tr_b16 a[136:137],v241 offset:9280\n"
"ds_read_b64_tr_b16 a[138:139],v241 offset:13888\n"
"v_mfma_f32_16x16x32_bf16 v[84:87],a[200:203],a[116:119],v[84:87]\n"
"s_waitcnt lgkmcnt(12)\n"
"v_mfma_f32_16x16x32_bf16 v[88:91],a[204:207],a[112:115],v[88:91]\n"
"ds_read_b64_tr_b16 a[140:141],v241 offset:9312\n"
"ds_read_b64_tr_b16 a[142:143],v241 offset:13920\n"
"v_mfma_f32_16x16x32_bf16 v[92:95],a[204:207],a[116:119],v[92:95]\n"
"v_mfma_f32_16x16x32_bf16 v[96:99],a[208:211],a[112:115],v[96:99]\n"
"ds_read_b64_tr_b16 a[144:145],v241 offset:9344\n"
"ds_read_b64_tr_b16 a[146:147],v241 offset:13952\n"
"v_mfma_f32_16x16x32_bf16 v[100:103],a[208:211],a[116:119],v[100:103]\n"
"buffer_load_dwordx4 v247,s[56:59],s62 offen offset:1024 lds\n"
"v_mfma_f32_16x16x32_bf16 v[104:107],a[212:215],a[112:115],v[104:107]\n"
"ds_read_b64_tr_b16 a[148:149],v241 offset:9376\n"
"ds_read_b64_tr_b16 a[150:151],v241 offset:13984\n"
"v_mfma_f32_16x16x32_bf16 v[108:111],a[212:215],a[116:119],v[108:111]\n"
"s_waitcnt lgkmcnt(12)\n"
"v_mfma_f32_16x16x32_bf16 v[112:115],a[216:219],a[112:115],v[112:115]\n"
"ds_read_b64_tr_b16 a[152:153],v241 offset:9408\n"
"ds_read_b64_tr_b16 a[154:155],v241 offset:14016\n"
"v_mfma_f32_16x16x32_bf16 v[116:119],a[216:219],a[116:119],v[116:119]\n"
"v_mfma_f32_16x16x32_bf16 v[120:123],a[220:223],a[112:115],v[120:123]\n"
"ds_read_b64_tr_b16 a[156:157],v241 offset:9440\n"
"ds_read_b64_tr_b16 a[158:159],v241 offset:14048\n"
"v_mfma_f32_16x16x32_bf16 v[124:127],a[220:223],a[116:119],v[124:127]\n"
"s_add_u32 m0,s65,118784\n"
"v_mfma_f32_16x16x32_bf16 v[128:131],a[224:227],a[112:115],v[128:131]\n"
"ds_read_b64_tr_b16 a[160:161],v241 offset:9472\n"
"ds_read_b64_tr_b16 a[162:163],v241 offset:14080\n"
"v_mfma_f32_16x16x32_bf16 v[132:135],a[224:227],a[116:119],v[132:135]\n"
"buffer_load_dwordx4 v248,s[56:59],s62 offen offset:0 lds\n"
"s_waitcnt lgkmcnt(12) vmcnt(20)\n"
"v_mfma_f32_16x16x32_bf16 v[64:67],a[128:131],a[120:123],v[64:67]\n"
"v_mfma_f32_16x16x32_bf16 v[68:71],a[128:131],a[124:127],v[68:71]\n"
"v_mfma_f32_16x16x32_bf16 v[72:75],a[132:135],a[120:123],v[72:75]\n"
"v_mfma_f32_16x16x32_bf16 v[76:79],a[132:135],a[124:127],v[76:79]\n"
"s_waitcnt vmcnt(11) lgkmcnt(0)\n"
"s_barrier\n"
"v_mfma_f32_16x16x32_bf16 v[80:83],a[136:139],a[120:123],v[80:83]\n"
"ds_read_b64_tr_b16 a[192:193],v241 offset:18432\n"
"ds_read_b64_tr_b16 a[194:195],v241 offset:23040\n"
"v_mfma_f32_16x16x32_bf16 v[84:87],a[136:139],a[124:127],v[84:87]\n"
"ds_read_b64_tr_b16 a[196:197],v241 offset:18464\n"
"ds_read_b64_tr_b16 a[198:199],v241 offset:23072\n"
"v_mfma_f32_16x16x32_bf16 v[88:91],a[140:143],a[120:123],v[88:91]\n"
"ds_read_b64_tr_b16 a[200:201],v241 offset:18496\n"
"ds_read_b64_tr_b16 a[202:203],v241 offset:23104\n"
"v_mfma_f32_16x16x32_bf16 v[92:95],a[140:143],a[124:127],v[92:95]\n"
"buffer_load_dwordx4 v249,s[56:59],s62 offen offset:1024 lds\n"
"v_mfma_f32_16x16x32_bf16 v[96:99],a[144:147],a[120:123],v[96:99]\n"
"ds_read_b64_tr_b16 a[204:205],v241 offset:18528\n"
"ds_read_b64_tr_b16 a[206:207],v241 offset:23136\n"
"v_mfma_f32_16x16x32_bf16 v[100:103],a[144:147],a[124:127],v[100:103]\n"
"v_mfma_f32_16x16x32_bf16 v[104:107],a[148:151],a[120:123],v[104:107]\n"
"ds_read_b64_tr_b16 a[208:209],v241 offset:18560\n"
"ds_read_b64_tr_b16 a[210:211],v241 offset:23168\n"
"v_mfma_f32_16x16x32_bf16 v[108:111],a[148:151],a[124:127],v[108:111]\n"
"v_mfma_f32_16x16x32_bf16 v[112:115],a[152:155],a[120:123],v[112:115]\n"
"ds_read_b64_tr_b16 a[212:213],v241 offset:18592\n"
"ds_read_b64_tr_b16 a[214:215],v241 offset:23200\n"
"v_mfma_f32_16x16x32_bf16 v[116:119],a[152:155],a[124:127],v[116:119]\n"
"ds_read_b64_tr_b16 a[216:217],v241 offset:18624\n"
"ds_read_b64_tr_b16 a[218:219],v241 offset:23232\n"
"s_add_u32 m0,s65,126976\n"
"v_mfma_f32_16x16x32_bf16 v[120:123],a[156:159],a[120:123],v[120:123]\n"
"ds_read_b64_tr_b16 a[220:221],v241 offset:18656\n"
"ds_read_b64_tr_b16 a[222:223],v241 offset:23264\n"
"v_mfma_f32_16x16x32_bf16 v[124:127],a[156:159],a[124:127],v[124:127]\n"
"buffer_load_dwordx4 v250,s[56:59],s62 offen offset:0 lds\n"
"v_mfma_f32_16x16x32_bf16 v[128:131],a[160:163],a[120:123],v[128:131]\n"
"ds_read_b64_tr_b16 a[224:225],v241 offset:18688\n"
"ds_read_b64_tr_b16 a[226:227],v241 offset:23296\n"
"v_mfma_f32_16x16x32_bf16 v[132:135],a[160:163],a[124:127],v[132:135]\n"
"s_waitcnt lgkmcnt(12) vmcnt(11)\n"
"v_mfma_f32_16x16x32_bf16 v[64:67],a[192:195],a[0:3],v[64:67]\n"
"ds_read_b64_tr_b16 a[128:129],v241 offset:27648\n"
"ds_read_b64_tr_b16 a[130:131],v241 offset:32256\n"
"v_mfma_f32_16x16x32_bf16 v[68:71],a[192:195],a[4:7],v[68:71]\n"
"buffer_load_dwordx4 v251,s[56:59],s62 offen offset:1024 lds\n"
"v_mfma_f32_16x16x32_bf16 v[72:75],a[196:199],a[0:3],v[72:75]\n"
"ds_read_b64_tr_b16 a[132:133],v241 offset:27680\n"
"ds_read_b64_tr_b16 a[134:135],v241 offset:32288\n"
"v_mfma_f32_16x16x32_bf16 v[76:79],a[196:199],a[4:7],v[76:79]\n"
"v_mfma_f32_16x16x32_bf16 v[80:83],a[200:203],a[0:3],v[80:83]\n"
"ds_read_b64_tr_b16 a[136:137],v241 offset:27712\n"
"ds_read_b64_tr_b16 a[138:139],v241 offset:32320\n"
"v_mfma_f32_16x16x32_bf16 v[84:87],a[200:203],a[4:7],v[84:87]\n"
"s_waitcnt lgkmcnt(12)\n"
"v_mfma_f32_16x16x32_bf16 v[88:91],a[204:207],a[0:3],v[88:91]\n"
"ds_read_b64_tr_b16 a[140:141],v241 offset:27744\n"
"ds_read_b64_tr_b16 a[142:143],v241 offset:32352\n"
"v_mfma_f32_16x16x32_bf16 v[92:95],a[204:207],a[4:7],v[92:95]\n"
"s_add_u32 m0,s65,135168\n"
"v_mfma_f32_16x16x32_bf16 v[96:99],a[208:211],a[0:3],v[96:99]\n"
"ds_read_b64_tr_b16 a[144:145],v241 offset:27776\n"
"ds_read_b64_tr_b16 a[146:147],v241 offset:32384\n"
"v_mfma_f32_16x16x32_bf16 v[100:103],a[208:211],a[4:7],v[100:103]\n"
"buffer_load_dwordx4 v252,s[56:59],s62 offen offset:0 lds\n"
"v_mfma_f32_16x16x32_bf16 v[104:107],a[212:215],a[0:3],v[104:107]\n"
"ds_read_b64_tr_b16 a[148:149],v241 offset:27808\n"
"ds_read_b64_tr_b16 a[150:151],v241 offset:32416\n"
"v_mfma_f32_16x16x32_bf16 v[108:111],a[212:215],a[4:7],v[108:111]\n"
"s_waitcnt lgkmcnt(12)\n"
"v_mfma_f32_16x16x32_bf16 v[112:115],a[216:219],a[0:3],v[112:115]\n"
"ds_read_b64_tr_b16 a[152:153],v241 offset:27840\n"
"ds_read_b64_tr_b16 a[154:155],v241 offset:32448\n"
"v_mfma_f32_16x16x32_bf16 v[116:119],a[216:219],a[4:7],v[116:119]\n"
"v_mfma_f32_16x16x32_bf16 v[120:123],a[220:223],a[0:3],v[120:123]\n"
"ds_read_b64_tr_b16 a[156:157],v241 offset:27872\n"
"ds_read_b64_tr_b16 a[158:159],v241 offset:32480\n"
"v_mfma_f32_16x16x32_bf16 v[124:127],a[220:223],a[4:7],v[124:127]\n"
"buffer_load_dwordx4 v253,s[56:59],s62 offen offset:1024 lds\n"
"v_mfma_f32_16x16x32_bf16 v[128:131],a[224:227],a[0:3],v[128:131]\n"
"ds_read_b64_tr_b16 a[160:161],v241 offset:27904\n"
"ds_read_b64_tr_b16 a[162:163],v241 offset:32512\n"
"v_mfma_f32_16x16x32_bf16 v[132:135],a[224:227],a[4:7],v[132:135]\n"
"s_waitcnt lgkmcnt(12) vmcnt(12)\n"
"v_mfma_f32_16x16x32_bf16 v[64:67],a[128:131],a[8:11],v[64:67]\n"
"ds_read_b64_tr_b16 a[192:193],v241 offset:36864\n"
"ds_read_b64_tr_b16 a[194:195],v241 offset:41472\n"
"v_mfma_f32_16x16x32_bf16 v[68:71],a[128:131],a[12:15],v[68:71]\n"
"v_mfma_f32_16x16x32_bf16 v[72:75],a[132:135],a[8:11],v[72:75]\n"
"ds_read_b64_tr_b16 a[196:197],v241 offset:36896\n"
"ds_read_b64_tr_b16 a[198:199],v241 offset:41504\n"
"v_mfma_f32_16x16x32_bf16 v[76:79],a[132:135],a[12:15],v[76:79]\n"
"s_add_u32 m0,s61,143360\n"
"v_mfma_f32_16x16x32_bf16 v[80:83],a[136:139],a[8:11],v[80:83]\n"
"ds_read_b64_tr_b16 a[200:201],v241 offset:36928\n"
"ds_read_b64_tr_b16 a[202:203],v241 offset:41536\n"
"v_mfma_f32_16x16x32_bf16 v[84:87],a[136:139],a[12:15],v[84:87]\n"
"buffer_load_dwordx4 v254,s[56:59],s62 offen offset:0 lds\n"
"s_waitcnt lgkmcnt(12)\n"
"v_mfma_f32_16x16x32_bf16 v[88:91],a[140:143],a[8:11],v[88:91]\n"
"ds_read_b64_tr_b16 a[204:205],v241 offset:36960\n"
"ds_read_b64_tr_b16 a[206:207],v241 offset:41568\n"
"v_mfma_f32_16x16x32_bf16 v[92:95],a[140:143],a[12:15],v[92:95]\n"
"v_mfma_f32_16x16x32_bf16 v[96:99],a[144:147],a[8:11],v[96:99]\n"
"ds_read_b64_tr_b16 a[208:209],v241 offset:36992\n"
"ds_read_b64_tr_b16 a[210:211],v241 offset:41600\n"
"v_mfma_f32_16x16x32_bf16 v[100:103],a[144:147],a[12:15],v[100:103]\n"
"s_add_u32 s73,s14,8192\n"
"v_mfma_f32_16x16x32_bf16 v[104:107],a[148:151],a[8:11],v[104:107]\n"
"ds_read_b64_tr_b16 a[212:213],v241 offset:37024\n"
"ds_read_b64_tr_b16 a[214:215],v241 offset:41632\n"
"v_mfma_f32_16x16x32_bf16 v[108:111],a[148:151],a[12:15],v[108:111]\n"
"buffer_load_dwordx4 a[64:67],v9,s[44:47],s73 offen offset:0 sc1\n"
"s_waitcnt lgkmcnt(12)\n"
"v_mfma_f32_16x16x32_bf16 v[112:115],a[152:155],a[8:11],v[112:115]\n"
"ds_read_b64_tr_b16 a[216:217],v241 offset:37056\n"
"ds_read_b64_tr_b16 a[218:219],v241 offset:41664\n"
"v_mfma_f32_16x16x32_bf16 v[116:119],a[152:155],a[12:15],v[116:119]\n"
"v_mfma_f32_16x16x32_bf16 v[120:123],a[156:159],a[8:11],v[120:123]\n"
"ds_read_b64_tr_b16 a[220:221],v241 offset:37088\n"
"ds_read_b64_tr_b16 a[222:223],v241 offset:41696\n"
"v_mfma_f32_16x16x32_bf16 v[124:127],a[156:159],a[12:15],v[124:127]\n"
"v_mfma_f32_16x16x32_bf16 v[128:131],a[160:163],a[8:11],v[128:131]\n"
"ds_read_b64_tr_b16 a[224:225],v241 offset:37120\n"
"ds_read_b64_tr_b16 a[226:227],v241 offset:41728\n"
"v_mfma_f32_16x16x32_bf16 v[132:135],a[160:163],a[12:15],v[132:135]\n"
"buffer_load_dwordx4 a[68:71],v9,s[44:47],s73 offen offset:1024 sc1\n"
"s_waitcnt lgkmcnt(12) vmcnt(13)\n"
"v_mfma_f32_16x16x32_bf16 v[64:67],a[192:195],a[32:35],v[64:67]\n"
"ds_read_b64_tr_b16 a[128:129],v241 offset:46080\n"
"ds_read_b64_tr_b16 a[130:131],v241 offset:50688\n"
"v_mfma_f32_16x16x32_bf16 v[68:71],a[192:195],a[36:39],v[68:71]\n"
"v_mfma_f32_16x16x32_bf16 v[72:75],a[196:199],a[32:35],v[72:75]\n"
"ds_read_b64_tr_b16 a[132:133],v241 offset:46112\n"
"ds_read_b64_tr_b16 a[134:135],v241 offset:50720\n"
"v_mfma_f32_16x16x32_bf16 v[76:79],a[196:199],a[36:39],v[76:79]\n"
"buffer_load_dwordx4 a[72:75],v9,s[44:47],s73 offen offset:2048 sc1\n"
"v_mfma_f32_16x16x32_bf16 v[80:83],a[200:203],a[32:35],v[80:83]\n"
"ds_read_b64_tr_b16 a[136:137],v241 offset:46144\n"
"ds_read_b64_tr_b16 a[138:139],v241 offset:50752\n"
"v_mfma_f32_16x16x32_bf16 v[84:87],a[200:203],a[36:39],v[84:87]\n"
"s_waitcnt lgkmcnt(12)\n"
"v_mfma_f32_16x16x32_bf16 v[88:91],a[204:207],a[32:35],v[88:91]\n"
"ds_read_b64_tr_b16 a[140:141],v241 offset:46176\n"
"ds_read_b64_tr_b16 a[142:143],v241 offset:50784\n"
"v_mfma_f32_16x16x32_bf16 v[92:95],a[204:207],a[36:39],v[92:95]\n"
"buffer_load_dwordx4 a[76:79],v9,s[44:47],s73 offen offset:3072 sc1\n"
"v_mfma_f32_16x16x32_bf16 v[96:99],a[208:211],a[32:35],v[96:99]\n"
"ds_read_b64_tr_b16 a[144:145],v241 offset:46208\n"
"ds_read_b64_tr_b16 a[146:147],v241 offset:50816\n"
"v_mfma_f32_16x16x32_bf16 v[100:103],a[208:211],a[36:39],v[100:103]\n"
"v_mfma_f32_16x16x32_bf16 v[104:107],a[212:215],a[32:35],v[104:107]\n"
"ds_read_b64_tr_b16 a[148:149],v241 offset:46240\n"
"ds_read_b64_tr_b16 a[150:151],v241 offset:50848\n"
"v_mfma_f32_16x16x32_bf16 v[108:111],a[212:215],a[36:39],v[108:111]\n"
"s_add_u32 s73,s14,12288\n"
"s_waitcnt lgkmcnt(12)\n"
"v_mfma_f32_16x16x32_bf16 v[112:115],a[216:219],a[32:35],v[112:115]\n"
"ds_read_b64_tr_b16 a[152:153],v241 offset:46272\n"
"ds_read_b64_tr_b16 a[154:155],v241 offset:50880\n"
"v_mfma_f32_16x16x32_bf16 v[116:119],a[216:219],a[36:39],v[116:119]\n"
"buffer_load_dwordx4 a[96:99],v9,s[44:47],s73 offen offset:0 sc1\n"
"v_mfma_f32_16x16x32_bf16 v[120:123],a[220:223],a[32:35],v[120:123]\n"
"ds_read_b64_tr_b16 a[156:157],v241 offset:46304\n"
"ds_read_b64_tr_b16 a[158:159],v241 offset:50912\n"
"v_mfma_f32_16x16x32_bf16 v[124:127],a[220:223],a[36:39],v[124:127]\n"
"v_mfma_f32_16x16x32_bf16 v[128:131],a[224:227],a[32:35],v[128:131]\n"
"ds_read_b64_tr_b16 a[160:161],v241 offset:46336\n"
"ds_read_b64_tr_b16 a[162:163],v241 offset:50944\n"
"v_mfma_f32_16x16x32_bf16 v[132:135],a[224:227],a[36:39],v[132:135]\n"
"buffer_load_dwordx4 a[100:103],v9,s[44:47],s73 offen offset:1024 sc1\n"
"s_waitcnt lgkmcnt(12) vmcnt(15)\n"
"v_mfma_f32_16x16x32_bf16 v[64:67],a[128:131],a[40:43],v[64:67]\n"
"v_mfma_f32_16x16x32_bf16 v[68:71],a[128:131],a[44:47],v[68:71]\n"
"v_mfma_f32_16x16x32_bf16 v[72:75],a[132:135],a[40:43],v[72:75]\n"
"v_mfma_f32_16x16x32_bf16 v[76:79],a[132:135],a[44:47],v[76:79]\n"
"s_waitcnt vmcnt(6) lgkmcnt(0)\n"
"s_barrier\n"
"v_mfma_f32_16x16x32_bf16 v[80:83],a[136:139],a[40:43],v[80:83]\n"
"ds_read_b64_tr_b16 a[192:193],v242 offset:0\n"
"ds_read_b64_tr_b16 a[194:195],v242 offset:4608\n"
"v_mfma_f32_16x16x32_bf16 v[84:87],a[136:139],a[44:47],v[84:87]\n"
"ds_read_b64_tr_b16 a[196:197],v242 offset:32\n"
"ds_read_b64_tr_b16 a[198:199],v242 offset:4640\n"
"v_mfma_f32_16x16x32_bf16 v[88:91],a[140:143],a[40:43],v[88:91]\n"
"ds_read_b64_tr_b16 a[200:201],v242 offset:9216\n"
"ds_read_b64_tr_b16 a[202:203],v242 offset:13824\n"
"v_mfma_f32_16x16x32_bf16 v[92:95],a[140:143],a[44:47],v[92:95]\n"
"buffer_load_dwordx4 a[104:107],v9,s[44:47],s73 offen offset:2048 sc1\n"
"v_mfma_f32_16x16x32_bf16 v[96:99],a[144:147],a[40:43],v[96:99]\n"
"ds_read_b64_tr_b16 a[204:205],v242 offset:9248\n"
"ds_read_b64_tr_b16 a[206:207],v242 offset:13856\n"
"v_mfma_f32_16x16x32_bf16 v[100:103],a[144:147],a[44:47],v[100:103]\n"
"buffer_load_dwordx4 a[108:111],v9,s[44:47],s73 offen offset:3072 sc1\n"
"v_mfma_f32_16x16x32_bf16 v[104:107],a[148:151],a[40:43],v[104:107]\n"
"ds_read_b64_tr_b16 a[208:209],v242 offset:18432\n"
"ds_read_b64_tr_b16 a[210:211],v242 offset:23040\n"
"v_mfma_f32_16x16x32_bf16 v[108:111],a[148:151],a[44:47],v[108:111]\n"
"v_mfma_f32_16x16x32_bf16 v[112:115],a[152:155],a[40:43],v[112:115]\n"
"ds_read_b64_tr_b16 a[212:213],v242 offset:18464\n"
"ds_read_b64_tr_b16 a[214:215],v242 offset:23072\n"
"v_mfma_f32_16x16x32_bf16 v[116:119],a[152:155],a[44:47],v[116:119]\n"
"ds_read_b64_tr_b16 a[216:217],v242 offset:27648\n"
"ds_read_b64_tr_b16 a[218:219],v242 offset:32256\n"
"v_mfma_f32_16x16x32_bf16 v[120:123],a[156:159],a[40:43],v[120:123]\n"
"ds_read_b64_tr_b16 a[220:221],v242 offset:27680\n"
"ds_read_b64_tr_b16 a[222:223],v242 offset:32288\n"
"v_mfma_f32_16x16x32_bf16 v[124:127],a[156:159],a[44:47],v[124:127]\n"
"v_mfma_f32_16x16x32_bf16 v[128:131],a[160:163],a[40:43],v[128:131]\n"
"ds_read_b64_tr_b16 a[224:225],v242 offset:64\n"
"ds_read_b64_tr_b16 a[226:227],v242 offset:4672\n"
"v_mfma_f32_16x16x32_bf16 v[132:135],a[160:163],a[44:47],v[132:135]\n"
"s_waitcnt lgkmcnt(12) vmcnt(4)\n"
"v_mfma_f32_16x16x32_bf16 v[64:67],a[192:195],a[64:67],v[64:67]\n"
"ds_read_b64_tr_b16 a[128:129],v242 offset:96\n"
"ds_read_b64_tr_b16 a[130:131],v242 offset:4704\n"
"v_mfma_f32_16x16x32_bf16 v[68:71],a[192:195],a[68:71],v[68:71]\n"
"v_mfma_f32_16x16x32_bf16 v[72:75],a[196:199],a[64:67],v[72:75]\n"
"ds_read_b64_tr_b16 a[132:133],v242 offset:9280\n"
"ds_read_b64_tr_b16 a[134:135],v242 offset:13888\n"
"v_mfma_f32_16x16x32_bf16 v[76:79],a[196:199],a[68:71],v[76:79]\n"
"v_mfma_f32_16x16x32_bf16 v[64:67],a[200:203],a[72:75],v[64:67]\n"
"ds_read_b64_tr_b16 a[136:137],v242 offset:9312\n"
"ds_read_b64_tr_b16 a[138:139],v242 offset:13920\n"
"v_mfma_f32_16x16x32_bf16 v[68:71],a[200:203],a[76:79],v[68:71]\n"
"s_waitcnt lgkmcnt(12) vmcnt(2)\n"
"v_mfma_f32_16x16x32_bf16 v[72:75],a[204:207],a[72:75],v[72:75]\n"
"ds_read_b64_tr_b16 a[140:141],v242 offset:18496\n"
"ds_read_b64_tr_b16 a[142:143],v242 offset:23104\n"
"v_mfma_f32_16x16x32_bf16 v[76:79],a[204:207],a[76:79],v[76:79]\n"
"v_mfma_f32_16x16x32_bf16 v[64:67],a[208:211],a[96:99],v[64:67]\n"
"ds_read_b64_tr_b16 a[144:145],v242 offset:18528\n"
"ds_read_b64_tr_b16 a[146:147],v242 offset:23136\n"
"v_mfma_f32_16x16x32_bf16 v[68:71],a[208:211],a[100:103],v[68:71]\n"
"v_mfma_f32_16x16x32_bf16 v[72:75],a[212:215],a[96:99],v[72:75]\n"
"ds_read_b64_tr_b16 a[148:149],v242 offset:27712\n"
"ds_read_b64_tr_b16 a[150:151],v242 offset:32320\n"
"v_mfma_f32_16x16x32_bf16 v[76:79],a[212:215],a[100:103],v[76:79]\n"
"s_waitcnt lgkmcnt(12) vmcnt(0)\n"
"v_mfma_f32_16x16x32_bf16 v[64:67],a[216:219],a[104:107],v[64:67]\n"
"ds_read_b64_tr_b16 a[152:153],v242 offset:27744\n"
"ds_read_b64_tr_b16 a[154:155],v242 offset:32352\n"
"v_mfma_f32_16x16x32_bf16 v[68:71],a[216:219],a[108:111],v[68:71]\n"
"v_mfma_f32_16x16x32_bf16 v[72:75],a[220:223],a[104:107],v[72:75]\n"
"ds_read_b64_tr_b16 a[156:157],v242 offset:128\n"
"ds_read_b64_tr_b16 a[158:159],v242 offset:4736\n"
"v_mfma_f32_16x16x32_bf16 v[76:79],a[220:223],a[108:111],v[76:79]\n"
"v_mfma_f32_16x16x32_bf16 v[80:83],a[224:227],a[64:67],v[80:83]\n"
"ds_read_b64_tr_b16 a[160:161],v242 offset:160\n"
"ds_read_b64_tr_b16 a[162:163],v242 offset:4768\n"
"v_mfma_f32_16x16x32_bf16 v[84:87],a[224:227],a[68:71],v[84:87]\n"
"s_waitcnt lgkmcnt(12)\n"
"v_mfma_f32_16x16x32_bf16 v[88:91],a[128:131],a[64:67],v[88:91]\n"
"ds_read_b64_tr_b16 a[192:193],v242 offset:9344\n"
"ds_read_b64_tr_b16 a[194:195],v242 offset:13952\n"
"v_mfma_f32_16x16x32_bf16 v[92:95],a[128:131],a[68:71],v[92:95]\n"
"v_mfma_f32_16x16x32_bf16 v[80:83],a[132:135],a[72:75],v[80:83]\n"
"ds_read_b64_tr_b16 a[196:197],v242 offset:9376\n"
"ds_read_b64_tr_b16 a[198:199],v242 offset:13984\n"
"v_cvt_pk_bf16_f32 v16,v64,v65\n"
"v_mfma_f32_16x16x32_bf16 v[84:87],a[132:135],a[76:79],v[84:87]\n"
"v_cvt_pk_bf16_f32 v17,v66,v67\n"
"v_cvt_pk_bf16_f32 v18,v72,v73\n"
"v_cvt_pk_bf16_f32 v19,v74,v75\n"
"v_mfma_f32_16x16x32_bf16 v[88:91],a[136:139],a[72:75],v[88:91]\n"
"ds_read_b64_tr_b16 a[200:201],v242 offset:18560\n"
"ds_read_b64_tr_b16 a[202:203],v242 offset:23168\n"
"s_add_u32 s76,s77,0\n"
"v_mfma_f32_16x16x32_bf16 v[92:95],a[136:139],a[76:79],v[92:95]\n"
"buffer_store_dwordx4 v[16:19],v238,s[88:91],s76 offen offset:0 sc1\n"
"v_cvt_pk_bf16_f32 v20,v68,v69\n"
"v_cvt_pk_bf16_f32 v21,v70,v71\n"
"s_waitcnt lgkmcnt(12)\n"
"v_mfma_f32_16x16x32_bf16 v[80:83],a[140:143],a[96:99],v[80:83]\n"
"ds_read_b64_tr_b16 a[204:205],v242 offset:18592\n"
"ds_read_b64_tr_b16 a[206:207],v242 offset:23200\n"
"v_cvt_pk_bf16_f32 v22,v76,v77\n"
"v_mfma_f32_16x16x32_bf16 v[84:87],a[140:143],a[100:103],v[84:87]\n"
"v_cvt_pk_bf16_f32 v23,v78,v79\n"
"L_pgg208_%=:\n"
"s_cbranch_execz L_hrg_%=\n"
"s_add_u32 s76,s77,73728\n"
"buffer_store_dwordx4 v[20:23],v238,s[88:91],s76 offen offset:0 sc1\n"
"v_mfma_f32_16x16x32_bf16 v[88:91],a[144:147],a[96:99],v[88:91]\n"
"ds_read_b64_tr_b16 a[208:209],v242 offset:27776\n"
"ds_read_b64_tr_b16 a[210:211],v242 offset:32384\n"
"v_mfma_f32_16x16x32_bf16 v[92:95],a[144:147],a[100:103],v[92:95]\n"
"v_mfma_f32_16x16x32_bf16 v[80:83],a[148:151],a[104:107],v[80:83]\n"
"ds_read_b64_tr_b16 a[212:213],v242 offset:27808\n"
"ds_read_b64_tr_b16 a[214:215],v242 offset:32416\n"
"v_mfma_f32_16x16x32_bf16 v[84:87],a[148:151],a[108:111],v[84:87]\n"
"s_waitcnt lgkmcnt(12)\n"
"v_mfma_f32_16x16x32_bf16 v[88:91],a[152:155],a[104:107],v[88:91]\n"
"ds_read_b64_tr_b16 a[216:217],v242 offset:192\n"
"ds_read_b64_tr_b16 a[218:219],v242 offset:4800\n"
"v_mfma_f32_16x16x32_bf16 v[92:95],a[152:155],a[108:111],v[92:95]\n"
"v_mfma_f32_16x16x32_bf16 v[96:99],a[156:159],a[64:67],v[96:99]\n"
"ds_read_b64_tr_b16 a[220:221],v242 offset:224\n"
"ds_read_b64_tr_b16 a[222:223],v242 offset:4832\n"
"v_mfma_f32_16x16x32_bf16 v[100:103],a[156:159],a[68:71],v[100:103]\n"
"v_mfma_f32_16x16x32_bf16 v[104:107],a[160:163],a[64:67],v[104:107]\n"
"ds_read_b64_tr_b16 a[224:225],v242 offset:9408\n"
"ds_read_b64_tr_b16 a[226:227],v242 offset:14016\n"
"v_mfma_f32_16x16x32_bf16 v[108:111],a[160:163],a[68:71],v[108:111]\n"
"s_waitcnt lgkmcnt(12)\n"
"v_mfma_f32_16x16x32_bf16 v[96:99],a[192:195],a[72:75],v[96:99]\n"
"ds_read_b64_tr_b16 a[128:129],v242 offset:9440\n"
"ds_read_b64_tr_b16 a[130:131],v242 offset:14048\n"
"v_cvt_pk_bf16_f32 v24,v80,v81\n"
"v_mfma_f32_16x16x32_bf16 v[100:103],a[192:195],a[76:79],v[100:103]\n"
"v_cvt_pk_bf16_f32 v25,v82,v83\n"
"v_cvt_pk_bf16_f32 v26,v88,v89\n"
"v_cvt_pk_bf16_f32 v27,v90,v91\n"
"v_mfma_f32_16x16x32_bf16 v[104:107],a[196:199],a[72:75],v[104:107]\n"
"ds_read_b64_tr_b16 a[132:133],v242 offset:18624\n"
"ds_read_b64_tr_b16 a[134:135],v242 offset:23232\n"
"s_add_u32 s76,s77,0\n"
"v_mfma_f32_16x16x32_bf16 v[108:111],a[196:199],a[76:79],v[108:111]\n"
"buffer_store_dwordx4 v[24:27],v238,s[88:91],s76 offen offset:512 sc1\n"
"v_cvt_pk_bf16_f32 v28,v84,v85\n"
"v_cvt_pk_bf16_f32 v29,v86,v87\n"
"v_mfma_f32_16x16x32_bf16 v[96:99],a[200:203],a[96:99],v[96:99]\n"
"ds_read_b64_tr_b16 a[136:137],v242 offset:18656\n"
"ds_read_b64_tr_b16 a[138:139],v242 offset:23264\n"
"v_cvt_pk_bf16_f32 v30,v92,v93\n"
"v_mfma_f32_16x16x32_bf16 v[100:103],a[200:203],a[100:103],v[100:103]\n"
"v_cvt_pk_bf16_f32 v31,v94,v95\n"
"s_add_u32 s76,s77,73728\n"
"buffer_store_dwordx4 v[28:31],v238,s[88:91],s76 offen offset:512 sc1\n"
"s_waitcnt lgkmcnt(12)\n"
"v_mfma_f32_16x16x32_bf16 v[104:107],a[204:207],a[96:99],v[104:107]\n"
"ds_read_b64_tr_b16 a[140:141],v242 offset:27840\n"
"ds_read_b64_tr_b16 a[142:143],v242 offset:32448\n"
"v_mfma_f32_16x16x32_bf16 v[108:111],a[204:207],a[100:103],v[108:111]\n"
"v_mfma_f32_16x16x32_bf16 v[96:99],a[208:211],a[104:107],v[96:99]\n"
"ds_read_b64_tr_b16 a[144:145],v242 offset:27872\n"
"ds_read_b64_tr_b16 a[146:147],v242 offset:32480\n"
"v_mfma_f32_16x16x32_bf16 v[100:103],a[208:211],a[108:111],v[100:103]\n"
"v_mfma_f32_16x16x32_bf16 v[104:107],a[212:215],a[104:107],v[104:107]\n"
"ds_read_b64_tr_b16 a[148:149],v242 offset:256\n"
"ds_read_b64_tr_b16 a[150:151],v242 offset:4864\n"
"v_mfma_f32_16x16x32_bf16 v[108:111],a[212:215],a[108:111],v[108:111]\n"
"s_waitcnt lgkmcnt(12)\n"
"v_mfma_f32_16x16x32_bf16 v[112:115],a[216:219],a[64:67],v[112:115]\n"
"ds_read_b64_tr_b16 a[152:153],v242 offset:9472\n"
"ds_read_b64_tr_b16 a[154:155],v242 offset:14080\n"
"v_mfma_f32_16x16x32_bf16 v[116:119],a[216:219],a[68:71],v[116:119]\n"
"v_mfma_f32_16x16x32_bf16 v[120:123],a[220:223],a[64:67],v[120:123]\n"
"ds_read_b64_tr_b16 a[156:157],v242 offset:18688\n"
"ds_read_b64_tr_b16 a[158:159],v242 offset:23296\n"
"v_mfma_f32_16x16x32_bf16 v[124:127],a[220:223],a[68:71],v[124:127]\n"
"v_mfma_f32_16x16x32_bf16 v[112:115],a[224:227],a[72:75],v[112:115]\n"
"ds_read_b64_tr_b16 a[160:161],v242 offset:27904\n"
"ds_read_b64_tr_b16 a[162:163],v242 offset:32512\n"
"v_cvt_pk_bf16_f32 v32,v96,v97\n"
"v_mfma_f32_16x16x32_bf16 v[116:119],a[224:227],a[76:79],v[116:119]\n"
"v_cvt_pk_bf16_f32 v33,v98,v99\n"
"v_cvt_pk_bf16_f32 v34,v104,v105\n"
"v_cvt_pk_bf16_f32 v35,v106,v107\n"
"s_waitcnt lgkmcnt(12)\n"
"v_mfma_f32_16x16x32_bf16 v[120:123],a[128:131],a[72:75],v[120:123]\n"
"s_add_u32 s76,s77,4096\n"
"buffer_store_dwordx4 v[32:35],v238,s[88:91],s76 offen offset:0 sc1\n"
"v_cvt_pk_bf16_f32 v36,v100,v101\n"
"v_mfma_f32_16x16x32_bf16 v[124:127],a[128:131],a[76:79],v[124:127]\n"
"v_cvt_pk_bf16_f32 v37,v102,v103\n"
"v_cvt_pk_bf16_f32 v38,v108,v109\n"
"v_cvt_pk_bf16_f32 v39,v110,v111\n"
"v_mfma_f32_16x16x32_bf16 v[112:115],a[132:135],a[96:99],v[112:115]\n"
"s_add_u32 s76,s77,77824\n"
"buffer_store_dwordx4 v[36:39],v238,s[88:91],s76 offen offset:0 sc1\n"
"v_mfma_f32_16x16x32_bf16 v[116:119],a[132:135],a[100:103],v[116:119]\n"
"v_mfma_f32_16x16x32_bf16 v[120:123],a[136:139],a[96:99],v[120:123]\n"
"v_mfma_f32_16x16x32_bf16 v[124:127],a[136:139],a[100:103],v[124:127]\n"
"s_waitcnt lgkmcnt(6)\n"
"v_mfma_f32_16x16x32_bf16 v[112:115],a[140:143],a[104:107],v[112:115]\n"
"v_mfma_f32_16x16x32_bf16 v[116:119],a[140:143],a[108:111],v[116:119]\n"
"v_mfma_f32_16x16x32_bf16 v[120:123],a[144:147],a[104:107],v[120:123]\n"
"v_mfma_f32_16x16x32_bf16 v[124:127],a[144:147],a[108:111],v[124:127]\n"
"v_mfma_f32_16x16x32_bf16 v[128:131],a[148:151],a[64:67],v[128:131]\n"
"v_mfma_f32_16x16x32_bf16 v[132:135],a[148:151],a[68:71],v[132:135]\n"
"s_waitcnt lgkmcnt(0)\n"
"v_mfma_f32_16x16x32_bf16 v[128:131],a[152:155],a[72:75],v[128:131]\n"
"v_mfma_f32_16x16x32_bf16 v[132:135],a[152:155],a[76:79],v[132:135]\n"
"v_mfma_f32_16x16x32_bf16 v[128:131],a[156:159],a[96:99],v[128:131]\n"
"v_cvt_pk_bf16_f32 v40,v112,v113\n"
"v_cvt_pk_bf16_f32 v41,v114,v115\n"
"v_cvt_pk_bf16_f32 v42,v120,v121\n"
"v_mfma_f32_16x16x32_bf16 v[132:135],a[156:159],a[100:103],v[132:135]\n"
"v_cvt_pk_bf16_f32 v43,v122,v123\n"
"s_add_u32 s76,s77,4096\n"
"buffer_store_dwordx4 v[40:43],v238,s[88:91],s76 offen offset:512 sc1\n"
"v_mfma_f32_16x16x32_bf16 v[128:131],a[160:163],a[104:107],v[128:131]\n"
"v_cvt_pk_bf16_f32 v44,v116,v117\n"
"v_cvt_pk_bf16_f32 v45,v118,v119\n"
"v_cvt_pk_bf16_f32 v46,v124,v125\n"
"v_mfma_f32_16x16x32_bf16 v[132:135],a[160:163],a[108:111],v[132:135]\n"
"v_cvt_pk_bf16_f32 v47,v126,v127\n"
"s_add_u32 s76,s77,77824\n"
"buffer_store_dwordx4 v[44:47],v238,s[88:91],s76 offen offset:512 sc1\n"
"s_nop 7\n"
"v_cvt_pk_bf16_f32 v48,v128,v129\n"
"v_cvt_pk_bf16_f32 v49,v130,v131\n"
"v_cvt_pk_bf16_f32 v50,v132,v133\n"
"v_cvt_pk_bf16_f32 v51,v134,v135\n"
"s_nop 0\n"
"v_permlane32_swap_b32 v48,v50\n"
"v_permlane32_swap_b32 v49,v51\n"
"buffer_store_dwordx4 v[48:51],v239,s[88:91],s75 offen sc1\n"
"ds_read_b128 v[144:147],v224 offset:0\n"
"ds_read_b128 v[148:151],v224 offset:64\n"
"ds_read_b128 v[152:155],v224 offset:256\n"
"ds_read_b128 v[156:159],v224 offset:320\n"
"ds_read_b128 v[160:163],v225 offset:0\n"
"ds_read_b128 v[184:187],v224 offset:2304\n"
"ds_read_b128 v[188:191],v224 offset:2368\n"
"ds_read_b128 v[192:195],v224 offset:2560\n"
"ds_read_b128 v[196:199],v224 offset:2624\n"
"ds_read_b128 v[200:203],v225 offset:2304\n"
"ds_read_b128 v[164:167],v224 offset:4608\n"
"ds_read_b128 v[168:171],v224 offset:4672\n"
"ds_read_b128 v[172:175],v224 offset:4864\n"
"ds_read_b128 v[176:179],v224 offset:4928\n"
"ds_read_b128 v[180:183],v225 offset:4608\n"
"s_add_u32 m0,s60,0\n"
"s_add_u32 s62,s22,0\n"
"buffer_load_dwordx4 v208,s[40:43],s62 offen offset:0 lds\n"
"buffer_load_dwordx4 v209,s[40:43],s62 offen offset:1024 lds\n"
"buffer_load_dwordx4 v210,s[40:43],s62 offen offset:2048 lds\n"
"buffer_load_dwordx4 v211,s[40:43],s62 offen offset:3072 lds\n"
"s_add_u32 m0,s60,4096\n"
"s_nop 0\n"
"buffer_load_dwordx4 v212,s[40:43],s62 offen offset:0 lds\n"
"buffer_load_dwordx4 v213,s[40:43],s62 offen offset:1024 lds\n"
"buffer_load_dwordx4 v214,s[40:43],s62 offen offset:2048 lds\n"
"buffer_load_dwordx4 v215,s[40:43],s62 offen offset:3072 lds\n"
"s_add_u32 m0,s60,8192\n"
"s_nop 0\n"
"buffer_load_dwordx4 v216,s[40:43],s62 offen offset:0 lds\n"
"s_mov_b32 s77,25165824\n"
"s_mul_i32 s78,s34,589824\n"
"s_add_u32 s77,s77,s78\n"
"s_mul_i32 s78,s36,147456\n"
"s_add_u32 s77,s77,s78\n"
"s_mul_i32 s78,s35,36864\n"
"s_add_u32 s77,s77,s78\n"
"s_mul_i32 s78,s37,9216\n"
"s_add_u32 s77,s77,s78\n"
"s_add_u32 s76,s77,4096\n"
"s_add_u32 s75,s77,8192\n"
"s_mov_b32 s78,0xffff0000\n"
"s_mov_b64 exec,0\n"
"s_cmp_eq_u32 s37,1\n"
"s_cbranch_scc1 L_pgs240_%=\n"
"s_cmp_eq_u32 s37,2\n"
"s_cbranch_scc1 L_pgs256_%=\n"
"s_cmp_eq_u32 s37,3\n"
"s_cbranch_scc1 L_pgs272_%=\n"
"s_branch L_pgs224_%=\n"
"L_hrs_%=:\n"
"s_mov_b64 exec,-1\n"
"s_sleep 0\n"
"buffer_load_dwordx4 v[48:51],v9,s[88:91],s75 offen sc1\n"
"buffer_load_dwordx4 v[16:19],v9,s[88:91],s77 offen offset:0 sc1\n"
"buffer_load_dwordx4 v[20:23],v9,s[88:91],s77 offen offset:1024 sc1\n"
"buffer_load_dwordx4 v[24:27],v9,s[88:91],s77 offen offset:2048 sc1\n"
"buffer_load_dwordx4 v[28:31],v9,s[88:91],s77 offen offset:3072 sc1\n"
"buffer_load_dwordx4 v[32:35],v9,s[88:91],s76 offen offset:0 sc1\n"
"buffer_load_dwordx4 v[36:39],v9,s[88:91],s76 offen offset:1024 sc1\n"
"buffer_load_dwordx4 v[40:43],v9,s[88:91],s76 offen offset:2048 sc1\n"
"buffer_load_dwordx4 v[44:47],v9,s[88:91],s76 offen offset:3072 sc1\n"
"s_add_u32 m0,s60,36864\n"
"s_add_u32 s62,s22,1179648\n"
"buffer_load_dwordx4 v208,s[40:43],s62 offen offset:0 lds\n"
"buffer_load_dwordx4 v209,s[40:43],s62 offen offset:1024 lds\n"
"buffer_load_dwordx4 v210,s[40:43],s62 offen offset:2048 lds\n"
"buffer_load_dwordx4 v211,s[40:43],s62 offen offset:3072 lds\n"
"s_add_u32 m0,s60,40960\n"
"s_nop 0\n"
"buffer_load_dwordx4 v212,s[40:43],s62 offen offset:0 lds\n"
"buffer_load_dwordx4 v213,s[40:43],s62 offen offset:1024 lds\n"
"buffer_load_dwordx4 v214,s[40:43],s62 offen offset:2048 lds\n"
"buffer_load_dwordx4 v215,s[40:43],s62 offen offset:3072 lds\n"
"L_pb_%=:\n"
"v_mov_b32 v230,0\n"
"v_mov_b32 v232,0\n"
"s_waitcnt lgkmcnt(0)\n"
"v_lshlrev_b32 v108,16,v52\n"
"v_lshlrev_b32 v110,16,v53\n"
"v_and_b32 v109,s78,v52\n"
"v_and_b32 v111,s78,v53\n"
"v_lshlrev_b32 v112,16,v54\n"
"v_lshlrev_b32 v114,16,v55\n"
"v_and_b32 v113,s78,v54\n"
"v_and_b32 v115,s78,v55\n"
"v_lshlrev_b32 v116,16,v56\n"
"v_lshlrev_b32 v118,16,v57\n"
"v_and_b32 v117,s78,v56\n"
"v_and_b32 v119,s78,v57\n"
"v_lshlrev_b32 v120,16,v58\n"
"v_lshlrev_b32 v122,16,v59\n"
"v_and_b32 v121,s78,v58\n"
"v_and_b32 v123,s78,v59\n"
"v_lshlrev_b32 v124,16,v60\n"
"v_lshlrev_b32 v126,16,v61\n"
"v_and_b32 v125,s78,v60\n"
"v_and_b32 v127,s78,v61\n"
"s_waitcnt vmcnt(16)\n"
"v_lshlrev_b32 v84,16,v48\n"
"v_and_b32 v85,s78,v48\n"
"v_lshlrev_b32 v86,16,v49\n"
"v_and_b32 v87,s78,v49\n"
"v_lshlrev_b32 v88,16,v50\n"
"v_and_b32 v89,s78,v50\n"
"v_lshlrev_b32 v90,16,v51\n"
"v_and_b32 v91,s78,v51\n"
"s_nop 0\n"
"v_permlane32_swap_b32 v84,v88\n"
"v_permlane32_swap_b32 v85,v89\n"
"v_permlane32_swap_b32 v86,v90\n"
"v_permlane32_swap_b32 v87,v91\n"
"v_pk_add_f32 v[84:85],v[84:85],v[88:89]\n"
"v_pk_add_f32 v[86:87],v[86:87],v[90:91]\n"
"v_mov_b32 v88,v84\n"
"v_mov_b32 v89,v85\n"
"v_mov_b32 v90,v86\n"
"v_mov_b32 v91,v87\n"
"v_permlane16_swap_b32 v84,v88\n"
"v_permlane16_swap_b32 v85,v89\n"
"v_permlane16_swap_b32 v86,v90\n"
"v_permlane16_swap_b32 v87,v91\n"
"v_pk_add_f32 v[80:81],v[84:85],v[88:89]\n"
"v_add_f32 v82,v86,v90\n"
"v_add_f32 v83,v87,v91\n"
"v_cmp_u_f32 vcc,v80,v82\n"
"s_cbranch_vccnz L_rt3_%=\n"
"L_rb3_%=:\n"
"v_pk_fma_f32 v[124:125],v[160:161],v[80:81],v[124:125]\n"
"v_pk_fma_f32 v[126:127],v[162:163],v[82:83],v[126:127]\n"
"v_add_f32 v230,v230,v124\n"
"v_fmac_f32 v232,v124,v124\n"
"v_add_f32 v230,v230,v125\n"
"v_fmac_f32 v232,v125,v125\n"
"v_add_f32 v230,v230,v126\n"
"v_fmac_f32 v232,v126,v126\n"
"v_add_f32 v230,v230,v127\n"
"v_fmac_f32 v232,v127,v127\n"
"s_waitcnt vmcnt(14)\n"
"v_lshlrev_b32 v64,16,v16\n"
"v_and_b32 v65,s78,v16\n"
"v_lshlrev_b32 v66,16,v17\n"
"v_and_b32 v67,s78,v17\n"
"v_lshlrev_b32 v68,16,v18\n"
"v_and_b32 v69,s78,v18\n"
"v_lshlrev_b32 v70,16,v19\n"
"v_and_b32 v71,s78,v19\n"
"v_lshlrev_b32 v84,16,v20\n"
"v_and_b32 v85,s78,v20\n"
"v_lshlrev_b32 v86,16,v21\n"
"v_and_b32 v87,s78,v21\n"
"v_lshlrev_b32 v88,16,v22\n"
"v_and_b32 v89,s78,v22\n"
"v_lshlrev_b32 v90,16,v23\n"
"v_and_b32 v91,s78,v23\n"
"v_pk_add_f32 v[64:65],v[64:65],v[84:85]\n"
"v_pk_add_f32 v[66:67],v[66:67],v[86:87]\n"
"v_pk_add_f32 v[68:69],v[68:69],v[88:89]\n"
"v_pk_add_f32 v[70:71],v[70:71],v[90:91]\n"
"s_waitcnt vmcnt(12)\n"
"v_lshlrev_b32 v92,16,v24\n"
"v_and_b32 v93,s78,v24\n"
"v_lshlrev_b32 v94,16,v25\n"
"v_and_b32 v95,s78,v25\n"
"v_lshlrev_b32 v96,16,v26\n"
"v_and_b32 v97,s78,v26\n"
"v_lshlrev_b32 v98,16,v27\n"
"v_and_b32 v99,s78,v27\n"
"v_lshlrev_b32 v100,16,v28\n"
"v_and_b32 v101,s78,v28\n"
"v_lshlrev_b32 v102,16,v29\n"
"v_and_b32 v103,s78,v29\n"
"v_lshlrev_b32 v104,16,v30\n"
"v_and_b32 v105,s78,v30\n"
"v_lshlrev_b32 v106,16,v31\n"
"v_and_b32 v107,s78,v31\n"
"v_pk_add_f32 v[92:93],v[92:93],v[100:101]\n"
"v_pk_add_f32 v[94:95],v[94:95],v[102:103]\n"
"v_pk_add_f32 v[96:97],v[96:97],v[104:105]\n"
"v_pk_add_f32 v[98:99],v[98:99],v[106:107]\n"
"v_pk_add_f32 v[64:65],v[64:65],v[92:93]\n"
"v_pk_add_f32 v[66:67],v[66:67],v[94:95]\n"
"v_pk_add_f32 v[68:69],v[68:69],v[96:97]\n"
"v_pk_add_f32 v[70:71],v[70:71],v[98:99]\n"
"v_pk_fma_f32 v[108:109],v[144:145],v[64:65],v[108:109]\n"
"v_pk_fma_f32 v[110:111],v[146:147],v[66:67],v[110:111]\n"
"v_pk_fma_f32 v[112:113],v[148:149],v[68:69],v[112:113]\n"
"v_pk_fma_f32 v[114:115],v[150:151],v[70:71],v[114:115]\n"
"v_add_f32 v230,v230,v108\n"
"v_fmac_f32 v232,v108,v108\n"
"v_add_f32 v230,v230,v109\n"
"v_fmac_f32 v232,v109,v109\n"
"v_add_f32 v230,v230,v110\n"
"v_fmac_f32 v232,v110,v110\n"
"v_add_f32 v230,v230,v111\n"
"v_fmac_f32 v232,v111,v111\n"
"v_add_f32 v230,v230,v112\n"
"v_fmac_f32 v232,v112,v112\n"
"v_add_f32 v230,v230,v113\n"
"v_fmac_f32 v232,v113,v113\n"
"v_add_f32 v230,v230,v114\n"
"v_fmac_f32 v232,v114,v114\n"
"v_add_f32 v230,v230,v115\n"
"v_fmac_f32 v232,v115,v115\n"
"s_waitcnt vmcnt(10)\n"
"v_lshlrev_b32 v72,16,v32\n"
"v_and_b32 v73,s78,v32\n"
"v_lshlrev_b32 v74,16,v33\n"
"v_and_b32 v75,s78,v33\n"
"v_lshlrev_b32 v76,16,v34\n"
"v_and_b32 v77,s78,v34\n"
"v_lshlrev_b32 v78,16,v35\n"
"v_and_b32 v79,s78,v35\n"
"v_lshlrev_b32 v84,16,v36\n"
"v_and_b32 v85,s78,v36\n"
"v_lshlrev_b32 v86,16,v37\n"
"v_and_b32 v87,s78,v37\n"
"v_lshlrev_b32 v88,16,v38\n"
"v_and_b32 v89,s78,v38\n"
"v_lshlrev_b32 v90,16,v39\n"
"v_and_b32 v91,s78,v39\n"
"v_pk_add_f32 v[72:73],v[72:73],v[84:85]\n"
"v_pk_add_f32 v[74:75],v[74:75],v[86:87]\n"
"v_pk_add_f32 v[76:77],v[76:77],v[88:89]\n"
"v_pk_add_f32 v[78:79],v[78:79],v[90:91]\n"
"s_waitcnt vmcnt(8)\n"
"v_lshlrev_b32 v92,16,v40\n"
"v_and_b32 v93,s78,v40\n"
"v_lshlrev_b32 v94,16,v41\n"
"v_and_b32 v95,s78,v41\n"
"v_lshlrev_b32 v96,16,v42\n"
"v_and_b32 v97,s78,v42\n"
"v_lshlrev_b32 v98,16,v43\n"
"v_and_b32 v99,s78,v43\n"
"v_lshlrev_b32 v100,16,v44\n"
"v_and_b32 v101,s78,v44\n"
"v_lshlrev_b32 v102,16,v45\n"
"v_and_b32 v103,s78,v45\n"
"v_lshlrev_b32 v104,16,v46\n"
"v_and_b32 v105,s78,v46\n"
"v_lshlrev_b32 v106,16,v47\n"
"v_and_b32 v107,s78,v47\n"
"v_pk_add_f32 v[92:93],v[92:93],v[100:101]\n"
"v_pk_add_f32 v[94:95],v[94:95],v[102:103]\n"
"v_pk_add_f32 v[96:97],v[96:97],v[104:105]\n"
"v_pk_add_f32 v[98:99],v[98:99],v[106:107]\n"
"v_pk_add_f32 v[72:73],v[72:73],v[92:93]\n"
"v_pk_add_f32 v[74:75],v[74:75],v[94:95]\n"
"v_pk_add_f32 v[76:77],v[76:77],v[96:97]\n"
"v_pk_add_f32 v[78:79],v[78:79],v[98:99]\n"
"v_pk_fma_f32 v[116:117],v[152:153],v[72:73],v[116:117]\n"
"v_pk_fma_f32 v[118:119],v[154:155],v[74:75],v[118:119]\n"
"v_pk_fma_f32 v[120:121],v[156:157],v[76:77],v[120:121]\n"
"v_pk_fma_f32 v[122:123],v[158:159],v[78:79],v[122:123]\n"
"v_add_f32 v230,v230,v116\n"
"v_fmac_f32 v232,v116,v116\n"
"v_add_f32 v230,v230,v117\n"
"v_fmac_f32 v232,v117,v117\n"
"v_add_f32 v230,v230,v118\n"
"v_fmac_f32 v232,v118,v118\n"
"v_add_f32 v230,v230,v119\n"
"v_fmac_f32 v232,v119,v119\n"
"v_add_f32 v230,v230,v120\n"
"v_fmac_f32 v232,v120,v120\n"
"v_add_f32 v230,v230,v121\n"
"v_fmac_f32 v232,v121,v121\n"
"v_add_f32 v230,v230,v122\n"
"v_fmac_f32 v232,v122,v122\n"
"v_add_f32 v230,v230,v123\n"
"v_fmac_f32 v232,v123,v123\n"
"v_cmp_u_f32 vcc,v230,v230\n"
"s_cbranch_vccnz L_rt4_%=\n"
"L_rb4_%=:\n"
"s_add_u32 m0,s60,45056\n"
"s_nop 0\n"
"buffer_load_dwordx4 v216,s[40:43],s62 offen offset:0 lds\n"
"s_add_u32 m0,s60,73728\n"
"s_add_u32 s62,s22,2359296\n"
"buffer_load_dwordx4 v208,s[40:43],s62 offen offset:0 lds\n"
"buffer_load_dwordx4 v209,s[40:43],s62 offen offset:1024 lds\n"
"buffer_load_dwordx4 v210,s[40:43],s62 offen offset:2048 lds\n"
"buffer_load_dwordx4 v211,s[40:43],s62 offen offset:3072 lds\n"
"v_permlane32_swap_b32 v230,v232\n"
"v_add_f32 v230,v230,v232\n"
"v_mov_b32 v232,v230\n"
"s_nop 1\n"
"v_permlane16_swap_b32 v230,v232\n"
"v_add_f32 v230,v230,v232\n"
"s_nop 1\n"
"v_add_f32_dpp v230,v230,v230 row_ror:8 row_mask:0xf bank_mask:0xf\n"
"ds_write_b32 v218,v230\n"
"s_add_u32 m0,s60,77824\n"
"s_nop 0\n"
"buffer_load_dwordx4 v212,s[40:43],s62 offen offset:0 lds\n"
"buffer_load_dwordx4 v213,s[40:43],s62 offen offset:1024 lds\n"
"buffer_load_dwordx4 v214,s[40:43],s62 offen offset:2048 lds\n"
"buffer_load_dwordx4 v215,s[40:43],s62 offen offset:3072 lds\n"
"s_add_u32 m0,s60,81920\n"
"s_nop 0\n"
"buffer_load_dwordx4 v216,s[40:43],s62 offen offset:0 lds\n"
"s_waitcnt lgkmcnt(0)\n"
"s_barrier\n"
"ds_read_b128 v[64:67],v219\n"
"ds_read_b128 v[68:71],v219 offset:16\n"
"v_mov_b32 v234,0x3ae38e39\n"
"s_waitcnt lgkmcnt(0)\n"
"v_add_f32 v64,v64,v65\n"
"v_add_f32 v66,v66,v67\n"
"v_add_f32 v64,v64,v66\n"
"v_add_f32 v68,v68,v69\n"
"v_add_f32 v70,v70,v71\n"
"v_add_f32 v68,v68,v70\n"
"v_mul_f32 v64,0xbae38e39,v64\n"
"v_fmaak_f32 v68,v234,v68,0x3727c5ac\n"
"v_fma_f32 v68,-v64,v64,v68\n"
"v_rsq_f32 v68,v68\n"
"s_nop 0\n"
"v_mul_f32 v64,v64,v68\n"
"v_pk_fma_f32 v[88:89],v[124:125],v[68:69],v[64:65] op_sel_hi:[1,0,0]\n"
"v_pk_fma_f32 v[90:91],v[126:127],v[68:69],v[64:65] op_sel_hi:[1,0,0]\n"
"v_pk_fma_f32 v[88:89],v[88:89],v[180:181],v[200:201]\n"
"v_pk_fma_f32 v[90:91],v[90:91],v[182:183],v[202:203]\n"
"v_cvt_pk_bf16_f32 v24,v88,v89\n"
"v_cvt_pk_bf16_f32 v25,v90,v91\n"
"buffer_store_dwordx2 v[24:25],v221,s[88:91],s66 offen sc1\n"
"v_pk_fma_f32 v[80:81],v[116:117],v[68:69],v[64:65] op_sel_hi:[1,0,0]\n"
"v_pk_fma_f32 v[82:83],v[118:119],v[68:69],v[64:65] op_sel_hi:[1,0,0]\n"
"v_pk_fma_f32 v[84:85],v[120:121],v[68:69],v[64:65] op_sel_hi:[1,0,0]\n"
"v_pk_fma_f32 v[86:87],v[122:123],v[68:69],v[64:65] op_sel_hi:[1,0,0]\n"
"v_pk_fma_f32 v[80:81],v[80:81],v[172:173],v[192:193]\n"
"v_pk_fma_f32 v[82:83],v[82:83],v[174:175],v[194:195]\n"
"v_pk_fma_f32 v[84:85],v[84:85],v[176:177],v[196:197]\n"
"v_pk_fma_f32 v[86:87],v[86:87],v[178:179],v[198:199]\n"
"v_cvt_pk_bf16_f32 v20,v80,v81\n"
"v_cvt_pk_bf16_f32 v21,v82,v83\n"
"v_cvt_pk_bf16_f32 v22,v84,v85\n"
"v_cvt_pk_bf16_f32 v23,v86,v87\n"
"buffer_store_dwordx4 v[20:23],v220,s[88:91],s64 offen offset:2048 sc1\n"
"v_pk_fma_f32 v[72:73],v[108:109],v[68:69],v[64:65] op_sel_hi:[1,0,0]\n"
"v_pk_fma_f32 v[74:75],v[110:111],v[68:69],v[64:65] op_sel_hi:[1,0,0]\n"
"v_pk_fma_f32 v[76:77],v[112:113],v[68:69],v[64:65] op_sel_hi:[1,0,0]\n"
"v_pk_fma_f32 v[78:79],v[114:115],v[68:69],v[64:65] op_sel_hi:[1,0,0]\n"
"v_pk_fma_f32 v[72:73],v[72:73],v[164:165],v[184:185]\n"
"v_pk_fma_f32 v[74:75],v[74:75],v[166:167],v[186:187]\n"
"v_pk_fma_f32 v[76:77],v[76:77],v[168:169],v[188:189]\n"
"v_pk_fma_f32 v[78:79],v[78:79],v[170:171],v[190:191]\n"
"v_cvt_pk_bf16_f32 v16,v72,v73\n"
"v_cvt_pk_bf16_f32 v17,v74,v75\n"
"v_cvt_pk_bf16_f32 v18,v76,v77\n"
"v_cvt_pk_bf16_f32 v19,v78,v79\n"
"buffer_store_dwordx4 v[16:19],v220,s[88:91],s64 offen offset:0 sc1\n"
"s_add_u32 s76,s70,4096\n"
"buffer_store_dwordx4 v[108:111],v9,s[88:91],s70 offen offset:0\n"
"buffer_store_dwordx4 v[112:115],v9,s[88:91],s70 offen offset:1024\n"
"buffer_store_dwordx4 v[116:119],v9,s[88:91],s70 offen offset:2048\n"
"buffer_store_dwordx4 v[120:123],v9,s[88:91],s70 offen offset:3072\n"
"buffer_store_dwordx4 v[124:127],v9,s[88:91],s76 offen offset:0\n"
"s_add_u32 m0,s60,110592\n"
"s_add_u32 s62,s22,3538944\n"
"buffer_load_dwordx4 v208,s[40:43],s62 offen offset:0 lds\n"
"buffer_load_dwordx4 v209,s[40:43],s62 offen offset:1024 lds\n"
"buffer_load_dwordx4 v210,s[40:43],s62 offen offset:2048 lds\n"
"buffer_load_dwordx4 v211,s[40:43],s62 offen offset:3072 lds\n"
"s_add_u32 m0,s60,114688\n"
"s_nop 0\n"
"buffer_load_dwordx4 v212,s[40:43],s62 offen offset:0 lds\n"
"buffer_load_dwordx4 v213,s[40:43],s62 offen offset:1024 lds\n"
"buffer_load_dwordx4 v214,s[40:43],s62 offen offset:2048 lds\n"
"buffer_load_dwordx4 v215,s[40:43],s62 offen offset:3072 lds\n"
"s_add_u32 m0,s60,118784\n"
"s_nop 0\n"
"buffer_load_dwordx4 v216,s[40:43],s62 offen offset:0 lds\n"
"ds_read_b64_tr_b16 a[144:145],v240 offset:0\n"
"ds_read_b64_tr_b16 a[146:147],v240 offset:4608\n"
"ds_read_b64_tr_b16 a[148:149],v240 offset:9216\n"
"ds_read_b64_tr_b16 a[150:151],v240 offset:13824\n"
"ds_read_b64_tr_b16 a[152:153],v240 offset:18432\n"
"ds_read_b64_tr_b16 a[154:155],v240 offset:23040\n"
"ds_read_b64_tr_b16 a[156:157],v240 offset:27648\n"
"ds_read_b64_tr_b16 a[158:159],v240 offset:32256\n"
"ds_read_b64_tr_b16 a[160:161],v240 offset:32\n"
"ds_read_b64_tr_b16 a[162:163],v240 offset:4640\n"
"ds_read_b64_tr_b16 a[164:165],v240 offset:64\n"
"ds_read_b64_tr_b16 a[166:167],v240 offset:4672\n"
"ds_read_b64_tr_b16 a[168:169],v240 offset:96\n"
"ds_read_b64_tr_b16 a[170:171],v240 offset:4704\n"
"ds_read_b64_tr_b16 a[172:173],v240 offset:128\n"
"ds_read_b64_tr_b16 a[174:175],v240 offset:4736\n"
"ds_read_b64_tr_b16 a[176:177],v240 offset:160\n"
"ds_read_b64_tr_b16 a[178:179],v240 offset:4768\n"
"s_waitcnt vmcnt(14)\n"
"s_sleep 4\n"
"s_mov_b32 s15,0\n"
"buffer_load_dwordx4 a[0:3],v9,s[52:55],s15 offen offset:0 sc1\n"
"s_mov_b32 s15,16384\n"
"buffer_load_dwordx4 a[4:7],v9,s[52:55],s15 offen offset:2048 sc1\n"
"s_mov_b32 s15,0\n"
"buffer_load_dwordx4 a[8:11],v9,s[52:55],s15 offen offset:1024 sc1\n"
"s_mov_b32 s15,16384\n"
"buffer_load_dwordx4 a[12:15],v9,s[52:55],s15 offen offset:3072 sc1\n"
"s_mov_b32 s15,0\n"
"buffer_load_dwordx4 a[16:19],v9,s[52:55],s15 offen offset:2048 sc1\n"
"s_mov_b32 s15,20480\n"
"buffer_load_dwordx4 a[20:23],v9,s[52:55],s15 offen offset:0 sc1\n"
"s_mov_b32 s15,0\n"
"buffer_load_dwordx4 a[24:27],v9,s[52:55],s15 offen offset:3072 sc1\n"
"s_mov_b32 s15,20480\n"
"buffer_load_dwordx4 a[28:31],v9,s[52:55],s15 offen offset:1024 sc1\n"
"s_waitcnt lgkmcnt(12) vmcnt(2)\n"
"v_mfma_f32_16x16x32_bf16 v[64:67],a[144:147],a[0:3],0\n"
"ds_read_b64_tr_b16 a[180:181],v240 offset:192\n"
"ds_read_b64_tr_b16 a[182:183],v240 offset:4800\n"
"v_mfma_f32_16x16x32_bf16 v[68:71],a[144:147],a[4:7],0\n"
"s_nop 1\n"
"v_mfma_f32_16x16x32_bf16 v[24:27],a[148:151],a[8:11],v[64:67]\n"
"ds_read_b64_tr_b16 a[184:185],v240 offset:224\n"
"ds_read_b64_tr_b16 a[186:187],v240 offset:4832\n"
"v_mfma_f32_16x16x32_bf16 v[28:31],a[148:151],a[12:15],v[68:71]\n"
"v_mfma_f32_16x16x32_bf16 v[24:27],a[152:155],a[16:19],v[24:27]\n"
"ds_read_b64_tr_b16 a[188:189],v240 offset:256\n"
"ds_read_b64_tr_b16 a[190:191],v240 offset:4864\n"
"v_mfma_f32_16x16x32_bf16 v[28:31],a[152:155],a[20:23],v[28:31]\n"
"s_waitcnt lgkmcnt(12) vmcnt(0)\n"
"v_mfma_f32_16x16x32_bf16 v[24:27],a[156:159],a[24:27],v[24:27]\n"
"ds_read_b64_tr_b16 a[192:193],v240 offset:9248\n"
"ds_read_b64_tr_b16 a[194:195],v240 offset:13856\n"
"v_mfma_f32_16x16x32_bf16 v[28:31],a[156:159],a[28:31],v[28:31]\n"
"v_cmp_u_f32 vcc,v64,v68\n"
"s_cbranch_vccnz L_fzc0_0_%=\n"
"L_frc0_0_%=:\n"
"s_mov_b32 s15,4096\n"
"buffer_load_dwordx4 a[32:35],v9,s[52:55],s15 offen offset:0 sc1\n"
"s_mov_b32 s15,20480\n"
"buffer_load_dwordx4 a[36:39],v9,s[52:55],s15 offen offset:2048 sc1\n"
"s_mov_b32 s15,4096\n"
"buffer_load_dwordx4 a[40:43],v9,s[52:55],s15 offen offset:1024 sc1\n"
"s_mov_b32 s15,20480\n"
"buffer_load_dwordx4 a[44:47],v9,s[52:55],s15 offen offset:3072 sc1\n"
"s_mov_b32 s15,4096\n"
"buffer_load_dwordx4 a[48:51],v9,s[52:55],s15 offen offset:2048 sc1\n"
"s_mov_b32 s15,24576\n"
"buffer_load_dwordx4 a[52:55],v9,s[52:55],s15 offen offset:0 sc1\n"
"s_mov_b32 s15,4096\n"
"buffer_load_dwordx4 a[56:59],v9,s[52:55],s15 offen offset:3072 sc1\n"
"s_mov_b32 s15,24576\n"
"buffer_load_dwordx4 a[60:63],v9,s[52:55],s15 offen offset:1024 sc1\n"
"v_mfma_f32_16x16x32_bf16 v[72:75],a[160:163],a[0:3],0\n"
"ds_read_b64_tr_b16 a[196:197],v240 offset:9280\n"
"ds_read_b64_tr_b16 a[198:199],v240 offset:13888\n"
"s_mov_b32 s15,8192\n"
"v_mfma_f32_16x16x32_bf16 v[76:79],a[160:163],a[4:7],0\n"
"buffer_load_dwordx4 a[64:67],v9,s[52:55],s15 offen offset:0 sc1\n"
"v_mfma_f32_16x16x32_bf16 v[80:83],a[164:167],a[0:3],0\n"
"ds_read_b64_tr_b16 a[200:201],v240 offset:9312\n"
"ds_read_b64_tr_b16 a[202:203],v240 offset:13920\n"
"v_mfma_f32_16x16x32_bf16 v[84:87],a[164:167],a[4:7],0\n"
"s_waitcnt lgkmcnt(12)\n"
"v_mfma_f32_16x16x32_bf16 v[88:91],a[168:171],a[0:3],0\n"
"ds_read_b64_tr_b16 a[204:205],v240 offset:9344\n"
"ds_read_b64_tr_b16 a[206:207],v240 offset:13952\n"
"v_mfma_f32_16x16x32_bf16 v[92:95],a[168:171],a[4:7],0\n"
"s_mov_b32 s15,24576\n"
"v_cmp_u_f32 vcc,v24,v28\n"
"s_cbranch_vccnz L_fzc0_1_%=\n"
"L_frc0_1_%=:\n"
"v_mfma_f32_16x16x32_bf16 v[96:99],a[172:175],a[0:3],0\n"
"ds_read_b64_tr_b16 a[208:209],v240 offset:9376\n"
"ds_read_b64_tr_b16 a[210:211],v240 offset:13984\n"
"v_mfma_f32_16x16x32_bf16 v[100:103],a[172:175],a[4:7],0\n"
"buffer_load_dwordx4 a[68:71],v9,s[52:55],s15 offen offset:2048 sc1\n"
"v_mfma_f32_16x16x32_bf16 v[104:107],a[176:179],a[0:3],0\n"
"ds_read_b64_tr_b16 a[212:213],v240 offset:9408\n"
"ds_read_b64_tr_b16 a[214:215],v240 offset:14016\n"
"v_mfma_f32_16x16x32_bf16 v[108:111],a[176:179],a[4:7],0\n"
"s_mov_b32 s15,8192\n"
"s_waitcnt lgkmcnt(12)\n"
"v_mfma_f32_16x16x32_bf16 v[112:115],a[180:183],a[0:3],0\n"
"ds_read_b64_tr_b16 a[144:145],v240 offset:9440\n"
"ds_read_b64_tr_b16 a[146:147],v240 offset:14048\n"
"v_mfma_f32_16x16x32_bf16 v[116:119],a[180:183],a[4:7],0\n"
"buffer_load_dwordx4 a[72:75],v9,s[52:55],s15 offen offset:1024 sc1\n"
"v_mfma_f32_16x16x32_bf16 v[120:123],a[184:187],a[0:3],0\n"
"ds_read_b64_tr_b16 a[148:149],v240 offset:9472\n"
"ds_read_b64_tr_b16 a[150:151],v240 offset:14080\n"
"v_mfma_f32_16x16x32_bf16 v[124:127],a[184:187],a[4:7],0\n"
"v_mfma_f32_16x16x32_bf16 v[128:131],a[188:191],a[0:3],0\n"
"ds_read_b64_tr_b16 a[152:153],v240 offset:18464\n"
"ds_read_b64_tr_b16 a[154:155],v240 offset:23072\n"
"v_mfma_f32_16x16x32_bf16 v[132:135],a[188:191],a[4:7],0\n"
"s_mov_b32 s15,24576\n"
"s_waitcnt lgkmcnt(12)\n"
"v_mfma_f32_16x16x32_bf16 v[72:75],a[192:195],a[8:11],v[72:75]\n"
"ds_read_b64_tr_b16 a[156:157],v240 offset:18496\n"
"ds_read_b64_tr_b16 a[158:159],v240 offset:23104\n"
"v_mfma_f32_16x16x32_bf16 v[76:79],a[192:195],a[12:15],v[76:79]\n"
"buffer_load_dwordx4 a[76:79],v9,s[52:55],s15 offen offset:3072 sc1\n"
"v_mfma_f32_16x16x32_bf16 v[80:83],a[196:199],a[8:11],v[80:83]\n"
"ds_read_b64_tr_b16 a[160:161],v240 offset:18528\n"
"ds_read_b64_tr_b16 a[162:163],v240 offset:23136\n"
"v_mfma_f32_16x16x32_bf16 v[84:87],a[196:199],a[12:15],v[84:87]\n"
"s_mov_b32 s15,8192\n"
"v_mfma_f32_16x16x32_bf16 v[88:91],a[200:203],a[8:11],v[88:91]\n"
"ds_read_b64_tr_b16 a[164:165],v240 offset:18560\n"
"ds_read_b64_tr_b16 a[166:167],v240 offset:23168\n"
"L_pgs224_%=:\n"
"s_cbranch_execz L_pgs288_%=\n"
"v_mfma_f32_16x16x32_bf16 v[92:95],a[200:203],a[12:15],v[92:95]\n"
"buffer_load_dwordx4 a[80:83],v9,s[52:55],s15 offen offset:2048 sc1\n"
"s_waitcnt lgkmcnt(12)\n"
"v_mfma_f32_16x16x32_bf16 v[96:99],a[204:207],a[8:11],v[96:99]\n"
"ds_read_b64_tr_b16 a[168:169],v240 offset:18592\n"
"ds_read_b64_tr_b16 a[170:171],v240 offset:23200\n"
"v_mfma_f32_16x16x32_bf16 v[100:103],a[204:207],a[12:15],v[100:103]\n"
"v_mfma_f32_16x16x32_bf16 v[104:107],a[208:211],a[8:11],v[104:107]\n"
"ds_read_b64_tr_b16 a[172:173],v240 offset:18624\n"
"ds_read_b64_tr_b16 a[174:175],v240 offset:23232\n"
"v_mfma_f32_16x16x32_bf16 v[108:111],a[208:211],a[12:15],v[108:111]\n"
"s_mov_b32 s15,28672\n"
"v_mfma_f32_16x16x32_bf16 v[112:115],a[212:215],a[8:11],v[112:115]\n"
"ds_read_b64_tr_b16 a[176:177],v240 offset:18656\n"
"ds_read_b64_tr_b16 a[178:179],v240 offset:23264\n"
"v_mfma_f32_16x16x32_bf16 v[116:119],a[212:215],a[12:15],v[116:119]\n"
"buffer_load_dwordx4 a[84:87],v9,s[52:55],s15 offen offset:0 sc1\n"
"s_waitcnt lgkmcnt(12)\n"
"v_mfma_f32_16x16x32_bf16 v[120:123],a[144:147],a[8:11],v[120:123]\n"
"ds_read_b64_tr_b16 a[180:181],v240 offset:18688\n"
"ds_read_b64_tr_b16 a[182:183],v240 offset:23296\n"
"v_mfma_f32_16x16x32_bf16 v[124:127],a[144:147],a[12:15],v[124:127]\n"
"s_mov_b32 s15,8192\n"
"v_mfma_f32_16x16x32_bf16 v[128:131],a[148:151],a[8:11],v[128:131]\n"
"ds_read_b64_tr_b16 a[184:185],v240 offset:27680\n"
"ds_read_b64_tr_b16 a[186:187],v240 offset:32288\n"
"v_mfma_f32_16x16x32_bf16 v[132:135],a[148:151],a[12:15],v[132:135]\n"
"buffer_load_dwordx4 a[88:91],v9,s[52:55],s15 offen offset:3072 sc1\n"
"v_mfma_f32_16x16x32_bf16 v[72:75],a[152:155],a[16:19],v[72:75]\n"
"ds_read_b64_tr_b16 a[188:189],v240 offset:27712\n"
"ds_read_b64_tr_b16 a[190:191],v240 offset:32320\n"
"v_mfma_f32_16x16x32_bf16 v[76:79],a[152:155],a[20:23],v[76:79]\n"
"s_waitcnt lgkmcnt(12)\n"
"v_mfma_f32_16x16x32_bf16 v[80:83],a[156:159],a[16:19],v[80:83]\n"
"ds_read_b64_tr_b16 a[192:193],v240 offset:27744\n"
"ds_read_b64_tr_b16 a[194:195],v240 offset:32352\n"
"v_mfma_f32_16x16x32_bf16 v[84:87],a[156:159],a[20:23],v[84:87]\n"
"s_mov_b32 s15,28672\n"
"v_mfma_f32_16x16x32_bf16 v[88:91],a[160:163],a[16:19],v[88:91]\n"
"ds_read_b64_tr_b16 a[196:197],v240 offset:27776\n"
"ds_read_b64_tr_b16 a[198:199],v240 offset:32384\n"
"v_mfma_f32_16x16x32_bf16 v[92:95],a[160:163],a[20:23],v[92:95]\n"
"buffer_load_dwordx4 a[92:95],v9,s[52:55],s15 offen offset:1024 sc1\n"
"v_mfma_f32_16x16x32_bf16 v[96:99],a[164:167],a[16:19],v[96:99]\n"
"ds_read_b64_tr_b16 a[200:201],v240 offset:27808\n"
"ds_read_b64_tr_b16 a[202:203],v240 offset:32416\n"
"v_mfma_f32_16x16x32_bf16 v[100:103],a[164:167],a[20:23],v[100:103]\n"
"s_mov_b32 s15,12288\n"
"s_waitcnt lgkmcnt(12)\n"
"v_mfma_f32_16x16x32_bf16 v[104:107],a[168:171],a[16:19],v[104:107]\n"
"ds_read_b64_tr_b16 a[204:205],v240 offset:27840\n"
"ds_read_b64_tr_b16 a[206:207],v240 offset:32448\n"
"v_mfma_f32_16x16x32_bf16 v[108:111],a[168:171],a[20:23],v[108:111]\n"
"buffer_load_dwordx4 a[96:99],v9,s[52:55],s15 offen offset:0 sc1\n"
"v_mfma_f32_16x16x32_bf16 v[112:115],a[172:175],a[16:19],v[112:115]\n"
"ds_read_b64_tr_b16 a[208:209],v240 offset:27872\n"
"ds_read_b64_tr_b16 a[210:211],v240 offset:32480\n"
"v_mfma_f32_16x16x32_bf16 v[116:119],a[172:175],a[20:23],v[116:119]\n"
"v_mfma_f32_16x16x32_bf16 v[120:123],a[176:179],a[16:19],v[120:123]\n"
"ds_read_b64_tr_b16 a[212:213],v240 offset:27904\n"
"ds_read_b64_tr_b16 a[214:215],v240 offset:32512\n"
"v_mfma_f32_16x16x32_bf16 v[124:127],a[176:179],a[20:23],v[124:127]\n"
"s_mov_b32 s15,28672\n"
"s_waitcnt lgkmcnt(12)\n"
"v_mfma_f32_16x16x32_bf16 v[128:131],a[180:183],a[16:19],v[128:131]\n"
"v_mfma_f32_16x16x32_bf16 v[132:135],a[180:183],a[20:23],v[132:135]\n"
"buffer_load_dwordx4 a[100:103],v9,s[52:55],s15 offen offset:2048 sc1\n"
"v_mfma_f32_16x16x32_bf16 v[72:75],a[184:187],a[24:27],v[72:75]\n"
"v_mfma_f32_16x16x32_bf16 v[76:79],a[184:187],a[28:31],v[76:79]\n"
"s_waitcnt lgkmcnt(0)\n"
"s_barrier\n"
"v_mfma_f32_16x16x32_bf16 v[80:83],a[188:191],a[24:27],v[80:83]\n"
"ds_read_b64_tr_b16 a[144:145],v240 offset:36864\n"
"ds_read_b64_tr_b16 a[146:147],v240 offset:41472\n"
"v_mfma_f32_16x16x32_bf16 v[84:87],a[188:191],a[28:31],v[84:87]\n"
"ds_read_b64_tr_b16 a[148:149],v240 offset:46080\n"
"ds_read_b64_tr_b16 a[150:151],v240 offset:50688\n"
"s_mov_b32 s15,12288\n"
"v_mfma_f32_16x16x32_bf16 v[88:91],a[192:195],a[24:27],v[88:91]\n"
"ds_read_b64_tr_b16 a[152:153],v241 offset:0\n"
"ds_read_b64_tr_b16 a[154:155],v241 offset:4608\n"
"v_mfma_f32_16x16x32_bf16 v[92:95],a[192:195],a[28:31],v[92:95]\n"
"buffer_load_dwordx4 a[104:107],v9,s[52:55],s15 offen offset:1024 sc1\n"
"v_mfma_f32_16x16x32_bf16 v[96:99],a[196:199],a[24:27],v[96:99]\n"
"ds_read_b64_tr_b16 a[156:157],v241 offset:9216\n"
"ds_read_b64_tr_b16 a[158:159],v241 offset:13824\n"
"v_mfma_f32_16x16x32_bf16 v[100:103],a[196:199],a[28:31],v[100:103]\n"
"s_mov_b32 s15,28672\n"
"v_mfma_f32_16x16x32_bf16 v[104:107],a[200:203],a[24:27],v[104:107]\n"
"ds_read_b64_tr_b16 a[160:161],v240 offset:36896\n"
"ds_read_b64_tr_b16 a[162:163],v240 offset:41504\n"
"v_mfma_f32_16x16x32_bf16 v[108:111],a[200:203],a[28:31],v[108:111]\n"
"buffer_load_dwordx4 a[108:111],v9,s[52:55],s15 offen offset:3072 sc1\n"
"v_mfma_f32_16x16x32_bf16 v[112:115],a[204:207],a[24:27],v[112:115]\n"
"ds_read_b64_tr_b16 a[164:165],v240 offset:36928\n"
"ds_read_b64_tr_b16 a[166:167],v240 offset:41536\n"
"v_mfma_f32_16x16x32_bf16 v[116:119],a[204:207],a[28:31],v[116:119]\n"
"ds_read_b64_tr_b16 a[168:169],v240 offset:36960\n"
"ds_read_b64_tr_b16 a[170:171],v240 offset:41568\n"
"s_mov_b32 s15,12288\n"
"v_mfma_f32_16x16x32_bf16 v[120:123],a[208:211],a[24:27],v[120:123]\n"
"ds_read_b64_tr_b16 a[172:173],v240 offset:36992\n"
"ds_read_b64_tr_b16 a[174:175],v240 offset:41600\n"
"v_mfma_f32_16x16x32_bf16 v[124:127],a[208:211],a[28:31],v[124:127]\n"
"buffer_load_dwordx4 a[112:115],v9,s[52:55],s15 offen offset:2048 sc1\n"
"v_mfma_f32_16x16x32_bf16 v[128:131],a[212:215],a[24:27],v[128:131]\n"
"ds_read_b64_tr_b16 a[176:177],v240 offset:37024\n"
"ds_read_b64_tr_b16 a[178:179],v240 offset:41632\n"
"v_mfma_f32_16x16x32_bf16 v[132:135],a[212:215],a[28:31],v[132:135]\n"
"s_waitcnt lgkmcnt(12) vmcnt(15)\n"
"v_mfma_f32_16x16x32_bf16 v[16:19],a[144:147],a[32:35],v[24:27]\n"
"ds_read_b64_tr_b16 a[180:181],v240 offset:37056\n"
"ds_read_b64_tr_b16 a[182:183],v240 offset:41664\n"
"v_mfma_f32_16x16x32_bf16 v[20:23],a[144:147],a[36:39],v[28:31]\n"
"s_mov_b32 s15,32768\n"
"s_nop 1\n"
"v_mfma_f32_16x16x32_bf16 v[64:67],a[148:151],a[40:43],v[16:19]\n"
"ds_read_b64_tr_b16 a[184:185],v240 offset:37088\n"
"ds_read_b64_tr_b16 a[186:187],v240 offset:41696\n"
"v_mfma_f32_16x16x32_bf16 v[68:71],a[148:151],a[44:47],v[20:23]\n"
"buffer_load_dwordx4 a[116:119],v9,s[52:55],s15 offen offset:0 sc1\n"
"v_mfma_f32_16x16x32_bf16 v[64:67],a[152:155],a[48:51],v[64:67]\n"
"ds_read_b64_tr_b16 a[188:189],v240 offset:37120\n"
"ds_read_b64_tr_b16 a[190:191],v240 offset:41728\n"
"v_mfma_f32_16x16x32_bf16 v[68:71],a[152:155],a[52:55],v[68:71]\n"
"s_waitcnt lgkmcnt(12) vmcnt(14)\n"
"v_mfma_f32_16x16x32_bf16 v[64:67],a[156:159],a[56:59],v[64:67]\n"
"ds_read_b64_tr_b16 a[192:193],v240 offset:46112\n"
"ds_read_b64_tr_b16 a[194:195],v240 offset:50720\n"
"v_mfma_f32_16x16x32_bf16 v[68:71],a[156:159],a[60:63],v[68:71]\n"
"s_mov_b32 s15,12288\n"
"v_cmp_u_f32 vcc,v16,v20\n"
"s_cbranch_vccnz L_fzc4_0_%=\n"
"L_frc4_0_%=:\n"
"v_mfma_f32_16x16x32_bf16 v[72:75],a[160:163],a[32:35],v[72:75]\n"
"ds_read_b64_tr_b16 a[196:197],v240 offset:46144\n"
"ds_read_b64_tr_b16 a[198:199],v240 offset:50752\n"
"v_mfma_f32_16x16x32_bf16 v[76:79],a[160:163],a[36:39],v[76:79]\n"
"buffer_load_dwordx4 a[120:123],v9,s[52:55],s15 offen offset:3072 sc1\n"
"v_mfma_f32_16x16x32_bf16 v[80:83],a[164:167],a[32:35],v[80:83]\n"
"ds_read_b64_tr_b16 a[200:201],v240 offset:46176\n"
"ds_read_b64_tr_b16 a[202:203],v240 offset:50784\n"
"v_mfma_f32_16x16x32_bf16 v[84:87],a[164:167],a[36:39],v[84:87]\n"
"s_waitcnt lgkmcnt(12)\n"
"v_mfma_f32_16x16x32_bf16 v[88:91],a[168:171],a[32:35],v[88:91]\n"
"ds_read_b64_tr_b16 a[204:205],v240 offset:46208\n"
"ds_read_b64_tr_b16 a[206:207],v240 offset:50816\n"
"v_mfma_f32_16x16x32_bf16 v[92:95],a[168:171],a[36:39],v[92:95]\n"
"s_mov_b32 s15,32768\n"
"v_cmp_u_f32 vcc,v64,v68\n"
"s_cbranch_vccnz L_fzc4_1_%=\n"
"L_frc4_1_%=:\n"
"v_mfma_f32_16x16x32_bf16 v[96:99],a[172:175],a[32:35],v[96:99]\n"
"ds_read_b64_tr_b16 a[208:209],v240 offset:46240\n"
"ds_read_b64_tr_b16 a[210:211],v240 offset:50848\n"
"v_mfma_f32_16x16x32_bf16 v[100:103],a[172:175],a[36:39],v[100:103]\n"
"buffer_load_dwordx4 a[124:127],v9,s[52:55],s15 offen offset:1024 sc1\n"
"s_add_u32 m0,s60,0\n"
"s_mov_b32 s62,s23\n"
"v_mfma_f32_16x16x32_bf16 v[104:107],a[176:179],a[32:35],v[104:107]\n"
"ds_read_b64_tr_b16 a[212:213],v240 offset:46272\n"
"ds_read_b64_tr_b16 a[214:215],v240 offset:50880\n"
"v_mfma_f32_16x16x32_bf16 v[108:111],a[176:179],a[36:39],v[108:111]\n"
"buffer_load_dwordx4 v208,s[40:43],s62 offen offset:0 lds\n"
"s_waitcnt lgkmcnt(12)\n"
"v_mfma_f32_16x16x32_bf16 v[112:115],a[180:183],a[32:35],v[112:115]\n"
"ds_read_b64_tr_b16 a[144:145],v240 offset:46304\n"
"ds_read_b64_tr_b16 a[146:147],v240 offset:50912\n"
"v_mfma_f32_16x16x32_bf16 v[116:119],a[180:183],a[36:39],v[116:119]\n"
"v_mfma_f32_16x16x32_bf16 v[120:123],a[184:187],a[32:35],v[120:123]\n"
"ds_read_b64_tr_b16 a[148:149],v240 offset:46336\n"
"ds_read_b64_tr_b16 a[150:151],v240 offset:50944\n"
"v_mfma_f32_16x16x32_bf16 v[124:127],a[184:187],a[36:39],v[124:127]\n"
"v_mfma_f32_16x16x32_bf16 v[128:131],a[188:191],a[32:35],v[128:131]\n"
"ds_read_b64_tr_b16 a[152:153],v241 offset:32\n"
"ds_read_b64_tr_b16 a[154:155],v241 offset:4640\n"
"v_mfma_f32_16x16x32_bf16 v[132:135],a[188:191],a[36:39],v[132:135]\n"
"s_waitcnt lgkmcnt(12)\n"
"v_mfma_f32_16x16x32_bf16 v[72:75],a[192:195],a[40:43],v[72:75]\n"
"ds_read_b64_tr_b16 a[156:157],v241 offset:64\n"
"ds_read_b64_tr_b16 a[158:159],v241 offset:4672\n"
"v_mfma_f32_16x16x32_bf16 v[76:79],a[192:195],a[44:47],v[76:79]\n"
"buffer_load_dwordx4 v209,s[40:43],s62 offen offset:1024 lds\n"
"v_mfma_f32_16x16x32_bf16 v[80:83],a[196:199],a[40:43],v[80:83]\n"
"ds_read_b64_tr_b16 a[160:161],v241 offset:96\n"
"ds_read_b64_tr_b16 a[162:163],v241 offset:4704\n"
"v_mfma_f32_16x16x32_bf16 v[84:87],a[196:199],a[44:47],v[84:87]\n"
"v_mfma_f32_16x16x32_bf16 v[88:91],a[200:203],a[40:43],v[88:91]\n"
"ds_read_b64_tr_b16 a[164:165],v241 offset:128\n"
"ds_read_b64_tr_b16 a[166:167],v241 offset:4736\n"
"v_mfma_f32_16x16x32_bf16 v[92:95],a[200:203],a[44:47],v[92:95]\n"
"s_waitcnt lgkmcnt(12)\n"
"v_mfma_f32_16x16x32_bf16 v[96:99],a[204:207],a[40:43],v[96:99]\n"
"ds_read_b64_tr_b16 a[168:169],v241 offset:160\n"
"ds_read_b64_tr_b16 a[170:171],v241 offset:4768\n"
"v_mfma_f32_16x16x32_bf16 v[100:103],a[204:207],a[44:47],v[100:103]\n"
"buffer_load_dwordx4 v210,s[40:43],s62 offen offset:2048 lds\n"
"v_mfma_f32_16x16x32_bf16 v[104:107],a[208:211],a[40:43],v[104:107]\n"
"ds_read_b64_tr_b16 a[172:173],v241 offset:192\n"
"ds_read_b64_tr_b16 a[174:175],v241 offset:4800\n"
"v_mfma_f32_16x16x32_bf16 v[108:111],a[208:211],a[44:47],v[108:111]\n"
"v_mfma_f32_16x16x32_bf16 v[112:115],a[212:215],a[40:43],v[112:115]\n"
"ds_read_b64_tr_b16 a[176:177],v241 offset:224\n"
"ds_read_b64_tr_b16 a[178:179],v241 offset:4832\n"
"v_mfma_f32_16x16x32_bf16 v[116:119],a[212:215],a[44:47],v[116:119]\n"
"s_waitcnt lgkmcnt(12)\n"
"v_mfma_f32_16x16x32_bf16 v[120:123],a[144:147],a[40:43],v[120:123]\n"
"ds_read_b64_tr_b16 a[180:181],v241 offset:256\n"
"ds_read_b64_tr_b16 a[182:183],v241 offset:4864\n"
"v_mfma_f32_16x16x32_bf16 v[124:127],a[144:147],a[44:47],v[124:127]\n"
"v_mfma_f32_16x16x32_bf16 v[128:131],a[148:151],a[40:43],v[128:131]\n"
"ds_read_b64_tr_b16 a[184:185],v241 offset:9248\n"
"ds_read_b64_tr_b16 a[186:187],v241 offset:13856\n"
"v_mfma_f32_16x16x32_bf16 v[132:135],a[148:151],a[44:47],v[132:135]\n"
"buffer_load_dwordx4 v211,s[40:43],s62 offen offset:3072 lds\n"
"v_mfma_f32_16x16x32_bf16 v[72:75],a[152:155],a[48:51],v[72:75]\n"
"ds_read_b64_tr_b16 a[188:189],v241 offset:9280\n"
"ds_read_b64_tr_b16 a[190:191],v241 offset:13888\n"
"v_mfma_f32_16x16x32_bf16 v[76:79],a[152:155],a[52:55],v[76:79]\n"
"s_waitcnt lgkmcnt(12)\n"
"v_mfma_f32_16x16x32_bf16 v[80:83],a[156:159],a[48:51],v[80:83]\n"
"ds_read_b64_tr_b16 a[192:193],v241 offset:9312\n"
"ds_read_b64_tr_b16 a[194:195],v241 offset:13920\n"
"v_mfma_f32_16x16x32_bf16 v[84:87],a[156:159],a[52:55],v[84:87]\n"
"s_add_u32 m0,s60,4096\n"
"v_mfma_f32_16x16x32_bf16 v[88:91],a[160:163],a[48:51],v[88:91]\n"
"ds_read_b64_tr_b16 a[196:197],v241 offset:9344\n"
"ds_read_b64_tr_b16 a[198:199],v241 offset:13952\n"
"v_mfma_f32_16x16x32_bf16 v[92:95],a[160:163],a[52:55],v[92:95]\n"
"buffer_load_dwordx4 v212,s[40:43],s62 offen offset:0 lds\n"
"v_mfma_f32_16x16x32_bf16 v[96:99],a[164:167],a[48:51],v[96:99]\n"
"ds_read_b64_tr_b16 a[200:201],v241 offset:9376\n"
"ds_read_b64_tr_b16 a[202:203],v241 offset:13984\n"
"v_mfma_f32_16x16x32_bf16 v[100:103],a[164:167],a[52:55],v[100:103]\n"
"s_waitcnt lgkmcnt(12)\n"
"v_mfma_f32_16x16x32_bf16 v[104:107],a[168:171],a[48:51],v[104:107]\n"
"ds_read_b64_tr_b16 a[204:205],v241 offset:9408\n"
"ds_read_b64_tr_b16 a[206:207],v241 offset:14016\n"
"v_mfma_f32_16x16x32_bf16 v[108:111],a[168:171],a[52:55],v[108:111]\n"
"v_mfma_f32_16x16x32_bf16 v[112:115],a[172:175],a[48:51],v[112:115]\n"
"ds_read_b64_tr_b16 a[208:209],v241 offset:9440\n"
"ds_read_b64_tr_b16 a[210:211],v241 offset:14048\n"
"v_mfma_f32_16x16x32_bf16 v[116:119],a[172:175],a[52:55],v[116:119]\n"
"v_mfma_f32_16x16x32_bf16 v[120:123],a[176:179],a[48:51],v[120:123]\n"
"ds_read_b64_tr_b16 a[212:213],v241 offset:9472\n"
"ds_read_b64_tr_b16 a[214:215],v241 offset:14080\n"
"v_mfma_f32_16x16x32_bf16 v[124:127],a[176:179],a[52:55],v[124:127]\n"
"buffer_load_dwordx4 v213,s[40:43],s62 offen offset:1024 lds\n"
"s_waitcnt lgkmcnt(12)\n"
"v_mfma_f32_16x16x32_bf16 v[128:131],a[180:183],a[48:51],v[128:131]\n"
"v_mfma_f32_16x16x32_bf16 v[132:135],a[180:183],a[52:55],v[132:135]\n"
"v_mfma_f32_16x16x32_bf16 v[72:75],a[184:187],a[56:59],v[72:75]\n"
"v_mfma_f32_16x16x32_bf16 v[76:79],a[184:187],a[60:63],v[76:79]\n"
"s_waitcnt lgkmcnt(0)\n"
"s_barrier\n"
"v_mfma_f32_16x16x32_bf16 v[80:83],a[188:191],a[56:59],v[80:83]\n"
"ds_read_b64_tr_b16 a[144:145],v241 offset:18432\n"
"ds_read_b64_tr_b16 a[146:147],v241 offset:23040\n"
"v_mfma_f32_16x16x32_bf16 v[84:87],a[188:191],a[60:63],v[84:87]\n"
"ds_read_b64_tr_b16 a[148:149],v241 offset:27648\n"
"ds_read_b64_tr_b16 a[150:151],v241 offset:32256\n"
"v_mfma_f32_16x16x32_bf16 v[88:91],a[192:195],a[56:59],v[88:91]\n"
"ds_read_b64_tr_b16 a[152:153],v241 offset:36864\n"
"ds_read_b64_tr_b16 a[154:155],v241 offset:41472\n"
"v_mfma_f32_16x16x32_bf16 v[92:95],a[192:195],a[60:63],v[92:95]\n"
"buffer_load_dwordx4 v214,s[40:43],s62 offen offset:2048 lds\n"
"v_mfma_f32_16x16x32_bf16 v[96:99],a[196:199],a[56:59],v[96:99]\n"
"ds_read_b64_tr_b16 a[156:157],v241 offset:46080\n"
"ds_read_b64_tr_b16 a[158:159],v241 offset:50688\n"
"v_mfma_f32_16x16x32_bf16 v[100:103],a[196:199],a[60:63],v[100:103]\n"
"v_mfma_f32_16x16x32_bf16 v[104:107],a[200:203],a[56:59],v[104:107]\n"
"ds_read_b64_tr_b16 a[160:161],v241 offset:18464\n"
"ds_read_b64_tr_b16 a[162:163],v241 offset:23072\n"
"v_mfma_f32_16x16x32_bf16 v[108:111],a[200:203],a[60:63],v[108:111]\n"
"v_mfma_f32_16x16x32_bf16 v[112:115],a[204:207],a[56:59],v[112:115]\n"
"ds_read_b64_tr_b16 a[164:165],v241 offset:18496\n"
"ds_read_b64_tr_b16 a[166:167],v241 offset:23104\n"
"v_mfma_f32_16x16x32_bf16 v[116:119],a[204:207],a[60:63],v[116:119]\n"
"ds_read_b64_tr_b16 a[168:169],v241 offset:18528\n"
"ds_read_b64_tr_b16 a[170:171],v241 offset:23136\n"
"v_mfma_f32_16x16x32_bf16 v[120:123],a[208:211],a[56:59],v[120:123]\n"
"ds_read_b64_tr_b16 a[172:173],v241 offset:18560\n"
"ds_read_b64_tr_b16 a[174:175],v241 offset:23168\n"
"v_mfma_f32_16x16x32_bf16 v[124:127],a[208:211],a[60:63],v[124:127]\n"
"buffer_load_dwordx4 v215,s[40:43],s62 offen offset:3072 lds\n"
"v_mfma_f32_16x16x32_bf16 v[128:131],a[212:215],a[56:59],v[128:131]\n"
"ds_read_b64_tr_b16 a[176:177],v241 offset:18592\n"
"ds_read_b64_tr_b16 a[178:179],v241 offset:23200\n"
"v_mfma_f32_16x16x32_bf16 v[132:135],a[212:215],a[60:63],v[132:135]\n"
"s_add_u32 m0,s60,8192\n"
"s_waitcnt lgkmcnt(12) vmcnt(18)\n"
"v_mfma_f32_16x16x32_bf16 v[24:27],a[144:147],a[64:67],v[64:67]\n"
"ds_read_b64_tr_b16 a[180:181],v241 offset:18624\n"
"ds_read_b64_tr_b16 a[182:183],v241 offset:23232\n"
"v_mfma_f32_16x16x32_bf16 v[28:31],a[144:147],a[68:71],v[68:71]\n"
"buffer_load_dwordx4 v216,s[40:43],s62 offen offset:0 lds\n"
"s_nop 1\n"
"v_mfma_f32_16x16x32_bf16 v[16:19],a[148:151],a[72:75],v[24:27]\n"
"ds_read_b64_tr_b16 a[184:185],v241 offset:18656\n"
"ds_read_b64_tr_b16 a[186:187],v241 offset:23264\n"
"v_mfma_f32_16x16x32_bf16 v[20:23],a[148:151],a[76:79],v[28:31]\n"
"v_mfma_f32_16x16x32_bf16 v[16:19],a[152:155],a[80:83],v[16:19]\n"
"ds_read_b64_tr_b16 a[188:189],v241 offset:18688\n"
"ds_read_b64_tr_b16 a[190:191],v241 offset:23296\n"
"v_mfma_f32_16x16x32_bf16 v[20:23],a[152:155],a[84:87],v[20:23]\n"
"s_waitcnt lgkmcnt(12) vmcnt(17)\n"
"v_mfma_f32_16x16x32_bf16 v[16:19],a[156:159],a[88:91],v[16:19]\n"
"ds_read_b64_tr_b16 a[192:193],v241 offset:27680\n"
"ds_read_b64_tr_b16 a[194:195],v241 offset:32288\n"
"v_mfma_f32_16x16x32_bf16 v[20:23],a[156:159],a[92:95],v[20:23]\n"
"s_mov_b32 s15,16384\n"
"v_cmp_u_f32 vcc,v24,v28\n"
"s_cbranch_vccnz L_fzc8_0_%=\n"
"L_frc8_0_%=:\n"
"v_mfma_f32_16x16x32_bf16 v[72:75],a[160:163],a[64:67],v[72:75]\n"
"ds_read_b64_tr_b16 a[196:197],v241 offset:27712\n"
"ds_read_b64_tr_b16 a[198:199],v241 offset:32320\n"
"v_mfma_f32_16x16x32_bf16 v[76:79],a[160:163],a[68:71],v[76:79]\n"
"buffer_load_dwordx4 a[128:131],v9,s[52:55],s15 offen offset:0 sc1\n"
"v_mfma_f32_16x16x32_bf16 v[80:83],a[164:167],a[64:67],v[80:83]\n"
"ds_read_b64_tr_b16 a[200:201],v241 offset:27744\n"
"ds_read_b64_tr_b16 a[202:203],v241 offset:32352\n"
"v_mfma_f32_16x16x32_bf16 v[84:87],a[164:167],a[68:71],v[84:87]\n"
"s_waitcnt lgkmcnt(12)\n"
"v_mfma_f32_16x16x32_bf16 v[88:91],a[168:171],a[64:67],v[88:91]\n"
"ds_read_b64_tr_b16 a[204:205],v241 offset:27776\n"
"ds_read_b64_tr_b16 a[206:207],v241 offset:32384\n"
"v_mfma_f32_16x16x32_bf16 v[92:95],a[168:171],a[68:71],v[92:95]\n"
"s_mov_b32 s15,32768\n"
"v_cmp_u_f32 vcc,v16,v20\n"
"s_cbranch_vccnz L_fzc8_1_%=\n"
"L_frc8_1_%=:\n"
"v_mfma_f32_16x16x32_bf16 v[96:99],a[172:175],a[64:67],v[96:99]\n"
"ds_read_b64_tr_b16 a[208:209],v241 offset:27808\n"
"ds_read_b64_tr_b16 a[210:211],v241 offset:32416\n"
"v_mfma_f32_16x16x32_bf16 v[100:103],a[172:175],a[68:71],v[100:103]\n"
"buffer_load_dwordx4 a[132:135],v9,s[52:55],s15 offen offset:2048 sc1\n"
"v_mfma_f32_16x16x32_bf16 v[104:107],a[176:179],a[64:67],v[104:107]\n"
"ds_read_b64_tr_b16 a[212:213],v241 offset:27840\n"
"ds_read_b64_tr_b16 a[214:215],v241 offset:32448\n"
"v_mfma_f32_16x16x32_bf16 v[108:111],a[176:179],a[68:71],v[108:111]\n"
"s_mov_b32 s15,16384\n"
"s_waitcnt lgkmcnt(12)\n"
"v_mfma_f32_16x16x32_bf16 v[112:115],a[180:183],a[64:67],v[112:115]\n"
"ds_read_b64_tr_b16 a[144:145],v241 offset:27872\n"
"ds_read_b64_tr_b16 a[146:147],v241 offset:32480\n"
"v_mfma_f32_16x16x32_bf16 v[116:119],a[180:183],a[68:71],v[116:119]\n"
"buffer_load_dwordx4 a[136:139],v9,s[52:55],s15 offen offset:1024 sc1\n"
"v_mfma_f32_16x16x32_bf16 v[120:123],a[184:187],a[64:67],v[120:123]\n"
"ds_read_b64_tr_b16 a[148:149],v241 offset:27904\n"
"ds_read_b64_tr_b16 a[150:151],v241 offset:32512\n"
"v_mfma_f32_16x16x32_bf16 v[124:127],a[184:187],a[68:71],v[124:127]\n"
"s_mov_b32 s15,32768\n"
"v_mfma_f32_16x16x32_bf16 v[128:131],a[188:191],a[64:67],v[128:131]\n"
"ds_read_b64_tr_b16 a[152:153],v241 offset:36896\n"
"ds_read_b64_tr_b16 a[154:155],v241 offset:41504\n"
"v_mfma_f32_16x16x32_bf16 v[132:135],a[188:191],a[68:71],v[132:135]\n"
"buffer_load_dwordx4 a[140:143],v9,s[52:55],s15 offen offset:3072 sc1\n"
"s_waitcnt lgkmcnt(12)\n"
"v_mfma_f32_16x16x32_bf16 v[72:75],a[192:195],a[72:75],v[72:75]\n"
"ds_read_b64_tr_b16 a[156:157],v241 offset:36928\n"
"ds_read_b64_tr_b16 a[158:159],v241 offset:41536\n"
"v_mfma_f32_16x16x32_bf16 v[76:79],a[192:195],a[76:79],v[76:79]\n"
"v_mfma_f32_16x16x32_bf16 v[80:83],a[196:199],a[72:75],v[80:83]\n"
"ds_read_b64_tr_b16 a[160:161],v241 offset:36960\n"
"ds_read_b64_tr_b16 a[162:163],v241 offset:41568\n"
"v_mfma_f32_16x16x32_bf16 v[84:87],a[196:199],a[76:79],v[84:87]\n"
"s_add_u32 m0,s60,36864\n"
"s_add_u32 s62,s30,0\n"
"v_mfma_f32_16x16x32_bf16 v[88:91],a[200:203],a[72:75],v[88:91]\n"
"ds_read_b64_tr_b16 a[164:165],v241 offset:36992\n"
"ds_read_b64_tr_b16 a[166:167],v241 offset:41600\n"
"v_mfma_f32_16x16x32_bf16 v[92:95],a[200:203],a[76:79],v[92:95]\n"
"buffer_load_dwordx4 v208,s[40:43],s62 offen offset:0 lds\n"
"s_waitcnt lgkmcnt(12)\n"
"v_mfma_f32_16x16x32_bf16 v[96:99],a[204:207],a[72:75],v[96:99]\n"
"ds_read_b64_tr_b16 a[168:169],v241 offset:37024\n"
"ds_read_b64_tr_b16 a[170:171],v241 offset:41632\n"
"v_mfma_f32_16x16x32_bf16 v[100:103],a[204:207],a[76:79],v[100:103]\n"
"v_mfma_f32_16x16x32_bf16 v[104:107],a[208:211],a[72:75],v[104:107]\n"
"ds_read_b64_tr_b16 a[172:173],v241 offset:37056\n"
"ds_read_b64_tr_b16 a[174:175],v241 offset:41664\n"
"v_mfma_f32_16x16x32_bf16 v[108:111],a[208:211],a[76:79],v[108:111]\n"
"v_mfma_f32_16x16x32_bf16 v[112:115],a[212:215],a[72:75],v[112:115]\n"
"ds_read_b64_tr_b16 a[176:177],v241 offset:37088\n"
"ds_read_b64_tr_b16 a[178:179],v241 offset:41696\n"
"v_mfma_f32_16x16x32_bf16 v[116:119],a[212:215],a[76:79],v[116:119]\n"
"buffer_load_dwordx4 v209,s[40:43],s62 offen offset:1024 lds\n"
"s_waitcnt lgkmcnt(12)\n"
"v_mfma_f32_16x16x32_bf16 v[120:123],a[144:147],a[72:75],v[120:123]\n"
"ds_read_b64_tr_b16 a[180:181],v241 offset:37120\n"
"ds_read_b64_tr_b16 a[182:183],v241 offset:41728\n"
"s_mov_b32 s75,73398272\n"
"v_mfma_f32_16x16x32_bf16 v[124:127],a[144:147],a[76:79],v[124:127]\n"
"s_mul_i32 s78,s34,524288\n"
"s_add_u32 s75,s75,s78\n"
"v_mfma_f32_16x16x32_bf16 v[128:131],a[148:151],a[72:75],v[128:131]\n"
"ds_read_b64_tr_b16 a[184:185],v241 offset:46112\n"
"ds_read_b64_tr_b16 a[186:187],v241 offset:50720\n"
"v_mfma_f32_16x16x32_bf16 v[132:135],a[148:151],a[76:79],v[132:135]\n"
"s_add_u32 s44,s8,s75\n"
"s_addc_u32 s45,s9,0\n"
"s_and_b32 s45,s45,0xffff\n"
"v_mfma_f32_16x16x32_bf16 v[72:75],a[152:155],a[80:83],v[72:75]\n"
"ds_read_b64_tr_b16 a[188:189],v241 offset:46144\n"
"ds_read_b64_tr_b16 a[190:191],v241 offset:50752\n"
"s_mov_b32 s46,526336\n"
"v_mfma_f32_16x16x32_bf16 v[76:79],a[152:155],a[84:87],v[76:79]\n"
"s_mov_b32 s47,0x20000\n"
"s_mov_b32 s75,56623104\n"
"s_mul_i32 s78,s36,2359296\n"
"s_waitcnt lgkmcnt(12)\n"
"v_mfma_f32_16x16x32_bf16 v[80:83],a[156:159],a[80:83],v[80:83]\n"
"ds_read_b64_tr_b16 a[192:193],v241 offset:46176\n"
"ds_read_b64_tr_b16 a[194:195],v241 offset:50784\n"
"s_add_u32 s75,s75,s78\n"
"v_mfma_f32_16x16x32_bf16 v[84:87],a[156:159],a[84:87],v[84:87]\n"
"buffer_load_dwordx4 v210,s[40:43],s62 offen offset:2048 lds\n"
"s_mul_i32 s78,s35,589824\n"
"v_mfma_f32_16x16x32_bf16 v[88:91],a[160:163],a[80:83],v[88:91]\n"
"ds_read_b64_tr_b16 a[196:197],v241 offset:46208\n"
"ds_read_b64_tr_b16 a[198:199],v241 offset:50816\n"
"s_add_u32 s75,s75,s78\n"
"v_mfma_f32_16x16x32_bf16 v[92:95],a[160:163],a[84:87],v[92:95]\n"
"s_add_u32 s56,s8,s75\n"
"s_addc_u32 s57,s9,0\n"
"s_and_b32 s57,s57,0xffff\n"
"v_mfma_f32_16x16x32_bf16 v[96:99],a[164:167],a[80:83],v[96:99]\n"
"ds_read_b64_tr_b16 a[200:201],v241 offset:46240\n"
"ds_read_b64_tr_b16 a[202:203],v241 offset:50848\n"
"s_mov_b32 s58,589824\n"
"v_mfma_f32_16x16x32_bf16 v[100:103],a[164:167],a[84:87],v[100:103]\n"
"s_mov_b32 s59,0x20000\n"
"s_xor_b32 s75,s37,0\n"
"s_lshl_b32 s76,s75,12\n"
"s_waitcnt lgkmcnt(12)\n"
"v_mfma_f32_16x16x32_bf16 v[104:107],a[168:171],a[80:83],v[104:107]\n"
"ds_read_b64_tr_b16 a[204:205],v241 offset:46272\n"
"ds_read_b64_tr_b16 a[206:207],v241 offset:50880\n"
"s_lshl_b32 s75,s75,13\n"
"v_mfma_f32_16x16x32_bf16 v[108:111],a[168:171],a[84:87],v[108:111]\n"
"buffer_load_dwordx4 v211,s[40:43],s62 offen offset:3072 lds\n"
"s_add_u32 s76,s76,147456\n"
"v_mfma_f32_16x16x32_bf16 v[112:115],a[172:175],a[80:83],v[112:115]\n"
"ds_read_b64_tr_b16 a[208:209],v241 offset:46304\n"
"ds_read_b64_tr_b16 a[210:211],v241 offset:50912\n"
"v_add_u32 v12,s75,v9\n"
"v_mfma_f32_16x16x32_bf16 v[116:119],a[172:175],a[84:87],v[116:119]\n"
"v_add_u32 v13,73728,v12\n"
"v_add_u32 v14,s76,v9\n"
"s_mov_b32 s84,s75\n"
"v_mfma_f32_16x16x32_bf16 v[120:123],a[176:179],a[80:83],v[120:123]\n"
"ds_read_b64_tr_b16 a[212:213],v241 offset:46336\n"
"ds_read_b64_tr_b16 a[214:215],v241 offset:50944\n"
"s_mov_b32 s85,s76\n"
"v_mfma_f32_16x16x32_bf16 v[124:127],a[176:179],a[84:87],v[124:127]\n"
"s_mov_b32 s31,32768\n"
"s_mul_i32 s78,s37,36864\n"
"s_add_u32 s31,s31,s78\n"
"s_waitcnt lgkmcnt(12)\n"
"v_mfma_f32_16x16x32_bf16 v[128:131],a[180:183],a[80:83],v[128:131]\n"
"v_add_u32 v255,s31,v9\n"
"s_xor_b32 s80,s37,0\n"
"s_add_u32 s80,s80,1\n"
"v_mfma_f32_16x16x32_bf16 v[132:135],a[180:183],a[84:87],v[132:135]\n"
"s_lshl_b32 s80,s80,11\n"
"s_xor_b32 s81,s37,1\n"
"s_add_u32 s81,s81,0\n"
"v_mfma_f32_16x16x32_bf16 v[72:75],a[184:187],a[88:91],v[72:75]\n"
"s_lshl_b32 s81,s81,11\n"
"s_xor_b32 s82,s37,2\n"
"s_add_u32 s82,s82,1\n"
"v_mfma_f32_16x16x32_bf16 v[76:79],a[184:187],a[92:95],v[76:79]\n"
"s_waitcnt lgkmcnt(0)\n"
"s_barrier\n"
"s_lshl_b32 s82,s82,11\n"
"s_xor_b32 s83,s37,3\n"
"v_mfma_f32_16x16x32_bf16 v[80:83],a[188:191],a[88:91],v[80:83]\n"
"ds_read_b64_tr_b16 a[144:145],v242 offset:0\n"
"ds_read_b64_tr_b16 a[146:147],v242 offset:4608\n"
"s_add_u32 s83,s83,0\n"
"v_mfma_f32_16x16x32_bf16 v[84:87],a[188:191],a[92:95],v[84:87]\n"
"ds_read_b64_tr_b16 a[148:149],v242 offset:9216\n"
"ds_read_b64_tr_b16 a[150:151],v242 offset:13824\n"
"s_add_u32 m0,s60,40960\n"
"v_mfma_f32_16x16x32_bf16 v[88:91],a[192:195],a[88:91],v[88:91]\n"
"ds_read_b64_tr_b16 a[152:153],v242 offset:18432\n"
"ds_read_b64_tr_b16 a[154:155],v242 offset:23040\n"
"s_lshl_b32 s83,s83,11\n"
"v_mfma_f32_16x16x32_bf16 v[92:95],a[192:195],a[92:95],v[92:95]\n"
"buffer_load_dwordx4 v212,s[40:43],s62 offen offset:0 lds\n"
"v_mov_b32 v237,0xbdd2d3e8\n"
"v_mfma_f32_16x16x32_bf16 v[96:99],a[196:199],a[88:91],v[96:99]\n"
"ds_read_b64_tr_b16 a[156:157],v242 offset:27648\n"
"ds_read_b64_tr_b16 a[158:159],v242 offset:32256\n"
"v_mov_b32 v224,0\n"
"v_mfma_f32_16x16x32_bf16 v[100:103],a[196:199],a[92:95],v[100:103]\n"
"buffer_load_dwordx4 v213,s[40:43],s62 offen offset:1024 lds\n"
"v_mov_b32 v225,0\n"
"v_mfma_f32_16x16x32_bf16 v[104:107],a[200:203],a[88:91],v[104:107]\n"
"ds_read_b64_tr_b16 a[160:161],v242 offset:32\n"
"ds_read_b64_tr_b16 a[162:163],v242 offset:4640\n"
"v_mov_b32 v226,0\n"
"v_mfma_f32_16x16x32_bf16 v[108:111],a[200:203],a[92:95],v[108:111]\n"
"v_mov_b32 v227,0\n"
"v_mov_b32 v60,0\n"
"v_mov_b32 v61,0\n"
"v_mfma_f32_16x16x32_bf16 v[112:115],a[204:207],a[88:91],v[112:115]\n"
"ds_read_b64_tr_b16 a[164:165],v242 offset:64\n"
"ds_read_b64_tr_b16 a[166:167],v242 offset:4672\n"
"v_mov_b32 v62,0\n"
"v_mfma_f32_16x16x32_bf16 v[116:119],a[204:207],a[92:95],v[116:119]\n"
"ds_read_b64_tr_b16 a[168:169],v242 offset:96\n"
"ds_read_b64_tr_b16 a[170:171],v242 offset:4704\n"
"v_mov_b32 v63,0\n"
"v_mfma_f32_16x16x32_bf16 v[120:123],a[208:211],a[88:91],v[120:123]\n"
"ds_read_b64_tr_b16 a[172:173],v242 offset:128\n"
"ds_read_b64_tr_b16 a[174:175],v242 offset:4736\n"
"v_mov_b32 v228,0xbdd2d3e8\n"
"v_mfma_f32_16x16x32_bf16 v[124:127],a[208:211],a[92:95],v[124:127]\n"
"v_mov_b32 v229,0xc0135761\n"
"s_mov_b32 s77,524288\n"
"s_mul_i32 s78,s34,32768\n"
"v_mfma_f32_16x16x32_bf16 v[128:131],a[212:215],a[88:91],v[128:131]\n"
"ds_read_b64_tr_b16 a[176:177],v242 offset:160\n"
"ds_read_b64_tr_b16 a[178:179],v242 offset:4768\n"
"s_add_u32 s77,s77,s78\n"
"v_mfma_f32_16x16x32_bf16 v[132:135],a[212:215],a[92:95],v[132:135]\n"
"buffer_load_dwordx4 v214,s[40:43],s62 offen offset:2048 lds\n"
"s_mul_i32 s78,s37,8192\n"
"s_waitcnt lgkmcnt(12) vmcnt(22)\n"
"v_mfma_f32_16x16x32_bf16 v[64:67],a[144:147],a[96:99],v[16:19]\n"
"ds_read_b64_tr_b16 a[180:181],v242 offset:192\n"
"ds_read_b64_tr_b16 a[182:183],v242 offset:4800\n"
"s_add_u32 s77,s77,s78\n"
"v_mfma_f32_16x16x32_bf16 v[68:71],a[144:147],a[100:103],v[20:23]\n"
"s_mov_b32 s76,s77\n"
"s_mul_i32 s78,s36,2048\n"
"s_add_u32 s76,s76,s78\n"
"s_nop 1\n"
"v_mfma_f32_16x16x32_bf16 v[24:27],a[148:151],a[104:107],v[64:67]\n"
"ds_read_b64_tr_b16 a[184:185],v242 offset:224\n"
"ds_read_b64_tr_b16 a[186:187],v242 offset:4832\n"
"v_mfma_f32_16x16x32_bf16 v[28:31],a[148:151],a[108:111],v[68:71]\n"
"s_mul_i32 s78,s35,512\n"
"s_add_u32 s76,s76,s78\n"
"v_lshlrev_b32 v238,3,v8\n"
"v_mfma_f32_16x16x32_bf16 v[24:27],a[152:155],a[112:115],v[24:27]\n"
"ds_read_b64_tr_b16 a[188:189],v242 offset:256\n"
"ds_read_b64_tr_b16 a[190:191],v242 offset:4864\n"
"s_mov_b32 s75,s85\n"
"v_mfma_f32_16x16x32_bf16 v[28:31],a[152:155],a[116:119],v[28:31]\n"
"buffer_load_dwordx4 v215,s[40:43],s62 offen offset:3072 lds\n"
"v_and_b32 v234,15,v8\n"
"s_waitcnt lgkmcnt(12) vmcnt(21)\n"
"v_mfma_f32_16x16x32_bf16 v[24:27],a[156:159],a[120:123],v[24:27]\n"
"ds_read_b64_tr_b16 a[192:193],v242 offset:9248\n"
"ds_read_b64_tr_b16 a[194:195],v242 offset:13856\n"
"v_lshlrev_b32 v234,6,v234\n"
"v_mfma_f32_16x16x32_bf16 v[28:31],a[156:159],a[124:127],v[28:31]\n"
"v_lshrrev_b32 v235,4,v8\n"
"v_lshl_add_u32 v234,v235,4,v234\n"
"L_pgs240_%=:\n"
"s_cbranch_execz L_pgs304_%=\n"
"v_add_u32 v234,s75,v234\n"
"v_cmp_u_f32 vcc,v64,v68\n"
"s_cbranch_vccnz L_fzc12_0_%=\n"
"L_frc12_0_%=:\n"
"v_mfma_f32_16x16x32_bf16 v[72:75],a[160:163],a[96:99],v[72:75]\n"
"ds_read_b64_tr_b16 a[196:197],v242 offset:9280\n"
"ds_read_b64_tr_b16 a[198:199],v242 offset:13888\n"
"v_mfma_f32_16x16x32_bf16 v[76:79],a[160:163],a[100:103],v[76:79]\n"
"v_bfe_u32 v235,v8,2,2\n"
"v_lshrrev_b32 v236,5,v8\n"
"v_lshl_add_u32 v235,v236,3,v235\n"
"v_mfma_f32_16x16x32_bf16 v[80:83],a[164:167],a[96:99],v[80:83]\n"
"ds_read_b64_tr_b16 a[200:201],v242 offset:9312\n"
"ds_read_b64_tr_b16 a[202:203],v242 offset:13920\n"
"v_lshlrev_b32 v235,6,v235\n"
"v_mfma_f32_16x16x32_bf16 v[84:87],a[164:167],a[100:103],v[84:87]\n"
"v_and_b32 v236,3,v8\n"
"v_lshl_add_u32 v235,v236,4,v235\n"
"v_bfe_u32 v236,v8,4,1\n"
"s_waitcnt lgkmcnt(12)\n"
"v_mfma_f32_16x16x32_bf16 v[88:91],a[168:171],a[96:99],v[88:91]\n"
"ds_read_b64_tr_b16 a[204:205],v242 offset:9344\n"
"ds_read_b64_tr_b16 a[206:207],v242 offset:13952\n"
"v_lshl_add_u32 v235,v236,3,v235\n"
"v_mfma_f32_16x16x32_bf16 v[92:95],a[168:171],a[100:103],v[92:95]\n"
"s_add_u32 m0,s60,45056\n"
"v_add_u32 v235,s75,v235\n"
"v_cmp_u_f32 vcc,v24,v28\n"
"s_cbranch_vccnz L_fzc12_1_%=\n"
"L_frc12_1_%=:\n"
"v_mfma_f32_16x16x32_bf16 v[96:99],a[172:175],a[96:99],v[96:99]\n"
"ds_read_b64_tr_b16 a[208:209],v242 offset:9376\n"
"ds_read_b64_tr_b16 a[210:211],v242 offset:13984\n"
"v_mfma_f32_16x16x32_bf16 v[100:103],a[172:175],a[100:103],v[100:103]\n"
"buffer_load_dwordx4 v216,s[40:43],s62 offen offset:0 lds\n"
"v_mfma_f32_16x16x32_bf16 v[104:107],a[176:179],a[96:99],v[104:107]\n"
"ds_read_b64_tr_b16 a[212:213],v242 offset:9408\n"
"ds_read_b64_tr_b16 a[214:215],v242 offset:14016\n"
"v_mfma_f32_16x16x32_bf16 v[108:111],a[176:179],a[100:103],v[108:111]\n"
"s_add_u32 m0,s60,73728\n"
"s_add_u32 s62,s30,1179648\n"
"s_waitcnt lgkmcnt(12)\n"
"v_mfma_f32_16x16x32_bf16 v[112:115],a[180:183],a[96:99],v[112:115]\n"
"ds_read_b64_tr_b16 a[144:145],v242 offset:9440\n"
"ds_read_b64_tr_b16 a[146:147],v242 offset:14048\n"
"v_mfma_f32_16x16x32_bf16 v[116:119],a[180:183],a[100:103],v[116:119]\n"
"buffer_load_dwordx4 v208,s[40:43],s62 offen offset:0 lds\n"
"v_mfma_f32_16x16x32_bf16 v[120:123],a[184:187],a[96:99],v[120:123]\n"
"ds_read_b64_tr_b16 a[148:149],v242 offset:9472\n"
"ds_read_b64_tr_b16 a[150:151],v242 offset:14080\n"
"v_mfma_f32_16x16x32_bf16 v[124:127],a[184:187],a[100:103],v[124:127]\n"
"v_mfma_f32_16x16x32_bf16 v[128:131],a[188:191],a[96:99],v[128:131]\n"
"ds_read_b64_tr_b16 a[152:153],v242 offset:18464\n"
"ds_read_b64_tr_b16 a[154:155],v242 offset:23072\n"
"v_mfma_f32_16x16x32_bf16 v[132:135],a[188:191],a[100:103],v[132:135]\n"
"s_waitcnt lgkmcnt(12)\n"
"v_mfma_f32_16x16x32_bf16 v[72:75],a[192:195],a[104:107],v[72:75]\n"
"ds_read_b64_tr_b16 a[156:157],v242 offset:18496\n"
"ds_read_b64_tr_b16 a[158:159],v242 offset:23104\n"
"v_mfma_f32_16x16x32_bf16 v[76:79],a[192:195],a[108:111],v[76:79]\n"
"v_mfma_f32_16x16x32_bf16 v[80:83],a[196:199],a[104:107],v[80:83]\n"
"ds_read_b64_tr_b16 a[160:161],v242 offset:18528\n"
"ds_read_b64_tr_b16 a[162:163],v242 offset:23136\n"
"v_mfma_f32_16x16x32_bf16 v[84:87],a[196:199],a[108:111],v[84:87]\n"
"buffer_load_dwordx4 v209,s[40:43],s62 offen offset:1024 lds\n"
"v_mfma_f32_16x16x32_bf16 v[88:91],a[200:203],a[104:107],v[88:91]\n"
"ds_read_b64_tr_b16 a[164:165],v242 offset:18560\n"
"ds_read_b64_tr_b16 a[166:167],v242 offset:23168\n"
"v_mfma_f32_16x16x32_bf16 v[92:95],a[200:203],a[108:111],v[92:95]\n"
"s_waitcnt lgkmcnt(12)\n"
"v_mfma_f32_16x16x32_bf16 v[96:99],a[204:207],a[104:107],v[96:99]\n"
"ds_read_b64_tr_b16 a[168:169],v242 offset:18592\n"
"ds_read_b64_tr_b16 a[170:171],v242 offset:23200\n"
"v_mfma_f32_16x16x32_bf16 v[100:103],a[204:207],a[108:111],v[100:103]\n"
"v_mfma_f32_16x16x32_bf16 v[104:107],a[208:211],a[104:107],v[104:107]\n"
"ds_read_b64_tr_b16 a[172:173],v242 offset:18624\n"
"ds_read_b64_tr_b16 a[174:175],v242 offset:23232\n"
"v_mfma_f32_16x16x32_bf16 v[108:111],a[208:211],a[108:111],v[108:111]\n"
"buffer_load_dwordx4 v210,s[40:43],s62 offen offset:2048 lds\n"
"v_mfma_f32_16x16x32_bf16 v[112:115],a[212:215],a[104:107],v[112:115]\n"
"ds_read_b64_tr_b16 a[176:177],v242 offset:18656\n"
"ds_read_b64_tr_b16 a[178:179],v242 offset:23264\n"
"v_mfma_f32_16x16x32_bf16 v[116:119],a[212:215],a[108:111],v[116:119]\n"
"s_waitcnt lgkmcnt(12)\n"
"v_mfma_f32_16x16x32_bf16 v[120:123],a[144:147],a[104:107],v[120:123]\n"
"ds_read_b64_tr_b16 a[180:181],v242 offset:18688\n"
"ds_read_b64_tr_b16 a[182:183],v242 offset:23296\n"
"v_mfma_f32_16x16x32_bf16 v[124:127],a[144:147],a[108:111],v[124:127]\n"
"v_mfma_f32_16x16x32_bf16 v[128:131],a[148:151],a[104:107],v[128:131]\n"
"ds_read_b64_tr_b16 a[184:185],v242 offset:27680\n"
"ds_read_b64_tr_b16 a[186:187],v242 offset:32288\n"
"v_mfma_f32_16x16x32_bf16 v[132:135],a[148:151],a[108:111],v[132:135]\n"
"v_mfma_f32_16x16x32_bf16 v[72:75],a[152:155],a[112:115],v[72:75]\n"
"ds_read_b64_tr_b16 a[188:189],v242 offset:27712\n"
"ds_read_b64_tr_b16 a[190:191],v242 offset:32320\n"
"v_mfma_f32_16x16x32_bf16 v[76:79],a[152:155],a[116:119],v[76:79]\n"
"buffer_load_dwordx4 v211,s[40:43],s62 offen offset:3072 lds\n"
"s_waitcnt lgkmcnt(12)\n"
"v_mfma_f32_16x16x32_bf16 v[80:83],a[156:159],a[112:115],v[80:83]\n"
"ds_read_b64_tr_b16 a[192:193],v242 offset:27744\n"
"ds_read_b64_tr_b16 a[194:195],v242 offset:32352\n"
"v_mfma_f32_16x16x32_bf16 v[84:87],a[156:159],a[116:119],v[84:87]\n"
"v_mfma_f32_16x16x32_bf16 v[88:91],a[160:163],a[112:115],v[88:91]\n"
"ds_read_b64_tr_b16 a[196:197],v242 offset:27776\n"
"ds_read_b64_tr_b16 a[198:199],v242 offset:32384\n"
"v_mfma_f32_16x16x32_bf16 v[92:95],a[160:163],a[116:119],v[92:95]\n"
"s_add_u32 m0,s60,77824\n"
"v_mfma_f32_16x16x32_bf16 v[96:99],a[164:167],a[112:115],v[96:99]\n"
"ds_read_b64_tr_b16 a[200:201],v242 offset:27808\n"
"ds_read_b64_tr_b16 a[202:203],v242 offset:32416\n"
"v_mfma_f32_16x16x32_bf16 v[100:103],a[164:167],a[116:119],v[100:103]\n"
"buffer_load_dwordx4 v212,s[40:43],s62 offen offset:0 lds\n"
"s_waitcnt lgkmcnt(12)\n"
"v_mfma_f32_16x16x32_bf16 v[104:107],a[168:171],a[112:115],v[104:107]\n"
"ds_read_b64_tr_b16 a[204:205],v242 offset:27840\n"
"ds_read_b64_tr_b16 a[206:207],v242 offset:32448\n"
"v_mfma_f32_16x16x32_bf16 v[108:111],a[168:171],a[116:119],v[108:111]\n"
"v_mfma_f32_16x16x32_bf16 v[112:115],a[172:175],a[112:115],v[112:115]\n"
"ds_read_b64_tr_b16 a[208:209],v242 offset:27872\n"
"ds_read_b64_tr_b16 a[210:211],v242 offset:32480\n"
"v_mfma_f32_16x16x32_bf16 v[116:119],a[172:175],a[116:119],v[116:119]\n"
"v_mfma_f32_16x16x32_bf16 v[120:123],a[176:179],a[112:115],v[120:123]\n"
"ds_read_b64_tr_b16 a[212:213],v242 offset:27904\n"
"ds_read_b64_tr_b16 a[214:215],v242 offset:32512\n"
"v_mfma_f32_16x16x32_bf16 v[124:127],a[176:179],a[116:119],v[124:127]\n"
"s_waitcnt lgkmcnt(12)\n"
"v_mfma_f32_16x16x32_bf16 v[128:131],a[180:183],a[112:115],v[128:131]\n"
"v_mfma_f32_16x16x32_bf16 v[132:135],a[180:183],a[116:119],v[132:135]\n"
"buffer_load_dwordx4 v213,s[40:43],s62 offen offset:1024 lds\n"
"v_mfma_f32_16x16x32_bf16 v[72:75],a[184:187],a[120:123],v[72:75]\n"
"v_mfma_f32_16x16x32_bf16 v[76:79],a[184:187],a[124:127],v[76:79]\n"
"s_waitcnt vmcnt(19) lgkmcnt(0)\n"
"s_barrier\n"
"v_mfma_f32_16x16x32_bf16 v[80:83],a[188:191],a[120:123],v[80:83]\n"
"ds_read_b64_tr_b16 a[144:145],v240 offset:0\n"
"ds_read_b64_tr_b16 a[146:147],v240 offset:4608\n"
"v_mfma_f32_16x16x32_bf16 v[84:87],a[188:191],a[124:127],v[84:87]\n"
"ds_read_b64_tr_b16 a[148:149],v240 offset:9216\n"
"ds_read_b64_tr_b16 a[150:151],v240 offset:13824\n"
"v_mfma_f32_16x16x32_bf16 v[88:91],a[192:195],a[120:123],v[88:91]\n"
"ds_read_b64_tr_b16 a[152:153],v240 offset:32\n"
"ds_read_b64_tr_b16 a[154:155],v240 offset:4640\n"
"v_mfma_f32_16x16x32_bf16 v[92:95],a[192:195],a[124:127],v[92:95]\n"
"buffer_load_dwordx4 v214,s[40:43],s62 offen offset:2048 lds\n"
"v_mfma_f32_16x16x32_bf16 v[96:99],a[196:199],a[120:123],v[96:99]\n"
"ds_read_b64_tr_b16 a[156:157],v240 offset:64\n"
"ds_read_b64_tr_b16 a[158:159],v240 offset:4672\n"
"v_mfma_f32_16x16x32_bf16 v[100:103],a[196:199],a[124:127],v[100:103]\n"
"v_mfma_f32_16x16x32_bf16 v[104:107],a[200:203],a[120:123],v[104:107]\n"
"ds_read_b64_tr_b16 a[160:161],v240 offset:96\n"
"ds_read_b64_tr_b16 a[162:163],v240 offset:4704\n"
"v_mfma_f32_16x16x32_bf16 v[108:111],a[200:203],a[124:127],v[108:111]\n"
"v_mfma_f32_16x16x32_bf16 v[112:115],a[204:207],a[120:123],v[112:115]\n"
"ds_read_b64_tr_b16 a[164:165],v240 offset:128\n"
"ds_read_b64_tr_b16 a[166:167],v240 offset:4736\n"
"v_mfma_f32_16x16x32_bf16 v[116:119],a[204:207],a[124:127],v[116:119]\n"
"ds_read_b64_tr_b16 a[168:169],v240 offset:160\n"
"ds_read_b64_tr_b16 a[170:171],v240 offset:4768\n"
"v_mfma_f32_16x16x32_bf16 v[120:123],a[208:211],a[120:123],v[120:123]\n"
"ds_read_b64_tr_b16 a[172:173],v240 offset:192\n"
"ds_read_b64_tr_b16 a[174:175],v240 offset:4800\n"
"v_mfma_f32_16x16x32_bf16 v[124:127],a[208:211],a[124:127],v[124:127]\n"
"buffer_load_dwordx4 v215,s[40:43],s62 offen offset:3072 lds\n"
"v_mfma_f32_16x16x32_bf16 v[128:131],a[212:215],a[120:123],v[128:131]\n"
"ds_read_b64_tr_b16 a[176:177],v240 offset:224\n"
"ds_read_b64_tr_b16 a[178:179],v240 offset:4832\n"
"v_mfma_f32_16x16x32_bf16 v[132:135],a[212:215],a[124:127],v[132:135]\n"
"s_waitcnt lgkmcnt(12) vmcnt(17)\n"
"v_mfma_f32_16x16x32_bf16 v[16:19],a[144:147],a[128:131],v[24:27]\n"
"ds_read_b64_tr_b16 a[180:181],v240 offset:256\n"
"ds_read_b64_tr_b16 a[182:183],v240 offset:4864\n"
"v_mfma_f32_16x16x32_bf16 v[20:23],a[144:147],a[132:135],v[28:31]\n"
"s_nop 1\n"
"v_mfma_f32_16x16x32_bf16 v[64:67],a[148:151],a[136:139],v[16:19]\n"
"ds_read_b64_tr_b16 a[184:185],v240 offset:9248\n"
"ds_read_b64_tr_b16 a[186:187],v240 offset:13856\n"
"v_mfma_f32_16x16x32_bf16 v[68:71],a[148:151],a[140:143],v[20:23]\n"
"s_add_u32 m0,s60,81920\n"
"s_nop 4\n"
"v_cmp_u_f32 vcc,v16,v20\n"
"s_cbranch_vccnz L_fzc16_0_%=\n"
"L_frc16_0_%=:\n"
"v_mfma_f32_16x16x32_bf16 v[72:75],a[152:155],a[128:131],v[72:75]\n"
"ds_read_b64_tr_b16 a[188:189],v240 offset:9280\n"
"ds_read_b64_tr_b16 a[190:191],v240 offset:13888\n"
"v_mfma_f32_16x16x32_bf16 v[76:79],a[152:155],a[132:135],v[76:79]\n"
"buffer_load_dwordx4 v216,s[40:43],s62 offen offset:0 lds\n"
"s_waitcnt lgkmcnt(12)\n"
"v_mfma_f32_16x16x32_bf16 v[80:83],a[156:159],a[128:131],v[80:83]\n"
"ds_read_b64_tr_b16 a[192:193],v240 offset:9312\n"
"ds_read_b64_tr_b16 a[194:195],v240 offset:13920\n"
"v_mfma_f32_16x16x32_bf16 v[84:87],a[156:159],a[132:135],v[84:87]\n"
"v_mfma_f32_16x16x32_bf16 v[88:91],a[160:163],a[128:131],v[88:91]\n"
"ds_read_b64_tr_b16 a[196:197],v240 offset:9344\n"
"ds_read_b64_tr_b16 a[198:199],v240 offset:13952\n"
"v_mfma_f32_16x16x32_bf16 v[92:95],a[160:163],a[132:135],v[92:95]\n"
"v_cmp_u_f32 vcc,v64,v68\n"
"s_cbranch_vccnz L_fzc16_1_%=\n"
"L_frc16_1_%=:\n"
"v_mfma_f32_16x16x32_bf16 v[96:99],a[164:167],a[128:131],v[96:99]\n"
"ds_read_b64_tr_b16 a[200:201],v240 offset:9376\n"
"ds_read_b64_tr_b16 a[202:203],v240 offset:13984\n"
"v_mfma_f32_16x16x32_bf16 v[100:103],a[164:167],a[132:135],v[100:103]\n"
"s_waitcnt lgkmcnt(12)\n"
"v_mfma_f32_16x16x32_bf16 v[104:107],a[168:171],a[128:131],v[104:107]\n"
"ds_read_b64_tr_b16 a[204:205],v240 offset:9408\n"
"ds_read_b64_tr_b16 a[206:207],v240 offset:14016\n"
"v_mfma_f32_16x16x32_bf16 v[108:111],a[168:171],a[132:135],v[108:111]\n"
"v_mfma_f32_16x16x32_bf16 v[112:115],a[172:175],a[128:131],v[112:115]\n"
"ds_read_b64_tr_b16 a[208:209],v240 offset:9440\n"
"ds_read_b64_tr_b16 a[210:211],v240 offset:14048\n"
"v_mfma_f32_16x16x32_bf16 v[116:119],a[172:175],a[132:135],v[116:119]\n"
"v_mfma_f32_16x16x32_bf16 v[120:123],a[176:179],a[128:131],v[120:123]\n"
"ds_read_b64_tr_b16 a[212:213],v240 offset:9472\n"
"ds_read_b64_tr_b16 a[214:215],v240 offset:14080\n"
"v_mfma_f32_16x16x32_bf16 v[124:127],a[176:179],a[132:135],v[124:127]\n"
"s_waitcnt lgkmcnt(12)\n"
"v_mfma_f32_16x16x32_bf16 v[128:131],a[180:183],a[128:131],v[128:131]\n"
"ds_read_b64_tr_b16 a[144:145],v240 offset:18432\n"
"ds_read_b64_tr_b16 a[146:147],v240 offset:23040\n"
"v_mfma_f32_16x16x32_bf16 v[132:135],a[180:183],a[132:135],v[132:135]\n"
"v_mfma_f32_16x16x32_bf16 v[72:75],a[184:187],a[136:139],v[72:75]\n"
"ds_read_b64_tr_b16 a[148:149],v240 offset:18464\n"
"ds_read_b64_tr_b16 a[150:151],v240 offset:23072\n"
"v_mfma_f32_16x16x32_bf16 v[76:79],a[184:187],a[140:143],v[76:79]\n"
"v_mfma_f32_16x16x32_bf16 v[80:83],a[188:191],a[136:139],v[80:83]\n"
"ds_read_b64_tr_b16 a[152:153],v240 offset:18496\n"
"ds_read_b64_tr_b16 a[154:155],v240 offset:23104\n"
"v_mfma_f32_16x16x32_bf16 v[84:87],a[188:191],a[140:143],v[84:87]\n"
"s_waitcnt lgkmcnt(12)\n"
"v_mfma_f32_16x16x32_bf16 v[88:91],a[192:195],a[136:139],v[88:91]\n"
"ds_read_b64_tr_b16 a[156:157],v240 offset:18528\n"
"ds_read_b64_tr_b16 a[158:159],v240 offset:23136\n"
"v_mfma_f32_16x16x32_bf16 v[92:95],a[192:195],a[140:143],v[92:95]\n"
"v_mfma_f32_16x16x32_bf16 v[96:99],a[196:199],a[136:139],v[96:99]\n"
"ds_read_b64_tr_b16 a[160:161],v240 offset:18560\n"
"ds_read_b64_tr_b16 a[162:163],v240 offset:23168\n"
"v_mfma_f32_16x16x32_bf16 v[100:103],a[196:199],a[140:143],v[100:103]\n"
"v_mfma_f32_16x16x32_bf16 v[104:107],a[200:203],a[136:139],v[104:107]\n"
"ds_read_b64_tr_b16 a[164:165],v240 offset:18592\n"
"ds_read_b64_tr_b16 a[166:167],v240 offset:23200\n"
"v_mfma_f32_16x16x32_bf16 v[108:111],a[200:203],a[140:143],v[108:111]\n"
"s_waitcnt lgkmcnt(12)\n"
"v_mfma_f32_16x16x32_bf16 v[112:115],a[204:207],a[136:139],v[112:115]\n"
"ds_read_b64_tr_b16 a[168:169],v240 offset:18624\n"
"ds_read_b64_tr_b16 a[170:171],v240 offset:23232\n"
"v_mfma_f32_16x16x32_bf16 v[116:119],a[204:207],a[140:143],v[116:119]\n"
"v_mfma_f32_16x16x32_bf16 v[120:123],a[208:211],a[136:139],v[120:123]\n"
"ds_read_b64_tr_b16 a[172:173],v240 offset:18656\n"
"ds_read_b64_tr_b16 a[174:175],v240 offset:23264\n"
"v_mfma_f32_16x16x32_bf16 v[124:127],a[208:211],a[140:143],v[124:127]\n"
"v_mfma_f32_16x16x32_bf16 v[128:131],a[212:215],a[136:139],v[128:131]\n"
"ds_read_b64_tr_b16 a[176:177],v240 offset:18688\n"
"ds_read_b64_tr_b16 a[178:179],v240 offset:23296\n"
"v_mfma_f32_16x16x32_bf16 v[132:135],a[212:215],a[140:143],v[132:135]\n"
"s_waitcnt lgkmcnt(12)\n"
"v_mfma_f32_16x16x32_bf16 v[136:139],a[144:147],a[0:3],0\n"
"ds_read_b64_tr_b16 a[180:181],v240 offset:27648\n"
"ds_read_b64_tr_b16 a[182:183],v240 offset:32256\n"
"v_mfma_f32_16x16x32_bf16 v[140:143],a[144:147],a[4:7],0\n"
"v_mfma_f32_16x16x32_bf16 v[144:147],a[148:151],a[0:3],0\n"
"ds_read_b64_tr_b16 a[184:185],v240 offset:27680\n"
"ds_read_b64_tr_b16 a[186:187],v240 offset:32288\n"
"v_mfma_f32_16x16x32_bf16 v[148:151],a[148:151],a[4:7],0\n"
"v_mul_f32 v36,v64,v64\n"
"v_mul_f32 v37,v65,v65\n"
"v_mul_f32 v38,v66,v66\n"
"v_mul_f32 v39,v67,v67\n"
"v_mul_f32 v40,v68,v68\n"
"v_mul_f32 v41,v69,v69\n"
"v_pk_mul_f32 v[42:43],v[70:71],v[70:71]\n"
"v_pk_fma_f32 v[36:37],v[228:229],v[36:37],v[228:229] op_sel:[0,0,1] op_sel_hi:[0,1,1]\n"
"v_pk_fma_f32 v[38:39],v[228:229],v[38:39],v[228:229] op_sel:[0,0,1] op_sel_hi:[0,1,1]\n"
"v_pk_fma_f32 v[40:41],v[228:229],v[40:41],v[228:229] op_sel:[0,0,1] op_sel_hi:[0,1,1]\n"
"v_pk_fma_f32 v[42:43],v[228:229],v[42:43],v[228:229] op_sel:[0,0,1] op_sel_hi:[0,1,1]\n"
"v_pk_mul_f32 v[36:37],v[36:37],v[64:65]\n"
"v_pk_mul_f32 v[38:39],v[38:39],v[66:67]\n"
"v_pk_mul_f32 v[40:41],v[40:41],v[68:69]\n"
"v_pk_mul_f32 v[42:43],v[42:43],v[70:71]\n"
"v_exp_f32 v36,v36\n"
"v_exp_f32 v37,v37\n"
"v_exp_f32 v38,v38\n"
"v_exp_f32 v39,v39\n"
"v_exp_f32 v40,v40\n"
"v_exp_f32 v41,v41\n"
"v_exp_f32 v42,v42\n"
"v_exp_f32 v43,v43\n"
"s_add_u32 m0,s60,110592\n"
"s_add_u32 s62,s30,2359296\n"
"buffer_load_dwordx4 v208,s[40:43],s62 offen offset:0 lds\n"
"v_pk_add_f32 v[36:37],1.0,v[36:37] op_sel_hi:[0,1]\n"
"v_pk_add_f32 v[38:39],1.0,v[38:39] op_sel_hi:[0,1]\n"
"v_pk_add_f32 v[40:41],1.0,v[40:41] op_sel_hi:[0,1]\n"
"v_pk_add_f32 v[42:43],1.0,v[42:43] op_sel_hi:[0,1]\n"
"v_rcp_f32 v36,v36\n"
"v_rcp_f32 v37,v37\n"
"v_rcp_f32 v38,v38\n"
"v_rcp_f32 v39,v39\n"
"v_rcp_f32 v40,v40\n"
"v_rcp_f32 v41,v41\n"
"v_rcp_f32 v42,v42\n"
"v_rcp_f32 v43,v43\n"
"v_pk_mul_f32 v[64:65],v[64:65],v[36:37]\n"
"v_pk_mul_f32 v[66:67],v[66:67],v[38:39]\n"
"v_pk_mul_f32 v[68:69],v[68:69],v[40:41]\n"
"v_pk_mul_f32 v[70:71],v[70:71],v[42:43]\n"
"v_pk_add_f32 v[224:225],v[224:225],v[64:65]\n"
"v_pk_fma_f32 v[60:61],v[64:65],v[64:65],v[60:61]\n"
"v_pk_add_f32 v[226:227],v[226:227],v[68:69]\n"
"v_pk_fma_f32 v[62:63],v[68:69],v[68:69],v[62:63]\n"
"v_pk_add_f32 v[224:225],v[224:225],v[66:67]\n"
"v_pk_fma_f32 v[60:61],v[66:67],v[66:67],v[60:61]\n"
"v_pk_add_f32 v[226:227],v[226:227],v[70:71]\n"
"v_pk_fma_f32 v[62:63],v[70:71],v[70:71],v[62:63]\n"
"buffer_load_dwordx4 v209,s[40:43],s62 offen offset:1024 lds\n"
"v_pk_mul_f32 v[44:45],v[72:73],v[72:73]\n"
"v_pk_mul_f32 v[46:47],v[74:75],v[74:75]\n"
"v_pk_mul_f32 v[48:49],v[76:77],v[76:77]\n"
"v_pk_mul_f32 v[50:51],v[78:79],v[78:79]\n"
"v_pk_fma_f32 v[44:45],v[228:229],v[44:45],v[228:229] op_sel:[0,0,1] op_sel_hi:[0,1,1]\n"
"v_pk_fma_f32 v[46:47],v[228:229],v[46:47],v[228:229] op_sel:[0,0,1] op_sel_hi:[0,1,1]\n"
"v_pk_fma_f32 v[48:49],v[228:229],v[48:49],v[228:229] op_sel:[0,0,1] op_sel_hi:[0,1,1]\n"
"v_pk_fma_f32 v[50:51],v[228:229],v[50:51],v[228:229] op_sel:[0,0,1] op_sel_hi:[0,1,1]\n"
"v_pk_mul_f32 v[44:45],v[44:45],v[72:73]\n"
"v_pk_mul_f32 v[46:47],v[46:47],v[74:75]\n"
"v_pk_mul_f32 v[48:49],v[48:49],v[76:77]\n"
"v_pk_mul_f32 v[50:51],v[50:51],v[78:79]\n"
"v_exp_f32 v44,v44\n"
"v_exp_f32 v45,v45\n"
"v_exp_f32 v46,v46\n"
"v_exp_f32 v47,v47\n"
"v_exp_f32 v48,v48\n"
"v_exp_f32 v49,v49\n"
"v_exp_f32 v50,v50\n"
"v_exp_f32 v51,v51\n"
"buffer_load_dwordx4 v210,s[40:43],s62 offen offset:2048 lds\n"
"v_pk_add_f32 v[44:45],1.0,v[44:45] op_sel_hi:[0,1]\n"
"v_pk_add_f32 v[46:47],1.0,v[46:47] op_sel_hi:[0,1]\n"
"v_pk_add_f32 v[48:49],1.0,v[48:49] op_sel_hi:[0,1]\n"
"v_pk_add_f32 v[50:51],1.0,v[50:51] op_sel_hi:[0,1]\n"
"v_rcp_f32 v44,v44\n"
"v_rcp_f32 v45,v45\n"
"v_rcp_f32 v46,v46\n"
"v_rcp_f32 v47,v47\n"
"v_rcp_f32 v48,v48\n"
"v_rcp_f32 v49,v49\n"
"v_rcp_f32 v50,v50\n"
"v_rcp_f32 v51,v51\n"
"v_pk_mul_f32 v[72:73],v[72:73],v[44:45]\n"
"v_pk_mul_f32 v[74:75],v[74:75],v[46:47]\n"
"v_pk_mul_f32 v[76:77],v[76:77],v[48:49]\n"
"v_pk_mul_f32 v[78:79],v[78:79],v[50:51]\n"
"v_pk_add_f32 v[224:225],v[224:225],v[72:73]\n"
"v_pk_fma_f32 v[60:61],v[72:73],v[72:73],v[60:61]\n"
"v_pk_add_f32 v[226:227],v[226:227],v[76:77]\n"
"v_pk_fma_f32 v[62:63],v[76:77],v[76:77],v[62:63]\n"
"v_pk_add_f32 v[224:225],v[224:225],v[74:75]\n"
"v_pk_fma_f32 v[60:61],v[74:75],v[74:75],v[60:61]\n"
"v_pk_add_f32 v[226:227],v[226:227],v[78:79]\n"
"v_pk_fma_f32 v[62:63],v[78:79],v[78:79],v[62:63]\n"
"v_pk_mul_f32 v[52:53],v[80:81],v[80:81]\n"
"v_pk_mul_f32 v[54:55],v[82:83],v[82:83]\n"
"v_pk_mul_f32 v[56:57],v[84:85],v[84:85]\n"
"v_pk_mul_f32 v[58:59],v[86:87],v[86:87]\n"
"v_pk_fma_f32 v[52:53],v[228:229],v[52:53],v[228:229] op_sel:[0,0,1] op_sel_hi:[0,1,1]\n"
"v_pk_fma_f32 v[54:55],v[228:229],v[54:55],v[228:229] op_sel:[0,0,1] op_sel_hi:[0,1,1]\n"
"v_pk_fma_f32 v[56:57],v[228:229],v[56:57],v[228:229] op_sel:[0,0,1] op_sel_hi:[0,1,1]\n"
"v_pk_fma_f32 v[58:59],v[228:229],v[58:59],v[228:229] op_sel:[0,0,1] op_sel_hi:[0,1,1]\n"
"v_pk_mul_f32 v[52:53],v[52:53],v[80:81]\n"
"v_pk_mul_f32 v[54:55],v[54:55],v[82:83]\n"
"v_pk_mul_f32 v[56:57],v[56:57],v[84:85]\n"
"v_pk_mul_f32 v[58:59],v[58:59],v[86:87]\n"
"v_exp_f32 v52,v52\n"
"v_exp_f32 v53,v53\n"
"v_exp_f32 v54,v54\n"
"v_exp_f32 v55,v55\n"
"v_exp_f32 v56,v56\n"
"v_exp_f32 v57,v57\n"
"v_exp_f32 v58,v58\n"
"v_exp_f32 v59,v59\n"
"buffer_load_dwordx4 v211,s[40:43],s62 offen offset:3072 lds\n"
"v_pk_add_f32 v[52:53],1.0,v[52:53] op_sel_hi:[0,1]\n"
"v_pk_add_f32 v[54:55],1.0,v[54:55] op_sel_hi:[0,1]\n"
"v_pk_add_f32 v[56:57],1.0,v[56:57] op_sel_hi:[0,1]\n"
"v_pk_add_f32 v[58:59],1.0,v[58:59] op_sel_hi:[0,1]\n"
"v_rcp_f32 v52,v52\n"
"v_rcp_f32 v53,v53\n"
"v_rcp_f32 v54,v54\n"
"v_rcp_f32 v55,v55\n"
"v_rcp_f32 v56,v56\n"
"v_rcp_f32 v57,v57\n"
"v_rcp_f32 v58,v58\n"
"v_rcp_f32 v59,v59\n"
"v_pk_mul_f32 v[80:81],v[80:81],v[52:53]\n"
"v_pk_mul_f32 v[82:83],v[82:83],v[54:55]\n"
"v_pk_mul_f32 v[84:85],v[84:85],v[56:57]\n"
"v_pk_mul_f32 v[86:87],v[86:87],v[58:59]\n"
"v_pk_add_f32 v[224:225],v[224:225],v[80:81]\n"
"v_pk_fma_f32 v[60:61],v[80:81],v[80:81],v[60:61]\n"
"v_pk_add_f32 v[226:227],v[226:227],v[84:85]\n"
"v_pk_fma_f32 v[62:63],v[84:85],v[84:85],v[62:63]\n"
"v_pk_add_f32 v[224:225],v[224:225],v[82:83]\n"
"v_pk_fma_f32 v[60:61],v[82:83],v[82:83],v[60:61]\n"
"v_pk_add_f32 v[226:227],v[226:227],v[86:87]\n"
"v_pk_fma_f32 v[62:63],v[86:87],v[86:87],v[62:63]\n"
"s_add_u32 m0,s60,114688\n"
"s_nop 0\n"
"buffer_load_dwordx4 v212,s[40:43],s62 offen offset:0 lds\n"
"v_pk_mul_f32 v[36:37],v[88:89],v[88:89]\n"
"v_pk_mul_f32 v[38:39],v[90:91],v[90:91]\n"
"v_pk_mul_f32 v[40:41],v[92:93],v[92:93]\n"
"v_pk_mul_f32 v[42:43],v[94:95],v[94:95]\n"
"v_pk_fma_f32 v[36:37],v[228:229],v[36:37],v[228:229] op_sel:[0,0,1] op_sel_hi:[0,1,1]\n"
"v_pk_fma_f32 v[38:39],v[228:229],v[38:39],v[228:229] op_sel:[0,0,1] op_sel_hi:[0,1,1]\n"
"v_pk_fma_f32 v[40:41],v[228:229],v[40:41],v[228:229] op_sel:[0,0,1] op_sel_hi:[0,1,1]\n"
"v_pk_fma_f32 v[42:43],v[228:229],v[42:43],v[228:229] op_sel:[0,0,1] op_sel_hi:[0,1,1]\n"
"v_pk_mul_f32 v[36:37],v[36:37],v[88:89]\n"
"v_pk_mul_f32 v[38:39],v[38:39],v[90:91]\n"
"v_pk_mul_f32 v[40:41],v[40:41],v[92:93]\n"
"v_pk_mul_f32 v[42:43],v[42:43],v[94:95]\n"
"v_exp_f32 v36,v36\n"
"v_exp_f32 v37,v37\n"
"v_exp_f32 v38,v38\n"
"v_exp_f32 v39,v39\n"
"v_exp_f32 v40,v40\n"
"v_exp_f32 v41,v41\n"
"v_exp_f32 v42,v42\n"
"v_exp_f32 v43,v43\n"
"buffer_load_dwordx4 v213,s[40:43],s62 offen offset:1024 lds\n"
"v_pk_add_f32 v[36:37],1.0,v[36:37] op_sel_hi:[0,1]\n"
"v_pk_add_f32 v[38:39],1.0,v[38:39] op_sel_hi:[0,1]\n"
"v_pk_add_f32 v[40:41],1.0,v[40:41] op_sel_hi:[0,1]\n"
"v_pk_add_f32 v[42:43],1.0,v[42:43] op_sel_hi:[0,1]\n"
"v_rcp_f32 v36,v36\n"
"v_rcp_f32 v37,v37\n"
"v_rcp_f32 v38,v38\n"
"v_rcp_f32 v39,v39\n"
"v_rcp_f32 v40,v40\n"
"v_rcp_f32 v41,v41\n"
"v_rcp_f32 v42,v42\n"
"v_rcp_f32 v43,v43\n"
"v_pk_mul_f32 v[88:89],v[88:89],v[36:37]\n"
"v_pk_mul_f32 v[90:91],v[90:91],v[38:39]\n"
"v_pk_mul_f32 v[92:93],v[92:93],v[40:41]\n"
"v_pk_mul_f32 v[94:95],v[94:95],v[42:43]\n"
"v_pk_add_f32 v[224:225],v[224:225],v[88:89]\n"
"v_pk_fma_f32 v[60:61],v[88:89],v[88:89],v[60:61]\n"
"v_pk_add_f32 v[226:227],v[226:227],v[92:93]\n"
"v_pk_fma_f32 v[62:63],v[92:93],v[92:93],v[62:63]\n"
"v_pk_add_f32 v[224:225],v[224:225],v[90:91]\n"
"v_pk_fma_f32 v[60:61],v[90:91],v[90:91],v[60:61]\n"
"v_pk_add_f32 v[226:227],v[226:227],v[94:95]\n"
"v_pk_fma_f32 v[62:63],v[94:95],v[94:95],v[62:63]\n"
"v_pk_mul_f32 v[44:45],v[96:97],v[96:97]\n"
"v_pk_mul_f32 v[46:47],v[98:99],v[98:99]\n"
"v_pk_mul_f32 v[48:49],v[100:101],v[100:101]\n"
"v_pk_mul_f32 v[50:51],v[102:103],v[102:103]\n"
"v_pk_fma_f32 v[44:45],v[228:229],v[44:45],v[228:229] op_sel:[0,0,1] op_sel_hi:[0,1,1]\n"
"v_pk_fma_f32 v[46:47],v[228:229],v[46:47],v[228:229] op_sel:[0,0,1] op_sel_hi:[0,1,1]\n"
"v_pk_fma_f32 v[48:49],v[228:229],v[48:49],v[228:229] op_sel:[0,0,1] op_sel_hi:[0,1,1]\n"
"v_pk_fma_f32 v[50:51],v[228:229],v[50:51],v[228:229] op_sel:[0,0,1] op_sel_hi:[0,1,1]\n"
"v_pk_mul_f32 v[44:45],v[44:45],v[96:97]\n"
"v_pk_mul_f32 v[46:47],v[46:47],v[98:99]\n"
"v_pk_mul_f32 v[48:49],v[48:49],v[100:101]\n"
"v_pk_mul_f32 v[50:51],v[50:51],v[102:103]\n"
"v_exp_f32 v44,v44\n"
"v_exp_f32 v45,v45\n"
"v_exp_f32 v46,v46\n"
"v_exp_f32 v47,v47\n"
"v_exp_f32 v48,v48\n"
"v_exp_f32 v49,v49\n"
"v_exp_f32 v50,v50\n"
"v_exp_f32 v51,v51\n"
"buffer_load_dwordx4 v214,s[40:43],s62 offen offset:2048 lds\n"
"v_pk_add_f32 v[44:45],1.0,v[44:45] op_sel_hi:[0,1]\n"
"v_pk_add_f32 v[46:47],1.0,v[46:47] op_sel_hi:[0,1]\n"
"v_pk_add_f32 v[48:49],1.0,v[48:49] op_sel_hi:[0,1]\n"
"v_pk_add_f32 v[50:51],1.0,v[50:51] op_sel_hi:[0,1]\n"
"v_rcp_f32 v44,v44\n"
"v_rcp_f32 v45,v45\n"
"v_rcp_f32 v46,v46\n"
"v_rcp_f32 v47,v47\n"
"v_rcp_f32 v48,v48\n"
"v_rcp_f32 v49,v49\n"
"v_rcp_f32 v50,v50\n"
"v_rcp_f32 v51,v51\n"
"v_pk_mul_f32 v[96:97],v[96:97],v[44:45]\n"
"v_pk_mul_f32 v[98:99],v[98:99],v[46:47]\n"
"v_pk_mul_f32 v[100:101],v[100:101],v[48:49]\n"
"v_pk_mul_f32 v[102:103],v[102:103],v[50:51]\n"
"v_pk_add_f32 v[224:225],v[224:225],v[96:97]\n"
"v_pk_fma_f32 v[60:61],v[96:97],v[96:97],v[60:61]\n"
"v_pk_add_f32 v[226:227],v[226:227],v[100:101]\n"
"v_pk_fma_f32 v[62:63],v[100:101],v[100:101],v[62:63]\n"
"v_pk_add_f32 v[224:225],v[224:225],v[98:99]\n"
"v_pk_fma_f32 v[60:61],v[98:99],v[98:99],v[60:61]\n"
"v_pk_add_f32 v[226:227],v[226:227],v[102:103]\n"
"v_pk_fma_f32 v[62:63],v[102:103],v[102:103],v[62:63]\n"
"buffer_load_dwordx4 v215,s[40:43],s62 offen offset:3072 lds\n"
"v_pk_mul_f32 v[52:53],v[104:105],v[104:105]\n"
"v_pk_mul_f32 v[54:55],v[106:107],v[106:107]\n"
"v_pk_mul_f32 v[56:57],v[108:109],v[108:109]\n"
"v_pk_mul_f32 v[58:59],v[110:111],v[110:111]\n"
"v_pk_fma_f32 v[52:53],v[228:229],v[52:53],v[228:229] op_sel:[0,0,1] op_sel_hi:[0,1,1]\n"
"v_pk_fma_f32 v[54:55],v[228:229],v[54:55],v[228:229] op_sel:[0,0,1] op_sel_hi:[0,1,1]\n"
"v_pk_fma_f32 v[56:57],v[228:229],v[56:57],v[228:229] op_sel:[0,0,1] op_sel_hi:[0,1,1]\n"
"v_pk_fma_f32 v[58:59],v[228:229],v[58:59],v[228:229] op_sel:[0,0,1] op_sel_hi:[0,1,1]\n"
"v_pk_mul_f32 v[52:53],v[52:53],v[104:105]\n"
"v_pk_mul_f32 v[54:55],v[54:55],v[106:107]\n"
"v_pk_mul_f32 v[56:57],v[56:57],v[108:109]\n"
"v_pk_mul_f32 v[58:59],v[58:59],v[110:111]\n"
"v_exp_f32 v52,v52\n"
"v_exp_f32 v53,v53\n"
"v_exp_f32 v54,v54\n"
"v_exp_f32 v55,v55\n"
"v_exp_f32 v56,v56\n"
"v_exp_f32 v57,v57\n"
"v_exp_f32 v58,v58\n"
"v_exp_f32 v59,v59\n"
"s_add_u32 m0,s60,118784\n"
"s_nop 0\n"
"buffer_load_dwordx4 v216,s[40:43],s62 offen offset:0 lds\n"
"v_pk_add_f32 v[52:53],1.0,v[52:53] op_sel_hi:[0,1]\n"
"v_pk_add_f32 v[54:55],1.0,v[54:55] op_sel_hi:[0,1]\n"
"v_pk_add_f32 v[56:57],1.0,v[56:57] op_sel_hi:[0,1]\n"
"v_pk_add_f32 v[58:59],1.0,v[58:59] op_sel_hi:[0,1]\n"
"v_rcp_f32 v52,v52\n"
"v_rcp_f32 v53,v53\n"
"v_rcp_f32 v54,v54\n"
"v_rcp_f32 v55,v55\n"
"v_rcp_f32 v56,v56\n"
"v_rcp_f32 v57,v57\n"
"v_rcp_f32 v58,v58\n"
"v_rcp_f32 v59,v59\n"
"v_pk_mul_f32 v[104:105],v[104:105],v[52:53]\n"
"v_pk_mul_f32 v[106:107],v[106:107],v[54:55]\n"
"v_pk_mul_f32 v[108:109],v[108:109],v[56:57]\n"
"v_pk_mul_f32 v[110:111],v[110:111],v[58:59]\n"
"v_pk_add_f32 v[224:225],v[224:225],v[104:105]\n"
"v_pk_fma_f32 v[60:61],v[104:105],v[104:105],v[60:61]\n"
"v_pk_add_f32 v[226:227],v[226:227],v[108:109]\n"
"v_pk_fma_f32 v[62:63],v[108:109],v[108:109],v[62:63]\n"
"v_pk_add_f32 v[224:225],v[224:225],v[106:107]\n"
"v_pk_fma_f32 v[60:61],v[106:107],v[106:107],v[60:61]\n"
"v_pk_add_f32 v[226:227],v[226:227],v[110:111]\n"
"v_pk_fma_f32 v[62:63],v[110:111],v[110:111],v[62:63]\n"
"v_pk_mul_f32 v[36:37],v[112:113],v[112:113]\n"
"v_pk_mul_f32 v[38:39],v[114:115],v[114:115]\n"
"v_pk_mul_f32 v[40:41],v[116:117],v[116:117]\n"
"v_pk_mul_f32 v[42:43],v[118:119],v[118:119]\n"
"v_pk_fma_f32 v[36:37],v[228:229],v[36:37],v[228:229] op_sel:[0,0,1] op_sel_hi:[0,1,1]\n"
"v_pk_fma_f32 v[38:39],v[228:229],v[38:39],v[228:229] op_sel:[0,0,1] op_sel_hi:[0,1,1]\n"
"v_pk_fma_f32 v[40:41],v[228:229],v[40:41],v[228:229] op_sel:[0,0,1] op_sel_hi:[0,1,1]\n"
"v_pk_fma_f32 v[42:43],v[228:229],v[42:43],v[228:229] op_sel:[0,0,1] op_sel_hi:[0,1,1]\n"
"v_pk_mul_f32 v[36:37],v[36:37],v[112:113]\n"
"v_pk_mul_f32 v[38:39],v[38:39],v[114:115]\n"
"v_pk_mul_f32 v[40:41],v[40:41],v[116:117]\n"
"v_pk_mul_f32 v[42:43],v[42:43],v[118:119]\n"
"v_exp_f32 v36,v36\n"
"v_exp_f32 v37,v37\n"
"v_exp_f32 v38,v38\n"
"v_exp_f32 v39,v39\n"
"v_exp_f32 v40,v40\n"
"v_exp_f32 v41,v41\n"
"v_exp_f32 v42,v42\n"
"v_exp_f32 v43,v43\n"
"v_pk_add_f32 v[36:37],1.0,v[36:37] op_sel_hi:[0,1]\n"
"v_pk_add_f32 v[38:39],1.0,v[38:39] op_sel_hi:[0,1]\n"
"v_pk_add_f32 v[40:41],1.0,v[40:41] op_sel_hi:[0,1]\n"
"v_pk_add_f32 v[42:43],1.0,v[42:43] op_sel_hi:[0,1]\n"
"v_rcp_f32 v36,v36\n"
"v_rcp_f32 v37,v37\n"
"v_rcp_f32 v38,v38\n"
"v_rcp_f32 v39,v39\n"
"v_rcp_f32 v40,v40\n"
"v_rcp_f32 v41,v41\n"
"v_rcp_f32 v42,v42\n"
"v_rcp_f32 v43,v43\n"
"v_pk_mul_f32 v[112:113],v[112:113],v[36:37]\n"
"v_pk_mul_f32 v[114:115],v[114:115],v[38:39]\n"
"v_pk_mul_f32 v[116:117],v[116:117],v[40:41]\n"
"v_pk_mul_f32 v[118:119],v[118:119],v[42:43]\n"
"v_pk_add_f32 v[224:225],v[224:225],v[112:113]\n"
"v_pk_fma_f32 v[60:61],v[112:113],v[112:113],v[60:61]\n"
"v_pk_add_f32 v[226:227],v[226:227],v[116:117]\n"
"v_pk_fma_f32 v[62:63],v[116:117],v[116:117],v[62:63]\n"
"v_pk_add_f32 v[224:225],v[224:225],v[114:115]\n"
"v_pk_fma_f32 v[60:61],v[114:115],v[114:115],v[60:61]\n"
"v_pk_add_f32 v[226:227],v[226:227],v[118:119]\n"
"v_pk_fma_f32 v[62:63],v[118:119],v[118:119],v[62:63]\n"
"v_pk_mul_f32 v[44:45],v[120:121],v[120:121]\n"
"v_pk_mul_f32 v[46:47],v[122:123],v[122:123]\n"
"L_pgs256_%=:\n"
"s_cbranch_execz L_pgs320_%=\n"
"v_pk_mul_f32 v[48:49],v[124:125],v[124:125]\n"
"v_pk_mul_f32 v[50:51],v[126:127],v[126:127]\n"
"v_pk_fma_f32 v[44:45],v[228:229],v[44:45],v[228:229] op_sel:[0,0,1] op_sel_hi:[0,1,1]\n"
"v_pk_fma_f32 v[46:47],v[228:229],v[46:47],v[228:229] op_sel:[0,0,1] op_sel_hi:[0,1,1]\n"
"v_pk_fma_f32 v[48:49],v[228:229],v[48:49],v[228:229] op_sel:[0,0,1] op_sel_hi:[0,1,1]\n"
"v_pk_fma_f32 v[50:51],v[228:229],v[50:51],v[228:229] op_sel:[0,0,1] op_sel_hi:[0,1,1]\n"
"v_pk_mul_f32 v[44:45],v[44:45],v[120:121]\n"
"v_pk_mul_f32 v[46:47],v[46:47],v[122:123]\n"
"v_pk_mul_f32 v[48:49],v[48:49],v[124:125]\n"
"v_pk_mul_f32 v[50:51],v[50:51],v[126:127]\n"
"v_exp_f32 v44,v44\n"
"v_exp_f32 v45,v45\n"
"v_exp_f32 v46,v46\n"
"v_exp_f32 v47,v47\n"
"v_exp_f32 v48,v48\n"
"v_exp_f32 v49,v49\n"
"v_exp_f32 v50,v50\n"
"v_exp_f32 v51,v51\n"
"v_pk_add_f32 v[44:45],1.0,v[44:45] op_sel_hi:[0,1]\n"
"v_pk_add_f32 v[46:47],1.0,v[46:47] op_sel_hi:[0,1]\n"
"v_pk_add_f32 v[48:49],1.0,v[48:49] op_sel_hi:[0,1]\n"
"v_pk_add_f32 v[50:51],1.0,v[50:51] op_sel_hi:[0,1]\n"
"v_rcp_f32 v44,v44\n"
"v_rcp_f32 v45,v45\n"
"v_rcp_f32 v46,v46\n"
"v_rcp_f32 v47,v47\n"
"v_rcp_f32 v48,v48\n"
"v_rcp_f32 v49,v49\n"
"v_rcp_f32 v50,v50\n"
"v_rcp_f32 v51,v51\n"
"v_pk_mul_f32 v[120:121],v[120:121],v[44:45]\n"
"v_pk_mul_f32 v[122:123],v[122:123],v[46:47]\n"
"v_pk_mul_f32 v[124:125],v[124:125],v[48:49]\n"
"v_pk_mul_f32 v[126:127],v[126:127],v[50:51]\n"
"v_pk_add_f32 v[224:225],v[224:225],v[120:121]\n"
"v_pk_fma_f32 v[60:61],v[120:121],v[120:121],v[60:61]\n"
"v_pk_add_f32 v[226:227],v[226:227],v[124:125]\n"
"v_pk_fma_f32 v[62:63],v[124:125],v[124:125],v[62:63]\n"
"v_pk_add_f32 v[224:225],v[224:225],v[122:123]\n"
"v_pk_fma_f32 v[60:61],v[122:123],v[122:123],v[60:61]\n"
"v_pk_add_f32 v[226:227],v[226:227],v[126:127]\n"
"v_pk_fma_f32 v[62:63],v[126:127],v[126:127],v[62:63]\n"
"v_pk_mul_f32 v[52:53],v[128:129],v[128:129]\n"
"v_pk_mul_f32 v[54:55],v[130:131],v[130:131]\n"
"v_pk_mul_f32 v[56:57],v[132:133],v[132:133]\n"
"v_pk_mul_f32 v[58:59],v[134:135],v[134:135]\n"
"v_pk_fma_f32 v[52:53],v[228:229],v[52:53],v[228:229] op_sel:[0,0,1] op_sel_hi:[0,1,1]\n"
"v_pk_fma_f32 v[54:55],v[228:229],v[54:55],v[228:229] op_sel:[0,0,1] op_sel_hi:[0,1,1]\n"
"v_pk_fma_f32 v[56:57],v[228:229],v[56:57],v[228:229] op_sel:[0,0,1] op_sel_hi:[0,1,1]\n"
"v_pk_fma_f32 v[58:59],v[228:229],v[58:59],v[228:229] op_sel:[0,0,1] op_sel_hi:[0,1,1]\n"
"v_pk_mul_f32 v[52:53],v[52:53],v[128:129]\n"
"v_pk_mul_f32 v[54:55],v[54:55],v[130:131]\n"
"v_pk_mul_f32 v[56:57],v[56:57],v[132:133]\n"
"v_pk_mul_f32 v[58:59],v[58:59],v[134:135]\n"
"v_exp_f32 v52,v52\n"
"v_exp_f32 v53,v53\n"
"v_exp_f32 v54,v54\n"
"v_exp_f32 v55,v55\n"
"v_exp_f32 v56,v56\n"
"v_exp_f32 v57,v57\n"
"v_exp_f32 v58,v58\n"
"v_exp_f32 v59,v59\n"
"v_pk_add_f32 v[52:53],1.0,v[52:53] op_sel_hi:[0,1]\n"
"v_pk_add_f32 v[54:55],1.0,v[54:55] op_sel_hi:[0,1]\n"
"v_pk_add_f32 v[56:57],1.0,v[56:57] op_sel_hi:[0,1]\n"
"v_pk_add_f32 v[58:59],1.0,v[58:59] op_sel_hi:[0,1]\n"
"v_rcp_f32 v52,v52\n"
"v_rcp_f32 v53,v53\n"
"v_rcp_f32 v54,v54\n"
"v_rcp_f32 v55,v55\n"
"v_rcp_f32 v56,v56\n"
"v_rcp_f32 v57,v57\n"
"v_rcp_f32 v58,v58\n"
"v_rcp_f32 v59,v59\n"
"v_pk_mul_f32 v[128:129],v[128:129],v[52:53]\n"
"v_pk_mul_f32 v[130:131],v[130:131],v[54:55]\n"
"v_pk_mul_f32 v[132:133],v[132:133],v[56:57]\n"
"v_pk_mul_f32 v[134:135],v[134:135],v[58:59]\n"
"v_pk_add_f32 v[224:225],v[224:225],v[128:129]\n"
"v_pk_fma_f32 v[60:61],v[128:129],v[128:129],v[60:61]\n"
"v_pk_add_f32 v[226:227],v[226:227],v[132:133]\n"
"v_pk_fma_f32 v[62:63],v[132:133],v[132:133],v[62:63]\n"
"v_pk_add_f32 v[224:225],v[224:225],v[130:131]\n"
"v_pk_fma_f32 v[60:61],v[130:131],v[130:131],v[60:61]\n"
"v_pk_add_f32 v[226:227],v[226:227],v[134:135]\n"
"v_pk_fma_f32 v[62:63],v[134:135],v[134:135],v[62:63]\n"
"v_add_f32 v230,v224,v225\n"
"v_add_f32 v231,v226,v227\n"
"v_add_f32 v232,v60,v61\n"
"v_add_f32 v233,v62,v63\n"
"s_nop 1\n"
"v_permlane32_swap_b32 v230,v231\n"
"v_permlane32_swap_b32 v232,v233\n"
"v_add_f32 v230,v230,v231\n"
"v_add_f32 v232,v232,v233\n"
"s_nop 1\n"
"v_permlane16_swap_b32 v230,v232\n"
"v_add_f32 v230,v230,v232\n"
"v_mov_b32 v231,s38\n"
"buffer_store_dwordx2 v[230:231],v238,s[88:91],s76 offen sc1\n"
"s_add_u32 s76,s77,4096\n"
"v_mfma_f32_16x16x32_bf16 v[152:155],a[152:155],a[0:3],0\n"
"ds_read_b64_tr_b16 a[188:189],v240 offset:27712\n"
"ds_read_b64_tr_b16 a[190:191],v240 offset:32320\n"
"v_mfma_f32_16x16x32_bf16 v[156:159],a[152:155],a[4:7],0\n"
"s_waitcnt lgkmcnt(12)\n"
"v_mfma_f32_16x16x32_bf16 v[160:163],a[156:159],a[0:3],0\n"
"ds_read_b64_tr_b16 a[192:193],v240 offset:27744\n"
"ds_read_b64_tr_b16 a[194:195],v240 offset:32352\n"
"v_mfma_f32_16x16x32_bf16 v[164:167],a[156:159],a[4:7],0\n"
"v_mfma_f32_16x16x32_bf16 v[168:171],a[160:163],a[0:3],0\n"
"ds_read_b64_tr_b16 a[196:197],v240 offset:27776\n"
"ds_read_b64_tr_b16 a[198:199],v240 offset:32384\n"
"v_mfma_f32_16x16x32_bf16 v[172:175],a[160:163],a[4:7],0\n"
"v_mfma_f32_16x16x32_bf16 v[176:179],a[164:167],a[0:3],0\n"
"ds_read_b64_tr_b16 a[200:201],v240 offset:27808\n"
"ds_read_b64_tr_b16 a[202:203],v240 offset:32416\n"
"v_mfma_f32_16x16x32_bf16 v[180:183],a[164:167],a[4:7],0\n"
"s_waitcnt lgkmcnt(12)\n"
"v_mfma_f32_16x16x32_bf16 v[184:187],a[168:171],a[0:3],0\n"
"ds_read_b64_tr_b16 a[204:205],v240 offset:27840\n"
"ds_read_b64_tr_b16 a[206:207],v240 offset:32448\n"
"v_mfma_f32_16x16x32_bf16 v[188:191],a[168:171],a[4:7],0\n"
"v_mfma_f32_16x16x32_bf16 v[192:195],a[172:175],a[0:3],0\n"
"ds_read_b64_tr_b16 a[208:209],v240 offset:27872\n"
"ds_read_b64_tr_b16 a[210:211],v240 offset:32480\n"
"v_mfma_f32_16x16x32_bf16 v[196:199],a[172:175],a[4:7],0\n"
"v_mfma_f32_16x16x32_bf16 v[200:203],a[176:179],a[0:3],0\n"
"ds_read_b64_tr_b16 a[212:213],v240 offset:27904\n"
"ds_read_b64_tr_b16 a[214:215],v240 offset:32512\n"
"v_mfma_f32_16x16x32_bf16 v[204:207],a[176:179],a[4:7],0\n"
"s_lshl_b32 s15,s34,2\n"
"s_add_u32 s15,s15,s37\n"
"s_lshl_b32 s15,s15,13\n"
"s_add_u32 s16,s15,s80\n"
"s_waitcnt lgkmcnt(12)\n"
"v_mfma_f32_16x16x32_bf16 v[136:139],a[180:183],a[8:11],v[136:139]\n"
"v_mfma_f32_16x16x32_bf16 v[140:143],a[180:183],a[12:15],v[140:143]\n"
"buffer_load_dwordx4 a[0:3],v9,s[44:47],s16 offen offset:0 sc1\n"
"v_mfma_f32_16x16x32_bf16 v[144:147],a[184:187],a[8:11],v[144:147]\n"
"v_mfma_f32_16x16x32_bf16 v[148:151],a[184:187],a[12:15],v[148:151]\n"
"s_waitcnt vmcnt(20) lgkmcnt(0)\n"
"s_barrier\n"
"v_mfma_f32_16x16x32_bf16 v[152:155],a[188:191],a[8:11],v[152:155]\n"
"ds_read_b64_tr_b16 a[144:145],v240 offset:36864\n"
"ds_read_b64_tr_b16 a[146:147],v240 offset:41472\n"
"v_mfma_f32_16x16x32_bf16 v[156:159],a[188:191],a[12:15],v[156:159]\n"
"ds_read_b64_tr_b16 a[148:149],v240 offset:36896\n"
"ds_read_b64_tr_b16 a[150:151],v240 offset:41504\n"
"s_add_u32 m0,s60,0\n"
"s_add_u32 s62,s30,3538944\n"
"v_mfma_f32_16x16x32_bf16 v[160:163],a[192:195],a[8:11],v[160:163]\n"
"ds_read_b64_tr_b16 a[152:153],v240 offset:36928\n"
"ds_read_b64_tr_b16 a[154:155],v240 offset:41536\n"
"v_mfma_f32_16x16x32_bf16 v[164:167],a[192:195],a[12:15],v[164:167]\n"
"buffer_load_dwordx4 v208,s[40:43],s62 offen offset:0 lds\n"
"v_mfma_f32_16x16x32_bf16 v[168:171],a[196:199],a[8:11],v[168:171]\n"
"ds_read_b64_tr_b16 a[156:157],v240 offset:36960\n"
"ds_read_b64_tr_b16 a[158:159],v240 offset:41568\n"
"v_mfma_f32_16x16x32_bf16 v[172:175],a[196:199],a[12:15],v[172:175]\n"
"v_mfma_f32_16x16x32_bf16 v[176:179],a[200:203],a[8:11],v[176:179]\n"
"ds_read_b64_tr_b16 a[160:161],v240 offset:36992\n"
"ds_read_b64_tr_b16 a[162:163],v240 offset:41600\n"
"v_mfma_f32_16x16x32_bf16 v[180:183],a[200:203],a[12:15],v[180:183]\n"
"v_mfma_f32_16x16x32_bf16 v[184:187],a[204:207],a[8:11],v[184:187]\n"
"ds_read_b64_tr_b16 a[164:165],v240 offset:37024\n"
"ds_read_b64_tr_b16 a[166:167],v240 offset:41632\n"
"v_mfma_f32_16x16x32_bf16 v[188:191],a[204:207],a[12:15],v[188:191]\n"
"ds_read_b64_tr_b16 a[168:169],v240 offset:37056\n"
"ds_read_b64_tr_b16 a[170:171],v240 offset:41664\n"
"v_mfma_f32_16x16x32_bf16 v[192:195],a[208:211],a[8:11],v[192:195]\n"
"ds_read_b64_tr_b16 a[172:173],v240 offset:37088\n"
"ds_read_b64_tr_b16 a[174:175],v240 offset:41696\n"
"v_mfma_f32_16x16x32_bf16 v[196:199],a[208:211],a[12:15],v[196:199]\n"
"buffer_load_dwordx4 v209,s[40:43],s62 offen offset:1024 lds\n"
"v_mfma_f32_16x16x32_bf16 v[200:203],a[212:215],a[8:11],v[200:203]\n"
"ds_read_b64_tr_b16 a[176:177],v240 offset:37120\n"
"ds_read_b64_tr_b16 a[178:179],v240 offset:41728\n"
"v_mfma_f32_16x16x32_bf16 v[204:207],a[212:215],a[12:15],v[204:207]\n"
"s_waitcnt lgkmcnt(12)\n"
"v_mfma_f32_16x16x32_bf16 v[136:139],a[144:147],a[16:19],v[136:139]\n"
"ds_read_b64_tr_b16 a[180:181],v240 offset:46080\n"
"ds_read_b64_tr_b16 a[182:183],v240 offset:50688\n"
"v_mfma_f32_16x16x32_bf16 v[140:143],a[144:147],a[20:23],v[140:143]\n"
"v_mfma_f32_16x16x32_bf16 v[144:147],a[148:151],a[16:19],v[144:147]\n"
"ds_read_b64_tr_b16 a[184:185],v240 offset:46112\n"
"ds_read_b64_tr_b16 a[186:187],v240 offset:50720\n"
"v_mfma_f32_16x16x32_bf16 v[148:151],a[148:151],a[20:23],v[148:151]\n"
"buffer_load_dwordx4 v210,s[40:43],s62 offen offset:2048 lds\n"
"v_mfma_f32_16x16x32_bf16 v[152:155],a[152:155],a[16:19],v[152:155]\n"
"ds_read_b64_tr_b16 a[188:189],v240 offset:46144\n"
"ds_read_b64_tr_b16 a[190:191],v240 offset:50752\n"
"v_mfma_f32_16x16x32_bf16 v[156:159],a[152:155],a[20:23],v[156:159]\n"
"s_waitcnt lgkmcnt(12)\n"
"v_mfma_f32_16x16x32_bf16 v[160:163],a[156:159],a[16:19],v[160:163]\n"
"ds_read_b64_tr_b16 a[192:193],v240 offset:46176\n"
"ds_read_b64_tr_b16 a[194:195],v240 offset:50784\n"
"v_mfma_f32_16x16x32_bf16 v[164:167],a[156:159],a[20:23],v[164:167]\n"
"v_mfma_f32_16x16x32_bf16 v[168:171],a[160:163],a[16:19],v[168:171]\n"
"ds_read_b64_tr_b16 a[196:197],v240 offset:46208\n"
"ds_read_b64_tr_b16 a[198:199],v240 offset:50816\n"
"v_mfma_f32_16x16x32_bf16 v[172:175],a[160:163],a[20:23],v[172:175]\n"
"v_mfma_f32_16x16x32_bf16 v[176:179],a[164:167],a[16:19],v[176:179]\n"
"ds_read_b64_tr_b16 a[200:201],v240 offset:46240\n"
"ds_read_b64_tr_b16 a[202:203],v240 offset:50848\n"
"v_mfma_f32_16x16x32_bf16 v[180:183],a[164:167],a[20:23],v[180:183]\n"
"buffer_load_dwordx4 v211,s[40:43],s62 offen offset:3072 lds\n"
"s_waitcnt lgkmcnt(12)\n"
"v_mfma_f32_16x16x32_bf16 v[184:187],a[168:171],a[16:19],v[184:187]\n"
"ds_read_b64_tr_b16 a[204:205],v240 offset:46272\n"
"ds_read_b64_tr_b16 a[206:207],v240 offset:50880\n"
"v_mfma_f32_16x16x32_bf16 v[188:191],a[168:171],a[20:23],v[188:191]\n"
"v_mfma_f32_16x16x32_bf16 v[192:195],a[172:175],a[16:19],v[192:195]\n"
"ds_read_b64_tr_b16 a[208:209],v240 offset:46304\n"
"ds_read_b64_tr_b16 a[210:211],v240 offset:50912\n"
"v_mfma_f32_16x16x32_bf16 v[196:199],a[172:175],a[20:23],v[196:199]\n"
"s_add_u32 m0,s60,4096\n"
"v_mfma_f32_16x16x32_bf16 v[200:203],a[176:179],a[16:19],v[200:203]\n"
"ds_read_b64_tr_b16 a[212:213],v240 offset:46336\n"
"ds_read_b64_tr_b16 a[214:215],v240 offset:50944\n"
"v_mfma_f32_16x16x32_bf16 v[204:207],a[176:179],a[20:23],v[204:207]\n"
"buffer_load_dwordx4 v212,s[40:43],s62 offen offset:0 lds\n"
"s_waitcnt lgkmcnt(12)\n"
"v_mfma_f32_16x16x32_bf16 v[136:139],a[180:183],a[24:27],v[136:139]\n"
"ds_read_b64_tr_b16 a[144:145],v241 offset:0\n"
"ds_read_b64_tr_b16 a[146:147],v241 offset:4608\n"
"v_mfma_f32_16x16x32_bf16 v[140:143],a[180:183],a[28:31],v[140:143]\n"
"v_mfma_f32_16x16x32_bf16 v[144:147],a[184:187],a[24:27],v[144:147]\n"
"ds_read_b64_tr_b16 a[148:149],v241 offset:32\n"
"ds_read_b64_tr_b16 a[150:151],v241 offset:4640\n"
"v_mfma_f32_16x16x32_bf16 v[148:151],a[184:187],a[28:31],v[148:151]\n"
"v_mfma_f32_16x16x32_bf16 v[152:155],a[188:191],a[24:27],v[152:155]\n"
"ds_read_b64_tr_b16 a[152:153],v241 offset:64\n"
"ds_read_b64_tr_b16 a[154:155],v241 offset:4672\n"
"v_mfma_f32_16x16x32_bf16 v[156:159],a[188:191],a[28:31],v[156:159]\n"
"s_waitcnt lgkmcnt(12)\n"
"v_mfma_f32_16x16x32_bf16 v[160:163],a[192:195],a[24:27],v[160:163]\n"
"ds_read_b64_tr_b16 a[156:157],v241 offset:96\n"
"ds_read_b64_tr_b16 a[158:159],v241 offset:4704\n"
"v_mfma_f32_16x16x32_bf16 v[164:167],a[192:195],a[28:31],v[164:167]\n"
"buffer_load_dwordx4 v213,s[40:43],s62 offen offset:1024 lds\n"
"v_mfma_f32_16x16x32_bf16 v[168:171],a[196:199],a[24:27],v[168:171]\n"
"ds_read_b64_tr_b16 a[160:161],v241 offset:128\n"
"ds_read_b64_tr_b16 a[162:163],v241 offset:4736\n"
"v_mfma_f32_16x16x32_bf16 v[172:175],a[196:199],a[28:31],v[172:175]\n"
"v_mfma_f32_16x16x32_bf16 v[176:179],a[200:203],a[24:27],v[176:179]\n"
"ds_read_b64_tr_b16 a[164:165],v241 offset:160\n"
"ds_read_b64_tr_b16 a[166:167],v241 offset:4768\n"
"v_mfma_f32_16x16x32_bf16 v[180:183],a[200:203],a[28:31],v[180:183]\n"
"s_waitcnt lgkmcnt(12)\n"
"v_mfma_f32_16x16x32_bf16 v[184:187],a[204:207],a[24:27],v[184:187]\n"
"ds_read_b64_tr_b16 a[168:169],v241 offset:192\n"
"ds_read_b64_tr_b16 a[170:171],v241 offset:4800\n"
"v_mfma_f32_16x16x32_bf16 v[188:191],a[204:207],a[28:31],v[188:191]\n"
"buffer_load_dwordx4 v214,s[40:43],s62 offen offset:2048 lds\n"
"v_mfma_f32_16x16x32_bf16 v[192:195],a[208:211],a[24:27],v[192:195]\n"
"ds_read_b64_tr_b16 a[172:173],v241 offset:224\n"
"ds_read_b64_tr_b16 a[174:175],v241 offset:4832\n"
"v_mfma_f32_16x16x32_bf16 v[196:199],a[208:211],a[28:31],v[196:199]\n"
"v_mfma_f32_16x16x32_bf16 v[200:203],a[212:215],a[24:27],v[200:203]\n"
"ds_read_b64_tr_b16 a[176:177],v241 offset:256\n"
"ds_read_b64_tr_b16 a[178:179],v241 offset:4864\n"
"v_mfma_f32_16x16x32_bf16 v[204:207],a[212:215],a[28:31],v[204:207]\n"
"s_waitcnt lgkmcnt(12)\n"
"v_mfma_f32_16x16x32_bf16 v[136:139],a[144:147],a[32:35],v[136:139]\n"
"ds_read_b64_tr_b16 a[180:181],v241 offset:9216\n"
"ds_read_b64_tr_b16 a[182:183],v241 offset:13824\n"
"v_mfma_f32_16x16x32_bf16 v[140:143],a[144:147],a[36:39],v[140:143]\n"
"buffer_load_dwordx2 v[16:17],v238,s[88:91],s77 offen offset:0 sc1\n"
"v_mfma_f32_16x16x32_bf16 v[144:147],a[148:151],a[32:35],v[144:147]\n"
"ds_read_b64_tr_b16 a[184:185],v241 offset:9248\n"
"ds_read_b64_tr_b16 a[186:187],v241 offset:13856\n"
"v_mfma_f32_16x16x32_bf16 v[148:151],a[148:151],a[36:39],v[148:151]\n"
"buffer_load_dwordx2 v[18:19],v238,s[88:91],s77 offen offset:512 sc1\n"
"v_mfma_f32_16x16x32_bf16 v[152:155],a[152:155],a[32:35],v[152:155]\n"
"ds_read_b64_tr_b16 a[188:189],v241 offset:9280\n"
"ds_read_b64_tr_b16 a[190:191],v241 offset:13888\n"
"v_mfma_f32_16x16x32_bf16 v[156:159],a[152:155],a[36:39],v[156:159]\n"
"buffer_load_dwordx2 v[20:21],v238,s[88:91],s77 offen offset:1024 sc1\n"
"s_waitcnt lgkmcnt(12)\n"
"v_mfma_f32_16x16x32_bf16 v[160:163],a[156:159],a[32:35],v[160:163]\n"
"ds_read_b64_tr_b16 a[192:193],v241 offset:9312\n"
"ds_read_b64_tr_b16 a[194:195],v241 offset:13920\n"
"v_mfma_f32_16x16x32_bf16 v[164:167],a[156:159],a[36:39],v[164:167]\n"
"buffer_load_dwordx2 v[22:23],v238,s[88:91],s77 offen offset:1536 sc1\n"
"v_mfma_f32_16x16x32_bf16 v[168:171],a[160:163],a[32:35],v[168:171]\n"
"ds_read_b64_tr_b16 a[196:197],v241 offset:9344\n"
"ds_read_b64_tr_b16 a[198:199],v241 offset:13952\n"
"v_mfma_f32_16x16x32_bf16 v[172:175],a[160:163],a[36:39],v[172:175]\n"
"buffer_load_dwordx2 v[24:25],v238,s[88:91],s77 offen offset:2048 sc1\n"
"v_mfma_f32_16x16x32_bf16 v[176:179],a[164:167],a[32:35],v[176:179]\n"
"ds_read_b64_tr_b16 a[200:201],v241 offset:9376\n"
"ds_read_b64_tr_b16 a[202:203],v241 offset:13984\n"
"v_mfma_f32_16x16x32_bf16 v[180:183],a[164:167],a[36:39],v[180:183]\n"
"buffer_load_dwordx2 v[26:27],v238,s[88:91],s77 offen offset:2560 sc1\n"
"s_waitcnt lgkmcnt(12)\n"
"v_mfma_f32_16x16x32_bf16 v[184:187],a[168:171],a[32:35],v[184:187]\n"
"ds_read_b64_tr_b16 a[204:205],v241 offset:9408\n"
"ds_read_b64_tr_b16 a[206:207],v241 offset:14016\n"
"v_mfma_f32_16x16x32_bf16 v[188:191],a[168:171],a[36:39],v[188:191]\n"
"buffer_load_dwordx2 v[28:29],v238,s[88:91],s77 offen offset:3072 sc1\n"
"v_mfma_f32_16x16x32_bf16 v[192:195],a[172:175],a[32:35],v[192:195]\n"
"ds_read_b64_tr_b16 a[208:209],v241 offset:9440\n"
"ds_read_b64_tr_b16 a[210:211],v241 offset:14048\n"
"v_mfma_f32_16x16x32_bf16 v[196:199],a[172:175],a[36:39],v[196:199]\n"
"buffer_load_dwordx2 v[30:31],v238,s[88:91],s77 offen offset:3584 sc1\n"
"v_mfma_f32_16x16x32_bf16 v[200:203],a[176:179],a[32:35],v[200:203]\n"
"ds_read_b64_tr_b16 a[212:213],v241 offset:9472\n"
"ds_read_b64_tr_b16 a[214:215],v241 offset:14080\n"
"v_mfma_f32_16x16x32_bf16 v[204:207],a[176:179],a[36:39],v[204:207]\n"
"buffer_load_dwordx2 v[32:33],v238,s[88:91],s76 offen offset:0 sc1\n"
"s_waitcnt lgkmcnt(12)\n"
"v_mfma_f32_16x16x32_bf16 v[136:139],a[180:183],a[40:43],v[136:139]\n"
"v_mfma_f32_16x16x32_bf16 v[140:143],a[180:183],a[44:47],v[140:143]\n"
"buffer_load_dwordx2 v[34:35],v238,s[88:91],s76 offen offset:512 sc1\n"
"v_mfma_f32_16x16x32_bf16 v[144:147],a[184:187],a[40:43],v[144:147]\n"
"v_mfma_f32_16x16x32_bf16 v[148:151],a[184:187],a[44:47],v[148:151]\n"
"s_waitcnt vmcnt(28) lgkmcnt(0)\n"
"s_barrier\n"
"buffer_load_dwordx2 v[36:37],v238,s[88:91],s76 offen offset:1024 sc1\n"
"v_mfma_f32_16x16x32_bf16 v[152:155],a[188:191],a[40:43],v[152:155]\n"
"ds_read_b64_tr_b16 a[144:145],v241 offset:18432\n"
"ds_read_b64_tr_b16 a[146:147],v241 offset:23040\n"
"v_mfma_f32_16x16x32_bf16 v[156:159],a[188:191],a[44:47],v[156:159]\n"
"ds_read_b64_tr_b16 a[148:149],v241 offset:18464\n"
"ds_read_b64_tr_b16 a[150:151],v241 offset:23072\n"
"buffer_load_dwordx2 v[38:39],v238,s[88:91],s76 offen offset:1536 sc1\n"
"v_mfma_f32_16x16x32_bf16 v[160:163],a[192:195],a[40:43],v[160:163]\n"
"ds_read_b64_tr_b16 a[152:153],v241 offset:18496\n"
"ds_read_b64_tr_b16 a[154:155],v241 offset:23104\n"
"v_mfma_f32_16x16x32_bf16 v[164:167],a[192:195],a[44:47],v[164:167]\n"
"buffer_load_dwordx2 v[40:41],v238,s[88:91],s76 offen offset:2048 sc1\n"
"v_mfma_f32_16x16x32_bf16 v[168:171],a[196:199],a[40:43],v[168:171]\n"
"ds_read_b64_tr_b16 a[156:157],v241 offset:18528\n"
"ds_read_b64_tr_b16 a[158:159],v241 offset:23136\n"
"v_mfma_f32_16x16x32_bf16 v[172:175],a[196:199],a[44:47],v[172:175]\n"
"buffer_load_dwordx2 v[42:43],v238,s[88:91],s76 offen offset:2560 sc1\n"
"v_mfma_f32_16x16x32_bf16 v[176:179],a[200:203],a[40:43],v[176:179]\n"
"ds_read_b64_tr_b16 a[160:161],v241 offset:18560\n"
"ds_read_b64_tr_b16 a[162:163],v241 offset:23168\n"
"v_mfma_f32_16x16x32_bf16 v[180:183],a[200:203],a[44:47],v[180:183]\n"
"buffer_load_dwordx2 v[44:45],v238,s[88:91],s76 offen offset:3072 sc1\n"
"v_mfma_f32_16x16x32_bf16 v[184:187],a[204:207],a[40:43],v[184:187]\n"
"ds_read_b64_tr_b16 a[164:165],v241 offset:18592\n"
"ds_read_b64_tr_b16 a[166:167],v241 offset:23200\n"
"v_mfma_f32_16x16x32_bf16 v[188:191],a[204:207],a[44:47],v[188:191]\n"
"ds_read_b64_tr_b16 a[168:169],v241 offset:18624\n"
"ds_read_b64_tr_b16 a[170:171],v241 offset:23232\n"
"buffer_load_dwordx2 v[46:47],v238,s[88:91],s76 offen offset:3584 sc1\n"
"v_mfma_f32_16x16x32_bf16 v[192:195],a[208:211],a[40:43],v[192:195]\n"
"ds_read_b64_tr_b16 a[172:173],v241 offset:18656\n"
"ds_read_b64_tr_b16 a[174:175],v241 offset:23264\n"
"v_mfma_f32_16x16x32_bf16 v[196:199],a[208:211],a[44:47],v[196:199]\n"
"buffer_load_dwordx4 v215,s[40:43],s62 offen offset:3072 lds\n"
"v_mfma_f32_16x16x32_bf16 v[200:203],a[212:215],a[40:43],v[200:203]\n"
"ds_read_b64_tr_b16 a[176:177],v241 offset:18688\n"
"ds_read_b64_tr_b16 a[178:179],v241 offset:23296\n"
"s_add_u32 m0,s60,8192\n"
"v_mfma_f32_16x16x32_bf16 v[204:207],a[212:215],a[44:47],v[204:207]\n"
"buffer_load_dwordx4 v216,s[40:43],s62 offen offset:0 lds\n"
"s_waitcnt lgkmcnt(12)\n"
"v_mfma_f32_16x16x32_bf16 v[136:139],a[144:147],a[48:51],v[136:139]\n"
"ds_read_b64_tr_b16 a[180:181],v241 offset:27648\n"
"ds_read_b64_tr_b16 a[182:183],v241 offset:32256\n"
"v_mfma_f32_16x16x32_bf16 v[140:143],a[144:147],a[52:55],v[140:143]\n"
"v_mfma_f32_16x16x32_bf16 v[144:147],a[148:151],a[48:51],v[144:147]\n"
"ds_read_b64_tr_b16 a[184:185],v241 offset:27680\n"
"ds_read_b64_tr_b16 a[186:187],v241 offset:32288\n"
"v_mfma_f32_16x16x32_bf16 v[148:151],a[148:151],a[52:55],v[148:151]\n"
"v_mfma_f32_16x16x32_bf16 v[152:155],a[152:155],a[48:51],v[152:155]\n"
"ds_read_b64_tr_b16 a[188:189],v241 offset:27712\n"
"ds_read_b64_tr_b16 a[190:191],v241 offset:32320\n"
"v_mfma_f32_16x16x32_bf16 v[156:159],a[152:155],a[52:55],v[156:159]\n"
"s_waitcnt lgkmcnt(12)\n"
"v_mfma_f32_16x16x32_bf16 v[160:163],a[156:159],a[48:51],v[160:163]\n"
"ds_read_b64_tr_b16 a[192:193],v241 offset:27744\n"
"ds_read_b64_tr_b16 a[194:195],v241 offset:32352\n"
"v_mfma_f32_16x16x32_bf16 v[164:167],a[156:159],a[52:55],v[164:167]\n"
"buffer_load_dwordx4 a[4:7],v9,s[44:47],s16 offen offset:1024 sc1\n"
"v_mfma_f32_16x16x32_bf16 v[168:171],a[160:163],a[48:51],v[168:171]\n"
"ds_read_b64_tr_b16 a[196:197],v241 offset:27776\n"
"ds_read_b64_tr_b16 a[198:199],v241 offset:32384\n"
"v_mfma_f32_16x16x32_bf16 v[172:175],a[160:163],a[52:55],v[172:175]\n"
"v_mfma_f32_16x16x32_bf16 v[176:179],a[164:167],a[48:51],v[176:179]\n"
"ds_read_b64_tr_b16 a[200:201],v241 offset:27808\n"
"ds_read_b64_tr_b16 a[202:203],v241 offset:32416\n"
"v_mfma_f32_16x16x32_bf16 v[180:183],a[164:167],a[52:55],v[180:183]\n"
"s_add_u32 s16,s15,s81\n"
"s_waitcnt lgkmcnt(12)\n"
"v_mfma_f32_16x16x32_bf16 v[184:187],a[168:171],a[48:51],v[184:187]\n"
"ds_read_b64_tr_b16 a[204:205],v241 offset:27840\n"
"ds_read_b64_tr_b16 a[206:207],v241 offset:32448\n"
"v_mfma_f32_16x16x32_bf16 v[188:191],a[168:171],a[52:55],v[188:191]\n"
"buffer_load_dwordx4 a[8:11],v9,s[44:47],s16 offen offset:2048 sc1\n"
"v_mfma_f32_16x16x32_bf16 v[192:195],a[172:175],a[48:51],v[192:195]\n"
"ds_read_b64_tr_b16 a[208:209],v241 offset:27872\n"
"ds_read_b64_tr_b16 a[210:211],v241 offset:32480\n"
"v_mfma_f32_16x16x32_bf16 v[196:199],a[172:175],a[52:55],v[196:199]\n"
"v_mfma_f32_16x16x32_bf16 v[200:203],a[176:179],a[48:51],v[200:203]\n"
"ds_read_b64_tr_b16 a[212:213],v241 offset:27904\n"
"ds_read_b64_tr_b16 a[214:215],v241 offset:32512\n"
"v_mfma_f32_16x16x32_bf16 v[204:207],a[176:179],a[52:55],v[204:207]\n"
"s_waitcnt lgkmcnt(12)\n"
"v_mfma_f32_16x16x32_bf16 v[136:139],a[180:183],a[56:59],v[136:139]\n"
"ds_read_b64_tr_b16 a[144:145],v241 offset:36864\n"
"ds_read_b64_tr_b16 a[146:147],v241 offset:41472\n"
"v_mfma_f32_16x16x32_bf16 v[140:143],a[180:183],a[60:63],v[140:143]\n"
"buffer_load_dwordx4 a[12:15],v9,s[44:47],s16 offen offset:3072 sc1\n"
"v_mfma_f32_16x16x32_bf16 v[144:147],a[184:187],a[56:59],v[144:147]\n"
"ds_read_b64_tr_b16 a[148:149],v241 offset:36896\n"
"ds_read_b64_tr_b16 a[150:151],v241 offset:41504\n"
"v_mfma_f32_16x16x32_bf16 v[148:151],a[184:187],a[60:63],v[148:151]\n"
"v_mfma_f32_16x16x32_bf16 v[152:155],a[188:191],a[56:59],v[152:155]\n"
"ds_read_b64_tr_b16 a[152:153],v241 offset:36928\n"
"ds_read_b64_tr_b16 a[154:155],v241 offset:41536\n"
"v_mfma_f32_16x16x32_bf16 v[156:159],a[188:191],a[60:63],v[156:159]\n"
"s_add_u32 s16,s15,s82\n"
"s_waitcnt lgkmcnt(12)\n"
"v_mfma_f32_16x16x32_bf16 v[160:163],a[192:195],a[56:59],v[160:163]\n"
"ds_read_b64_tr_b16 a[156:157],v241 offset:36960\n"
"ds_read_b64_tr_b16 a[158:159],v241 offset:41568\n"
"v_mfma_f32_16x16x32_bf16 v[164:167],a[192:195],a[60:63],v[164:167]\n"
"buffer_load_dwordx4 a[16:19],v9,s[44:47],s16 offen offset:0 sc1\n"
"v_mfma_f32_16x16x32_bf16 v[168:171],a[196:199],a[56:59],v[168:171]\n"
"ds_read_b64_tr_b16 a[160:161],v241 offset:36992\n"
"ds_read_b64_tr_b16 a[162:163],v241 offset:41600\n"
"v_mfma_f32_16x16x32_bf16 v[172:175],a[196:199],a[60:63],v[172:175]\n"
"v_mfma_f32_16x16x32_bf16 v[176:179],a[200:203],a[56:59],v[176:179]\n"
"ds_read_b64_tr_b16 a[164:165],v241 offset:37024\n"
"ds_read_b64_tr_b16 a[166:167],v241 offset:41632\n"
"v_mfma_f32_16x16x32_bf16 v[180:183],a[200:203],a[60:63],v[180:183]\n"
"s_waitcnt lgkmcnt(12)\n"
"v_mfma_f32_16x16x32_bf16 v[184:187],a[204:207],a[56:59],v[184:187]\n"
"ds_read_b64_tr_b16 a[168:169],v241 offset:37056\n"
"ds_read_b64_tr_b16 a[170:171],v241 offset:41664\n"
"v_mfma_f32_16x16x32_bf16 v[188:191],a[204:207],a[60:63],v[188:191]\n"
"buffer_load_dwordx4 a[20:23],v9,s[44:47],s16 offen offset:1024 sc1\n"
"v_mfma_f32_16x16x32_bf16 v[192:195],a[208:211],a[56:59],v[192:195]\n"
"ds_read_b64_tr_b16 a[172:173],v241 offset:37088\n"
"ds_read_b64_tr_b16 a[174:175],v241 offset:41696\n"
"v_mfma_f32_16x16x32_bf16 v[196:199],a[208:211],a[60:63],v[196:199]\n"
"v_mfma_f32_16x16x32_bf16 v[200:203],a[212:215],a[56:59],v[200:203]\n"
"ds_read_b64_tr_b16 a[176:177],v241 offset:37120\n"
"ds_read_b64_tr_b16 a[178:179],v241 offset:41728\n"
"v_mfma_f32_16x16x32_bf16 v[204:207],a[212:215],a[60:63],v[204:207]\n"
"s_add_u32 s16,s15,s83\n"
"s_waitcnt lgkmcnt(12)\n"
"v_mfma_f32_16x16x32_bf16 v[136:139],a[144:147],a[64:67],v[136:139]\n"
"ds_read_b64_tr_b16 a[180:181],v241 offset:46080\n"
"ds_read_b64_tr_b16 a[182:183],v241 offset:50688\n"
"v_mfma_f32_16x16x32_bf16 v[140:143],a[144:147],a[68:71],v[140:143]\n"
"buffer_load_dwordx4 a[24:27],v9,s[44:47],s16 offen offset:2048 sc1\n"
"v_mfma_f32_16x16x32_bf16 v[144:147],a[148:151],a[64:67],v[144:147]\n"
"ds_read_b64_tr_b16 a[184:185],v241 offset:46112\n"
"ds_read_b64_tr_b16 a[186:187],v241 offset:50720\n"
"v_mfma_f32_16x16x32_bf16 v[148:151],a[148:151],a[68:71],v[148:151]\n"
"v_mfma_f32_16x16x32_bf16 v[152:155],a[152:155],a[64:67],v[152:155]\n"
"ds_read_b64_tr_b16 a[188:189],v241 offset:46144\n"
"ds_read_b64_tr_b16 a[190:191],v241 offset:50752\n"
"s_waitcnt vmcnt(8)\n"
"v_min3_u32 v236,v17,v19,s38\n"
"v_max3_u32 v239,v17,v19,s38\n"
"v_min3_u32 v236,v21,v23,v236\n"
"v_max3_u32 v239,v21,v23,v239\n"
"v_min3_u32 v236,v25,v27,v236\n"
"v_max3_u32 v239,v25,v27,v239\n"
"v_min3_u32 v236,v29,v31,v236\n"
"v_max3_u32 v239,v29,v31,v239\n"
"v_min3_u32 v236,v33,v35,v236\n"
"v_max3_u32 v239,v33,v35,v239\n"
"v_min3_u32 v236,v37,v39,v236\n"
"v_max3_u32 v239,v37,v39,v239\n"
"v_min3_u32 v236,v41,v43,v236\n"
"v_max3_u32 v239,v41,v43,v239\n"
"v_min3_u32 v236,v45,v47,v236\n"
"v_max3_u32 v239,v45,v47,v239\n"
"v_cmp_ne_u32 vcc,v236,v239\n"
"s_cbranch_vccnz L_ex_%=\n"
"L_ok_%=:\n"
"v_add_f32 v16,v16,v32\n"
"v_add_f32 v18,v18,v34\n"
"v_add_f32 v20,v20,v36\n"
"v_add_f32 v22,v22,v38\n"
"v_add_f32 v24,v24,v40\n"
"v_add_f32 v26,v26,v42\n"
"v_add_f32 v28,v28,v44\n"
"v_add_f32 v30,v30,v46\n"
"v_add_f32 v16,v16,v24\n"
"v_add_f32 v18,v18,v26\n"
"v_add_f32 v20,v20,v28\n"
"v_add_f32 v22,v22,v30\n"
"v_add_f32 v16,v16,v20\n"
"v_add_f32 v18,v18,v22\n"
"v_add_f32 v16,v16,v18\n"
"v_mov_b32 v18,v16\n"
"s_nop 1\n"
"v_permlane32_swap_b32 v16,v18\n"
"v_mov_b32 v17,v16\n"
"v_mov_b32 v19,v18\n"
"s_nop 1\n"
"v_permlane16_swap_b32 v16,v17\n"
"v_permlane16_swap_b32 v18,v19\n"
"v_mov_b32 v236,0x39e38e39\n"
"v_mul_f32 v16,0xb9e38e39,v16\n"
"v_fmaak_f32 v17,v236,v17,0x3727c5ac\n"
"v_fma_f32 v17,-v16,v16,v17\n"
"v_rsq_f32 v17,v17\n"
"s_nop 0\n"
"v_mul_f32 v16,v16,v17\n"
"v_mul_f32 v18,0xb9e38e39,v18\n"
"v_fmaak_f32 v19,v236,v19,0x3727c5ac\n"
"v_fma_f32 v19,-v18,v18,v19\n"
"v_rsq_f32 v19,v19\n"
"s_nop 0\n"
"v_mul_f32 v18,v18,v19\n"
"s_mov_b32 s77,0\n"
"s_mul_i32 s78,s34,36864\n"
"s_add_u32 s77,s77,s78\n"
"s_mul_i32 s78,s37,9216\n"
"s_add_u32 s77,s77,s78\n"
"v_pk_fma_f32 v[64:65],v[64:65],v[16:17],v[16:17] op_sel:[0,1,0] op_sel_hi:[1,1,0]\n"
"v_pk_fma_f32 v[66:67],v[66:67],v[16:17],v[16:17] op_sel:[0,1,0] op_sel_hi:[1,1,0]\n"
"v_pk_fma_f32 v[68:69],v[68:69],v[18:19],v[18:19] op_sel:[0,1,0] op_sel_hi:[1,1,0]\n"
"v_pk_fma_f32 v[70:71],v[70:71],v[18:19],v[18:19] op_sel:[0,1,0] op_sel_hi:[1,1,0]\n"
"v_cvt_pk_bf16_f32 v36,v64,v65\n"
"v_cvt_pk_bf16_f32 v37,v66,v67\n"
"v_cvt_pk_bf16_f32 v38,v68,v69\n"
"v_cvt_pk_bf16_f32 v39,v70,v71\n"
"ds_write_b128 v234,v[36:39] offset:0\n"
"ds_read_b64_tr_b16 a[216:217],v235 offset:0\n"
"ds_read_b64_tr_b16 a[218:219],v235 offset:256\n"
"v_pk_fma_f32 v[72:73],v[72:73],v[16:17],v[16:17] op_sel:[0,1,0] op_sel_hi:[1,1,0]\n"
"v_pk_fma_f32 v[74:75],v[74:75],v[16:17],v[16:17] op_sel:[0,1,0] op_sel_hi:[1,1,0]\n"
"v_pk_fma_f32 v[76:77],v[76:77],v[18:19],v[18:19] op_sel:[0,1,0] op_sel_hi:[1,1,0]\n"
"v_pk_fma_f32 v[78:79],v[78:79],v[18:19],v[18:19] op_sel:[0,1,0] op_sel_hi:[1,1,0]\n"
"v_cvt_pk_bf16_f32 v40,v72,v73\n"
"v_cvt_pk_bf16_f32 v41,v74,v75\n"
"v_cvt_pk_bf16_f32 v42,v76,v77\n"
"v_cvt_pk_bf16_f32 v43,v78,v79\n"
"ds_write_b128 v234,v[40:43] offset:1024\n"
"ds_read_b64_tr_b16 a[220:221],v235 offset:1024\n"
"ds_read_b64_tr_b16 a[222:223],v235 offset:1280\n"
"v_pk_fma_f32 v[80:81],v[80:81],v[16:17],v[16:17] op_sel:[0,1,0] op_sel_hi:[1,1,0]\n"
"v_pk_fma_f32 v[82:83],v[82:83],v[16:17],v[16:17] op_sel:[0,1,0] op_sel_hi:[1,1,0]\n"
"v_pk_fma_f32 v[84:85],v[84:85],v[18:19],v[18:19] op_sel:[0,1,0] op_sel_hi:[1,1,0]\n"
"v_pk_fma_f32 v[86:87],v[86:87],v[18:19],v[18:19] op_sel:[0,1,0] op_sel_hi:[1,1,0]\n"
"v_cvt_pk_bf16_f32 v44,v80,v81\n"
"v_cvt_pk_bf16_f32 v45,v82,v83\n"
"v_cvt_pk_bf16_f32 v46,v84,v85\n"
"v_cvt_pk_bf16_f32 v47,v86,v87\n"
"ds_write_b128 v234,v[44:47] offset:2048\n"
"ds_read_b64_tr_b16 a[224:225],v235 offset:2048\n"
"ds_read_b64_tr_b16 a[226:227],v235 offset:2304\n"
"s_add_u32 s76,s77,0\n"
"s_waitcnt lgkmcnt(6)\n"
"buffer_store_dwordx4 a[216:219],v9,s[56:59],s76 offen offset:0 sc1\n"
"v_pk_fma_f32 v[88:89],v[88:89],v[16:17],v[16:17] op_sel:[0,1,0] op_sel_hi:[1,1,0]\n"
"v_pk_fma_f32 v[90:91],v[90:91],v[16:17],v[16:17] op_sel:[0,1,0] op_sel_hi:[1,1,0]\n"
"v_pk_fma_f32 v[92:93],v[92:93],v[18:19],v[18:19] op_sel:[0,1,0] op_sel_hi:[1,1,0]\n"
"v_pk_fma_f32 v[94:95],v[94:95],v[18:19],v[18:19] op_sel:[0,1,0] op_sel_hi:[1,1,0]\n"
"v_cvt_pk_bf16_f32 v48,v88,v89\n"
"v_cvt_pk_bf16_f32 v49,v90,v91\n"
"v_cvt_pk_bf16_f32 v50,v92,v93\n"
"v_cvt_pk_bf16_f32 v51,v94,v95\n"
"ds_write_b128 v234,v[48:51] offset:3072\n"
"ds_read_b64_tr_b16 a[228:229],v235 offset:3072\n"
"ds_read_b64_tr_b16 a[230:231],v235 offset:3328\n"
"s_waitcnt lgkmcnt(6)\n"
"buffer_store_dwordx4 a[220:223],v9,s[56:59],s76 offen offset:1024 sc1\n"
"v_pk_fma_f32 v[96:97],v[96:97],v[16:17],v[16:17] op_sel:[0,1,0] op_sel_hi:[1,1,0]\n"
"L_pgs272_%=:\n"
"s_cbranch_execz L_pgs336_%=\n"
"v_pk_fma_f32 v[98:99],v[98:99],v[16:17],v[16:17] op_sel:[0,1,0] op_sel_hi:[1,1,0]\n"
"v_pk_fma_f32 v[100:101],v[100:101],v[18:19],v[18:19] op_sel:[0,1,0] op_sel_hi:[1,1,0]\n"
"v_pk_fma_f32 v[102:103],v[102:103],v[18:19],v[18:19] op_sel:[0,1,0] op_sel_hi:[1,1,0]\n"
"v_cvt_pk_bf16_f32 v52,v96,v97\n"
"v_cvt_pk_bf16_f32 v53,v98,v99\n"
"v_cvt_pk_bf16_f32 v54,v100,v101\n"
"v_cvt_pk_bf16_f32 v55,v102,v103\n"
"ds_write_b128 v234,v[52:55] offset:0\n"
"ds_read_b64_tr_b16 a[232:233],v235 offset:0\n"
"ds_read_b64_tr_b16 a[234:235],v235 offset:256\n"
"s_waitcnt lgkmcnt(6)\n"
"buffer_store_dwordx4 a[224:227],v9,s[56:59],s76 offen offset:2048 sc1\n"
"v_pk_fma_f32 v[104:105],v[104:105],v[16:17],v[16:17] op_sel:[0,1,0] op_sel_hi:[1,1,0]\n"
"v_pk_fma_f32 v[106:107],v[106:107],v[16:17],v[16:17] op_sel:[0,1,0] op_sel_hi:[1,1,0]\n"
"v_pk_fma_f32 v[108:109],v[108:109],v[18:19],v[18:19] op_sel:[0,1,0] op_sel_hi:[1,1,0]\n"
"v_pk_fma_f32 v[110:111],v[110:111],v[18:19],v[18:19] op_sel:[0,1,0] op_sel_hi:[1,1,0]\n"
"v_cvt_pk_bf16_f32 v56,v104,v105\n"
"v_cvt_pk_bf16_f32 v57,v106,v107\n"
"v_cvt_pk_bf16_f32 v58,v108,v109\n"
"v_cvt_pk_bf16_f32 v59,v110,v111\n"
"ds_write_b128 v234,v[56:59] offset:1024\n"
"ds_read_b64_tr_b16 a[236:237],v235 offset:1024\n"
"ds_read_b64_tr_b16 a[238:239],v235 offset:1280\n"
"s_waitcnt lgkmcnt(6)\n"
"buffer_store_dwordx4 a[228:231],v9,s[56:59],s76 offen offset:3072 sc1\n"
"v_pk_fma_f32 v[112:113],v[112:113],v[16:17],v[16:17] op_sel:[0,1,0] op_sel_hi:[1,1,0]\n"
"v_pk_fma_f32 v[114:115],v[114:115],v[16:17],v[16:17] op_sel:[0,1,0] op_sel_hi:[1,1,0]\n"
"v_pk_fma_f32 v[116:117],v[116:117],v[18:19],v[18:19] op_sel:[0,1,0] op_sel_hi:[1,1,0]\n"
"v_pk_fma_f32 v[118:119],v[118:119],v[18:19],v[18:19] op_sel:[0,1,0] op_sel_hi:[1,1,0]\n"
"v_cvt_pk_bf16_f32 v36,v112,v113\n"
"v_cvt_pk_bf16_f32 v37,v114,v115\n"
"v_cvt_pk_bf16_f32 v38,v116,v117\n"
"v_cvt_pk_bf16_f32 v39,v118,v119\n"
"ds_write_b128 v234,v[36:39] offset:2048\n"
"ds_read_b64_tr_b16 a[240:241],v235 offset:2048\n"
"ds_read_b64_tr_b16 a[242:243],v235 offset:2304\n"
"s_add_u32 s76,s77,4096\n"
"s_waitcnt lgkmcnt(6)\n"
"buffer_store_dwordx4 a[232:235],v9,s[56:59],s76 offen offset:0 sc1\n"
"v_pk_fma_f32 v[120:121],v[120:121],v[16:17],v[16:17] op_sel:[0,1,0] op_sel_hi:[1,1,0]\n"
"v_pk_fma_f32 v[122:123],v[122:123],v[16:17],v[16:17] op_sel:[0,1,0] op_sel_hi:[1,1,0]\n"
"v_pk_fma_f32 v[124:125],v[124:125],v[18:19],v[18:19] op_sel:[0,1,0] op_sel_hi:[1,1,0]\n"
"v_pk_fma_f32 v[126:127],v[126:127],v[18:19],v[18:19] op_sel:[0,1,0] op_sel_hi:[1,1,0]\n"
"v_cvt_pk_bf16_f32 v40,v120,v121\n"
"v_cvt_pk_bf16_f32 v41,v122,v123\n"
"v_cvt_pk_bf16_f32 v42,v124,v125\n"
"v_cvt_pk_bf16_f32 v43,v126,v127\n"
"ds_write_b128 v234,v[40:43] offset:3072\n"
"ds_read_b64_tr_b16 a[244:245],v235 offset:3072\n"
"ds_read_b64_tr_b16 a[246:247],v235 offset:3328\n"
"s_waitcnt lgkmcnt(6)\n"
"buffer_store_dwordx4 a[236:239],v9,s[56:59],s76 offen offset:1024 sc1\n"
"v_pk_fma_f32 v[128:129],v[128:129],v[16:17],v[16:17] op_sel:[0,1,0] op_sel_hi:[1,1,0]\n"
"v_pk_fma_f32 v[130:131],v[130:131],v[16:17],v[16:17] op_sel:[0,1,0] op_sel_hi:[1,1,0]\n"
"v_pk_fma_f32 v[132:133],v[132:133],v[18:19],v[18:19] op_sel:[0,1,0] op_sel_hi:[1,1,0]\n"
"v_pk_fma_f32 v[134:135],v[134:135],v[18:19],v[18:19] op_sel:[0,1,0] op_sel_hi:[1,1,0]\n"
"v_cvt_pk_bf16_f32 v44,v128,v129\n"
"v_cvt_pk_bf16_f32 v45,v130,v131\n"
"v_cvt_pk_bf16_f32 v46,v132,v133\n"
"v_cvt_pk_bf16_f32 v47,v134,v135\n"
"ds_write_b128 v234,v[44:47] offset:0\n"
"ds_read_b64_tr_b16 a[248:249],v235 offset:0\n"
"ds_read_b64_tr_b16 a[250:251],v235 offset:256\n"
"s_waitcnt lgkmcnt(6)\n"
"buffer_store_dwordx4 a[240:243],v9,s[56:59],s76 offen offset:2048 sc1\n"
"s_mov_b32 s75,393216\n"
"s_mul_i32 s78,s36,2048\n"
"s_add_u32 s75,s75,s78\n"
"s_mul_i32 s78,s35,512\n"
"s_add_u32 s75,s75,s78\n"
"s_mul_i32 s78,s37,128\n"
"s_add_u32 s75,s75,s78\n"
"s_waitcnt lgkmcnt(3)\n"
"buffer_store_dwordx4 a[244:247],v9,s[56:59],s76 offen offset:3072 sc1\n"
"s_add_u32 s76,s77,8192\n"
"s_waitcnt lgkmcnt(0)\n"
"buffer_store_dwordx4 a[248:251],v9,s[56:59],s76 offen offset:0 sc1\n"
"v_mfma_f32_16x16x32_bf16 v[156:159],a[152:155],a[68:71],v[156:159]\n"
"s_mov_b32 s76,s75\n"
"s_mul_i32 s78,s34,4\n"
"s_add_u32 s76,s76,s78\n"
"v_mfma_f32_16x16x32_bf16 v[160:163],a[156:159],a[64:67],v[160:163]\n"
"ds_read_b64_tr_b16 a[192:193],v241 offset:46176\n"
"ds_read_b64_tr_b16 a[194:195],v241 offset:50784\n"
"v_mfma_f32_16x16x32_bf16 v[164:167],a[156:159],a[68:71],v[164:167]\n"
"buffer_load_dwordx4 a[28:31],v9,s[44:47],s16 offen offset:3072 sc1\n"
"v_mfma_f32_16x16x32_bf16 v[168:171],a[160:163],a[64:67],v[168:171]\n"
"ds_read_b64_tr_b16 a[196:197],v241 offset:46208\n"
"ds_read_b64_tr_b16 a[198:199],v241 offset:50816\n"
"v_mfma_f32_16x16x32_bf16 v[172:175],a[160:163],a[68:71],v[172:175]\n"
"v_mfma_f32_16x16x32_bf16 v[176:179],a[164:167],a[64:67],v[176:179]\n"
"ds_read_b64_tr_b16 a[200:201],v241 offset:46240\n"
"ds_read_b64_tr_b16 a[202:203],v241 offset:50848\n"
"v_mfma_f32_16x16x32_bf16 v[180:183],a[164:167],a[68:71],v[180:183]\n"
"s_add_u32 m0,s84,36864\n"
"s_cmp_le_u32 s34,1\n"
"s_cselect_b32 s72,8,4\n"
"s_add_u32 s72,s72,s37\n"
"s_lshl_b32 s72,s72,13\n"
"s_add_u32 s73,s72,s80\n"
"v_mfma_f32_16x16x32_bf16 v[184:187],a[168:171],a[64:67],v[184:187]\n"
"ds_read_b64_tr_b16 a[204:205],v241 offset:46272\n"
"ds_read_b64_tr_b16 a[206:207],v241 offset:50880\n"
"v_mfma_f32_16x16x32_bf16 v[188:191],a[168:171],a[68:71],v[188:191]\n"
"buffer_load_dwordx4 v9,s[44:47],s73 offen offset:0 sc1 lds\n"
"v_mfma_f32_16x16x32_bf16 v[192:195],a[172:175],a[64:67],v[192:195]\n"
"ds_read_b64_tr_b16 a[208:209],v241 offset:46304\n"
"ds_read_b64_tr_b16 a[210:211],v241 offset:50912\n"
"v_mfma_f32_16x16x32_bf16 v[196:199],a[172:175],a[68:71],v[196:199]\n"
"v_mfma_f32_16x16x32_bf16 v[200:203],a[176:179],a[64:67],v[200:203]\n"
"ds_read_b64_tr_b16 a[212:213],v241 offset:46336\n"
"ds_read_b64_tr_b16 a[214:215],v241 offset:50944\n"
"v_mfma_f32_16x16x32_bf16 v[204:207],a[176:179],a[68:71],v[204:207]\n"
"v_mfma_f32_16x16x32_bf16 v[136:139],a[180:183],a[72:75],v[136:139]\n"
"v_mfma_f32_16x16x32_bf16 v[140:143],a[180:183],a[76:79],v[140:143]\n"
"buffer_load_dwordx4 v9,s[44:47],s73 offen offset:1024 sc1 lds\n"
"v_mfma_f32_16x16x32_bf16 v[144:147],a[184:187],a[72:75],v[144:147]\n"
"v_mfma_f32_16x16x32_bf16 v[148:151],a[184:187],a[76:79],v[148:151]\n"
"s_waitcnt lgkmcnt(0)\n"
"s_barrier\n"
"v_mfma_f32_16x16x32_bf16 v[152:155],a[188:191],a[72:75],v[152:155]\n"
"ds_read_b64_tr_b16 a[144:145],v242 offset:0\n"
"ds_read_b64_tr_b16 a[146:147],v242 offset:4608\n"
"v_mfma_f32_16x16x32_bf16 v[156:159],a[188:191],a[76:79],v[156:159]\n"
"ds_read_b64_tr_b16 a[148:149],v242 offset:32\n"
"ds_read_b64_tr_b16 a[150:151],v242 offset:4640\n"
"s_add_u32 s73,s72,s81\n"
"v_mfma_f32_16x16x32_bf16 v[160:163],a[192:195],a[72:75],v[160:163]\n"
"ds_read_b64_tr_b16 a[152:153],v242 offset:64\n"
"ds_read_b64_tr_b16 a[154:155],v242 offset:4672\n"
"v_mfma_f32_16x16x32_bf16 v[164:167],a[192:195],a[76:79],v[164:167]\n"
"buffer_load_dwordx4 v9,s[44:47],s73 offen offset:2048 sc1 lds\n"
"v_mfma_f32_16x16x32_bf16 v[168:171],a[196:199],a[72:75],v[168:171]\n"
"ds_read_b64_tr_b16 a[156:157],v242 offset:96\n"
"ds_read_b64_tr_b16 a[158:159],v242 offset:4704\n"
"v_mfma_f32_16x16x32_bf16 v[172:175],a[196:199],a[76:79],v[172:175]\n"
"v_mfma_f32_16x16x32_bf16 v[176:179],a[200:203],a[72:75],v[176:179]\n"
"ds_read_b64_tr_b16 a[160:161],v242 offset:128\n"
"ds_read_b64_tr_b16 a[162:163],v242 offset:4736\n"
"v_mfma_f32_16x16x32_bf16 v[180:183],a[200:203],a[76:79],v[180:183]\n"
"v_mfma_f32_16x16x32_bf16 v[184:187],a[204:207],a[72:75],v[184:187]\n"
"ds_read_b64_tr_b16 a[164:165],v242 offset:160\n"
"ds_read_b64_tr_b16 a[166:167],v242 offset:4768\n"
"v_mfma_f32_16x16x32_bf16 v[188:191],a[204:207],a[76:79],v[188:191]\n"
"ds_read_b64_tr_b16 a[168:169],v242 offset:192\n"
"ds_read_b64_tr_b16 a[170:171],v242 offset:4800\n"
"v_mfma_f32_16x16x32_bf16 v[192:195],a[208:211],a[72:75],v[192:195]\n"
"ds_read_b64_tr_b16 a[172:173],v242 offset:224\n"
"ds_read_b64_tr_b16 a[174:175],v242 offset:4832\n"
"v_mfma_f32_16x16x32_bf16 v[196:199],a[208:211],a[76:79],v[196:199]\n"
"buffer_load_dwordx4 v9,s[44:47],s73 offen offset:3072 sc1 lds\n"
"v_mfma_f32_16x16x32_bf16 v[200:203],a[212:215],a[72:75],v[200:203]\n"
"ds_read_b64_tr_b16 a[176:177],v242 offset:256\n"
"ds_read_b64_tr_b16 a[178:179],v242 offset:4864\n"
"v_mfma_f32_16x16x32_bf16 v[204:207],a[212:215],a[76:79],v[204:207]\n"
"s_add_u32 m0,s84,40960\n"
"s_add_u32 s73,s72,s82\n"
"s_waitcnt lgkmcnt(12)\n"
"v_mfma_f32_16x16x32_bf16 v[136:139],a[144:147],a[80:83],v[136:139]\n"
"ds_read_b64_tr_b16 a[180:181],v242 offset:9216\n"
"ds_read_b64_tr_b16 a[182:183],v242 offset:13824\n"
"v_mfma_f32_16x16x32_bf16 v[140:143],a[144:147],a[84:87],v[140:143]\n"
"buffer_load_dwordx4 v9,s[44:47],s73 offen offset:0 sc1 lds\n"
"v_mfma_f32_16x16x32_bf16 v[144:147],a[148:151],a[80:83],v[144:147]\n"
"ds_read_b64_tr_b16 a[184:185],v242 offset:9248\n"
"ds_read_b64_tr_b16 a[186:187],v242 offset:13856\n"
"v_mfma_f32_16x16x32_bf16 v[148:151],a[148:151],a[84:87],v[148:151]\n"
"v_mfma_f32_16x16x32_bf16 v[152:155],a[152:155],a[80:83],v[152:155]\n"
"ds_read_b64_tr_b16 a[188:189],v242 offset:9280\n"
"ds_read_b64_tr_b16 a[190:191],v242 offset:13888\n"
"v_mfma_f32_16x16x32_bf16 v[156:159],a[152:155],a[84:87],v[156:159]\n"
"s_waitcnt lgkmcnt(12)\n"
"v_mfma_f32_16x16x32_bf16 v[160:163],a[156:159],a[80:83],v[160:163]\n"
"ds_read_b64_tr_b16 a[192:193],v242 offset:9312\n"
"ds_read_b64_tr_b16 a[194:195],v242 offset:13920\n"
"v_mfma_f32_16x16x32_bf16 v[164:167],a[156:159],a[84:87],v[164:167]\n"
"buffer_load_dwordx4 v9,s[44:47],s73 offen offset:1024 sc1 lds\n"
"v_mfma_f32_16x16x32_bf16 v[168:171],a[160:163],a[80:83],v[168:171]\n"
"ds_read_b64_tr_b16 a[196:197],v242 offset:9344\n"
"ds_read_b64_tr_b16 a[198:199],v242 offset:13952\n"
"v_mfma_f32_16x16x32_bf16 v[172:175],a[160:163],a[84:87],v[172:175]\n"
"v_mfma_f32_16x16x32_bf16 v[176:179],a[164:167],a[80:83],v[176:179]\n"
"ds_read_b64_tr_b16 a[200:201],v242 offset:9376\n"
"ds_read_b64_tr_b16 a[202:203],v242 offset:13984\n"
"v_mfma_f32_16x16x32_bf16 v[180:183],a[164:167],a[84:87],v[180:183]\n"
"s_add_u32 s73,s72,s83\n"
"s_waitcnt lgkmcnt(12)\n"
"v_mfma_f32_16x16x32_bf16 v[184:187],a[168:171],a[80:83],v[184:187]\n"
"ds_read_b64_tr_b16 a[204:205],v242 offset:9408\n"
"ds_read_b64_tr_b16 a[206:207],v242 offset:14016\n"
"v_mfma_f32_16x16x32_bf16 v[188:191],a[168:171],a[84:87],v[188:191]\n"
"buffer_load_dwordx4 v9,s[44:47],s73 offen offset:2048 sc1 lds\n"
"v_mfma_f32_16x16x32_bf16 v[192:195],a[172:175],a[80:83],v[192:195]\n"
"ds_read_b64_tr_b16 a[208:209],v242 offset:9440\n"
"ds_read_b64_tr_b16 a[210:211],v242 offset:14048\n"
"v_mfma_f32_16x16x32_bf16 v[196:199],a[172:175],a[84:87],v[196:199]\n"
"v_mfma_f32_16x16x32_bf16 v[200:203],a[176:179],a[80:83],v[200:203]\n"
"ds_read_b64_tr_b16 a[212:213],v242 offset:9472\n"
"ds_read_b64_tr_b16 a[214:215],v242 offset:14080\n"
"v_mfma_f32_16x16x32_bf16 v[204:207],a[176:179],a[84:87],v[204:207]\n"
"s_waitcnt lgkmcnt(12)\n"
"v_mfma_f32_16x16x32_bf16 v[136:139],a[180:183],a[88:91],v[136:139]\n"
"ds_read_b64_tr_b16 a[144:145],v242 offset:18432\n"
"ds_read_b64_tr_b16 a[146:147],v242 offset:23040\n"
"v_mfma_f32_16x16x32_bf16 v[140:143],a[180:183],a[92:95],v[140:143]\n"
"buffer_load_dwordx4 v9,s[44:47],s73 offen offset:3072 sc1 lds\n"
"v_mfma_f32_16x16x32_bf16 v[144:147],a[184:187],a[88:91],v[144:147]\n"
"ds_read_b64_tr_b16 a[148:149],v242 offset:18464\n"
"ds_read_b64_tr_b16 a[150:151],v242 offset:23072\n"
"v_mfma_f32_16x16x32_bf16 v[148:151],a[184:187],a[92:95],v[148:151]\n"
"v_mfma_f32_16x16x32_bf16 v[152:155],a[188:191],a[88:91],v[152:155]\n"
"ds_read_b64_tr_b16 a[152:153],v242 offset:18496\n"
"ds_read_b64_tr_b16 a[154:155],v242 offset:23104\n"
"v_mfma_f32_16x16x32_bf16 v[156:159],a[188:191],a[92:95],v[156:159]\n"
"s_cmp_le_u32 s34,0\n"
"s_cselect_b32 s15,4,0\n"
"s_add_u32 s15,s15,s37\n"
"s_lshl_b32 s15,s15,13\n"
"s_add_u32 s16,s15,s80\n"
"s_waitcnt lgkmcnt(12)\n"
"v_mfma_f32_16x16x32_bf16 v[160:163],a[192:195],a[88:91],v[160:163]\n"
"ds_read_b64_tr_b16 a[156:157],v242 offset:18528\n"
"ds_read_b64_tr_b16 a[158:159],v242 offset:23136\n"
"v_mfma_f32_16x16x32_bf16 v[164:167],a[192:195],a[92:95],v[164:167]\n"
"buffer_load_dwordx4 a[32:35],v9,s[44:47],s16 offen offset:0 sc1\n"
"v_mfma_f32_16x16x32_bf16 v[168:171],a[196:199],a[88:91],v[168:171]\n"
"ds_read_b64_tr_b16 a[160:161],v242 offset:18560\n"
"ds_read_b64_tr_b16 a[162:163],v242 offset:23168\n"
"v_mfma_f32_16x16x32_bf16 v[172:175],a[196:199],a[92:95],v[172:175]\n"
"v_mfma_f32_16x16x32_bf16 v[176:179],a[200:203],a[88:91],v[176:179]\n"
"ds_read_b64_tr_b16 a[164:165],v242 offset:18592\n"
"ds_read_b64_tr_b16 a[166:167],v242 offset:23200\n"
"v_mfma_f32_16x16x32_bf16 v[180:183],a[200:203],a[92:95],v[180:183]\n"
"s_waitcnt lgkmcnt(12)\n"
"v_mfma_f32_16x16x32_bf16 v[184:187],a[204:207],a[88:91],v[184:187]\n"
"ds_read_b64_tr_b16 a[168:169],v242 offset:18624\n"
"ds_read_b64_tr_b16 a[170:171],v242 offset:23232\n"
"v_mfma_f32_16x16x32_bf16 v[188:191],a[204:207],a[92:95],v[188:191]\n"
"buffer_load_dwordx4 a[36:39],v9,s[44:47],s16 offen offset:1024 sc1\n"
"v_mfma_f32_16x16x32_bf16 v[192:195],a[208:211],a[88:91],v[192:195]\n"
"ds_read_b64_tr_b16 a[172:173],v242 offset:18656\n"
"ds_read_b64_tr_b16 a[174:175],v242 offset:23264\n"
"v_mfma_f32_16x16x32_bf16 v[196:199],a[208:211],a[92:95],v[196:199]\n"
"v_mfma_f32_16x16x32_bf16 v[200:203],a[212:215],a[88:91],v[200:203]\n"
"ds_read_b64_tr_b16 a[176:177],v242 offset:18688\n"
"ds_read_b64_tr_b16 a[178:179],v242 offset:23296\n"
"v_mfma_f32_16x16x32_bf16 v[204:207],a[212:215],a[92:95],v[204:207]\n"
"s_add_u32 s16,s15,s81\n"
"s_waitcnt lgkmcnt(12)\n"
"v_mfma_f32_16x16x32_bf16 v[136:139],a[144:147],a[96:99],v[136:139]\n"
"ds_read_b64_tr_b16 a[180:181],v242 offset:27648\n"
"ds_read_b64_tr_b16 a[182:183],v242 offset:32256\n"
"v_mfma_f32_16x16x32_bf16 v[140:143],a[144:147],a[100:103],v[140:143]\n"
"buffer_load_dwordx4 a[40:43],v9,s[44:47],s16 offen offset:2048 sc1\n"
"v_mfma_f32_16x16x32_bf16 v[144:147],a[148:151],a[96:99],v[144:147]\n"
"ds_read_b64_tr_b16 a[184:185],v242 offset:27680\n"
"ds_read_b64_tr_b16 a[186:187],v242 offset:32288\n"
"v_mfma_f32_16x16x32_bf16 v[148:151],a[148:151],a[100:103],v[148:151]\n"
"v_mfma_f32_16x16x32_bf16 v[152:155],a[152:155],a[96:99],v[152:155]\n"
"ds_read_b64_tr_b16 a[188:189],v242 offset:27712\n"
"ds_read_b64_tr_b16 a[190:191],v242 offset:32320\n"
"v_mfma_f32_16x16x32_bf16 v[156:159],a[152:155],a[100:103],v[156:159]\n"
"s_waitcnt lgkmcnt(12)\n"
"v_mfma_f32_16x16x32_bf16 v[160:163],a[156:159],a[96:99],v[160:163]\n"
"ds_read_b64_tr_b16 a[192:193],v242 offset:27744\n"
"ds_read_b64_tr_b16 a[194:195],v242 offset:32352\n"
"v_mfma_f32_16x16x32_bf16 v[164:167],a[156:159],a[100:103],v[164:167]\n"
"buffer_load_dwordx4 a[44:47],v9,s[44:47],s16 offen offset:3072 sc1\n"
"v_mfma_f32_16x16x32_bf16 v[168:171],a[160:163],a[96:99],v[168:171]\n"
"ds_read_b64_tr_b16 a[196:197],v242 offset:27776\n"
"ds_read_b64_tr_b16 a[198:199],v242 offset:32384\n"
"v_mfma_f32_16x16x32_bf16 v[172:175],a[160:163],a[100:103],v[172:175]\n"
"v_mfma_f32_16x16x32_bf16 v[176:179],a[164:167],a[96:99],v[176:179]\n"
"ds_read_b64_tr_b16 a[200:201],v242 offset:27808\n"
"ds_read_b64_tr_b16 a[202:203],v242 offset:32416\n"
"v_mfma_f32_16x16x32_bf16 v[180:183],a[164:167],a[100:103],v[180:183]\n"
"s_add_u32 s16,s15,s82\n"
"s_waitcnt lgkmcnt(12)\n"
"v_mfma_f32_16x16x32_bf16 v[184:187],a[168:171],a[96:99],v[184:187]\n"
"ds_read_b64_tr_b16 a[204:205],v242 offset:27840\n"
"ds_read_b64_tr_b16 a[206:207],v242 offset:32448\n"
"v_mfma_f32_16x16x32_bf16 v[188:191],a[168:171],a[100:103],v[188:191]\n"
"buffer_load_dwordx4 a[48:51],v9,s[44:47],s16 offen offset:0 sc1\n"
"v_mfma_f32_16x16x32_bf16 v[192:195],a[172:175],a[96:99],v[192:195]\n"
"ds_read_b64_tr_b16 a[208:209],v242 offset:27872\n"
"ds_read_b64_tr_b16 a[210:211],v242 offset:32480\n"
"v_mfma_f32_16x16x32_bf16 v[196:199],a[172:175],a[100:103],v[196:199]\n"
"v_mfma_f32_16x16x32_bf16 v[200:203],a[176:179],a[96:99],v[200:203]\n"
"ds_read_b64_tr_b16 a[212:213],v242 offset:27904\n"
"ds_read_b64_tr_b16 a[214:215],v242 offset:32512\n"
"v_mfma_f32_16x16x32_bf16 v[204:207],a[176:179],a[100:103],v[204:207]\n"
"s_waitcnt lgkmcnt(12)\n"
"v_mfma_f32_16x16x32_bf16 v[136:139],a[180:183],a[104:107],v[136:139]\n"
"v_mfma_f32_16x16x32_bf16 v[140:143],a[180:183],a[108:111],v[140:143]\n"
"buffer_load_dwordx4 a[52:55],v9,s[44:47],s16 offen offset:1024 sc1\n"
"v_mfma_f32_16x16x32_bf16 v[144:147],a[184:187],a[104:107],v[144:147]\n"
"v_mfma_f32_16x16x32_bf16 v[148:151],a[184:187],a[108:111],v[148:151]\n"
"s_waitcnt vmcnt(30) lgkmcnt(0)\n"
"s_barrier\n"
"v_mfma_f32_16x16x32_bf16 v[152:155],a[188:191],a[104:107],v[152:155]\n"
"ds_read_b64_tr_b16 a[144:145],v240 offset:0\n"
"ds_read_b64_tr_b16 a[146:147],v240 offset:4608\n"
"s_waitcnt vmcnt(15)\n"
"v_mov_b32 v234,s38\n"
"buffer_store_dword v234,v11,s[88:91],s76 offen sc1\n"
"v_mfma_f32_16x16x32_bf16 v[156:159],a[188:191],a[108:111],v[156:159]\n"
"ds_read_b64_tr_b16 a[148:149],v240 offset:32\n"
"ds_read_b64_tr_b16 a[150:151],v240 offset:4640\n"
"s_add_u32 s16,s15,s83\n"
"v_mfma_f32_16x16x32_bf16 v[160:163],a[192:195],a[104:107],v[160:163]\n"
"ds_read_b64_tr_b16 a[152:153],v240 offset:64\n"
"ds_read_b64_tr_b16 a[154:155],v240 offset:4672\n"
"v_mfma_f32_16x16x32_bf16 v[164:167],a[192:195],a[108:111],v[164:167]\n"
"buffer_load_dwordx4 a[56:59],v9,s[44:47],s16 offen offset:2048 sc1\n"
"v_mfma_f32_16x16x32_bf16 v[168:171],a[196:199],a[104:107],v[168:171]\n"
"ds_read_b64_tr_b16 a[156:157],v240 offset:96\n"
"ds_read_b64_tr_b16 a[158:159],v240 offset:4704\n"
"v_mfma_f32_16x16x32_bf16 v[172:175],a[196:199],a[108:111],v[172:175]\n"
"v_mfma_f32_16x16x32_bf16 v[176:179],a[200:203],a[104:107],v[176:179]\n"
"ds_read_b64_tr_b16 a[160:161],v240 offset:128\n"
"ds_read_b64_tr_b16 a[162:163],v240 offset:4736\n"
"v_mfma_f32_16x16x32_bf16 v[180:183],a[200:203],a[108:111],v[180:183]\n"
"v_mfma_f32_16x16x32_bf16 v[184:187],a[204:207],a[104:107],v[184:187]\n"
"ds_read_b64_tr_b16 a[164:165],v240 offset:160\n"
"ds_read_b64_tr_b16 a[166:167],v240 offset:4768\n"
"v_mfma_f32_16x16x32_bf16 v[188:191],a[204:207],a[108:111],v[188:191]\n"
"ds_read_b64_tr_b16 a[168:169],v240 offset:192\n"
"ds_read_b64_tr_b16 a[170:171],v240 offset:4800\n"
"v_mfma_f32_16x16x32_bf16 v[192:195],a[208:211],a[104:107],v[192:195]\n"
"ds_read_b64_tr_b16 a[172:173],v240 offset:224\n"
"ds_read_b64_tr_b16 a[174:175],v240 offset:4832\n"
"v_mfma_f32_16x16x32_bf16 v[196:199],a[208:211],a[108:111],v[196:199]\n"
"buffer_load_dwordx4 a[60:63],v9,s[44:47],s16 offen offset:3072 sc1\n"
"v_mfma_f32_16x16x32_bf16 v[200:203],a[212:215],a[104:107],v[200:203]\n"
"ds_read_b64_tr_b16 a[176:177],v240 offset:256\n"
"ds_read_b64_tr_b16 a[178:179],v240 offset:4864\n"
"v_mfma_f32_16x16x32_bf16 v[204:207],a[212:215],a[108:111],v[204:207]\n"
"s_add_u32 m0,s84,73728\n"
"s_cmp_le_u32 s34,2\n"
"s_cselect_b32 s72,12,8\n"
"s_add_u32 s72,s72,s37\n"
"s_lshl_b32 s72,s72,13\n"
"s_add_u32 s73,s72,s80\n"
"s_waitcnt lgkmcnt(12)\n"
"v_mfma_f32_16x16x32_bf16 v[136:139],a[144:147],a[112:115],v[136:139]\n"
"ds_read_b64_tr_b16 a[180:181],v240 offset:9216\n"
"ds_read_b64_tr_b16 a[182:183],v240 offset:13824\n"
"v_mfma_f32_16x16x32_bf16 v[140:143],a[144:147],a[116:119],v[140:143]\n"
"buffer_load_dwordx4 v9,s[44:47],s73 offen offset:0 sc1 lds\n"
"v_mfma_f32_16x16x32_bf16 v[144:147],a[148:151],a[112:115],v[144:147]\n"
"ds_read_b64_tr_b16 a[184:185],v240 offset:9248\n"
"ds_read_b64_tr_b16 a[186:187],v240 offset:13856\n"
"v_mfma_f32_16x16x32_bf16 v[148:151],a[148:151],a[116:119],v[148:151]\n"
"v_mfma_f32_16x16x32_bf16 v[152:155],a[152:155],a[112:115],v[152:155]\n"
"ds_read_b64_tr_b16 a[188:189],v240 offset:9280\n"
"ds_read_b64_tr_b16 a[190:191],v240 offset:13888\n"
"v_mfma_f32_16x16x32_bf16 v[156:159],a[152:155],a[116:119],v[156:159]\n"
"s_waitcnt lgkmcnt(12)\n"
"v_mfma_f32_16x16x32_bf16 v[160:163],a[156:159],a[112:115],v[160:163]\n"
"ds_read_b64_tr_b16 a[192:193],v240 offset:9312\n"
"ds_read_b64_tr_b16 a[194:195],v240 offset:13920\n"
"v_mfma_f32_16x16x32_bf16 v[164:167],a[156:159],a[116:119],v[164:167]\n"
"buffer_load_dwordx4 v9,s[44:47],s73 offen offset:1024 sc1 lds\n"
"v_mfma_f32_16x16x32_bf16 v[168:171],a[160:163],a[112:115],v[168:171]\n"
"ds_read_b64_tr_b16 a[196:197],v240 offset:9344\n"
"ds_read_b64_tr_b16 a[198:199],v240 offset:13952\n"
"v_mfma_f32_16x16x32_bf16 v[172:175],a[160:163],a[116:119],v[172:175]\n"
"v_mfma_f32_16x16x32_bf16 v[176:179],a[164:167],a[112:115],v[176:179]\n"
"ds_read_b64_tr_b16 a[200:201],v240 offset:9376\n"
"ds_read_b64_tr_b16 a[202:203],v240 offset:13984\n"
"v_mfma_f32_16x16x32_bf16 v[180:183],a[164:167],a[116:119],v[180:183]\n"
"s_add_u32 s73,s72,s81\n"
"s_waitcnt lgkmcnt(12)\n"
"v_mfma_f32_16x16x32_bf16 v[184:187],a[168:171],a[112:115],v[184:187]\n"
"ds_read_b64_tr_b16 a[204:205],v240 offset:9408\n"
"ds_read_b64_tr_b16 a[206:207],v240 offset:14016\n"
"v_mfma_f32_16x16x32_bf16 v[188:191],a[168:171],a[116:119],v[188:191]\n"
"buffer_load_dwordx4 v9,s[44:47],s73 offen offset:2048 sc1 lds\n"
"v_mfma_f32_16x16x32_bf16 v[192:195],a[172:175],a[112:115],v[192:195]\n"
"ds_read_b64_tr_b16 a[208:209],v240 offset:9440\n"
"ds_read_b64_tr_b16 a[210:211],v240 offset:14048\n"
"v_mfma_f32_16x16x32_bf16 v[196:199],a[172:175],a[116:119],v[196:199]\n"
"v_mfma_f32_16x16x32_bf16 v[200:203],a[176:179],a[112:115],v[200:203]\n"
"ds_read_b64_tr_b16 a[212:213],v240 offset:9472\n"
"ds_read_b64_tr_b16 a[214:215],v240 offset:14080\n"
"v_mfma_f32_16x16x32_bf16 v[204:207],a[176:179],a[116:119],v[204:207]\n"
"s_waitcnt lgkmcnt(12)\n"
"v_mfma_f32_16x16x32_bf16 v[136:139],a[180:183],a[120:123],v[136:139]\n"
"ds_read_b64_tr_b16 a[144:145],v240 offset:18432\n"
"ds_read_b64_tr_b16 a[146:147],v240 offset:23040\n"
"v_mfma_f32_16x16x32_bf16 v[140:143],a[180:183],a[124:127],v[140:143]\n"
"buffer_load_dwordx4 v9,s[44:47],s73 offen offset:3072 sc1 lds\n"
"v_mfma_f32_16x16x32_bf16 v[144:147],a[184:187],a[120:123],v[144:147]\n"
"ds_read_b64_tr_b16 a[148:149],v240 offset:18464\n"
"ds_read_b64_tr_b16 a[150:151],v240 offset:23072\n"
"v_mfma_f32_16x16x32_bf16 v[148:151],a[184:187],a[124:127],v[148:151]\n"
"v_mfma_f32_16x16x32_bf16 v[152:155],a[188:191],a[120:123],v[152:155]\n"
"ds_read_b64_tr_b16 a[152:153],v240 offset:18496\n"
"ds_read_b64_tr_b16 a[154:155],v240 offset:23104\n"
"v_mfma_f32_16x16x32_bf16 v[156:159],a[188:191],a[124:127],v[156:159]\n"
"s_add_u32 m0,s84,77824\n"
"s_add_u32 s73,s72,s82\n"
"s_waitcnt lgkmcnt(12)\n"
"v_mfma_f32_16x16x32_bf16 v[160:163],a[192:195],a[120:123],v[160:163]\n"
"ds_read_b64_tr_b16 a[156:157],v240 offset:18528\n"
"ds_read_b64_tr_b16 a[158:159],v240 offset:23136\n"
"v_mfma_f32_16x16x32_bf16 v[164:167],a[192:195],a[124:127],v[164:167]\n"
"buffer_load_dwordx4 v9,s[44:47],s73 offen offset:0 sc1 lds\n"
"v_mfma_f32_16x16x32_bf16 v[168:171],a[196:199],a[120:123],v[168:171]\n"
"ds_read_b64_tr_b16 a[160:161],v240 offset:18560\n"
"ds_read_b64_tr_b16 a[162:163],v240 offset:23168\n"
"v_mfma_f32_16x16x32_bf16 v[172:175],a[196:199],a[124:127],v[172:175]\n"
"v_mfma_f32_16x16x32_bf16 v[176:179],a[200:203],a[120:123],v[176:179]\n"
"ds_read_b64_tr_b16 a[164:165],v240 offset:18592\n"
"ds_read_b64_tr_b16 a[166:167],v240 offset:23200\n"
"v_mfma_f32_16x16x32_bf16 v[180:183],a[200:203],a[124:127],v[180:183]\n"
"s_waitcnt lgkmcnt(12)\n"
"v_mfma_f32_16x16x32_bf16 v[184:187],a[204:207],a[120:123],v[184:187]\n"
"ds_read_b64_tr_b16 a[168:169],v240 offset:18624\n"
"ds_read_b64_tr_b16 a[170:171],v240 offset:23232\n"
"v_mfma_f32_16x16x32_bf16 v[188:191],a[204:207],a[124:127],v[188:191]\n"
"buffer_load_dwordx4 v9,s[44:47],s73 offen offset:1024 sc1 lds\n"
"v_mfma_f32_16x16x32_bf16 v[192:195],a[208:211],a[120:123],v[192:195]\n"
"ds_read_b64_tr_b16 a[172:173],v240 offset:18656\n"
"ds_read_b64_tr_b16 a[174:175],v240 offset:23264\n"
"v_mfma_f32_16x16x32_bf16 v[196:199],a[208:211],a[124:127],v[196:199]\n"
"v_mfma_f32_16x16x32_bf16 v[200:203],a[212:215],a[120:123],v[200:203]\n"
"ds_read_b64_tr_b16 a[176:177],v240 offset:18688\n"
"ds_read_b64_tr_b16 a[178:179],v240 offset:23296\n"
"v_mfma_f32_16x16x32_bf16 v[204:207],a[212:215],a[124:127],v[204:207]\n"
"s_add_u32 s73,s72,s83\n"
"s_waitcnt lgkmcnt(12)\n"
"v_mfma_f32_16x16x32_bf16 v[136:139],a[144:147],a[128:131],v[136:139]\n"
"ds_read_b64_tr_b16 a[180:181],v240 offset:27648\n"
"ds_read_b64_tr_b16 a[182:183],v240 offset:32256\n"
"v_mfma_f32_16x16x32_bf16 v[140:143],a[144:147],a[132:135],v[140:143]\n"
"buffer_load_dwordx4 v9,s[44:47],s73 offen offset:2048 sc1 lds\n"
"v_mfma_f32_16x16x32_bf16 v[144:147],a[148:151],a[128:131],v[144:147]\n"
"ds_read_b64_tr_b16 a[184:185],v240 offset:27680\n"
"ds_read_b64_tr_b16 a[186:187],v240 offset:32288\n"
"v_mfma_f32_16x16x32_bf16 v[148:151],a[148:151],a[132:135],v[148:151]\n"
"v_mfma_f32_16x16x32_bf16 v[152:155],a[152:155],a[128:131],v[152:155]\n"
"ds_read_b64_tr_b16 a[188:189],v240 offset:27712\n"
"ds_read_b64_tr_b16 a[190:191],v240 offset:32320\n"
"v_mfma_f32_16x16x32_bf16 v[156:159],a[152:155],a[132:135],v[156:159]\n"
"s_waitcnt lgkmcnt(12)\n"
"v_mfma_f32_16x16x32_bf16 v[160:163],a[156:159],a[128:131],v[160:163]\n"
"ds_read_b64_tr_b16 a[192:193],v240 offset:27744\n"
"ds_read_b64_tr_b16 a[194:195],v240 offset:32352\n"
"v_mfma_f32_16x16x32_bf16 v[164:167],a[156:159],a[132:135],v[164:167]\n"
"buffer_load_dwordx4 v9,s[44:47],s73 offen offset:3072 sc1 lds\n"
"v_min_u32 v61,28,v10\n"
"v_mfma_f32_16x16x32_bf16 v[168:171],a[160:163],a[128:131],v[168:171]\n"
"ds_read_b64_tr_b16 a[196:197],v240 offset:27776\n"
"ds_read_b64_tr_b16 a[198:199],v240 offset:32384\n"
"v_mfma_f32_16x16x32_bf16 v[172:175],a[160:163],a[132:135],v[172:175]\n"
"buffer_load_dword v60,v61,s[88:91],s75 offen sc1\n"
"v_mfma_f32_16x16x32_bf16 v[176:179],a[164:167],a[128:131],v[176:179]\n"
"ds_read_b64_tr_b16 a[200:201],v240 offset:27808\n"
"ds_read_b64_tr_b16 a[202:203],v240 offset:32416\n"
"v_mfma_f32_16x16x32_bf16 v[180:183],a[164:167],a[132:135],v[180:183]\n"
"s_add_u32 m0,s84,110592\n"
"s_cmp_le_u32 s34,3\n"
"s_cselect_b32 s72,16,12\n"
"s_add_u32 s72,s72,s37\n"
"s_lshl_b32 s72,s72,13\n"
"s_add_u32 s73,s72,s80\n"
"s_waitcnt lgkmcnt(12)\n"
"v_mfma_f32_16x16x32_bf16 v[184:187],a[168:171],a[128:131],v[184:187]\n"
"ds_read_b64_tr_b16 a[204:205],v240 offset:27840\n"
"ds_read_b64_tr_b16 a[206:207],v240 offset:32448\n"
"v_mfma_f32_16x16x32_bf16 v[188:191],a[168:171],a[132:135],v[188:191]\n"
"buffer_load_dwordx4 v9,s[44:47],s73 offen offset:0 sc1 lds\n"
"v_mfma_f32_16x16x32_bf16 v[192:195],a[172:175],a[128:131],v[192:195]\n"
"ds_read_b64_tr_b16 a[208:209],v240 offset:27872\n"
"ds_read_b64_tr_b16 a[210:211],v240 offset:32480\n"
"v_mfma_f32_16x16x32_bf16 v[196:199],a[172:175],a[132:135],v[196:199]\n"
"v_mfma_f32_16x16x32_bf16 v[200:203],a[176:179],a[128:131],v[200:203]\n"
"ds_read_b64_tr_b16 a[212:213],v240 offset:27904\n"
"ds_read_b64_tr_b16 a[214:215],v240 offset:32512\n"
"v_mfma_f32_16x16x32_bf16 v[204:207],a[176:179],a[132:135],v[204:207]\n"
"s_waitcnt lgkmcnt(12)\n"
"v_mfma_f32_16x16x32_bf16 v[136:139],a[180:183],a[136:139],v[136:139]\n"
"v_mfma_f32_16x16x32_bf16 v[140:143],a[180:183],a[140:143],v[140:143]\n"
"buffer_load_dwordx4 v9,s[44:47],s73 offen offset:1024 sc1 lds\n"
"v_mfma_f32_16x16x32_bf16 v[144:147],a[184:187],a[136:139],v[144:147]\n"
"v_mfma_f32_16x16x32_bf16 v[148:151],a[184:187],a[140:143],v[148:151]\n"
"s_waitcnt lgkmcnt(0)\n"
"s_barrier\n"
"v_mfma_f32_16x16x32_bf16 v[152:155],a[188:191],a[136:139],v[152:155]\n"
"v_mfma_f32_16x16x32_bf16 v[156:159],a[188:191],a[140:143],v[156:159]\n"
"s_add_u32 s73,s72,s81\n"
"v_mfma_f32_16x16x32_bf16 v[160:163],a[192:195],a[136:139],v[160:163]\n"
"v_mfma_f32_16x16x32_bf16 v[164:167],a[192:195],a[140:143],v[164:167]\n"
"buffer_load_dwordx4 v9,s[44:47],s73 offen offset:2048 sc1 lds\n"
"v_mfma_f32_16x16x32_bf16 v[168:171],a[196:199],a[136:139],v[168:171]\n"
"v_mfma_f32_16x16x32_bf16 v[172:175],a[196:199],a[140:143],v[172:175]\n"
"v_mfma_f32_16x16x32_bf16 v[176:179],a[200:203],a[136:139],v[176:179]\n"
"v_mfma_f32_16x16x32_bf16 v[180:183],a[200:203],a[140:143],v[180:183]\n"
"v_mfma_f32_16x16x32_bf16 v[184:187],a[204:207],a[136:139],v[184:187]\n"
"v_mfma_f32_16x16x32_bf16 v[188:191],a[204:207],a[140:143],v[188:191]\n"
"buffer_load_dwordx4 v9,s[44:47],s73 offen offset:3072 sc1 lds\n"
"v_mfma_f32_16x16x32_bf16 v[192:195],a[208:211],a[136:139],v[192:195]\n"
"v_mfma_f32_16x16x32_bf16 v[196:199],a[208:211],a[140:143],v[196:199]\n"
"v_mfma_f32_16x16x32_bf16 v[200:203],a[212:215],a[136:139],v[200:203]\n"
"v_mfma_f32_16x16x32_bf16 v[204:207],a[212:215],a[140:143],v[204:207]\n"
"s_add_u32 m0,s84,114688\n"
"s_add_u32 s73,s72,s82\n"
"buffer_load_dwordx4 v9,s[44:47],s73 offen offset:0 sc1 lds\n"
"buffer_load_dwordx4 v9,s[44:47],s73 offen offset:1024 sc1 lds\n"
"s_add_u32 s73,s72,s83\n"
"buffer_load_dwordx4 v9,s[44:47],s73 offen offset:2048 sc1 lds\n"
"buffer_load_dwordx4 v9,s[44:47],s73 offen offset:3072 sc1 lds\n"
"s_waitcnt vmcnt(34)\n"
"v_mfma_f32_16x16x32_bf16 v[64:67],a[216:219],a[0:3],0\n"
"v_mfma_f32_16x16x32_bf16 v[68:71],a[216:219],a[4:7],0\n"
"v_cvt_pk_bf16_f32 v16,v136,v137\n"
"v_cvt_pk_bf16_f32 v17,v138,v139\n"
"v_mfma_f32_16x16x32_bf16 v[72:75],a[216:219],a[8:11],0\n"
"s_add_u32 m0,s84,0\n"
"s_cmp_le_u32 s34,4\n"
"s_cselect_b32 s72,20,16\n"
"s_add_u32 s72,s72,s37\n"
"s_lshl_b32 s72,s72,13\n"
"s_add_u32 s73,s72,s80\n"
"buffer_load_dwordx4 v9,s[44:47],s73 offen offset:0 sc1 lds\n"
"v_mfma_f32_16x16x32_bf16 v[76:79],a[216:219],a[12:15],0\n"
"v_cvt_pk_bf16_f32 v20,v140,v141\n"
"v_cvt_pk_bf16_f32 v21,v142,v143\n"
"v_mfma_f32_16x16x32_bf16 a[64:67],a[216:219],a[16:19],0\n"
"v_cvt_pk_bf16_f32 v18,v144,v145\n"
"v_cvt_pk_bf16_f32 v19,v146,v147\n"
"v_mfma_f32_16x16x32_bf16 a[68:71],a[216:219],a[20:23],0\n"
"v_cvt_pk_bf16_f32 v22,v148,v149\n"
"v_cvt_pk_bf16_f32 v23,v150,v151\n"
"v_mfma_f32_16x16x32_bf16 a[72:75],a[216:219],a[24:27],0\n"
"buffer_load_dwordx4 v9,s[44:47],s73 offen offset:1024 sc1 lds\n"
"v_mfma_f32_16x16x32_bf16 a[76:79],a[216:219],a[28:31],0\n"
"v_cvt_pk_bf16_f32 v24,v152,v153\n"
"v_cvt_pk_bf16_f32 v25,v154,v155\n"
"s_waitcnt vmcnt(10)\n"
"v_cmp_ne_u32 vcc,s38,v60\n"
"s_cbranch_vccnz L_ap5_%=\n"
"L_aq5_%=:\n"
"s_add_u32 s75,s75,32\n"
"v_mfma_f32_16x16x32_bf16 v[80:83],a[220:223],a[0:3],0\n"
"v_mfma_f32_16x16x32_bf16 v[84:87],a[220:223],a[4:7],0\n"
"s_cmp_le_u32 s34,0\n"
"s_cselect_b32 s61,4,0\n"
"s_add_u32 s61,s61,s37\n"
"s_mul_i32 s61,s61,9216\n"
"buffer_load_dwordx4 a[252:255],v9,s[56:59],s61 offen offset:0 sc1\n"
"v_mfma_f32_16x16x32_bf16 v[88:91],a[220:223],a[8:11],0\n"
"s_add_u32 s73,s72,s81\n"
"buffer_load_dwordx4 v9,s[44:47],s73 offen offset:2048 sc1 lds\n"
"v_mfma_f32_16x16x32_bf16 v[92:95],a[220:223],a[12:15],0\n"
"v_cvt_pk_bf16_f32 v28,v156,v157\n"
"L_pgs288_%=:\n"
"s_cbranch_execz L_pgs352_%=\n"
"v_cvt_pk_bf16_f32 v29,v158,v159\n"
"v_mfma_f32_16x16x32_bf16 a[80:83],a[220:223],a[16:19],0\n"
"v_cvt_pk_bf16_f32 v26,v160,v161\n"
"v_cvt_pk_bf16_f32 v27,v162,v163\n"
"v_mfma_f32_16x16x32_bf16 a[84:87],a[220:223],a[20:23],0\n"
"buffer_load_dwordx4 v[208:211],v9,s[56:59],s61 offen offset:1024 sc1\n"
"v_cvt_pk_bf16_f32 v30,v164,v165\n"
"v_mfma_f32_16x16x32_bf16 a[88:91],a[220:223],a[24:27],0\n"
"buffer_load_dwordx4 v9,s[44:47],s73 offen offset:3072 sc1 lds\n"
"v_mfma_f32_16x16x32_bf16 a[92:95],a[220:223],a[28:31],0\n"
"v_cvt_pk_bf16_f32 v31,v166,v167\n"
"v_cvt_pk_bf16_f32 v32,v168,v169\n"
"v_mfma_f32_16x16x32_bf16 v[96:99],a[224:227],a[0:3],0\n"
"v_cvt_pk_bf16_f32 v33,v170,v171\n"
"v_mfma_f32_16x16x32_bf16 v[100:103],a[224:227],a[4:7],0\n"
"buffer_load_dwordx4 v[212:215],v9,s[56:59],s61 offen offset:2048 sc1\n"
"v_cvt_pk_bf16_f32 v36,v172,v173\n"
"v_mfma_f32_16x16x32_bf16 v[104:107],a[224:227],a[8:11],0\n"
"s_add_u32 m0,s84,4096\n"
"s_add_u32 s73,s72,s82\n"
"buffer_load_dwordx4 v9,s[44:47],s73 offen offset:0 sc1 lds\n"
"v_mfma_f32_16x16x32_bf16 v[108:111],a[224:227],a[12:15],0\n"
"v_cvt_pk_bf16_f32 v37,v174,v175\n"
"v_cvt_pk_bf16_f32 v34,v176,v177\n"
"v_mfma_f32_16x16x32_bf16 a[96:99],a[224:227],a[16:19],0\n"
"v_cvt_pk_bf16_f32 v35,v178,v179\n"
"v_cvt_pk_bf16_f32 v38,v180,v181\n"
"v_mfma_f32_16x16x32_bf16 a[100:103],a[224:227],a[20:23],0\n"
"buffer_load_dwordx4 v[216:219],v9,s[56:59],s61 offen offset:3072 sc1\n"
"v_cvt_pk_bf16_f32 v39,v182,v183\n"
"v_mfma_f32_16x16x32_bf16 a[104:107],a[224:227],a[24:27],0\n"
"buffer_load_dwordx4 v9,s[44:47],s73 offen offset:1024 sc1 lds\n"
"v_mfma_f32_16x16x32_bf16 a[108:111],a[224:227],a[28:31],0\n"
"v_cvt_pk_bf16_f32 v40,v184,v185\n"
"v_cvt_pk_bf16_f32 v41,v186,v187\n"
"v_mfma_f32_16x16x32_bf16 v[112:115],a[228:231],a[0:3],0\n"
"v_cvt_pk_bf16_f32 v44,v188,v189\n"
"v_mfma_f32_16x16x32_bf16 v[116:119],a[228:231],a[4:7],0\n"
"s_add_u32 s61,s61,4096\n"
"buffer_load_dwordx4 v[220:223],v9,s[56:59],s61 offen offset:0 sc1\n"
"v_mfma_f32_16x16x32_bf16 v[120:123],a[228:231],a[8:11],0\n"
"s_add_u32 s73,s72,s83\n"
"buffer_load_dwordx4 v9,s[44:47],s73 offen offset:2048 sc1 lds\n"
"v_mfma_f32_16x16x32_bf16 v[124:127],a[228:231],a[12:15],0\n"
"v_cvt_pk_bf16_f32 v45,v190,v191\n"
"v_cvt_pk_bf16_f32 v42,v192,v193\n"
"v_mfma_f32_16x16x32_bf16 a[112:115],a[228:231],a[16:19],0\n"
"v_cvt_pk_bf16_f32 v43,v194,v195\n"
"v_cvt_pk_bf16_f32 v46,v196,v197\n"
"v_mfma_f32_16x16x32_bf16 a[116:119],a[228:231],a[20:23],0\n"
"buffer_load_dwordx4 v[224:227],v9,s[56:59],s61 offen offset:1024 sc1\n"
"v_cvt_pk_bf16_f32 v47,v198,v199\n"
"v_mfma_f32_16x16x32_bf16 a[120:123],a[228:231],a[24:27],0\n"
"buffer_load_dwordx4 v9,s[44:47],s73 offen offset:3072 sc1 lds\n"
"v_mfma_f32_16x16x32_bf16 a[124:127],a[228:231],a[28:31],0\n"
"v_cvt_pk_bf16_f32 v48,v200,v201\n"
"v_cvt_pk_bf16_f32 v49,v202,v203\n"
"v_mfma_f32_16x16x32_bf16 v[128:131],a[232:235],a[0:3],0\n"
"v_cvt_pk_bf16_f32 v50,v204,v205\n"
"v_mfma_f32_16x16x32_bf16 v[132:135],a[232:235],a[4:7],0\n"
"buffer_load_dwordx4 v[228:231],v9,s[56:59],s61 offen offset:2048 sc1\n"
"v_cvt_pk_bf16_f32 v51,v206,v207\n"
"v_mfma_f32_16x16x32_bf16 v[136:139],a[232:235],a[8:11],0\n"
"s_mov_b32 m0,s85\n"
"s_cmp_le_u32 s34,5\n"
"s_cselect_b32 s72,24,20\n"
"s_add_u32 s72,s72,s37\n"
"s_lshl_b32 s72,s72,13\n"
"s_add_u32 s73,s72,s80\n"
"buffer_load_dwordx4 v9,s[44:47],s73 offen offset:0 sc1 lds\n"
"v_mfma_f32_16x16x32_bf16 v[140:143],a[232:235],a[12:15],0\n"
"s_mov_b32 s62,0xffff0000\n"
"v_mfma_f32_16x16x32_bf16 a[128:131],a[232:235],a[16:19],0\n"
"s_mov_b32 s77,0\n"
"s_mul_i32 s78,s34,256\n"
"v_mfma_f32_16x16x32_bf16 a[132:135],a[232:235],a[20:23],0\n"
"buffer_load_dwordx4 a[208:211],v9,s[56:59],s61 offen offset:3072 sc1\n"
"s_add_u32 s77,s77,s78\n"
"v_mfma_f32_16x16x32_bf16 a[136:139],a[232:235],a[24:27],0\n"
"buffer_load_dwordx4 v9,s[44:47],s73 offen offset:1024 sc1 lds\n"
"v_mfma_f32_16x16x32_bf16 a[140:143],a[232:235],a[28:31],0\n"
"s_mul_i32 s78,s37,64\n"
"s_add_u32 s77,s77,s78\n"
"v_mfma_f32_16x16x32_bf16 v[144:147],a[236:239],a[0:3],0\n"
"v_mfma_f32_16x16x32_bf16 v[148:151],a[236:239],a[4:7],0\n"
"s_add_u32 s61,s61,4096\n"
"buffer_load_dwordx4 a[212:215],v9,s[56:59],s61 offen offset:0 sc1\n"
"v_mfma_f32_16x16x32_bf16 v[152:155],a[236:239],a[8:11],0\n"
"s_add_u32 s73,s72,s81\n"
"buffer_load_dwordx4 v9,s[44:47],s73 offen offset:2048 sc1 lds\n"
"v_mfma_f32_16x16x32_bf16 v[156:159],a[236:239],a[12:15],0\n"
"s_add_u32 s48,s24,s77\n"
"s_addc_u32 s49,s25,0\n"
"v_mfma_f32_16x16x32_bf16 a[144:147],a[236:239],a[16:19],0\n"
"s_and_b32 s49,s49,0xffff\n"
"s_mov_b32 s50,64\n"
"v_mfma_f32_16x16x32_bf16 a[148:151],a[236:239],a[20:23],0\n"
"s_cmp_le_u32 s34,1\n"
"s_cselect_b32 s61,8,4\n"
"s_add_u32 s61,s61,s37\n"
"s_mul_i32 s61,s61,9216\n"
"buffer_load_dwordx4 a[216:219],v9,s[56:59],s61 offen offset:0 sc1\n"
"v_mfma_f32_16x16x32_bf16 a[152:155],a[236:239],a[24:27],0\n"
"buffer_load_dwordx4 v9,s[44:47],s73 offen offset:3072 sc1 lds\n"
"v_mfma_f32_16x16x32_bf16 a[156:159],a[236:239],a[28:31],0\n"
"s_mov_b32 s51,0x20000\n"
"v_and_b32 v236,15,v8\n"
"v_mfma_f32_16x16x32_bf16 v[160:163],a[240:243],a[0:3],0\n"
"v_lshlrev_b32 v236,1,v236\n"
"v_mfma_f32_16x16x32_bf16 v[164:167],a[240:243],a[4:7],0\n"
"buffer_load_dwordx4 a[220:223],v9,s[56:59],s61 offen offset:1024 sc1\n"
"buffer_load_ushort v232,v236,s[48:51],0 offen offset:0\n"
"v_mfma_f32_16x16x32_bf16 v[168:171],a[240:243],a[8:11],0\n"
"s_mov_b32 m0,s31\n"
"s_add_u32 s73,s72,s82\n"
"buffer_load_dwordx4 v9,s[44:47],s73 offen offset:0 sc1 lds\n"
"v_mfma_f32_16x16x32_bf16 v[172:175],a[240:243],a[12:15],0\n"
"buffer_load_ushort v233,v236,s[48:51],0 offen offset:32\n"
"v_lshlrev_b32 v52,16,v16\n"
"v_mfma_f32_16x16x32_bf16 a[160:163],a[240:243],a[16:19],0\n"
"v_and_b32 v53,s62,v16\n"
"v_lshlrev_b32 v54,16,v17\n"
"v_mfma_f32_16x16x32_bf16 a[164:167],a[240:243],a[20:23],0\n"
"buffer_load_dwordx4 a[224:227],v9,s[56:59],s61 offen offset:2048 sc1\n"
"v_and_b32 v55,s62,v17\n"
"v_mfma_f32_16x16x32_bf16 a[168:171],a[240:243],a[24:27],0\n"
"buffer_load_dwordx4 v9,s[44:47],s73 offen offset:1024 sc1 lds\n"
"v_mfma_f32_16x16x32_bf16 a[172:175],a[240:243],a[28:31],0\n"
"v_mul_f32 v56,v52,v52\n"
"v_mul_f32 v57,v53,v53\n"
"v_mfma_f32_16x16x32_bf16 v[176:179],a[244:247],a[0:3],0\n"
"v_mul_f32 v58,v54,v54\n"
"v_mfma_f32_16x16x32_bf16 v[192:195],a[248:251],a[0:3],0\n"
"buffer_load_dwordx4 a[228:231],v9,s[56:59],s61 offen offset:3072 sc1\n"
"v_mfma_f32_16x16x32_bf16 v[180:183],a[244:247],a[4:7],0\n"
"s_add_u32 s73,s72,s83\n"
"buffer_load_dwordx4 v9,s[44:47],s73 offen offset:2048 sc1 lds\n"
"v_mfma_f32_16x16x32_bf16 v[196:199],a[248:251],a[4:7],0\n"
"ds_read_b128 a[0:3],v12 offset:36864\n"
"v_mfma_f32_16x16x32_bf16 v[184:187],a[244:247],a[8:11],0\n"
"v_mul_f32 v59,v55,v55\n"
"v_fmaak_f32 v56,v237,v56,0xc0135761\n"
"v_mfma_f32_16x16x32_bf16 v[200:203],a[248:251],a[8:11],0\n"
"ds_read_b128 a[4:7],v12 offset:37888\n"
"s_add_u32 s61,s61,4096\n"
"buffer_load_dwordx4 a[232:235],v9,s[56:59],s61 offen offset:0 sc1\n"
"v_mfma_f32_16x16x32_bf16 v[188:191],a[244:247],a[12:15],0\n"
"buffer_load_dwordx4 v9,s[44:47],s73 offen offset:3072 sc1 lds\n"
"v_mfma_f32_16x16x32_bf16 v[204:207],a[248:251],a[12:15],0\n"
"ds_read_b128 a[8:11],v12 offset:38912\n"
"v_fmaak_f32 v57,v237,v57,0xc0135761\n"
"v_mfma_f32_16x16x32_bf16 a[176:179],a[244:247],a[16:19],0\n"
"v_fmaak_f32 v58,v237,v58,0xc0135761\n"
"v_fmaak_f32 v59,v237,v59,0xc0135761\n"
"v_mfma_f32_16x16x32_bf16 a[192:195],a[248:251],a[16:19],0\n"
"ds_read_b128 a[12:15],v12 offset:39936\n"
"buffer_load_dwordx4 a[236:239],v9,s[56:59],s61 offen offset:1024 sc1\n"
"v_mfma_f32_16x16x32_bf16 a[180:183],a[244:247],a[20:23],0\n"
"v_mul_f32 v56,v56,v52\n"
"v_mul_f32 v57,v57,v53\n"
"v_mfma_f32_16x16x32_bf16 a[196:199],a[248:251],a[20:23],0\n"
"ds_read_b128 a[16:19],v12 offset:40960\n"
"v_mul_f32 v58,v58,v54\n"
"v_mfma_f32_16x16x32_bf16 a[184:187],a[244:247],a[24:27],0\n"
"v_mul_f32 v59,v59,v55\n"
"v_mfma_f32_16x16x32_bf16 a[200:203],a[248:251],a[24:27],0\n"
"ds_read_b128 a[20:23],v12 offset:41984\n"
"buffer_load_dwordx4 a[240:243],v9,s[56:59],s61 offen offset:2048 sc1\n"
"v_mfma_f32_16x16x32_bf16 a[188:191],a[244:247],a[28:31],0\n"
"v_exp_f32 v56,v56\n"
"v_mfma_f32_16x16x32_bf16 a[204:207],a[248:251],a[28:31],0\n"
"ds_read_b128 a[24:27],v12 offset:43008\n"
"s_waitcnt vmcnt(13)\n"
"v_mfma_f32_16x16x32_bf16 v[64:67],a[252:255],a[32:35],v[64:67]\n"
"s_waitcnt lgkmcnt(5)\n"
"v_mfma_f32_16x16x32_bf16 v[64:67],a[216:219],a[0:3],v[64:67]\n"
"ds_read_b128 a[28:31],v12 offset:44032\n"
"buffer_load_dwordx4 a[244:247],v9,s[56:59],s61 offen offset:3072 sc1\n"
"s_waitcnt vmcnt(12)\n"
"v_mfma_f32_16x16x32_bf16 v[80:83],a[220:223],a[0:3],v[80:83]\n"
"v_mfma_f32_16x16x32_bf16 v[80:83],v[208:211],a[32:35],v[80:83]\n"
"v_exp_f32 v57,v57\n"
"v_mfma_f32_16x16x32_bf16 v[68:71],a[252:255],a[36:39],v[68:71]\n"
"v_exp_f32 v58,v58\n"
"v_mfma_f32_16x16x32_bf16 v[68:71],a[216:219],a[4:7],v[68:71]\n"
"s_add_u32 s61,s61,4096\n"
"buffer_load_dwordx4 a[248:251],v9,s[56:59],s61 offen offset:0 sc1\n"
"v_mfma_f32_16x16x32_bf16 v[84:87],a[220:223],a[4:7],v[84:87]\n"
"v_exp_f32 v59,v59\n"
"v_mfma_f32_16x16x32_bf16 v[84:87],v[208:211],a[36:39],v[84:87]\n"
"v_add_f32 v56,1.0,v56\n"
"v_add_f32 v57,1.0,v57\n"
"v_mfma_f32_16x16x32_bf16 v[72:75],a[252:255],a[40:43],v[72:75]\n"
"v_add_f32 v58,1.0,v58\n"
"v_add_f32 v59,1.0,v59\n"
"s_waitcnt lgkmcnt(4)\n"
"v_mfma_f32_16x16x32_bf16 v[72:75],a[216:219],a[8:11],v[72:75]\n"
"v_mfma_f32_16x16x32_bf16 v[88:91],a[220:223],a[8:11],v[88:91]\n"
"v_rcp_f32 v56,v56\n"
"v_mfma_f32_16x16x32_bf16 v[88:91],v[208:211],a[40:43],v[88:91]\n"
"v_rcp_f32 v57,v57\n"
"v_mfma_f32_16x16x32_bf16 v[76:79],a[252:255],a[44:47],v[76:79]\n"
"v_rcp_f32 v58,v58\n"
"v_mfma_f32_16x16x32_bf16 v[76:79],a[216:219],a[12:15],v[76:79]\n"
"v_rcp_f32 v59,v59\n"
"v_mfma_f32_16x16x32_bf16 v[92:95],a[220:223],a[12:15],v[92:95]\n"
"v_mul_f32 v52,v52,v56\n"
"v_mul_f32 v53,v53,v57\n"
"v_mfma_f32_16x16x32_bf16 v[92:95],v[208:211],a[44:47],v[92:95]\n"
"v_mul_f32 v54,v54,v58\n"
"v_mul_f32 v55,v55,v59\n"
"v_mfma_f32_16x16x32_bf16 a[64:67],a[252:255],a[48:51],a[64:67]\n"
"v_cvt_pk_bf16_f32 v16,v52,v53\n"
"v_cvt_pk_bf16_f32 v17,v54,v55\n"
"s_waitcnt lgkmcnt(2)\n"
"v_mfma_f32_16x16x32_bf16 a[64:67],a[216:219],a[16:19],a[64:67]\n"
"v_lshlrev_b32 v52,16,v20\n"
"v_mfma_f32_16x16x32_bf16 a[80:83],a[220:223],a[16:19],a[80:83]\n"
"v_and_b32 v53,s62,v20\n"
"v_lshlrev_b32 v54,16,v21\n"
"v_mfma_f32_16x16x32_bf16 a[80:83],v[208:211],a[48:51],a[80:83]\n"
"v_and_b32 v55,s62,v21\n"
"v_mul_f32 v56,v52,v52\n"
"v_mfma_f32_16x16x32_bf16 a[68:71],a[252:255],a[52:55],a[68:71]\n"
"v_mul_f32 v57,v53,v53\n"
"v_mul_f32 v58,v54,v54\n"
"v_mfma_f32_16x16x32_bf16 a[68:71],a[216:219],a[20:23],a[68:71]\n"
"v_mul_f32 v59,v55,v55\n"
"v_fmaak_f32 v56,v237,v56,0xc0135761\n"
"v_mfma_f32_16x16x32_bf16 a[84:87],a[220:223],a[20:23],a[84:87]\n"
"v_fmaak_f32 v57,v237,v57,0xc0135761\n"
"v_fmaak_f32 v58,v237,v58,0xc0135761\n"
"v_mfma_f32_16x16x32_bf16 a[84:87],v[208:211],a[52:55],a[84:87]\n"
"v_fmaak_f32 v59,v237,v59,0xc0135761\n"
"v_mul_f32 v56,v56,v52\n"
"v_mfma_f32_16x16x32_bf16 a[72:75],a[252:255],a[56:59],a[72:75]\n"
"v_mul_f32 v57,v57,v53\n"
"v_mul_f32 v58,v58,v54\n"
"s_waitcnt lgkmcnt(0)\n"
"v_mfma_f32_16x16x32_bf16 a[72:75],a[216:219],a[24:27],a[72:75]\n"
"v_mul_f32 v59,v59,v55\n"
"v_mfma_f32_16x16x32_bf16 a[88:91],a[220:223],a[24:27],a[88:91]\n"
"v_exp_f32 v56,v56\n"
"v_mfma_f32_16x16x32_bf16 a[88:91],v[208:211],a[56:59],a[88:91]\n"
"v_exp_f32 v57,v57\n"
"v_mfma_f32_16x16x32_bf16 a[76:79],a[252:255],a[60:63],a[76:79]\n"
"s_cmp_le_u32 s34,2\n"
"s_cselect_b32 s61,12,8\n"
"v_mfma_f32_16x16x32_bf16 a[76:79],a[216:219],a[28:31],a[76:79]\n"
"s_add_u32 s61,s61,s37\n"
"s_mul_i32 s61,s61,9216\n"
"v_mfma_f32_16x16x32_bf16 a[92:95],a[220:223],a[28:31],a[92:95]\n"
"buffer_load_dwordx4 a[252:255],v9,s[56:59],s61 offen offset:0 sc1\n"
"v_mfma_f32_16x16x32_bf16 a[92:95],v[208:211],a[60:63],a[92:95]\n"
"v_exp_f32 v58,v58\n"
"s_waitcnt vmcnt(10)\n"
"v_mfma_f32_16x16x32_bf16 v[96:99],v[212:215],a[32:35],v[96:99]\n"
"v_mfma_f32_16x16x32_bf16 v[96:99],a[224:227],a[0:3],v[96:99]\n"
"v_exp_f32 v59,v59\n"
"v_mfma_f32_16x16x32_bf16 v[100:103],a[224:227],a[4:7],v[100:103]\n"
"buffer_load_dwordx4 v[208:211],v9,s[56:59],s61 offen offset:1024 sc1\n"
"v_add_f32 v56,1.0,v56\n"
"v_mfma_f32_16x16x32_bf16 v[100:103],v[212:215],a[36:39],v[100:103]\n"
"v_add_f32 v57,1.0,v57\n"
"v_add_f32 v58,1.0,v58\n"
"v_mfma_f32_16x16x32_bf16 v[104:107],v[212:215],a[40:43],v[104:107]\n"
"s_cmp_le_u32 s34,3\n"
"s_cselect_b32 s17,16,12\n"
"v_mfma_f32_16x16x32_bf16 v[104:107],a[224:227],a[8:11],v[104:107]\n"
"s_add_u32 s17,s17,s37\n"
"s_mul_i32 s17,s17,9216\n"
"v_mfma_f32_16x16x32_bf16 v[108:111],a[224:227],a[12:15],v[108:111]\n"
"buffer_load_dwordx4 a[216:219],v9,s[56:59],s17 offen offset:0 sc1\n"
"v_add_f32 v59,1.0,v59\n"
"v_mfma_f32_16x16x32_bf16 v[108:111],v[212:215],a[44:47],v[108:111]\n"
"v_rcp_f32 v56,v56\n"
"v_mfma_f32_16x16x32_bf16 a[96:99],v[212:215],a[48:51],a[96:99]\n"
"v_rcp_f32 v57,v57\n"
"v_mfma_f32_16x16x32_bf16 a[96:99],a[224:227],a[16:19],a[96:99]\n"
"v_rcp_f32 v58,v58\n"
"v_mfma_f32_16x16x32_bf16 a[100:103],a[224:227],a[20:23],a[100:103]\n"
"buffer_load_dwordx4 a[220:223],v9,s[56:59],s17 offen offset:1024 sc1\n"
"v_mfma_f32_16x16x32_bf16 a[100:103],v[212:215],a[52:55],a[100:103]\n"
"v_rcp_f32 v59,v59\n"
"v_mfma_f32_16x16x32_bf16 a[104:107],v[212:215],a[56:59],a[104:107]\n"
"v_mul_f32 v52,v52,v56\n"
"v_mul_f32 v53,v53,v57\n"
"v_mfma_f32_16x16x32_bf16 a[104:107],a[224:227],a[24:27],a[104:107]\n"
"v_mul_f32 v54,v54,v58\n"
"v_mul_f32 v55,v55,v59\n"
"v_mfma_f32_16x16x32_bf16 a[108:111],a[224:227],a[28:31],a[108:111]\n"
"v_cvt_pk_bf16_f32 v20,v52,v53\n"
"v_cvt_pk_bf16_f32 v21,v54,v55\n"
"v_mfma_f32_16x16x32_bf16 a[108:111],v[212:215],a[60:63],a[108:111]\n"
"v_lshlrev_b32 v52,16,v18\n"
"v_and_b32 v53,s62,v18\n"
"s_waitcnt vmcnt(11)\n"
"v_mfma_f32_16x16x32_bf16 v[112:115],v[216:219],a[32:35],v[112:115]\n"
"buffer_load_dwordx4 v[212:215],v9,s[56:59],s61 offen offset:2048 sc1\n"
"v_mfma_f32_16x16x32_bf16 v[112:115],a[228:231],a[0:3],v[112:115]\n"
"v_lshlrev_b32 v54,16,v19\n"
"v_and_b32 v55,s62,v19\n"
"v_mfma_f32_16x16x32_bf16 v[116:119],a[228:231],a[4:7],v[116:119]\n"
"v_mul_f32 v56,v52,v52\n"
"v_mul_f32 v57,v53,v53\n"
"v_mfma_f32_16x16x32_bf16 v[116:119],v[216:219],a[36:39],v[116:119]\n"
"v_mul_f32 v58,v54,v54\n"
"v_mul_f32 v59,v55,v55\n"
"v_mfma_f32_16x16x32_bf16 v[120:123],v[216:219],a[40:43],v[120:123]\n"
"v_fmaak_f32 v56,v237,v56,0xc0135761\n"
"v_fmaak_f32 v57,v237,v57,0xc0135761\n"
"v_mfma_f32_16x16x32_bf16 v[120:123],a[228:231],a[8:11],v[120:123]\n"
"v_fmaak_f32 v58,v237,v58,0xc0135761\n"
"v_fmaak_f32 v59,v237,v59,0xc0135761\n"
"v_mfma_f32_16x16x32_bf16 v[124:127],a[228:231],a[12:15],v[124:127]\n"
"v_mul_f32 v56,v56,v52\n"
"v_mul_f32 v57,v57,v53\n"
"v_mfma_f32_16x16x32_bf16 v[124:127],v[216:219],a[44:47],v[124:127]\n"
"v_mul_f32 v58,v58,v54\n"
"v_mul_f32 v59,v59,v55\n"
"v_mfma_f32_16x16x32_bf16 a[112:115],v[216:219],a[48:51],a[112:115]\n"
"buffer_load_dwordx4 a[224:227],v9,s[56:59],s17 offen offset:2048 sc1\n"
"v_mfma_f32_16x16x32_bf16 a[112:115],a[228:231],a[16:19],a[112:115]\n"
"v_exp_f32 v56,v56\n"
"v_mfma_f32_16x16x32_bf16 a[116:119],a[228:231],a[20:23],a[116:119]\n"
"v_exp_f32 v57,v57\n"
"v_mfma_f32_16x16x32_bf16 a[116:119],v[216:219],a[52:55],a[116:119]\n"
"v_exp_f32 v58,v58\n"
"v_mfma_f32_16x16x32_bf16 a[120:123],v[216:219],a[56:59],a[120:123]\n"
"v_exp_f32 v59,v59\n"
"v_mfma_f32_16x16x32_bf16 a[120:123],a[228:231],a[24:27],a[120:123]\n"
"v_add_f32 v56,1.0,v56\n"
"v_add_f32 v57,1.0,v57\n"
"v_mfma_f32_16x16x32_bf16 a[124:127],a[228:231],a[28:31],a[124:127]\n"
"v_add_f32 v58,1.0,v58\n"
"v_add_f32 v59,1.0,v59\n"
"v_mfma_f32_16x16x32_bf16 a[124:127],v[216:219],a[60:63],a[124:127]\n"
"v_rcp_f32 v56,v56\n"
"s_waitcnt vmcnt(11)\n"
"v_mfma_f32_16x16x32_bf16 v[128:131],v[220:223],a[32:35],v[128:131]\n"
"buffer_load_dwordx4 v[216:219],v9,s[56:59],s61 offen offset:3072 sc1\n"
"v_mfma_f32_16x16x32_bf16 v[128:131],a[232:235],a[0:3],v[128:131]\n"
"v_rcp_f32 v57,v57\n"
"v_mfma_f32_16x16x32_bf16 v[132:135],a[232:235],a[4:7],v[132:135]\n"
"v_rcp_f32 v58,v58\n"
"v_mfma_f32_16x16x32_bf16 v[132:135],v[220:223],a[36:39],v[132:135]\n"
"v_rcp_f32 v59,v59\n"
"v_mfma_f32_16x16x32_bf16 v[136:139],v[220:223],a[40:43],v[136:139]\n"
"v_mul_f32 v52,v52,v56\n"
"v_mul_f32 v53,v53,v57\n"
"v_mfma_f32_16x16x32_bf16 v[136:139],a[232:235],a[8:11],v[136:139]\n"
"v_mul_f32 v54,v54,v58\n"
"v_mul_f32 v55,v55,v59\n"
"v_mfma_f32_16x16x32_bf16 v[140:143],a[232:235],a[12:15],v[140:143]\n"
"v_cvt_pk_bf16_f32 v18,v52,v53\n"
"v_cvt_pk_bf16_f32 v19,v54,v55\n"
"v_mfma_f32_16x16x32_bf16 v[140:143],v[220:223],a[44:47],v[140:143]\n"
"v_lshlrev_b32 v52,16,v22\n"
"v_and_b32 v53,s62,v22\n"
"v_mfma_f32_16x16x32_bf16 a[128:131],v[220:223],a[48:51],a[128:131]\n"
"buffer_load_dwordx4 a[228:231],v9,s[56:59],s17 offen offset:3072 sc1\n"
"s_add_u32 m0,s84,36864\n"
"v_mfma_f32_16x16x32_bf16 a[128:131],a[232:235],a[16:19],a[128:131]\n"
"s_cmp_le_u32 s34,6\n"
"s_cselect_b32 s72,28,24\n"
"v_mfma_f32_16x16x32_bf16 a[132:135],a[232:235],a[20:23],a[132:135]\n"
"s_add_u32 s72,s72,s37\n"
"v_lshlrev_b32 v54,16,v23\n"
"v_mfma_f32_16x16x32_bf16 a[132:135],v[220:223],a[52:55],a[132:135]\n"
"s_lshl_b32 s72,s72,13\n"
"s_add_u32 s73,s72,s80\n"
"v_mfma_f32_16x16x32_bf16 a[136:139],v[220:223],a[56:59],a[136:139]\n"
"buffer_load_dwordx4 v9,s[44:47],s73 offen offset:0 sc1 lds\n"
"v_mfma_f32_16x16x32_bf16 a[136:139],a[232:235],a[24:27],a[136:139]\n"
"v_and_b32 v55,s62,v23\n"
"v_mul_f32 v56,v52,v52\n"
"v_mfma_f32_16x16x32_bf16 a[140:143],a[232:235],a[28:31],a[140:143]\n"
"v_mul_f32 v57,v53,v53\n"
"v_mul_f32 v58,v54,v54\n"
"v_mfma_f32_16x16x32_bf16 a[140:143],v[220:223],a[60:63],a[140:143]\n"
"s_add_u32 s61,s61,4096\n"
"v_mul_f32 v59,v55,v55\n"
"s_waitcnt vmcnt(12)\n"
"v_mfma_f32_16x16x32_bf16 v[144:147],v[224:227],a[32:35],v[144:147]\n"
"buffer_load_dwordx4 v[220:223],v9,s[56:59],s61 offen offset:0 sc1\n"
"v_mfma_f32_16x16x32_bf16 v[144:147],a[236:239],a[0:3],v[144:147]\n"
"v_fmaak_f32 v56,v237,v56,0xc0135761\n"
"v_fmaak_f32 v57,v237,v57,0xc0135761\n"
"v_mfma_f32_16x16x32_bf16 v[148:151],a[236:239],a[4:7],v[148:151]\n"
"v_fmaak_f32 v58,v237,v58,0xc0135761\n"
"v_fmaak_f32 v59,v237,v59,0xc0135761\n"
"v_mfma_f32_16x16x32_bf16 v[148:151],v[224:227],a[36:39],v[148:151]\n"
"v_mul_f32 v56,v56,v52\n"
"v_mul_f32 v57,v57,v53\n"
"v_mfma_f32_16x16x32_bf16 v[152:155],v[224:227],a[40:43],v[152:155]\n"
"buffer_load_dwordx4 v9,s[44:47],s73 offen offset:1024 sc1 lds\n"
"v_mfma_f32_16x16x32_bf16 v[152:155],a[236:239],a[8:11],v[152:155]\n"
"v_mul_f32 v58,v58,v54\n"
"v_mul_f32 v59,v59,v55\n"
"v_mfma_f32_16x16x32_bf16 v[156:159],a[236:239],a[12:15],v[156:159]\n"
"v_exp_f32 v56,v56\n"
"v_mfma_f32_16x16x32_bf16 v[156:159],v[224:227],a[44:47],v[156:159]\n"
"s_add_u32 s17,s17,4096\n"
"v_mfma_f32_16x16x32_bf16 a[144:147],v[224:227],a[48:51],a[144:147]\n"
"buffer_load_dwordx4 a[232:235],v9,s[56:59],s17 offen offset:0 sc1\n"
"v_mfma_f32_16x16x32_bf16 a[144:147],a[236:239],a[16:19],a[144:147]\n"
"v_exp_f32 v57,v57\n"
"v_mfma_f32_16x16x32_bf16 a[148:151],a[236:239],a[20:23],a[148:151]\n"
"v_exp_f32 v58,v58\n"
"v_mfma_f32_16x16x32_bf16 a[148:151],v[224:227],a[52:55],a[148:151]\n"
"s_add_u32 s73,s72,s81\n"
"v_mfma_f32_16x16x32_bf16 a[152:155],v[224:227],a[56:59],a[152:155]\n"
"buffer_load_dwordx4 v9,s[44:47],s73 offen offset:2048 sc1 lds\n"
"v_mfma_f32_16x16x32_bf16 a[152:155],a[236:239],a[24:27],a[152:155]\n"
"v_exp_f32 v59,v59\n"
"v_mfma_f32_16x16x32_bf16 a[156:159],a[236:239],a[28:31],a[156:159]\n"
"v_add_f32 v56,1.0,v56\n"
"v_add_f32 v57,1.0,v57\n"
"v_mfma_f32_16x16x32_bf16 a[156:159],v[224:227],a[60:63],a[156:159]\n"
"v_add_f32 v58,1.0,v58\n"
"v_add_f32 v59,1.0,v59\n"
"s_waitcnt vmcnt(15)\n"
"v_mfma_f32_16x16x32_bf16 v[160:163],v[228:231],a[32:35],v[160:163]\n"
"buffer_load_dwordx4 v[224:227],v9,s[56:59],s61 offen offset:1024 sc1\n"
"v_mfma_f32_16x16x32_bf16 v[160:163],a[240:243],a[0:3],v[160:163]\n"
"v_rcp_f32 v56,v56\n"
"v_mfma_f32_16x16x32_bf16 v[164:167],a[240:243],a[4:7],v[164:167]\n"
"v_rcp_f32 v57,v57\n"
"v_mfma_f32_16x16x32_bf16 v[164:167],v[228:231],a[36:39],v[164:167]\n"
"v_rcp_f32 v58,v58\n"
"v_mfma_f32_16x16x32_bf16 v[168:171],v[228:231],a[40:43],v[168:171]\n"
"v_rcp_f32 v59,v59\n"
"v_mfma_f32_16x16x32_bf16 v[168:171],a[240:243],a[8:11],v[168:171]\n"
"v_mul_f32 v52,v52,v56\n"
"v_mul_f32 v53,v53,v57\n"
"v_mfma_f32_16x16x32_bf16 v[172:175],a[240:243],a[12:15],v[172:175]\n"
"v_mul_f32 v54,v54,v58\n"
"v_mul_f32 v55,v55,v59\n"
"v_mfma_f32_16x16x32_bf16 v[172:175],v[228:231],a[44:47],v[172:175]\n"
"v_cvt_pk_bf16_f32 v22,v52,v53\n"
"v_cvt_pk_bf16_f32 v23,v54,v55\n"
"v_mfma_f32_16x16x32_bf16 a[160:163],v[228:231],a[48:51],a[160:163]\n"
"buffer_load_dwordx4 a[236:239],v9,s[56:59],s17 offen offset:1024 sc1\n"
"v_lshlrev_b32 v52,16,v24\n"
"v_mfma_f32_16x16x32_bf16 a[160:163],a[240:243],a[16:19],a[160:163]\n"
"v_and_b32 v53,s62,v24\n"
"v_lshlrev_b32 v54,16,v25\n"
"v_mfma_f32_16x16x32_bf16 a[164:167],a[240:243],a[20:23],a[164:167]\n"
"v_and_b32 v55,s62,v25\n"
"v_mul_f32 v56,v52,v52\n"
"v_mfma_f32_16x16x32_bf16 a[164:167],v[228:231],a[52:55],a[164:167]\n"
"v_mul_f32 v57,v53,v53\n"
"v_mul_f32 v58,v54,v54\n"
"v_mfma_f32_16x16x32_bf16 a[168:171],v[228:231],a[56:59],a[168:171]\n"
"buffer_load_dwordx4 v9,s[44:47],s73 offen offset:3072 sc1 lds\n"
"v_mfma_f32_16x16x32_bf16 a[168:171],a[240:243],a[24:27],a[168:171]\n"
"v_mul_f32 v59,v55,v55\n"
"v_fmaak_f32 v56,v237,v56,0xc0135761\n"
"v_mfma_f32_16x16x32_bf16 a[172:175],a[240:243],a[28:31],a[172:175]\n"
"v_fmaak_f32 v57,v237,v57,0xc0135761\n"
"v_fmaak_f32 v58,v237,v58,0xc0135761\n"
"v_mfma_f32_16x16x32_bf16 a[172:175],v[228:231],a[60:63],a[172:175]\n"
"v_fmaak_f32 v59,v237,v59,0xc0135761\n"
"v_mul_f32 v56,v56,v52\n"
"s_waitcnt vmcnt(17)\n"
"v_mfma_f32_16x16x32_bf16 v[176:179],a[208:211],a[32:35],v[176:179]\n"
"buffer_load_dwordx4 v[228:231],v9,s[56:59],s61 offen offset:2048 sc1\n"
"v_mfma_f32_16x16x32_bf16 v[176:179],a[244:247],a[0:3],v[176:179]\n"
"v_mul_f32 v57,v57,v53\n"
"v_mul_f32 v58,v58,v54\n"
"s_waitcnt vmcnt(17)\n"
"v_mfma_f32_16x16x32_bf16 v[192:195],a[248:251],a[0:3],v[192:195]\n"
"v_mul_f32 v59,v59,v55\n"
"v_mfma_f32_16x16x32_bf16 v[192:195],a[212:215],a[32:35],v[192:195]\n"
"s_add_u32 m0,s84,40960\n"
"s_add_u32 s73,s72,s82\n"
"v_mfma_f32_16x16x32_bf16 v[180:183],a[208:211],a[36:39],v[180:183]\n"
"buffer_load_dwordx4 v9,s[44:47],s73 offen offset:0 sc1 lds\n"
"v_mfma_f32_16x16x32_bf16 v[180:183],a[244:247],a[4:7],v[180:183]\n"
"ds_read_b128 a[32:35],v13 offset:0\n"
"v_mfma_f32_16x16x32_bf16 v[196:199],a[248:251],a[4:7],v[196:199]\n"
"ds_read_b128 a[0:3],v13 offset:36864\n"
"v_mfma_f32_16x16x32_bf16 v[196:199],a[212:215],a[36:39],v[196:199]\n"
"v_exp_f32 v56,v56\n"
"v_mfma_f32_16x16x32_bf16 v[184:187],a[208:211],a[40:43],v[184:187]\n"
"buffer_load_dwordx4 a[240:243],v9,s[56:59],s17 offen offset:2048 sc1\n"
"v_mfma_f32_16x16x32_bf16 v[184:187],a[244:247],a[8:11],v[184:187]\n"
"ds_read_b128 a[36:39],v13 offset:1024\n"
"v_mfma_f32_16x16x32_bf16 v[200:203],a[248:251],a[8:11],v[200:203]\n"
"ds_read_b128 a[4:7],v13 offset:37888\n"
"v_mfma_f32_16x16x32_bf16 v[200:203],a[212:215],a[40:43],v[200:203]\n"
"v_exp_f32 v57,v57\n"
"v_mfma_f32_16x16x32_bf16 v[188:191],a[208:211],a[44:47],v[188:191]\n"
"buffer_load_dwordx4 v9,s[44:47],s73 offen offset:1024 sc1 lds\n"
"v_mfma_f32_16x16x32_bf16 v[188:191],a[244:247],a[12:15],v[188:191]\n"
"ds_read_b128 a[40:43],v13 offset:2048\n"
"v_mfma_f32_16x16x32_bf16 v[204:207],a[248:251],a[12:15],v[204:207]\n"
"ds_read_b128 a[8:11],v13 offset:38912\n"
"v_mfma_f32_16x16x32_bf16 v[204:207],a[212:215],a[44:47],v[204:207]\n"
"v_exp_f32 v58,v58\n"
"v_mfma_f32_16x16x32_bf16 a[176:179],a[208:211],a[48:51],a[176:179]\n"
"v_exp_f32 v59,v59\n"
"v_mfma_f32_16x16x32_bf16 a[176:179],a[244:247],a[16:19],a[176:179]\n"
"ds_read_b128 a[44:47],v13 offset:3072\n"
"v_add_f32 v56,1.0,v56\n"
"v_mfma_f32_16x16x32_bf16 a[192:195],a[248:251],a[16:19],a[192:195]\n"
"ds_read_b128 a[12:15],v13 offset:39936\n"
"v_add_f32 v57,1.0,v57\n"
"v_mfma_f32_16x16x32_bf16 a[192:195],a[212:215],a[48:51],a[192:195]\n"
"v_add_f32 v58,1.0,v58\n"
"v_add_f32 v59,1.0,v59\n"
"v_mfma_f32_16x16x32_bf16 a[180:183],a[208:211],a[52:55],a[180:183]\n"
"v_rcp_f32 v56,v56\n"
"v_mfma_f32_16x16x32_bf16 a[180:183],a[244:247],a[20:23],a[180:183]\n"
"ds_read_b128 a[48:51],v13 offset:4096\n"
"v_mfma_f32_16x16x32_bf16 a[196:199],a[248:251],a[20:23],a[196:199]\n"
"ds_read_b128 a[16:19],v13 offset:40960\n"
"v_mfma_f32_16x16x32_bf16 a[196:199],a[212:215],a[52:55],a[196:199]\n"
"v_rcp_f32 v57,v57\n"
"v_mfma_f32_16x16x32_bf16 a[184:187],a[208:211],a[56:59],a[184:187]\n"
"v_rcp_f32 v58,v58\n"
"v_mfma_f32_16x16x32_bf16 a[184:187],a[244:247],a[24:27],a[184:187]\n"
"ds_read_b128 a[52:55],v13 offset:5120\n"
"v_mfma_f32_16x16x32_bf16 a[200:203],a[248:251],a[24:27],a[200:203]\n"
"ds_read_b128 a[20:23],v13 offset:41984\n"
"v_mfma_f32_16x16x32_bf16 a[200:203],a[212:215],a[56:59],a[200:203]\n"
"s_add_u32 s73,s72,s83\n"
"v_mfma_f32_16x16x32_bf16 a[188:191],a[208:211],a[60:63],a[188:191]\n"
"buffer_load_dwordx4 v9,s[44:47],s73 offen offset:2048 sc1 lds\n"
"v_mfma_f32_16x16x32_bf16 a[188:191],a[244:247],a[28:31],a[188:191]\n"
"ds_read_b128 a[56:59],v13 offset:6144\n"
"v_mfma_f32_16x16x32_bf16 a[204:207],a[248:251],a[28:31],a[204:207]\n"
"buffer_load_dwordx4 a[208:211],v9,s[56:59],s61 offen offset:3072 sc1\n"
"ds_read_b128 a[24:27],v13 offset:43008\n"
"v_mfma_f32_16x16x32_bf16 a[204:207],a[212:215],a[60:63],a[204:207]\n"
"v_rcp_f32 v59,v59\n"
"s_waitcnt lgkmcnt(10)\n"
"s_waitcnt vmcnt(19)\n"
"v_mfma_f32_16x16x32_bf16 v[64:67],a[252:255],a[32:35],v[64:67]\n"
"v_mfma_f32_16x16x32_bf16 v[64:67],a[216:219],a[0:3],v[64:67]\n"
"s_add_u32 s61,s61,4096\n"
"ds_read_b128 a[60:63],v13 offset:7168\n"
"s_waitcnt vmcnt(18)\n"
"v_mfma_f32_16x16x32_bf16 v[80:83],a[220:223],a[0:3],v[80:83]\n"
"buffer_load_dwordx4 a[212:215],v9,s[56:59],s61 offen offset:0 sc1\n"
"ds_read_b128 a[28:31],v13 offset:44032\n"
"v_mfma_f32_16x16x32_bf16 v[80:83],v[208:211],a[32:35],v[80:83]\n"
"v_mul_f32 v52,v52,v56\n"
"v_mul_f32 v53,v53,v57\n"
"v_mfma_f32_16x16x32_bf16 v[68:71],a[252:255],a[36:39],v[68:71]\n"
"buffer_load_dwordx4 v9,s[44:47],s73 offen offset:3072 sc1 lds\n"
"v_mfma_f32_16x16x32_bf16 v[68:71],a[216:219],a[4:7],v[68:71]\n"
"v_mul_f32 v54,v54,v58\n"
"v_mul_f32 v55,v55,v59\n"
"v_mfma_f32_16x16x32_bf16 v[84:87],a[220:223],a[4:7],v[84:87]\n"
"buffer_load_dwordx4 a[244:247],v9,s[56:59],s17 offen offset:3072 sc1\n"
"v_cvt_pk_bf16_f32 v24,v52,v53\n"
"v_mfma_f32_16x16x32_bf16 v[84:87],v[208:211],a[36:39],v[84:87]\n"
"v_cvt_pk_bf16_f32 v25,v54,v55\n"
"v_lshlrev_b32 v52,16,v28\n"
"s_waitcnt lgkmcnt(8)\n"
"v_mfma_f32_16x16x32_bf16 v[72:75],a[252:255],a[40:43],v[72:75]\n"
"v_and_b32 v53,s62,v28\n"
"v_mfma_f32_16x16x32_bf16 v[72:75],a[216:219],a[8:11],v[72:75]\n"
"s_add_u32 s17,s17,4096\n"
"v_lshlrev_b32 v54,16,v29\n"
"v_mfma_f32_16x16x32_bf16 v[88:91],a[220:223],a[8:11],v[88:91]\n"
"buffer_load_dwordx4 a[248:251],v9,s[56:59],s17 offen offset:0 sc1\n"
"v_and_b32 v55,s62,v29\n"
"v_mfma_f32_16x16x32_bf16 v[88:91],v[208:211],a[40:43],v[88:91]\n"
"v_mul_f32 v56,v52,v52\n"
"v_mul_f32 v57,v53,v53\n"
"v_mfma_f32_16x16x32_bf16 v[76:79],a[252:255],a[44:47],v[76:79]\n"
"v_mul_f32 v58,v54,v54\n"
"v_mul_f32 v59,v55,v55\n"
"v_mfma_f32_16x16x32_bf16 v[76:79],a[216:219],a[12:15],v[76:79]\n"
"v_fmaak_f32 v56,v237,v56,0xc0135761\n"
"v_fmaak_f32 v57,v237,v57,0xc0135761\n"
"v_mfma_f32_16x16x32_bf16 v[92:95],a[220:223],a[12:15],v[92:95]\n"
"v_fmaak_f32 v58,v237,v58,0xc0135761\n"
"v_fmaak_f32 v59,v237,v59,0xc0135761\n"
"v_mfma_f32_16x16x32_bf16 v[92:95],v[208:211],a[44:47],v[92:95]\n"
"v_mul_f32 v56,v56,v52\n"
"v_mul_f32 v57,v57,v53\n"
"s_waitcnt lgkmcnt(4)\n"
"v_mfma_f32_16x16x32_bf16 a[64:67],a[252:255],a[48:51],a[64:67]\n"
"v_mul_f32 v58,v58,v54\n"
"v_mfma_f32_16x16x32_bf16 a[64:67],a[216:219],a[16:19],a[64:67]\n"
"v_mul_f32 v59,v59,v55\n"
"v_mfma_f32_16x16x32_bf16 a[80:83],a[220:223],a[16:19],a[80:83]\n"
"v_exp_f32 v56,v56\n"
"v_mfma_f32_16x16x32_bf16 a[80:83],v[208:211],a[48:51],a[80:83]\n"
"v_exp_f32 v57,v57\n"
"v_mfma_f32_16x16x32_bf16 a[68:71],a[252:255],a[52:55],a[68:71]\n"
"v_exp_f32 v58,v58\n"
"v_mfma_f32_16x16x32_bf16 a[68:71],a[216:219],a[20:23],a[68:71]\n"
"v_exp_f32 v59,v59\n"
"v_mfma_f32_16x16x32_bf16 a[84:87],a[220:223],a[20:23],a[84:87]\n"
"v_add_f32 v56,1.0,v56\n"
"v_add_f32 v57,1.0,v57\n"
"v_mfma_f32_16x16x32_bf16 a[84:87],v[208:211],a[52:55],a[84:87]\n"
"v_add_f32 v58,1.0,v58\n"
"v_add_f32 v59,1.0,v59\n"
"s_waitcnt lgkmcnt(0)\n"
"v_mfma_f32_16x16x32_bf16 a[72:75],a[252:255],a[56:59],a[72:75]\n"
"s_add_u32 m0,s84,73728\n"
"v_mfma_f32_16x16x32_bf16 a[72:75],a[216:219],a[24:27],a[72:75]\n"
"s_cmp_le_u32 s34,7\n"
"s_cselect_b32 s72,32,28\n"
"v_mfma_f32_16x16x32_bf16 a[88:91],a[220:223],a[24:27],a[88:91]\n"
"s_add_u32 s72,s72,s37\n"
"v_mfma_f32_16x16x32_bf16 a[88:91],v[208:211],a[56:59],a[88:91]\n"
"s_lshl_b32 s72,s72,13\n"
"s_add_u32 s73,s72,s80\n"
"v_mfma_f32_16x16x32_bf16 a[76:79],a[252:255],a[60:63],a[76:79]\n"
"s_cmp_le_u32 s34,4\n"
"s_cselect_b32 s61,20,16\n"
"buffer_load_dwordx4 v9,s[44:47],s73 offen offset:0 sc1 lds\n"
"v_mfma_f32_16x16x32_bf16 a[76:79],a[216:219],a[28:31],a[76:79]\n"
"s_add_u32 s61,s61,s37\n"
"s_mul_i32 s61,s61,9216\n"
"v_mfma_f32_16x16x32_bf16 a[92:95],a[220:223],a[28:31],a[92:95]\n"
"buffer_load_dwordx4 a[252:255],v9,s[56:59],s61 offen offset:0 sc1\n"
"v_mfma_f32_16x16x32_bf16 a[92:95],v[208:211],a[60:63],a[92:95]\n"
"L_pgs304_%=:\n"
"s_cbranch_execz L_pgs368_%=\n"
"v_rcp_f32 v56,v56\n"
"s_waitcnt vmcnt(22)\n"
"v_mfma_f32_16x16x32_bf16 v[96:99],v[212:215],a[32:35],v[96:99]\n"
"v_mfma_f32_16x16x32_bf16 v[96:99],a[224:227],a[0:3],v[96:99]\n"
"v_rcp_f32 v57,v57\n"
"v_mfma_f32_16x16x32_bf16 v[100:103],a[224:227],a[4:7],v[100:103]\n"
"buffer_load_dwordx4 v[208:211],v9,s[56:59],s61 offen offset:1024 sc1\n"
"v_mfma_f32_16x16x32_bf16 v[100:103],v[212:215],a[36:39],v[100:103]\n"
"v_rcp_f32 v58,v58\n"
"v_mfma_f32_16x16x32_bf16 v[104:107],v[212:215],a[40:43],v[104:107]\n"
"s_cmp_le_u32 s34,5\n"
"s_cselect_b32 s17,24,20\n"
"buffer_load_dwordx4 v9,s[44:47],s73 offen offset:1024 sc1 lds\n"
"v_mfma_f32_16x16x32_bf16 v[104:107],a[224:227],a[8:11],v[104:107]\n"
"s_add_u32 s17,s17,s37\n"
"s_mul_i32 s17,s17,9216\n"
"v_mfma_f32_16x16x32_bf16 v[108:111],a[224:227],a[12:15],v[108:111]\n"
"buffer_load_dwordx4 a[216:219],v9,s[56:59],s17 offen offset:0 sc1\n"
"v_mfma_f32_16x16x32_bf16 v[108:111],v[212:215],a[44:47],v[108:111]\n"
"v_rcp_f32 v59,v59\n"
"v_mfma_f32_16x16x32_bf16 a[96:99],v[212:215],a[48:51],a[96:99]\n"
"v_mul_f32 v52,v52,v56\n"
"v_mul_f32 v53,v53,v57\n"
"v_mfma_f32_16x16x32_bf16 a[96:99],a[224:227],a[16:19],a[96:99]\n"
"v_mul_f32 v54,v54,v58\n"
"v_mul_f32 v55,v55,v59\n"
"v_mfma_f32_16x16x32_bf16 a[100:103],a[224:227],a[20:23],a[100:103]\n"
"buffer_load_dwordx4 a[220:223],v9,s[56:59],s17 offen offset:1024 sc1\n"
"v_cvt_pk_bf16_f32 v28,v52,v53\n"
"v_mfma_f32_16x16x32_bf16 a[100:103],v[212:215],a[52:55],a[100:103]\n"
"s_add_u32 s73,s72,s81\n"
"v_cvt_pk_bf16_f32 v29,v54,v55\n"
"v_mfma_f32_16x16x32_bf16 a[104:107],v[212:215],a[56:59],a[104:107]\n"
"buffer_load_dwordx4 v9,s[44:47],s73 offen offset:2048 sc1 lds\n"
"v_mfma_f32_16x16x32_bf16 a[104:107],a[224:227],a[24:27],a[104:107]\n"
"v_lshlrev_b32 v52,16,v26\n"
"v_and_b32 v53,s62,v26\n"
"v_mfma_f32_16x16x32_bf16 a[108:111],a[224:227],a[28:31],a[108:111]\n"
"v_lshlrev_b32 v54,16,v27\n"
"v_and_b32 v55,s62,v27\n"
"v_mfma_f32_16x16x32_bf16 a[108:111],v[212:215],a[60:63],a[108:111]\n"
"v_mul_f32 v56,v52,v52\n"
"v_mul_f32 v57,v53,v53\n"
"s_waitcnt vmcnt(25)\n"
"v_mfma_f32_16x16x32_bf16 v[112:115],v[216:219],a[32:35],v[112:115]\n"
"buffer_load_dwordx4 v[212:215],v9,s[56:59],s61 offen offset:2048 sc1\n"
"v_mfma_f32_16x16x32_bf16 v[112:115],a[228:231],a[0:3],v[112:115]\n"
"v_mul_f32 v58,v54,v54\n"
"v_mul_f32 v59,v55,v55\n"
"v_mfma_f32_16x16x32_bf16 v[116:119],a[228:231],a[4:7],v[116:119]\n"
"v_fmaak_f32 v56,v237,v56,0xc0135761\n"
"v_fmaak_f32 v57,v237,v57,0xc0135761\n"
"v_mfma_f32_16x16x32_bf16 v[116:119],v[216:219],a[36:39],v[116:119]\n"
"v_fmaak_f32 v58,v237,v58,0xc0135761\n"
"v_fmaak_f32 v59,v237,v59,0xc0135761\n"
"v_mfma_f32_16x16x32_bf16 v[120:123],v[216:219],a[40:43],v[120:123]\n"
"buffer_load_dwordx4 v9,s[44:47],s73 offen offset:3072 sc1 lds\n"
"v_mfma_f32_16x16x32_bf16 v[120:123],a[228:231],a[8:11],v[120:123]\n"
"v_mul_f32 v56,v56,v52\n"
"v_mul_f32 v57,v57,v53\n"
"v_mfma_f32_16x16x32_bf16 v[124:127],a[228:231],a[12:15],v[124:127]\n"
"v_mul_f32 v58,v58,v54\n"
"v_mul_f32 v59,v59,v55\n"
"v_mfma_f32_16x16x32_bf16 v[124:127],v[216:219],a[44:47],v[124:127]\n"
"v_exp_f32 v56,v56\n"
"v_mfma_f32_16x16x32_bf16 a[112:115],v[216:219],a[48:51],a[112:115]\n"
"buffer_load_dwordx4 a[224:227],v9,s[56:59],s17 offen offset:2048 sc1\n"
"v_mfma_f32_16x16x32_bf16 a[112:115],a[228:231],a[16:19],a[112:115]\n"
"v_exp_f32 v57,v57\n"
"v_mfma_f32_16x16x32_bf16 a[116:119],a[228:231],a[20:23],a[116:119]\n"
"v_exp_f32 v58,v58\n"
"v_mfma_f32_16x16x32_bf16 a[116:119],v[216:219],a[52:55],a[116:119]\n"
"s_add_u32 m0,s84,77824\n"
"s_add_u32 s73,s72,s82\n"
"v_mfma_f32_16x16x32_bf16 a[120:123],v[216:219],a[56:59],a[120:123]\n"
"buffer_load_dwordx4 v9,s[44:47],s73 offen offset:0 sc1 lds\n"
"v_mfma_f32_16x16x32_bf16 a[120:123],a[228:231],a[24:27],a[120:123]\n"
"v_exp_f32 v59,v59\n"
"v_mfma_f32_16x16x32_bf16 a[124:127],a[228:231],a[28:31],a[124:127]\n"
"v_add_f32 v56,1.0,v56\n"
"v_add_f32 v57,1.0,v57\n"
"v_mfma_f32_16x16x32_bf16 a[124:127],v[216:219],a[60:63],a[124:127]\n"
"v_add_f32 v58,1.0,v58\n"
"v_add_f32 v59,1.0,v59\n"
"s_waitcnt vmcnt(25)\n"
"v_mfma_f32_16x16x32_bf16 v[128:131],v[220:223],a[32:35],v[128:131]\n"
"buffer_load_dwordx4 v[216:219],v9,s[56:59],s61 offen offset:3072 sc1\n"
"v_mfma_f32_16x16x32_bf16 v[128:131],a[232:235],a[0:3],v[128:131]\n"
"v_rcp_f32 v56,v56\n"
"v_mfma_f32_16x16x32_bf16 v[132:135],a[232:235],a[4:7],v[132:135]\n"
"v_rcp_f32 v57,v57\n"
"v_mfma_f32_16x16x32_bf16 v[132:135],v[220:223],a[36:39],v[132:135]\n"
"v_rcp_f32 v58,v58\n"
"v_mfma_f32_16x16x32_bf16 v[136:139],v[220:223],a[40:43],v[136:139]\n"
"buffer_load_dwordx4 v9,s[44:47],s73 offen offset:1024 sc1 lds\n"
"v_mfma_f32_16x16x32_bf16 v[136:139],a[232:235],a[8:11],v[136:139]\n"
"v_rcp_f32 v59,v59\n"
"v_mfma_f32_16x16x32_bf16 v[140:143],a[232:235],a[12:15],v[140:143]\n"
"v_mul_f32 v52,v52,v56\n"
"v_mul_f32 v53,v53,v57\n"
"v_mfma_f32_16x16x32_bf16 v[140:143],v[220:223],a[44:47],v[140:143]\n"
"v_mul_f32 v54,v54,v58\n"
"v_mul_f32 v55,v55,v59\n"
"v_mfma_f32_16x16x32_bf16 a[128:131],v[220:223],a[48:51],a[128:131]\n"
"buffer_load_dwordx4 a[228:231],v9,s[56:59],s17 offen offset:3072 sc1\n"
"v_cvt_pk_bf16_f32 v26,v52,v53\n"
"v_mfma_f32_16x16x32_bf16 a[128:131],a[232:235],a[16:19],a[128:131]\n"
"v_cvt_pk_bf16_f32 v27,v54,v55\n"
"v_lshlrev_b32 v52,16,v30\n"
"v_mfma_f32_16x16x32_bf16 a[132:135],a[232:235],a[20:23],a[132:135]\n"
"v_and_b32 v53,s62,v30\n"
"v_lshlrev_b32 v54,16,v31\n"
"v_mfma_f32_16x16x32_bf16 a[132:135],v[220:223],a[52:55],a[132:135]\n"
"v_and_b32 v55,s62,v31\n"
"v_mul_f32 v56,v52,v52\n"
"v_mfma_f32_16x16x32_bf16 a[136:139],v[220:223],a[56:59],a[136:139]\n"
"v_mul_f32 v57,v53,v53\n"
"v_mul_f32 v58,v54,v54\n"
"v_mfma_f32_16x16x32_bf16 a[136:139],a[232:235],a[24:27],a[136:139]\n"
"v_mul_f32 v59,v55,v55\n"
"v_fmaak_f32 v56,v237,v56,0xc0135761\n"
"v_mfma_f32_16x16x32_bf16 a[140:143],a[232:235],a[28:31],a[140:143]\n"
"v_fmaak_f32 v57,v237,v57,0xc0135761\n"
"v_fmaak_f32 v58,v237,v58,0xc0135761\n"
"v_mfma_f32_16x16x32_bf16 a[140:143],v[220:223],a[60:63],a[140:143]\n"
"s_add_u32 s61,s61,4096\n"
"v_fmaak_f32 v59,v237,v59,0xc0135761\n"
"s_waitcnt vmcnt(25)\n"
"v_mfma_f32_16x16x32_bf16 v[144:147],v[224:227],a[32:35],v[144:147]\n"
"buffer_load_dwordx4 v[220:223],v9,s[56:59],s61 offen offset:0 sc1\n"
"v_mfma_f32_16x16x32_bf16 v[144:147],a[236:239],a[0:3],v[144:147]\n"
"v_mul_f32 v56,v56,v52\n"
"v_mul_f32 v57,v57,v53\n"
"v_mfma_f32_16x16x32_bf16 v[148:151],a[236:239],a[4:7],v[148:151]\n"
"v_mul_f32 v58,v58,v54\n"
"v_mul_f32 v59,v59,v55\n"
"v_mfma_f32_16x16x32_bf16 v[148:151],v[224:227],a[36:39],v[148:151]\n"
"s_add_u32 s73,s72,s83\n"
"v_mfma_f32_16x16x32_bf16 v[152:155],v[224:227],a[40:43],v[152:155]\n"
"buffer_load_dwordx4 v9,s[44:47],s73 offen offset:2048 sc1 lds\n"
"v_mfma_f32_16x16x32_bf16 v[152:155],a[236:239],a[8:11],v[152:155]\n"
"v_exp_f32 v56,v56\n"
"v_mfma_f32_16x16x32_bf16 v[156:159],a[236:239],a[12:15],v[156:159]\n"
"v_exp_f32 v57,v57\n"
"v_mfma_f32_16x16x32_bf16 v[156:159],v[224:227],a[44:47],v[156:159]\n"
"s_add_u32 s17,s17,4096\n"
"v_mfma_f32_16x16x32_bf16 a[144:147],v[224:227],a[48:51],a[144:147]\n"
"buffer_load_dwordx4 a[232:235],v9,s[56:59],s17 offen offset:0 sc1\n"
"v_mfma_f32_16x16x32_bf16 a[144:147],a[236:239],a[16:19],a[144:147]\n"
"v_exp_f32 v58,v58\n"
"v_mfma_f32_16x16x32_bf16 a[148:151],a[236:239],a[20:23],a[148:151]\n"
"v_exp_f32 v59,v59\n"
"v_mfma_f32_16x16x32_bf16 a[148:151],v[224:227],a[52:55],a[148:151]\n"
"v_add_f32 v56,1.0,v56\n"
"v_add_f32 v57,1.0,v57\n"
"v_mfma_f32_16x16x32_bf16 a[152:155],v[224:227],a[56:59],a[152:155]\n"
"buffer_load_dwordx4 v9,s[44:47],s73 offen offset:3072 sc1 lds\n"
"v_mfma_f32_16x16x32_bf16 a[152:155],a[236:239],a[24:27],a[152:155]\n"
"v_add_f32 v58,1.0,v58\n"
"v_add_f32 v59,1.0,v59\n"
"v_mfma_f32_16x16x32_bf16 a[156:159],a[236:239],a[28:31],a[156:159]\n"
"v_rcp_f32 v56,v56\n"
"v_mfma_f32_16x16x32_bf16 a[156:159],v[224:227],a[60:63],a[156:159]\n"
"v_rcp_f32 v57,v57\n"
"s_waitcnt vmcnt(25)\n"
"v_mfma_f32_16x16x32_bf16 v[160:163],v[228:231],a[32:35],v[160:163]\n"
"buffer_load_dwordx4 v[224:227],v9,s[56:59],s61 offen offset:1024 sc1\n"
"v_mfma_f32_16x16x32_bf16 v[160:163],a[240:243],a[0:3],v[160:163]\n"
"v_rcp_f32 v58,v58\n"
"v_mfma_f32_16x16x32_bf16 v[164:167],a[240:243],a[4:7],v[164:167]\n"
"v_rcp_f32 v59,v59\n"
"v_mfma_f32_16x16x32_bf16 v[164:167],v[228:231],a[36:39],v[164:167]\n"
"v_mul_f32 v52,v52,v56\n"
"v_mul_f32 v53,v53,v57\n"
"v_mfma_f32_16x16x32_bf16 v[168:171],v[228:231],a[40:43],v[168:171]\n"
"v_mul_f32 v54,v54,v58\n"
"v_mul_f32 v55,v55,v59\n"
"v_mfma_f32_16x16x32_bf16 v[168:171],a[240:243],a[8:11],v[168:171]\n"
"v_cvt_pk_bf16_f32 v30,v52,v53\n"
"v_cvt_pk_bf16_f32 v31,v54,v55\n"
"v_mfma_f32_16x16x32_bf16 v[172:175],a[240:243],a[12:15],v[172:175]\n"
"v_min_u32 v61,28,v10\n"
"buffer_load_dword v60,v61,s[88:91],s75 offen sc1\n"
"v_mfma_f32_16x16x32_bf16 v[172:175],v[228:231],a[44:47],v[172:175]\n"
"v_lshlrev_b32 v52,16,v32\n"
"v_and_b32 v53,s62,v32\n"
"v_mfma_f32_16x16x32_bf16 a[160:163],v[228:231],a[48:51],a[160:163]\n"
"buffer_load_dwordx4 a[236:239],v9,s[56:59],s17 offen offset:1024 sc1\n"
"s_add_u32 m0,s84,110592\n"
"v_mfma_f32_16x16x32_bf16 a[160:163],a[240:243],a[16:19],a[160:163]\n"
"s_cmp_le_u32 s34,8\n"
"s_cselect_b32 s72,36,32\n"
"v_mfma_f32_16x16x32_bf16 a[164:167],a[240:243],a[20:23],a[164:167]\n"
"s_add_u32 s72,s72,s37\n"
"v_lshlrev_b32 v54,16,v33\n"
"v_mfma_f32_16x16x32_bf16 a[164:167],v[228:231],a[52:55],a[164:167]\n"
"s_lshl_b32 s72,s72,13\n"
"s_add_u32 s73,s72,s80\n"
"v_mfma_f32_16x16x32_bf16 a[168:171],v[228:231],a[56:59],a[168:171]\n"
"buffer_load_dwordx4 v9,s[44:47],s73 offen offset:0 sc1 lds\n"
"v_mfma_f32_16x16x32_bf16 a[168:171],a[240:243],a[24:27],a[168:171]\n"
"v_and_b32 v55,s62,v33\n"
"v_mul_f32 v56,v52,v52\n"
"v_mfma_f32_16x16x32_bf16 a[172:175],a[240:243],a[28:31],a[172:175]\n"
"v_mul_f32 v57,v53,v53\n"
"v_mul_f32 v58,v54,v54\n"
"v_mfma_f32_16x16x32_bf16 a[172:175],v[228:231],a[60:63],a[172:175]\n"
"v_mul_f32 v59,v55,v55\n"
"v_fmaak_f32 v56,v237,v56,0xc0135761\n"
"s_waitcnt vmcnt(23)\n"
"v_mfma_f32_16x16x32_bf16 v[176:179],a[208:211],a[32:35],v[176:179]\n"
"buffer_load_dwordx4 v[228:231],v9,s[56:59],s61 offen offset:2048 sc1\n"
"v_mfma_f32_16x16x32_bf16 v[176:179],a[244:247],a[0:3],v[176:179]\n"
"v_fmaak_f32 v57,v237,v57,0xc0135761\n"
"v_fmaak_f32 v58,v237,v58,0xc0135761\n"
"s_waitcnt vmcnt(23)\n"
"v_mfma_f32_16x16x32_bf16 v[192:195],a[248:251],a[0:3],v[192:195]\n"
"v_fmaak_f32 v59,v237,v59,0xc0135761\n"
"v_mfma_f32_16x16x32_bf16 v[192:195],a[212:215],a[32:35],v[192:195]\n"
"v_mul_f32 v56,v56,v52\n"
"v_mul_f32 v57,v57,v53\n"
"v_mfma_f32_16x16x32_bf16 v[180:183],a[208:211],a[36:39],v[180:183]\n"
"buffer_load_dwordx4 v9,s[44:47],s73 offen offset:1024 sc1 lds\n"
"v_mfma_f32_16x16x32_bf16 v[180:183],a[244:247],a[4:7],v[180:183]\n"
"ds_read_b128 a[32:35],v12 offset:0\n"
"v_mfma_f32_16x16x32_bf16 v[196:199],a[248:251],a[4:7],v[196:199]\n"
"ds_read_b128 a[0:3],v14 offset:0\n"
"v_mfma_f32_16x16x32_bf16 v[196:199],a[212:215],a[36:39],v[196:199]\n"
"v_mul_f32 v58,v58,v54\n"
"v_mul_f32 v59,v59,v55\n"
"v_mfma_f32_16x16x32_bf16 v[184:187],a[208:211],a[40:43],v[184:187]\n"
"buffer_load_dwordx4 a[240:243],v9,s[56:59],s17 offen offset:2048 sc1\n"
"v_mfma_f32_16x16x32_bf16 v[184:187],a[244:247],a[8:11],v[184:187]\n"
"ds_read_b128 a[36:39],v12 offset:1024\n"
"v_mfma_f32_16x16x32_bf16 v[200:203],a[248:251],a[8:11],v[200:203]\n"
"ds_read_b128 a[4:7],v14 offset:1024\n"
"v_mfma_f32_16x16x32_bf16 v[200:203],a[212:215],a[40:43],v[200:203]\n"
"s_add_u32 s73,s72,s81\n"
"v_mfma_f32_16x16x32_bf16 v[188:191],a[208:211],a[44:47],v[188:191]\n"
"buffer_load_dwordx4 v9,s[44:47],s73 offen offset:2048 sc1 lds\n"
"v_mfma_f32_16x16x32_bf16 v[188:191],a[244:247],a[12:15],v[188:191]\n"
"ds_read_b128 a[40:43],v12 offset:2048\n"
"v_mfma_f32_16x16x32_bf16 v[204:207],a[248:251],a[12:15],v[204:207]\n"
"ds_read_b128 a[8:11],v14 offset:2048\n"
"v_mfma_f32_16x16x32_bf16 v[204:207],a[212:215],a[44:47],v[204:207]\n"
"v_exp_f32 v56,v56\n"
"v_mfma_f32_16x16x32_bf16 a[176:179],a[208:211],a[48:51],a[176:179]\n"
"v_exp_f32 v57,v57\n"
"v_mfma_f32_16x16x32_bf16 a[176:179],a[244:247],a[16:19],a[176:179]\n"
"ds_read_b128 a[44:47],v12 offset:3072\n"
"v_mfma_f32_16x16x32_bf16 a[192:195],a[248:251],a[16:19],a[192:195]\n"
"ds_read_b128 a[12:15],v14 offset:3072\n"
"v_mfma_f32_16x16x32_bf16 a[192:195],a[212:215],a[48:51],a[192:195]\n"
"v_exp_f32 v58,v58\n"
"v_mfma_f32_16x16x32_bf16 a[180:183],a[208:211],a[52:55],a[180:183]\n"
"v_exp_f32 v59,v59\n"
"v_mfma_f32_16x16x32_bf16 a[180:183],a[244:247],a[20:23],a[180:183]\n"
"ds_read_b128 a[48:51],v12 offset:4096\n"
"v_add_f32 v56,1.0,v56\n"
"v_mfma_f32_16x16x32_bf16 a[196:199],a[248:251],a[20:23],a[196:199]\n"
"ds_read_b128 a[16:19],v255 offset:0\n"
"v_add_f32 v57,1.0,v57\n"
"v_mfma_f32_16x16x32_bf16 a[196:199],a[212:215],a[52:55],a[196:199]\n"
"v_add_f32 v58,1.0,v58\n"
"v_add_f32 v59,1.0,v59\n"
"v_mfma_f32_16x16x32_bf16 a[184:187],a[208:211],a[56:59],a[184:187]\n"
"v_rcp_f32 v56,v56\n"
"v_mfma_f32_16x16x32_bf16 a[184:187],a[244:247],a[24:27],a[184:187]\n"
"ds_read_b128 a[52:55],v12 offset:5120\n"
"v_mfma_f32_16x16x32_bf16 a[200:203],a[248:251],a[24:27],a[200:203]\n"
"ds_read_b128 a[20:23],v255 offset:1024\n"
"v_mfma_f32_16x16x32_bf16 a[200:203],a[212:215],a[56:59],a[200:203]\n"
"v_rcp_f32 v57,v57\n"
"v_mfma_f32_16x16x32_bf16 a[188:191],a[208:211],a[60:63],a[188:191]\n"
"buffer_load_dwordx4 v9,s[44:47],s73 offen offset:3072 sc1 lds\n"
"v_mfma_f32_16x16x32_bf16 a[188:191],a[244:247],a[28:31],a[188:191]\n"
"ds_read_b128 a[56:59],v12 offset:6144\n"
"v_mfma_f32_16x16x32_bf16 a[204:207],a[248:251],a[28:31],a[204:207]\n"
"buffer_load_dwordx4 a[208:211],v9,s[56:59],s61 offen offset:3072 sc1\n"
"ds_read_b128 a[24:27],v255 offset:2048\n"
"v_mfma_f32_16x16x32_bf16 a[204:207],a[212:215],a[60:63],a[204:207]\n"
"v_rcp_f32 v58,v58\n"
"s_waitcnt lgkmcnt(10)\n"
"s_waitcnt vmcnt(23)\n"
"v_mfma_f32_16x16x32_bf16 v[64:67],a[252:255],a[32:35],v[64:67]\n"
"v_mfma_f32_16x16x32_bf16 v[64:67],a[216:219],a[0:3],v[64:67]\n"
"s_add_u32 s61,s61,4096\n"
"ds_read_b128 a[60:63],v12 offset:7168\n"
"s_waitcnt vmcnt(22)\n"
"v_mfma_f32_16x16x32_bf16 v[80:83],a[220:223],a[0:3],v[80:83]\n"
"buffer_load_dwordx4 a[212:215],v9,s[56:59],s61 offen offset:0 sc1\n"
"ds_read_b128 a[28:31],v255 offset:3072\n"
"v_mfma_f32_16x16x32_bf16 v[80:83],v[208:211],a[32:35],v[80:83]\n"
"s_add_u32 m0,s84,114688\n"
"s_add_u32 s73,s72,s82\n"
"v_mfma_f32_16x16x32_bf16 v[68:71],a[252:255],a[36:39],v[68:71]\n"
"buffer_load_dwordx4 v9,s[44:47],s73 offen offset:0 sc1 lds\n"
"v_mfma_f32_16x16x32_bf16 v[68:71],a[216:219],a[4:7],v[68:71]\n"
"v_rcp_f32 v59,v59\n"
"v_mfma_f32_16x16x32_bf16 v[84:87],a[220:223],a[4:7],v[84:87]\n"
"buffer_load_dwordx4 a[244:247],v9,s[56:59],s17 offen offset:3072 sc1\n"
"v_mul_f32 v52,v52,v56\n"
"v_mfma_f32_16x16x32_bf16 v[84:87],v[208:211],a[36:39],v[84:87]\n"
"v_mul_f32 v53,v53,v57\n"
"v_mul_f32 v54,v54,v58\n"
"s_waitcnt lgkmcnt(8)\n"
"v_mfma_f32_16x16x32_bf16 v[72:75],a[252:255],a[40:43],v[72:75]\n"
"v_mul_f32 v55,v55,v59\n"
"v_mfma_f32_16x16x32_bf16 v[72:75],a[216:219],a[8:11],v[72:75]\n"
"s_add_u32 s17,s17,4096\n"
"v_cvt_pk_bf16_f32 v32,v52,v53\n"
"v_mfma_f32_16x16x32_bf16 v[88:91],a[220:223],a[8:11],v[88:91]\n"
"buffer_load_dwordx4 a[248:251],v9,s[56:59],s17 offen offset:0 sc1\n"
"v_cvt_pk_bf16_f32 v33,v54,v55\n"
"v_mfma_f32_16x16x32_bf16 v[88:91],v[208:211],a[40:43],v[88:91]\n"
"v_lshlrev_b32 v52,16,v36\n"
"v_and_b32 v53,s62,v36\n"
"v_mfma_f32_16x16x32_bf16 v[76:79],a[252:255],a[44:47],v[76:79]\n"
"buffer_load_dwordx4 v9,s[44:47],s73 offen offset:1024 sc1 lds\n"
"v_mfma_f32_16x16x32_bf16 v[76:79],a[216:219],a[12:15],v[76:79]\n"
"v_lshlrev_b32 v54,16,v37\n"
"v_and_b32 v55,s62,v37\n"
"v_mfma_f32_16x16x32_bf16 v[92:95],a[220:223],a[12:15],v[92:95]\n"
"v_mul_f32 v56,v52,v52\n"
"v_mul_f32 v57,v53,v53\n"
"v_mfma_f32_16x16x32_bf16 v[92:95],v[208:211],a[44:47],v[92:95]\n"
"v_mul_f32 v58,v54,v54\n"
"v_mul_f32 v59,v55,v55\n"
"s_waitcnt lgkmcnt(4)\n"
"v_mfma_f32_16x16x32_bf16 a[64:67],a[252:255],a[48:51],a[64:67]\n"
"v_fmaak_f32 v56,v237,v56,0xc0135761\n"
"v_mfma_f32_16x16x32_bf16 a[64:67],a[216:219],a[16:19],a[64:67]\n"
"v_fmaak_f32 v57,v237,v57,0xc0135761\n"
"v_fmaak_f32 v58,v237,v58,0xc0135761\n"
"v_mfma_f32_16x16x32_bf16 a[80:83],a[220:223],a[16:19],a[80:83]\n"
"v_fmaak_f32 v59,v237,v59,0xc0135761\n"
"v_mul_f32 v56,v56,v52\n"
"v_mfma_f32_16x16x32_bf16 a[80:83],v[208:211],a[48:51],a[80:83]\n"
"v_mul_f32 v57,v57,v53\n"
"v_mul_f32 v58,v58,v54\n"
"v_mfma_f32_16x16x32_bf16 a[68:71],a[252:255],a[52:55],a[68:71]\n"
"v_mul_f32 v59,v59,v55\n"
"v_mfma_f32_16x16x32_bf16 a[68:71],a[216:219],a[20:23],a[68:71]\n"
"v_exp_f32 v56,v56\n"
"v_mfma_f32_16x16x32_bf16 a[84:87],a[220:223],a[20:23],a[84:87]\n"
"v_exp_f32 v57,v57\n"
"v_mfma_f32_16x16x32_bf16 a[84:87],v[208:211],a[52:55],a[84:87]\n"
"v_exp_f32 v58,v58\n"
"s_waitcnt lgkmcnt(0)\n"
"v_mfma_f32_16x16x32_bf16 a[72:75],a[252:255],a[56:59],a[72:75]\n"
"v_mfma_f32_16x16x32_bf16 a[72:75],a[216:219],a[24:27],a[72:75]\n"
"v_exp_f32 v59,v59\n"
"v_mfma_f32_16x16x32_bf16 a[88:91],a[220:223],a[24:27],a[88:91]\n"
"v_add_f32 v56,1.0,v56\n"
"v_add_f32 v57,1.0,v57\n"
"v_mfma_f32_16x16x32_bf16 a[88:91],v[208:211],a[56:59],a[88:91]\n"
"s_add_u32 s73,s72,s83\n"
"v_add_f32 v58,1.0,v58\n"
"s_waitcnt vmcnt(13)\n"
"v_cmp_ne_u32 vcc,s38,v60\n"
"s_cbranch_vccnz L_ap6_%=\n"
"L_aq6_%=:\n"
"v_mfma_f32_16x16x32_bf16 a[76:79],a[252:255],a[60:63],a[76:79]\n"
"s_cmp_le_u32 s34,6\n"
"s_cselect_b32 s61,28,24\n"
"buffer_load_dwordx4 v9,s[44:47],s73 offen offset:2048 sc1 lds\n"
"v_mfma_f32_16x16x32_bf16 a[76:79],a[216:219],a[28:31],a[76:79]\n"
"s_add_u32 s61,s61,s37\n"
"s_mul_i32 s61,s61,9216\n"
"v_mfma_f32_16x16x32_bf16 a[92:95],a[220:223],a[28:31],a[92:95]\n"
"buffer_load_dwordx4 a[252:255],v9,s[56:59],s61 offen offset:0 sc1\n"
"v_add_f32 v59,1.0,v59\n"
"v_mfma_f32_16x16x32_bf16 a[92:95],v[208:211],a[60:63],a[92:95]\n"
"v_rcp_f32 v56,v56\n"
"v_mfma_f32_16x16x32_bf16 v[96:99],v[212:215],a[32:35],v[96:99]\n"
"v_mfma_f32_16x16x32_bf16 v[96:99],a[224:227],a[0:3],v[96:99]\n"
"v_rcp_f32 v57,v57\n"
"v_mfma_f32_16x16x32_bf16 v[100:103],a[224:227],a[4:7],v[100:103]\n"
"buffer_load_dwordx4 v[208:211],v9,s[56:59],s61 offen offset:1024 sc1\n"
"v_mfma_f32_16x16x32_bf16 v[100:103],v[212:215],a[36:39],v[100:103]\n"
"v_rcp_f32 v58,v58\n"
"v_mfma_f32_16x16x32_bf16 v[104:107],v[212:215],a[40:43],v[104:107]\n"
"s_cmp_le_u32 s34,7\n"
"s_cselect_b32 s17,32,28\n"
"buffer_load_dwordx4 v9,s[44:47],s73 offen offset:3072 sc1 lds\n"
"v_mfma_f32_16x16x32_bf16 v[104:107],a[224:227],a[8:11],v[104:107]\n"
"s_add_u32 s17,s17,s37\n"
"s_mul_i32 s17,s17,9216\n"
"v_mfma_f32_16x16x32_bf16 v[108:111],a[224:227],a[12:15],v[108:111]\n"
"buffer_load_dwordx4 a[216:219],v9,s[56:59],s17 offen offset:0 sc1\n"
"v_mfma_f32_16x16x32_bf16 v[108:111],v[212:215],a[44:47],v[108:111]\n"
"v_rcp_f32 v59,v59\n"
"v_mfma_f32_16x16x32_bf16 a[96:99],v[212:215],a[48:51],a[96:99]\n"
"v_mul_f32 v52,v52,v56\n"
"v_mul_f32 v53,v53,v57\n"
"v_mfma_f32_16x16x32_bf16 a[96:99],a[224:227],a[16:19],a[96:99]\n"
"v_mul_f32 v54,v54,v58\n"
"v_mul_f32 v55,v55,v59\n"
"v_mfma_f32_16x16x32_bf16 a[100:103],a[224:227],a[20:23],a[100:103]\n"
"buffer_load_dwordx4 a[220:223],v9,s[56:59],s17 offen offset:1024 sc1\n"
"v_cvt_pk_bf16_f32 v36,v52,v53\n"
"v_mfma_f32_16x16x32_bf16 a[100:103],v[212:215],a[52:55],a[100:103]\n"
"v_cvt_pk_bf16_f32 v37,v54,v55\n"
"v_lshlrev_b32 v52,16,v34\n"
"v_mfma_f32_16x16x32_bf16 a[104:107],v[212:215],a[56:59],a[104:107]\n"
"v_and_b32 v53,s62,v34\n"
"v_lshlrev_b32 v54,16,v35\n"
"v_mfma_f32_16x16x32_bf16 a[104:107],a[224:227],a[24:27],a[104:107]\n"
"v_and_b32 v55,s62,v35\n"
"v_mul_f32 v56,v52,v52\n"
"v_mfma_f32_16x16x32_bf16 a[108:111],a[224:227],a[28:31],a[108:111]\n"
"v_mul_f32 v57,v53,v53\n"
"v_mul_f32 v58,v54,v54\n"
"v_mfma_f32_16x16x32_bf16 a[108:111],v[212:215],a[60:63],a[108:111]\n"
"v_mul_f32 v59,v55,v55\n"
"v_fmaak_f32 v56,v237,v56,0xc0135761\n"
"v_mfma_f32_16x16x32_bf16 v[112:115],v[216:219],a[32:35],v[112:115]\n"
"buffer_load_dwordx4 v[212:215],v9,s[56:59],s61 offen offset:2048 sc1\n"
"s_add_u32 m0,s84,0\n"
"v_mfma_f32_16x16x32_bf16 v[112:115],a[228:231],a[0:3],v[112:115]\n"
"s_cmp_le_u32 s34,9\n"
"s_cselect_b32 s72,40,36\n"
"v_mfma_f32_16x16x32_bf16 v[116:119],a[228:231],a[4:7],v[116:119]\n"
"s_add_u32 s72,s72,s37\n"
"v_fmaak_f32 v57,v237,v57,0xc0135761\n"
"v_mfma_f32_16x16x32_bf16 v[116:119],v[216:219],a[36:39],v[116:119]\n"
"s_lshl_b32 s72,s72,13\n"
"s_add_u32 s73,s72,s80\n"
"v_mfma_f32_16x16x32_bf16 v[120:123],v[216:219],a[40:43],v[120:123]\n"
"buffer_load_dwordx4 v9,s[44:47],s73 offen offset:0 sc1 lds\n"
"v_mfma_f32_16x16x32_bf16 v[120:123],a[228:231],a[8:11],v[120:123]\n"
"v_fmaak_f32 v58,v237,v58,0xc0135761\n"
"v_fmaak_f32 v59,v237,v59,0xc0135761\n"
"v_mfma_f32_16x16x32_bf16 v[124:127],a[228:231],a[12:15],v[124:127]\n"
"v_mul_f32 v56,v56,v52\n"
"v_mul_f32 v57,v57,v53\n"
"v_mfma_f32_16x16x32_bf16 v[124:127],v[216:219],a[44:47],v[124:127]\n"
"v_mul_f32 v58,v58,v54\n"
"v_mul_f32 v59,v59,v55\n"
"v_mfma_f32_16x16x32_bf16 a[112:115],v[216:219],a[48:51],a[112:115]\n"
"buffer_load_dwordx4 a[224:227],v9,s[56:59],s17 offen offset:2048 sc1\n"
"v_mfma_f32_16x16x32_bf16 a[112:115],a[228:231],a[16:19],a[112:115]\n"
"v_exp_f32 v56,v56\n"
"v_mfma_f32_16x16x32_bf16 a[116:119],a[228:231],a[20:23],a[116:119]\n"
"v_exp_f32 v57,v57\n"
"v_mfma_f32_16x16x32_bf16 a[116:119],v[216:219],a[52:55],a[116:119]\n"
"v_exp_f32 v58,v58\n"
"v_mfma_f32_16x16x32_bf16 a[120:123],v[216:219],a[56:59],a[120:123]\n"
"buffer_load_dwordx4 v9,s[44:47],s73 offen offset:1024 sc1 lds\n"
"v_mfma_f32_16x16x32_bf16 a[120:123],a[228:231],a[24:27],a[120:123]\n"
"v_exp_f32 v59,v59\n"
"v_mfma_f32_16x16x32_bf16 a[124:127],a[228:231],a[28:31],a[124:127]\n"
"v_add_f32 v56,1.0,v56\n"
"v_add_f32 v57,1.0,v57\n"
"v_mfma_f32_16x16x32_bf16 a[124:127],v[216:219],a[60:63],a[124:127]\n"
"v_add_f32 v58,1.0,v58\n"
"v_add_f32 v59,1.0,v59\n"
"v_mfma_f32_16x16x32_bf16 v[128:131],v[220:223],a[32:35],v[128:131]\n"
"buffer_load_dwordx4 v[216:219],v9,s[56:59],s61 offen offset:3072 sc1\n"
"v_mfma_f32_16x16x32_bf16 v[128:131],a[232:235],a[0:3],v[128:131]\n"
"v_rcp_f32 v56,v56\n"
"v_mfma_f32_16x16x32_bf16 v[132:135],a[232:235],a[4:7],v[132:135]\n"
"v_rcp_f32 v57,v57\n"
"v_mfma_f32_16x16x32_bf16 v[132:135],v[220:223],a[36:39],v[132:135]\n"
"s_add_u32 s73,s72,s81\n"
"v_mfma_f32_16x16x32_bf16 v[136:139],v[220:223],a[40:43],v[136:139]\n"
"buffer_load_dwordx4 v9,s[44:47],s73 offen offset:2048 sc1 lds\n"
"v_mfma_f32_16x16x32_bf16 v[136:139],a[232:235],a[8:11],v[136:139]\n"
"v_rcp_f32 v58,v58\n"
"v_mfma_f32_16x16x32_bf16 v[140:143],a[232:235],a[12:15],v[140:143]\n"
"v_rcp_f32 v59,v59\n"
"v_mfma_f32_16x16x32_bf16 v[140:143],v[220:223],a[44:47],v[140:143]\n"
"v_mul_f32 v52,v52,v56\n"
"v_mul_f32 v53,v53,v57\n"
"v_mfma_f32_16x16x32_bf16 a[128:131],v[220:223],a[48:51],a[128:131]\n"
"buffer_load_dwordx4 a[228:231],v9,s[56:59],s17 offen offset:3072 sc1\n"
"v_mul_f32 v54,v54,v58\n"
"v_mfma_f32_16x16x32_bf16 a[128:131],a[232:235],a[16:19],a[128:131]\n"
"v_mul_f32 v55,v55,v59\n"
"v_cvt_pk_bf16_f32 v34,v52,v53\n"
"v_mfma_f32_16x16x32_bf16 a[132:135],a[232:235],a[20:23],a[132:135]\n"
"v_cvt_pk_bf16_f32 v35,v54,v55\n"
"v_lshlrev_b32 v52,16,v38\n"
"v_mfma_f32_16x16x32_bf16 a[132:135],v[220:223],a[52:55],a[132:135]\n"
"v_and_b32 v53,s62,v38\n"
"v_lshlrev_b32 v54,16,v39\n"
"v_mfma_f32_16x16x32_bf16 a[136:139],v[220:223],a[56:59],a[136:139]\n"
"v_and_b32 v55,s62,v39\n"
"v_mul_f32 v56,v52,v52\n"
"v_mfma_f32_16x16x32_bf16 a[136:139],a[232:235],a[24:27],a[136:139]\n"
"v_mul_f32 v57,v53,v53\n"
"v_mul_f32 v58,v54,v54\n"
"v_mfma_f32_16x16x32_bf16 a[140:143],a[232:235],a[28:31],a[140:143]\n"
"v_mul_f32 v59,v55,v55\n"
"v_fmaak_f32 v56,v237,v56,0xc0135761\n"
"v_mfma_f32_16x16x32_bf16 a[140:143],v[220:223],a[60:63],a[140:143]\n"
"s_add_u32 s61,s61,4096\n"
"v_fmaak_f32 v57,v237,v57,0xc0135761\n"
"s_waitcnt vmcnt(25)\n"
"v_mfma_f32_16x16x32_bf16 v[144:147],v[224:227],a[32:35],v[144:147]\n"
"buffer_load_dwordx4 v[220:223],v9,s[56:59],s61 offen offset:0 sc1\n"
"v_mfma_f32_16x16x32_bf16 v[144:147],a[236:239],a[0:3],v[144:147]\n"
"v_fmaak_f32 v58,v237,v58,0xc0135761\n"
"v_fmaak_f32 v59,v237,v59,0xc0135761\n"
"v_mfma_f32_16x16x32_bf16 v[148:151],a[236:239],a[4:7],v[148:151]\n"
"v_mul_f32 v56,v56,v52\n"
"v_mul_f32 v57,v57,v53\n"
"v_mfma_f32_16x16x32_bf16 v[148:151],v[224:227],a[36:39],v[148:151]\n"
"v_mul_f32 v58,v58,v54\n"
"v_mul_f32 v59,v59,v55\n"
"v_mfma_f32_16x16x32_bf16 v[152:155],v[224:227],a[40:43],v[152:155]\n"
"buffer_load_dwordx4 v9,s[44:47],s73 offen offset:3072 sc1 lds\n"
"v_mfma_f32_16x16x32_bf16 v[152:155],a[236:239],a[8:11],v[152:155]\n"
"v_exp_f32 v56,v56\n"
"v_mfma_f32_16x16x32_bf16 v[156:159],a[236:239],a[12:15],v[156:159]\n"
"v_exp_f32 v57,v57\n"
"v_mfma_f32_16x16x32_bf16 v[156:159],v[224:227],a[44:47],v[156:159]\n"
"s_add_u32 s17,s17,4096\n"
"v_mfma_f32_16x16x32_bf16 a[144:147],v[224:227],a[48:51],a[144:147]\n"
"buffer_load_dwordx4 a[232:235],v9,s[56:59],s17 offen offset:0 sc1\n"
"v_mfma_f32_16x16x32_bf16 a[144:147],a[236:239],a[16:19],a[144:147]\n"
"v_exp_f32 v58,v58\n"
"v_mfma_f32_16x16x32_bf16 a[148:151],a[236:239],a[20:23],a[148:151]\n"
"v_exp_f32 v59,v59\n"
"v_mfma_f32_16x16x32_bf16 a[148:151],v[224:227],a[52:55],a[148:151]\n"
"s_add_u32 m0,s84,4096\n"
"s_add_u32 s73,s72,s82\n"
"v_mfma_f32_16x16x32_bf16 a[152:155],v[224:227],a[56:59],a[152:155]\n"
"buffer_load_dwordx4 v9,s[44:47],s73 offen offset:0 sc1 lds\n"
"v_mfma_f32_16x16x32_bf16 a[152:155],a[236:239],a[24:27],a[152:155]\n"
"v_add_f32 v56,1.0,v56\n"
"v_add_f32 v57,1.0,v57\n"
"v_mfma_f32_16x16x32_bf16 a[156:159],a[236:239],a[28:31],a[156:159]\n"
"v_add_f32 v58,1.0,v58\n"
"v_add_f32 v59,1.0,v59\n"
"v_mfma_f32_16x16x32_bf16 a[156:159],v[224:227],a[60:63],a[156:159]\n"
"v_rcp_f32 v56,v56\n"
"s_waitcnt vmcnt(25)\n"
"v_mfma_f32_16x16x32_bf16 v[160:163],v[228:231],a[32:35],v[160:163]\n"
"buffer_load_dwordx4 v[224:227],v9,s[56:59],s61 offen offset:1024 sc1\n"
"v_mfma_f32_16x16x32_bf16 v[160:163],a[240:243],a[0:3],v[160:163]\n"
"v_rcp_f32 v57,v57\n"
"v_mfma_f32_16x16x32_bf16 v[164:167],a[240:243],a[4:7],v[164:167]\n"
"v_rcp_f32 v58,v58\n"
"v_mfma_f32_16x16x32_bf16 v[164:167],v[228:231],a[36:39],v[164:167]\n"
"v_rcp_f32 v59,v59\n"
"v_mfma_f32_16x16x32_bf16 v[168:171],v[228:231],a[40:43],v[168:171]\n"
"buffer_load_dwordx4 v9,s[44:47],s73 offen offset:1024 sc1 lds\n"
"v_mfma_f32_16x16x32_bf16 v[168:171],a[240:243],a[8:11],v[168:171]\n"
"v_mul_f32 v52,v52,v56\n"
"v_mul_f32 v53,v53,v57\n"
"v_mfma_f32_16x16x32_bf16 v[172:175],a[240:243],a[12:15],v[172:175]\n"
"v_mul_f32 v54,v54,v58\n"
"v_mul_f32 v55,v55,v59\n"
"v_mfma_f32_16x16x32_bf16 v[172:175],v[228:231],a[44:47],v[172:175]\n"
"v_cvt_pk_bf16_f32 v38,v52,v53\n"
"v_cvt_pk_bf16_f32 v39,v54,v55\n"
"v_mfma_f32_16x16x32_bf16 a[160:163],v[228:231],a[48:51],a[160:163]\n"
"buffer_load_dwordx4 a[236:239],v9,s[56:59],s17 offen offset:1024 sc1\n"
"v_lshlrev_b32 v52,16,v40\n"
"v_mfma_f32_16x16x32_bf16 a[160:163],a[240:243],a[16:19],a[160:163]\n"
"v_and_b32 v53,s62,v40\n"
"v_lshlrev_b32 v54,16,v41\n"
"v_mfma_f32_16x16x32_bf16 a[164:167],a[240:243],a[20:23],a[164:167]\n"
"v_and_b32 v55,s62,v41\n"
"v_mul_f32 v56,v52,v52\n"
"v_mfma_f32_16x16x32_bf16 a[164:167],v[228:231],a[52:55],a[164:167]\n"
"v_mul_f32 v57,v53,v53\n"
"v_mul_f32 v58,v54,v54\n"
"v_mfma_f32_16x16x32_bf16 a[168:171],v[228:231],a[56:59],a[168:171]\n"
"v_mul_f32 v59,v55,v55\n"
"v_fmaak_f32 v56,v237,v56,0xc0135761\n"
"v_mfma_f32_16x16x32_bf16 a[168:171],a[240:243],a[24:27],a[168:171]\n"
"v_fmaak_f32 v57,v237,v57,0xc0135761\n"
"v_fmaak_f32 v58,v237,v58,0xc0135761\n"
"v_mfma_f32_16x16x32_bf16 a[172:175],a[240:243],a[28:31],a[172:175]\n"
"v_fmaak_f32 v59,v237,v59,0xc0135761\n"
"v_mul_f32 v56,v56,v52\n"
"v_mfma_f32_16x16x32_bf16 a[172:175],v[228:231],a[60:63],a[172:175]\n"
"v_mul_f32 v57,v57,v53\n"
"v_mul_f32 v58,v58,v54\n"
"s_waitcnt vmcnt(22)\n"
"v_mfma_f32_16x16x32_bf16 v[176:179],a[208:211],a[32:35],v[176:179]\n"
"buffer_load_dwordx4 v[228:231],v9,s[56:59],s61 offen offset:2048 sc1\n"
"v_mfma_f32_16x16x32_bf16 v[176:179],a[244:247],a[0:3],v[176:179]\n"
"v_mul_f32 v59,v59,v55\n"
"s_waitcnt vmcnt(22)\n"
"v_mfma_f32_16x16x32_bf16 v[192:195],a[248:251],a[0:3],v[192:195]\n"
"v_mfma_f32_16x16x32_bf16 v[192:195],a[212:215],a[32:35],v[192:195]\n"
"s_add_u32 s73,s72,s83\n"
"v_mfma_f32_16x16x32_bf16 v[180:183],a[208:211],a[36:39],v[180:183]\n"
"buffer_load_dwordx4 v9,s[44:47],s73 offen offset:2048 sc1 lds\n"
"v_mfma_f32_16x16x32_bf16 v[180:183],a[244:247],a[4:7],v[180:183]\n"
"ds_read_b128 a[32:35],v12 offset:36864\n"
"v_mfma_f32_16x16x32_bf16 v[196:199],a[248:251],a[4:7],v[196:199]\n"
"ds_read_b128 a[0:3],v13 offset:0\n"
"v_mfma_f32_16x16x32_bf16 v[196:199],a[212:215],a[36:39],v[196:199]\n"
"v_exp_f32 v56,v56\n"
"v_mfma_f32_16x16x32_bf16 v[184:187],a[208:211],a[40:43],v[184:187]\n"
"buffer_load_dwordx4 a[240:243],v9,s[56:59],s17 offen offset:2048 sc1\n"
"v_mfma_f32_16x16x32_bf16 v[184:187],a[244:247],a[8:11],v[184:187]\n"
"ds_read_b128 a[36:39],v12 offset:37888\n"
"v_mfma_f32_16x16x32_bf16 v[200:203],a[248:251],a[8:11],v[200:203]\n"
"ds_read_b128 a[4:7],v13 offset:1024\n"
"v_mfma_f32_16x16x32_bf16 v[200:203],a[212:215],a[40:43],v[200:203]\n"
"v_exp_f32 v57,v57\n"
"v_mfma_f32_16x16x32_bf16 v[188:191],a[208:211],a[44:47],v[188:191]\n"
"buffer_load_dwordx4 v9,s[44:47],s73 offen offset:3072 sc1 lds\n"
"v_mfma_f32_16x16x32_bf16 v[188:191],a[244:247],a[12:15],v[188:191]\n"
"ds_read_b128 a[40:43],v12 offset:38912\n"
"v_mfma_f32_16x16x32_bf16 v[204:207],a[248:251],a[12:15],v[204:207]\n"
"ds_read_b128 a[8:11],v13 offset:2048\n"
"v_mfma_f32_16x16x32_bf16 v[204:207],a[212:215],a[44:47],v[204:207]\n"
"v_exp_f32 v58,v58\n"
"v_mfma_f32_16x16x32_bf16 a[176:179],a[208:211],a[48:51],a[176:179]\n"
"v_exp_f32 v59,v59\n"
"v_mfma_f32_16x16x32_bf16 a[176:179],a[244:247],a[16:19],a[176:179]\n"
"ds_read_b128 a[44:47],v12 offset:39936\n"
"v_add_f32 v56,1.0,v56\n"
"v_mfma_f32_16x16x32_bf16 a[192:195],a[248:251],a[16:19],a[192:195]\n"
"ds_read_b128 a[12:15],v13 offset:3072\n"
"v_add_f32 v57,1.0,v57\n"
"v_mfma_f32_16x16x32_bf16 a[192:195],a[212:215],a[48:51],a[192:195]\n"
"v_add_f32 v58,1.0,v58\n"
"v_add_f32 v59,1.0,v59\n"
"v_mfma_f32_16x16x32_bf16 a[180:183],a[208:211],a[52:55],a[180:183]\n"
"v_rcp_f32 v56,v56\n"
"v_mfma_f32_16x16x32_bf16 a[180:183],a[244:247],a[20:23],a[180:183]\n"
"ds_read_b128 a[48:51],v12 offset:40960\n"
"v_mfma_f32_16x16x32_bf16 a[196:199],a[248:251],a[20:23],a[196:199]\n"
"ds_read_b128 a[16:19],v13 offset:4096\n"
"v_mfma_f32_16x16x32_bf16 a[196:199],a[212:215],a[52:55],a[196:199]\n"
"v_rcp_f32 v57,v57\n"
"v_mfma_f32_16x16x32_bf16 a[184:187],a[208:211],a[56:59],a[184:187]\n"
"s_mov_b32 m0,s85\n"
"v_mfma_f32_16x16x32_bf16 a[184:187],a[244:247],a[24:27],a[184:187]\n"
"ds_read_b128 a[52:55],v12 offset:41984\n"
"L_pgs320_%=:\n"
"s_cbranch_execz L_pgs384_%=\n"
"s_cmp_le_u32 s34,10\n"
"s_cselect_b32 s72,44,40\n"
"v_mfma_f32_16x16x32_bf16 a[200:203],a[248:251],a[24:27],a[200:203]\n"
"ds_read_b128 a[20:23],v13 offset:5120\n"
"s_add_u32 s72,s72,s37\n"
"v_mfma_f32_16x16x32_bf16 a[200:203],a[212:215],a[56:59],a[200:203]\n"
"s_lshl_b32 s72,s72,13\n"
"s_add_u32 s73,s72,s80\n"
"v_mfma_f32_16x16x32_bf16 a[188:191],a[208:211],a[60:63],a[188:191]\n"
"buffer_load_dwordx4 v9,s[44:47],s73 offen offset:0 sc1 lds\n"
"v_mfma_f32_16x16x32_bf16 a[188:191],a[244:247],a[28:31],a[188:191]\n"
"ds_read_b128 a[56:59],v12 offset:43008\n"
"v_mfma_f32_16x16x32_bf16 a[204:207],a[248:251],a[28:31],a[204:207]\n"
"buffer_load_dwordx4 a[208:211],v9,s[56:59],s61 offen offset:3072 sc1\n"
"ds_read_b128 a[24:27],v13 offset:6144\n"
"v_mfma_f32_16x16x32_bf16 a[204:207],a[212:215],a[60:63],a[204:207]\n"
"v_rcp_f32 v58,v58\n"
"s_waitcnt lgkmcnt(10)\n"
"s_waitcnt vmcnt(21)\n"
"v_mfma_f32_16x16x32_bf16 v[64:67],a[252:255],a[32:35],v[64:67]\n"
"v_mfma_f32_16x16x32_bf16 v[64:67],a[216:219],a[0:3],v[64:67]\n"
"s_add_u32 s61,s61,4096\n"
"ds_read_b128 a[60:63],v12 offset:44032\n"
"s_waitcnt vmcnt(20)\n"
"v_mfma_f32_16x16x32_bf16 v[80:83],a[220:223],a[0:3],v[80:83]\n"
"buffer_load_dwordx4 a[212:215],v9,s[56:59],s61 offen offset:0 sc1\n"
"ds_read_b128 a[28:31],v13 offset:7168\n"
"v_mfma_f32_16x16x32_bf16 v[80:83],v[208:211],a[32:35],v[80:83]\n"
"v_rcp_f32 v59,v59\n"
"v_mfma_f32_16x16x32_bf16 v[68:71],a[252:255],a[36:39],v[68:71]\n"
"buffer_load_dwordx4 v9,s[44:47],s73 offen offset:1024 sc1 lds\n"
"v_mfma_f32_16x16x32_bf16 v[68:71],a[216:219],a[4:7],v[68:71]\n"
"v_mul_f32 v52,v52,v56\n"
"v_mul_f32 v53,v53,v57\n"
"v_mfma_f32_16x16x32_bf16 v[84:87],a[220:223],a[4:7],v[84:87]\n"
"buffer_load_dwordx4 a[244:247],v9,s[56:59],s17 offen offset:3072 sc1\n"
"v_mul_f32 v54,v54,v58\n"
"v_mfma_f32_16x16x32_bf16 v[84:87],v[208:211],a[36:39],v[84:87]\n"
"v_mul_f32 v55,v55,v59\n"
"v_cvt_pk_bf16_f32 v40,v52,v53\n"
"s_waitcnt lgkmcnt(8)\n"
"v_mfma_f32_16x16x32_bf16 v[72:75],a[252:255],a[40:43],v[72:75]\n"
"v_cvt_pk_bf16_f32 v41,v54,v55\n"
"v_mfma_f32_16x16x32_bf16 v[72:75],a[216:219],a[8:11],v[72:75]\n"
"s_add_u32 s17,s17,4096\n"
"v_lshlrev_b32 v52,16,v44\n"
"v_mfma_f32_16x16x32_bf16 v[88:91],a[220:223],a[8:11],v[88:91]\n"
"buffer_load_dwordx4 a[248:251],v9,s[56:59],s17 offen offset:0 sc1\n"
"v_and_b32 v53,s62,v44\n"
"v_mfma_f32_16x16x32_bf16 v[88:91],v[208:211],a[40:43],v[88:91]\n"
"s_add_u32 s73,s72,s81\n"
"v_lshlrev_b32 v54,16,v45\n"
"v_mfma_f32_16x16x32_bf16 v[76:79],a[252:255],a[44:47],v[76:79]\n"
"buffer_load_dwordx4 v9,s[44:47],s73 offen offset:2048 sc1 lds\n"
"v_mfma_f32_16x16x32_bf16 v[76:79],a[216:219],a[12:15],v[76:79]\n"
"v_and_b32 v55,s62,v45\n"
"v_mul_f32 v56,v52,v52\n"
"v_mfma_f32_16x16x32_bf16 v[92:95],a[220:223],a[12:15],v[92:95]\n"
"v_mul_f32 v57,v53,v53\n"
"v_mul_f32 v58,v54,v54\n"
"v_mfma_f32_16x16x32_bf16 v[92:95],v[208:211],a[44:47],v[92:95]\n"
"v_mul_f32 v59,v55,v55\n"
"v_fmaak_f32 v56,v237,v56,0xc0135761\n"
"s_waitcnt lgkmcnt(4)\n"
"v_mfma_f32_16x16x32_bf16 a[64:67],a[252:255],a[48:51],a[64:67]\n"
"v_fmaak_f32 v57,v237,v57,0xc0135761\n"
"v_mfma_f32_16x16x32_bf16 a[64:67],a[216:219],a[16:19],a[64:67]\n"
"v_fmaak_f32 v58,v237,v58,0xc0135761\n"
"v_fmaak_f32 v59,v237,v59,0xc0135761\n"
"v_mfma_f32_16x16x32_bf16 a[80:83],a[220:223],a[16:19],a[80:83]\n"
"v_mul_f32 v56,v56,v52\n"
"v_mul_f32 v57,v57,v53\n"
"v_mfma_f32_16x16x32_bf16 a[80:83],v[208:211],a[48:51],a[80:83]\n"
"v_mul_f32 v58,v58,v54\n"
"v_mul_f32 v59,v59,v55\n"
"v_mfma_f32_16x16x32_bf16 a[68:71],a[252:255],a[52:55],a[68:71]\n"
"v_exp_f32 v56,v56\n"
"v_mfma_f32_16x16x32_bf16 a[68:71],a[216:219],a[20:23],a[68:71]\n"
"v_exp_f32 v57,v57\n"
"v_mfma_f32_16x16x32_bf16 a[84:87],a[220:223],a[20:23],a[84:87]\n"
"v_exp_f32 v58,v58\n"
"v_mfma_f32_16x16x32_bf16 a[84:87],v[208:211],a[52:55],a[84:87]\n"
"v_exp_f32 v59,v59\n"
"s_waitcnt lgkmcnt(0)\n"
"v_mfma_f32_16x16x32_bf16 a[72:75],a[252:255],a[56:59],a[72:75]\n"
"v_add_f32 v56,1.0,v56\n"
"v_mfma_f32_16x16x32_bf16 a[72:75],a[216:219],a[24:27],a[72:75]\n"
"v_add_f32 v57,1.0,v57\n"
"v_add_f32 v58,1.0,v58\n"
"v_mfma_f32_16x16x32_bf16 a[88:91],a[220:223],a[24:27],a[88:91]\n"
"v_add_f32 v59,1.0,v59\n"
"v_mfma_f32_16x16x32_bf16 a[88:91],v[208:211],a[56:59],a[88:91]\n"
"v_rcp_f32 v56,v56\n"
"v_mfma_f32_16x16x32_bf16 a[76:79],a[252:255],a[60:63],a[76:79]\n"
"s_cmp_le_u32 s34,8\n"
"s_cselect_b32 s61,36,32\n"
"buffer_load_dwordx4 v9,s[44:47],s73 offen offset:3072 sc1 lds\n"
"v_mfma_f32_16x16x32_bf16 a[76:79],a[216:219],a[28:31],a[76:79]\n"
"s_add_u32 s61,s61,s37\n"
"s_mul_i32 s61,s61,9216\n"
"v_mfma_f32_16x16x32_bf16 a[92:95],a[220:223],a[28:31],a[92:95]\n"
"buffer_load_dwordx4 a[252:255],v9,s[56:59],s61 offen offset:0 sc1\n"
"v_mfma_f32_16x16x32_bf16 a[92:95],v[208:211],a[60:63],a[92:95]\n"
"v_rcp_f32 v57,v57\n"
"s_waitcnt vmcnt(24)\n"
"v_mfma_f32_16x16x32_bf16 v[96:99],v[212:215],a[32:35],v[96:99]\n"
"v_mfma_f32_16x16x32_bf16 v[96:99],a[224:227],a[0:3],v[96:99]\n"
"v_rcp_f32 v58,v58\n"
"v_mfma_f32_16x16x32_bf16 v[100:103],a[224:227],a[4:7],v[100:103]\n"
"buffer_load_dwordx4 v[208:211],v9,s[56:59],s61 offen offset:1024 sc1\n"
"v_mfma_f32_16x16x32_bf16 v[100:103],v[212:215],a[36:39],v[100:103]\n"
"s_mov_b32 m0,s31\n"
"s_add_u32 s73,s72,s82\n"
"v_mfma_f32_16x16x32_bf16 v[104:107],v[212:215],a[40:43],v[104:107]\n"
"s_cmp_le_u32 s34,9\n"
"s_cselect_b32 s17,40,36\n"
"buffer_load_dwordx4 v9,s[44:47],s73 offen offset:0 sc1 lds\n"
"v_mfma_f32_16x16x32_bf16 v[104:107],a[224:227],a[8:11],v[104:107]\n"
"s_add_u32 s17,s17,s37\n"
"s_mul_i32 s17,s17,9216\n"
"v_mfma_f32_16x16x32_bf16 v[108:111],a[224:227],a[12:15],v[108:111]\n"
"buffer_load_dwordx4 a[216:219],v9,s[56:59],s17 offen offset:0 sc1\n"
"v_mfma_f32_16x16x32_bf16 v[108:111],v[212:215],a[44:47],v[108:111]\n"
"v_rcp_f32 v59,v59\n"
"v_mfma_f32_16x16x32_bf16 a[96:99],v[212:215],a[48:51],a[96:99]\n"
"v_mul_f32 v52,v52,v56\n"
"v_mul_f32 v53,v53,v57\n"
"v_mfma_f32_16x16x32_bf16 a[96:99],a[224:227],a[16:19],a[96:99]\n"
"v_mul_f32 v54,v54,v58\n"
"v_mul_f32 v55,v55,v59\n"
"v_mfma_f32_16x16x32_bf16 a[100:103],a[224:227],a[20:23],a[100:103]\n"
"buffer_load_dwordx4 a[220:223],v9,s[56:59],s17 offen offset:1024 sc1\n"
"v_cvt_pk_bf16_f32 v44,v52,v53\n"
"v_mfma_f32_16x16x32_bf16 a[100:103],v[212:215],a[52:55],a[100:103]\n"
"v_cvt_pk_bf16_f32 v45,v54,v55\n"
"v_lshlrev_b32 v52,16,v42\n"
"v_mfma_f32_16x16x32_bf16 a[104:107],v[212:215],a[56:59],a[104:107]\n"
"buffer_load_dwordx4 v9,s[44:47],s73 offen offset:1024 sc1 lds\n"
"v_mfma_f32_16x16x32_bf16 a[104:107],a[224:227],a[24:27],a[104:107]\n"
"v_and_b32 v53,s62,v42\n"
"v_lshlrev_b32 v54,16,v43\n"
"v_mfma_f32_16x16x32_bf16 a[108:111],a[224:227],a[28:31],a[108:111]\n"
"v_and_b32 v55,s62,v43\n"
"v_mul_f32 v56,v52,v52\n"
"v_mfma_f32_16x16x32_bf16 a[108:111],v[212:215],a[60:63],a[108:111]\n"
"v_mul_f32 v57,v53,v53\n"
"v_mul_f32 v58,v54,v54\n"
"s_waitcnt vmcnt(25)\n"
"v_mfma_f32_16x16x32_bf16 v[112:115],v[216:219],a[32:35],v[112:115]\n"
"buffer_load_dwordx4 v[212:215],v9,s[56:59],s61 offen offset:2048 sc1\n"
"v_mfma_f32_16x16x32_bf16 v[112:115],a[228:231],a[0:3],v[112:115]\n"
"v_mul_f32 v59,v55,v55\n"
"v_fmaak_f32 v56,v237,v56,0xc0135761\n"
"v_mfma_f32_16x16x32_bf16 v[116:119],a[228:231],a[4:7],v[116:119]\n"
"v_fmaak_f32 v57,v237,v57,0xc0135761\n"
"v_fmaak_f32 v58,v237,v58,0xc0135761\n"
"v_mfma_f32_16x16x32_bf16 v[116:119],v[216:219],a[36:39],v[116:119]\n"
"v_fmaak_f32 v59,v237,v59,0xc0135761\n"
"v_mul_f32 v56,v56,v52\n"
"v_mfma_f32_16x16x32_bf16 v[120:123],v[216:219],a[40:43],v[120:123]\n"
"v_mul_f32 v57,v57,v53\n"
"v_mul_f32 v58,v58,v54\n"
"v_mfma_f32_16x16x32_bf16 v[120:123],a[228:231],a[8:11],v[120:123]\n"
"v_mul_f32 v59,v59,v55\n"
"v_mfma_f32_16x16x32_bf16 v[124:127],a[228:231],a[12:15],v[124:127]\n"
"v_exp_f32 v56,v56\n"
"v_mfma_f32_16x16x32_bf16 v[124:127],v[216:219],a[44:47],v[124:127]\n"
"v_exp_f32 v57,v57\n"
"v_mfma_f32_16x16x32_bf16 a[112:115],v[216:219],a[48:51],a[112:115]\n"
"buffer_load_dwordx4 a[224:227],v9,s[56:59],s17 offen offset:2048 sc1\n"
"v_mfma_f32_16x16x32_bf16 a[112:115],a[228:231],a[16:19],a[112:115]\n"
"v_exp_f32 v58,v58\n"
"v_mfma_f32_16x16x32_bf16 a[116:119],a[228:231],a[20:23],a[116:119]\n"
"v_exp_f32 v59,v59\n"
"v_mfma_f32_16x16x32_bf16 a[116:119],v[216:219],a[52:55],a[116:119]\n"
"s_add_u32 s73,s72,s83\n"
"v_add_f32 v56,1.0,v56\n"
"v_mfma_f32_16x16x32_bf16 a[120:123],v[216:219],a[56:59],a[120:123]\n"
"buffer_load_dwordx4 v9,s[44:47],s73 offen offset:2048 sc1 lds\n"
"v_mfma_f32_16x16x32_bf16 a[120:123],a[228:231],a[24:27],a[120:123]\n"
"v_add_f32 v57,1.0,v57\n"
"v_add_f32 v58,1.0,v58\n"
"v_mfma_f32_16x16x32_bf16 a[124:127],a[228:231],a[28:31],a[124:127]\n"
"v_add_f32 v59,1.0,v59\n"
"v_mfma_f32_16x16x32_bf16 a[124:127],v[216:219],a[60:63],a[124:127]\n"
"v_rcp_f32 v56,v56\n"
"s_waitcnt vmcnt(25)\n"
"v_mfma_f32_16x16x32_bf16 v[128:131],v[220:223],a[32:35],v[128:131]\n"
"buffer_load_dwordx4 v[216:219],v9,s[56:59],s61 offen offset:3072 sc1\n"
"v_mfma_f32_16x16x32_bf16 v[128:131],a[232:235],a[0:3],v[128:131]\n"
"v_rcp_f32 v57,v57\n"
"v_mfma_f32_16x16x32_bf16 v[132:135],a[232:235],a[4:7],v[132:135]\n"
"v_rcp_f32 v58,v58\n"
"v_mfma_f32_16x16x32_bf16 v[132:135],v[220:223],a[36:39],v[132:135]\n"
"v_rcp_f32 v59,v59\n"
"v_mfma_f32_16x16x32_bf16 v[136:139],v[220:223],a[40:43],v[136:139]\n"
"buffer_load_dwordx4 v9,s[44:47],s73 offen offset:3072 sc1 lds\n"
"v_mfma_f32_16x16x32_bf16 v[136:139],a[232:235],a[8:11],v[136:139]\n"
"v_mul_f32 v52,v52,v56\n"
"v_mul_f32 v53,v53,v57\n"
"v_mfma_f32_16x16x32_bf16 v[140:143],a[232:235],a[12:15],v[140:143]\n"
"v_mul_f32 v54,v54,v58\n"
"v_mul_f32 v55,v55,v59\n"
"v_mfma_f32_16x16x32_bf16 v[140:143],v[220:223],a[44:47],v[140:143]\n"
"v_cvt_pk_bf16_f32 v42,v52,v53\n"
"v_cvt_pk_bf16_f32 v43,v54,v55\n"
"v_mfma_f32_16x16x32_bf16 a[128:131],v[220:223],a[48:51],a[128:131]\n"
"buffer_load_dwordx4 a[228:231],v9,s[56:59],s17 offen offset:3072 sc1\n"
"s_add_u32 m0,s84,36864\n"
"v_mfma_f32_16x16x32_bf16 a[128:131],a[232:235],a[16:19],a[128:131]\n"
"s_cmp_le_u32 s34,11\n"
"s_cselect_b32 s72,48,44\n"
"v_mfma_f32_16x16x32_bf16 a[132:135],a[232:235],a[20:23],a[132:135]\n"
"s_add_u32 s72,s72,s37\n"
"v_lshlrev_b32 v52,16,v46\n"
"v_mfma_f32_16x16x32_bf16 a[132:135],v[220:223],a[52:55],a[132:135]\n"
"s_lshl_b32 s72,s72,13\n"
"s_add_u32 s73,s72,s80\n"
"v_mfma_f32_16x16x32_bf16 a[136:139],v[220:223],a[56:59],a[136:139]\n"
"buffer_load_dwordx4 v9,s[44:47],s73 offen offset:0 sc1 lds\n"
"v_mfma_f32_16x16x32_bf16 a[136:139],a[232:235],a[24:27],a[136:139]\n"
"v_and_b32 v53,s62,v46\n"
"v_lshlrev_b32 v54,16,v47\n"
"v_mfma_f32_16x16x32_bf16 a[140:143],a[232:235],a[28:31],a[140:143]\n"
"v_and_b32 v55,s62,v47\n"
"v_mul_f32 v56,v52,v52\n"
"v_mfma_f32_16x16x32_bf16 a[140:143],v[220:223],a[60:63],a[140:143]\n"
"s_add_u32 s61,s61,4096\n"
"v_mul_f32 v57,v53,v53\n"
"s_waitcnt vmcnt(25)\n"
"v_mfma_f32_16x16x32_bf16 v[144:147],v[224:227],a[32:35],v[144:147]\n"
"buffer_load_dwordx4 v[220:223],v9,s[56:59],s61 offen offset:0 sc1\n"
"v_mfma_f32_16x16x32_bf16 v[144:147],a[236:239],a[0:3],v[144:147]\n"
"v_mul_f32 v58,v54,v54\n"
"v_mul_f32 v59,v55,v55\n"
"v_mfma_f32_16x16x32_bf16 v[148:151],a[236:239],a[4:7],v[148:151]\n"
"v_fmaak_f32 v56,v237,v56,0xc0135761\n"
"v_fmaak_f32 v57,v237,v57,0xc0135761\n"
"v_mfma_f32_16x16x32_bf16 v[148:151],v[224:227],a[36:39],v[148:151]\n"
"v_fmaak_f32 v58,v237,v58,0xc0135761\n"
"v_fmaak_f32 v59,v237,v59,0xc0135761\n"
"v_mfma_f32_16x16x32_bf16 v[152:155],v[224:227],a[40:43],v[152:155]\n"
"buffer_load_dwordx4 v9,s[44:47],s73 offen offset:1024 sc1 lds\n"
"v_mfma_f32_16x16x32_bf16 v[152:155],a[236:239],a[8:11],v[152:155]\n"
"v_mul_f32 v56,v56,v52\n"
"v_mul_f32 v57,v57,v53\n"
"v_mfma_f32_16x16x32_bf16 v[156:159],a[236:239],a[12:15],v[156:159]\n"
"v_mul_f32 v58,v58,v54\n"
"v_mul_f32 v59,v59,v55\n"
"v_mfma_f32_16x16x32_bf16 v[156:159],v[224:227],a[44:47],v[156:159]\n"
"s_add_u32 s17,s17,4096\n"
"v_mfma_f32_16x16x32_bf16 a[144:147],v[224:227],a[48:51],a[144:147]\n"
"buffer_load_dwordx4 a[232:235],v9,s[56:59],s17 offen offset:0 sc1\n"
"v_mfma_f32_16x16x32_bf16 a[144:147],a[236:239],a[16:19],a[144:147]\n"
"v_exp_f32 v56,v56\n"
"v_mfma_f32_16x16x32_bf16 a[148:151],a[236:239],a[20:23],a[148:151]\n"
"v_exp_f32 v57,v57\n"
"v_mfma_f32_16x16x32_bf16 a[148:151],v[224:227],a[52:55],a[148:151]\n"
"s_add_u32 s73,s72,s81\n"
"v_mfma_f32_16x16x32_bf16 a[152:155],v[224:227],a[56:59],a[152:155]\n"
"buffer_load_dwordx4 v9,s[44:47],s73 offen offset:2048 sc1 lds\n"
"v_mfma_f32_16x16x32_bf16 a[152:155],a[236:239],a[24:27],a[152:155]\n"
"v_exp_f32 v58,v58\n"
"v_mfma_f32_16x16x32_bf16 a[156:159],a[236:239],a[28:31],a[156:159]\n"
"v_exp_f32 v59,v59\n"
"v_mfma_f32_16x16x32_bf16 a[156:159],v[224:227],a[60:63],a[156:159]\n"
"v_add_f32 v56,1.0,v56\n"
"v_add_f32 v57,1.0,v57\n"
"s_waitcnt vmcnt(26)\n"
"v_mfma_f32_16x16x32_bf16 v[160:163],v[228:231],a[32:35],v[160:163]\n"
"buffer_load_dwordx4 v[224:227],v9,s[56:59],s61 offen offset:1024 sc1\n"
"v_mfma_f32_16x16x32_bf16 v[160:163],a[240:243],a[0:3],v[160:163]\n"
"v_add_f32 v58,1.0,v58\n"
"v_add_f32 v59,1.0,v59\n"
"v_mfma_f32_16x16x32_bf16 v[164:167],a[240:243],a[4:7],v[164:167]\n"
"v_rcp_f32 v56,v56\n"
"v_mfma_f32_16x16x32_bf16 v[164:167],v[228:231],a[36:39],v[164:167]\n"
"v_rcp_f32 v57,v57\n"
"v_mfma_f32_16x16x32_bf16 v[168:171],v[228:231],a[40:43],v[168:171]\n"
"buffer_load_dwordx4 v9,s[44:47],s73 offen offset:3072 sc1 lds\n"
"v_mfma_f32_16x16x32_bf16 v[168:171],a[240:243],a[8:11],v[168:171]\n"
"v_rcp_f32 v58,v58\n"
"v_mfma_f32_16x16x32_bf16 v[172:175],a[240:243],a[12:15],v[172:175]\n"
"v_rcp_f32 v59,v59\n"
"v_mfma_f32_16x16x32_bf16 v[172:175],v[228:231],a[44:47],v[172:175]\n"
"v_mul_f32 v52,v52,v56\n"
"v_mul_f32 v53,v53,v57\n"
"v_mfma_f32_16x16x32_bf16 a[160:163],v[228:231],a[48:51],a[160:163]\n"
"buffer_load_dwordx4 a[236:239],v9,s[56:59],s17 offen offset:1024 sc1\n"
"v_mul_f32 v54,v54,v58\n"
"v_mfma_f32_16x16x32_bf16 a[160:163],a[240:243],a[16:19],a[160:163]\n"
"v_mul_f32 v55,v55,v59\n"
"v_cvt_pk_bf16_f32 v46,v52,v53\n"
"v_mfma_f32_16x16x32_bf16 a[164:167],a[240:243],a[20:23],a[164:167]\n"
"v_cvt_pk_bf16_f32 v47,v54,v55\n"
"v_lshlrev_b32 v52,16,v48\n"
"v_mfma_f32_16x16x32_bf16 a[164:167],v[228:231],a[52:55],a[164:167]\n"
"s_add_u32 m0,s84,40960\n"
"s_add_u32 s73,s72,s82\n"
"v_mfma_f32_16x16x32_bf16 a[168:171],v[228:231],a[56:59],a[168:171]\n"
"buffer_load_dwordx4 v9,s[44:47],s73 offen offset:0 sc1 lds\n"
"v_mfma_f32_16x16x32_bf16 a[168:171],a[240:243],a[24:27],a[168:171]\n"
"v_and_b32 v53,s62,v48\n"
"v_lshlrev_b32 v54,16,v49\n"
"v_mfma_f32_16x16x32_bf16 a[172:175],a[240:243],a[28:31],a[172:175]\n"
"v_and_b32 v55,s62,v49\n"
"v_mul_f32 v56,v52,v52\n"
"v_mfma_f32_16x16x32_bf16 a[172:175],v[228:231],a[60:63],a[172:175]\n"
"v_mul_f32 v57,v53,v53\n"
"v_mul_f32 v58,v54,v54\n"
"s_waitcnt vmcnt(24)\n"
"v_mfma_f32_16x16x32_bf16 v[176:179],a[208:211],a[32:35],v[176:179]\n"
"buffer_load_dwordx4 v[228:231],v9,s[56:59],s61 offen offset:2048 sc1\n"
"v_mfma_f32_16x16x32_bf16 v[176:179],a[244:247],a[0:3],v[176:179]\n"
"v_mul_f32 v59,v55,v55\n"
"v_fmaak_f32 v56,v237,v56,0xc0135761\n"
"s_waitcnt vmcnt(24)\n"
"v_mfma_f32_16x16x32_bf16 v[192:195],a[248:251],a[0:3],v[192:195]\n"
"v_fmaak_f32 v57,v237,v57,0xc0135761\n"
"v_mfma_f32_16x16x32_bf16 v[192:195],a[212:215],a[32:35],v[192:195]\n"
"v_fmaak_f32 v58,v237,v58,0xc0135761\n"
"v_fmaak_f32 v59,v237,v59,0xc0135761\n"
"v_mfma_f32_16x16x32_bf16 v[180:183],a[208:211],a[36:39],v[180:183]\n"
"buffer_load_dwordx4 v9,s[44:47],s73 offen offset:1024 sc1 lds\n"
"v_mfma_f32_16x16x32_bf16 v[180:183],a[244:247],a[4:7],v[180:183]\n"
"ds_read_b128 a[32:35],v13 offset:36864\n"
"v_mfma_f32_16x16x32_bf16 v[196:199],a[248:251],a[4:7],v[196:199]\n"
"ds_read_b128 a[0:3],v12 offset:0\n"
"v_mfma_f32_16x16x32_bf16 v[196:199],a[212:215],a[36:39],v[196:199]\n"
"v_mul_f32 v56,v56,v52\n"
"v_mul_f32 v57,v57,v53\n"
"v_mfma_f32_16x16x32_bf16 v[184:187],a[208:211],a[40:43],v[184:187]\n"
"buffer_load_dwordx4 a[240:243],v9,s[56:59],s17 offen offset:2048 sc1\n"
"v_mul_f32 v58,v58,v54\n"
"v_mfma_f32_16x16x32_bf16 v[184:187],a[244:247],a[8:11],v[184:187]\n"
"ds_read_b128 a[36:39],v13 offset:37888\n"
"v_mul_f32 v59,v59,v55\n"
"v_mfma_f32_16x16x32_bf16 v[200:203],a[248:251],a[8:11],v[200:203]\n"
"ds_read_b128 a[4:7],v12 offset:1024\n"
"v_mfma_f32_16x16x32_bf16 v[200:203],a[212:215],a[40:43],v[200:203]\n"
"s_add_u32 s73,s72,s83\n"
"v_mfma_f32_16x16x32_bf16 v[188:191],a[208:211],a[44:47],v[188:191]\n"
"buffer_load_dwordx4 v9,s[44:47],s73 offen offset:2048 sc1 lds\n"
"v_mfma_f32_16x16x32_bf16 v[188:191],a[244:247],a[12:15],v[188:191]\n"
"ds_read_b128 a[40:43],v13 offset:38912\n"
"v_mfma_f32_16x16x32_bf16 v[204:207],a[248:251],a[12:15],v[204:207]\n"
"ds_read_b128 a[8:11],v12 offset:2048\n"
"v_mfma_f32_16x16x32_bf16 v[204:207],a[212:215],a[44:47],v[204:207]\n"
"v_exp_f32 v56,v56\n"
"v_mfma_f32_16x16x32_bf16 a[176:179],a[208:211],a[48:51],a[176:179]\n"
"v_exp_f32 v57,v57\n"
"v_mfma_f32_16x16x32_bf16 a[176:179],a[244:247],a[16:19],a[176:179]\n"
"ds_read_b128 a[44:47],v13 offset:39936\n"
"v_mfma_f32_16x16x32_bf16 a[192:195],a[248:251],a[16:19],a[192:195]\n"
"ds_read_b128 a[12:15],v12 offset:3072\n"
"v_mfma_f32_16x16x32_bf16 a[192:195],a[212:215],a[48:51],a[192:195]\n"
"v_exp_f32 v58,v58\n"
"v_mfma_f32_16x16x32_bf16 a[180:183],a[208:211],a[52:55],a[180:183]\n"
"buffer_load_dwordx4 v9,s[44:47],s73 offen offset:3072 sc1 lds\n"
"v_mfma_f32_16x16x32_bf16 a[180:183],a[244:247],a[20:23],a[180:183]\n"
"ds_read_b128 a[48:51],v13 offset:40960\n"
"v_mfma_f32_16x16x32_bf16 a[196:199],a[248:251],a[20:23],a[196:199]\n"
"ds_read_b128 a[16:19],v12 offset:4096\n"
"v_mfma_f32_16x16x32_bf16 a[196:199],a[212:215],a[52:55],a[196:199]\n"
"v_exp_f32 v59,v59\n"
"v_mfma_f32_16x16x32_bf16 a[184:187],a[208:211],a[56:59],a[184:187]\n"
"v_add_f32 v56,1.0,v56\n"
"v_add_f32 v57,1.0,v57\n"
"v_mfma_f32_16x16x32_bf16 a[184:187],a[244:247],a[24:27],a[184:187]\n"
"ds_read_b128 a[52:55],v13 offset:41984\n"
"v_add_f32 v58,1.0,v58\n"
"v_mfma_f32_16x16x32_bf16 a[200:203],a[248:251],a[24:27],a[200:203]\n"
"ds_read_b128 a[20:23],v12 offset:5120\n"
"v_add_f32 v59,1.0,v59\n"
"v_mfma_f32_16x16x32_bf16 a[200:203],a[212:215],a[56:59],a[200:203]\n"
"v_rcp_f32 v56,v56\n"
"v_mfma_f32_16x16x32_bf16 a[188:191],a[208:211],a[60:63],a[188:191]\n"
"v_rcp_f32 v57,v57\n"
"v_mfma_f32_16x16x32_bf16 a[188:191],a[244:247],a[28:31],a[188:191]\n"
"ds_read_b128 a[56:59],v13 offset:43008\n"
"v_mfma_f32_16x16x32_bf16 a[204:207],a[248:251],a[28:31],a[204:207]\n"
"buffer_load_dwordx4 a[208:211],v9,s[56:59],s61 offen offset:3072 sc1\n"
"ds_read_b128 a[24:27],v12 offset:6144\n"
"v_mfma_f32_16x16x32_bf16 a[204:207],a[212:215],a[60:63],a[204:207]\n"
"v_rcp_f32 v58,v58\n"
"s_waitcnt lgkmcnt(10)\n"
"s_waitcnt vmcnt(23)\n"
"v_mfma_f32_16x16x32_bf16 v[64:67],a[252:255],a[32:35],v[64:67]\n"
"v_mfma_f32_16x16x32_bf16 v[64:67],a[216:219],a[0:3],v[64:67]\n"
"s_add_u32 s61,s61,4096\n"
"ds_read_b128 a[60:63],v13 offset:44032\n"
"s_waitcnt vmcnt(22)\n"
"v_mfma_f32_16x16x32_bf16 v[80:83],a[220:223],a[0:3],v[80:83]\n"
"buffer_load_dwordx4 a[212:215],v9,s[56:59],s61 offen offset:0 sc1\n"
"ds_read_b128 a[28:31],v12 offset:7168\n"
"v_mfma_f32_16x16x32_bf16 v[80:83],v[208:211],a[32:35],v[80:83]\n"
"v_rcp_f32 v59,v59\n"
"v_mfma_f32_16x16x32_bf16 v[68:71],a[252:255],a[36:39],v[68:71]\n"
"v_mul_f32 v52,v52,v56\n"
"v_mul_f32 v53,v53,v57\n"
"v_mfma_f32_16x16x32_bf16 v[68:71],a[216:219],a[4:7],v[68:71]\n"
"v_mul_f32 v54,v54,v58\n"
"v_mul_f32 v55,v55,v59\n"
"v_mfma_f32_16x16x32_bf16 v[84:87],a[220:223],a[4:7],v[84:87]\n"
"buffer_load_dwordx4 a[244:247],v9,s[56:59],s17 offen offset:3072 sc1\n"
"v_cvt_pk_bf16_f32 v48,v52,v53\n"
"v_mfma_f32_16x16x32_bf16 v[84:87],v[208:211],a[36:39],v[84:87]\n"
"v_cvt_pk_bf16_f32 v49,v54,v55\n"
"v_lshlrev_b32 v52,16,v50\n"
"s_waitcnt lgkmcnt(8)\n"
"v_mfma_f32_16x16x32_bf16 v[72:75],a[252:255],a[40:43],v[72:75]\n"
"v_and_b32 v53,s62,v50\n"
"v_mfma_f32_16x16x32_bf16 v[72:75],a[216:219],a[8:11],v[72:75]\n"
"s_add_u32 s17,s17,4096\n"
"v_lshlrev_b32 v54,16,v51\n"
"v_mfma_f32_16x16x32_bf16 v[88:91],a[220:223],a[8:11],v[88:91]\n"
"buffer_load_dwordx4 a[248:251],v9,s[56:59],s17 offen offset:0 sc1\n"
"v_and_b32 v55,s62,v51\n"
"v_mfma_f32_16x16x32_bf16 v[88:91],v[208:211],a[40:43],v[88:91]\n"
"v_mul_f32 v56,v52,v52\n"
"v_mul_f32 v57,v53,v53\n"
"v_mfma_f32_16x16x32_bf16 v[76:79],a[252:255],a[44:47],v[76:79]\n"
"v_mul_f32 v58,v54,v54\n"
"v_mul_f32 v59,v55,v55\n"
"v_mfma_f32_16x16x32_bf16 v[76:79],a[216:219],a[12:15],v[76:79]\n"
"v_fmaak_f32 v56,v237,v56,0xc0135761\n"
"v_fmaak_f32 v57,v237,v57,0xc0135761\n"
"v_mfma_f32_16x16x32_bf16 v[92:95],a[220:223],a[12:15],v[92:95]\n"
"v_fmaak_f32 v58,v237,v58,0xc0135761\n"
"v_fmaak_f32 v59,v237,v59,0xc0135761\n"
"v_mfma_f32_16x16x32_bf16 v[92:95],v[208:211],a[44:47],v[92:95]\n"
"v_mul_f32 v56,v56,v52\n"
"v_mul_f32 v57,v57,v53\n"
"s_waitcnt lgkmcnt(4)\n"
"v_mfma_f32_16x16x32_bf16 a[64:67],a[252:255],a[48:51],a[64:67]\n"
"v_mul_f32 v58,v58,v54\n"
"v_mfma_f32_16x16x32_bf16 a[64:67],a[216:219],a[16:19],a[64:67]\n"
"v_mul_f32 v59,v59,v55\n"
"v_mfma_f32_16x16x32_bf16 a[80:83],a[220:223],a[16:19],a[80:83]\n"
"v_exp_f32 v56,v56\n"
"v_mfma_f32_16x16x32_bf16 a[80:83],v[208:211],a[48:51],a[80:83]\n"
"v_exp_f32 v57,v57\n"
"v_mfma_f32_16x16x32_bf16 a[68:71],a[252:255],a[52:55],a[68:71]\n"
"v_exp_f32 v58,v58\n"
"v_mfma_f32_16x16x32_bf16 a[68:71],a[216:219],a[20:23],a[68:71]\n"
"v_exp_f32 v59,v59\n"
"v_mfma_f32_16x16x32_bf16 a[84:87],a[220:223],a[20:23],a[84:87]\n"
"v_add_f32 v56,1.0,v56\n"
"v_add_f32 v57,1.0,v57\n"
"v_mfma_f32_16x16x32_bf16 a[84:87],v[208:211],a[52:55],a[84:87]\n"
"v_add_f32 v58,1.0,v58\n"
"v_add_f32 v59,1.0,v59\n"
"s_waitcnt lgkmcnt(0)\n"
"v_mfma_f32_16x16x32_bf16 a[72:75],a[252:255],a[56:59],a[72:75]\n"
"s_add_u32 m0,s84,73728\n"
"v_mfma_f32_16x16x32_bf16 a[72:75],a[216:219],a[24:27],a[72:75]\n"
"s_cmp_le_u32 s34,12\n"
"s_cselect_b32 s72,52,48\n"
"v_mfma_f32_16x16x32_bf16 a[88:91],a[220:223],a[24:27],a[88:91]\n"
"s_add_u32 s72,s72,s37\n"
"v_mfma_f32_16x16x32_bf16 a[88:91],v[208:211],a[56:59],a[88:91]\n"
"s_lshl_b32 s72,s72,13\n"
"s_add_u32 s73,s72,s80\n"
"v_mfma_f32_16x16x32_bf16 a[76:79],a[252:255],a[60:63],a[76:79]\n"
"s_cmp_le_u32 s34,10\n"
"s_cselect_b32 s61,44,40\n"
"buffer_load_dwordx4 v9,s[44:47],s73 offen offset:0 sc1 lds\n"
"v_mfma_f32_16x16x32_bf16 a[76:79],a[216:219],a[28:31],a[76:79]\n"
"s_add_u32 s61,s61,s37\n"
"s_mul_i32 s61,s61,9216\n"
"v_mfma_f32_16x16x32_bf16 a[92:95],a[220:223],a[28:31],a[92:95]\n"
"buffer_load_dwordx4 a[252:255],v9,s[56:59],s61 offen offset:0 sc1\n"
"v_mfma_f32_16x16x32_bf16 a[92:95],v[208:211],a[60:63],a[92:95]\n"
"v_rcp_f32 v56,v56\n"
"s_waitcnt vmcnt(24)\n"
"v_mfma_f32_16x16x32_bf16 v[96:99],v[212:215],a[32:35],v[96:99]\n"
"v_mfma_f32_16x16x32_bf16 v[96:99],a[224:227],a[0:3],v[96:99]\n"
"v_rcp_f32 v57,v57\n"
"v_mfma_f32_16x16x32_bf16 v[100:103],a[224:227],a[4:7],v[100:103]\n"
"buffer_load_dwordx4 v[208:211],v9,s[56:59],s61 offen offset:1024 sc1\n"
"v_mfma_f32_16x16x32_bf16 v[100:103],v[212:215],a[36:39],v[100:103]\n"
"v_rcp_f32 v58,v58\n"
"v_mfma_f32_16x16x32_bf16 v[104:107],v[212:215],a[40:43],v[104:107]\n"
"s_cmp_le_u32 s34,11\n"
"s_cselect_b32 s17,48,44\n"
"buffer_load_dwordx4 v9,s[44:47],s73 offen offset:1024 sc1 lds\n"
"v_mfma_f32_16x16x32_bf16 v[104:107],a[224:227],a[8:11],v[104:107]\n"
"s_add_u32 s17,s17,s37\n"
"s_mul_i32 s17,s17,9216\n"
"v_mfma_f32_16x16x32_bf16 v[108:111],a[224:227],a[12:15],v[108:111]\n"
"buffer_load_dwordx4 a[216:219],v9,s[56:59],s17 offen offset:0 sc1\n"
"v_mfma_f32_16x16x32_bf16 v[108:111],v[212:215],a[44:47],v[108:111]\n"
"v_rcp_f32 v59,v59\n"
"v_mfma_f32_16x16x32_bf16 a[96:99],v[212:215],a[48:51],a[96:99]\n"
"v_mul_f32 v52,v52,v56\n"
"v_mul_f32 v53,v53,v57\n"
"v_mfma_f32_16x16x32_bf16 a[96:99],a[224:227],a[16:19],a[96:99]\n"
"v_mul_f32 v54,v54,v58\n"
"v_mul_f32 v55,v55,v59\n"
"v_mfma_f32_16x16x32_bf16 a[100:103],a[224:227],a[20:23],a[100:103]\n"
"buffer_load_dwordx4 a[220:223],v9,s[56:59],s17 offen offset:1024 sc1\n"
"v_cvt_pk_bf16_f32 v50,v52,v53\n"
"v_mfma_f32_16x16x32_bf16 a[100:103],v[212:215],a[52:55],a[100:103]\n"
"s_add_u32 s73,s72,s81\n"
"v_cvt_pk_bf16_f32 v51,v54,v55\n"
"v_mfma_f32_16x16x32_bf16 a[104:107],v[212:215],a[56:59],a[104:107]\n"
"buffer_load_dwordx4 v9,s[44:47],s73 offen offset:2048 sc1 lds\n"
"v_mfma_f32_16x16x32_bf16 a[104:107],a[224:227],a[24:27],a[104:107]\n"
"v_add_u32 v238,0,v8\n"
"v_mul_u32_u24 v239,3641,v238\n"
"v_mfma_f32_16x16x32_bf16 a[108:111],a[224:227],a[28:31],a[108:111]\n"
"v_lshrrev_b32 v239,16,v239\n"
"v_mul_u32_u24 v243,18,v239\n"
"v_mfma_f32_16x16x32_bf16 a[108:111],v[212:215],a[60:63],a[108:111]\n"
"v_sub_u32 v238,v238,v243\n"
"v_add_u32 v238,s98,v238\n"
"s_waitcnt vmcnt(25)\n"
"v_mfma_f32_16x16x32_bf16 v[112:115],v[216:219],a[32:35],v[112:115]\n"
"buffer_load_dwordx4 v[212:215],v9,s[56:59],s61 offen offset:2048 sc1\n"
"v_mfma_f32_16x16x32_bf16 v[112:115],a[228:231],a[0:3],v[112:115]\n"
"v_subrev_u32 v243,18,v238\n"
"v_min_u32 v238,v238,v243\n"
"v_mfma_f32_16x16x32_bf16 v[116:119],a[228:231],a[4:7],v[116:119]\n"
"v_mul_u32_u24 v239,1152,v239\n"
"v_lshl_add_u32 v246,v238,4,v239\n"
"v_mfma_f32_16x16x32_bf16 v[116:119],v[216:219],a[36:39],v[116:119]\n"
"v_add_u32 v238,64,v8\n"
"v_mul_u32_u24 v239,3641,v238\n"
"v_mfma_f32_16x16x32_bf16 v[120:123],v[216:219],a[40:43],v[120:123]\n"
"v_lshrrev_b32 v239,16,v239\n"
"v_mul_u32_u24 v243,18,v239\n"
"v_mfma_f32_16x16x32_bf16 v[120:123],a[228:231],a[8:11],v[120:123]\n"
"v_sub_u32 v238,v238,v243\n"
"v_add_u32 v238,s98,v238\n"
"v_mfma_f32_16x16x32_bf16 v[124:127],a[228:231],a[12:15],v[124:127]\n"
"v_subrev_u32 v243,18,v238\n"
"v_min_u32 v238,v238,v243\n"
"v_mfma_f32_16x16x32_bf16 v[124:127],v[216:219],a[44:47],v[124:127]\n"
"v_mul_u32_u24 v239,1152,v239\n"
"v_lshl_add_u32 v247,v238,4,v239\n"
"v_mfma_f32_16x16x32_bf16 a[112:115],v[216:219],a[48:51],a[112:115]\n"
"buffer_load_dwordx4 a[224:227],v9,s[56:59],s17 offen offset:2048 sc1\n"
"v_subrev_u32 v247,1024,v247\n"
"v_mfma_f32_16x16x32_bf16 a[112:115],a[228:231],a[16:19],a[112:115]\n"
"v_add_u32 v238,128,v8\n"
"v_mul_u32_u24 v239,3641,v238\n"
"v_mfma_f32_16x16x32_bf16 a[116:119],a[228:231],a[20:23],a[116:119]\n"
"v_lshrrev_b32 v239,16,v239\n"
"v_mul_u32_u24 v243,18,v239\n"
"v_mfma_f32_16x16x32_bf16 a[116:119],v[216:219],a[52:55],a[116:119]\n"
"v_sub_u32 v238,v238,v243\n"
"v_add_u32 v238,s98,v238\n"
"v_mfma_f32_16x16x32_bf16 a[120:123],v[216:219],a[56:59],a[120:123]\n"
"buffer_load_dwordx4 v9,s[44:47],s73 offen offset:3072 sc1 lds\n"
"v_mfma_f32_16x16x32_bf16 a[120:123],a[228:231],a[24:27],a[120:123]\n"
"v_subrev_u32 v243,18,v238\n"
"v_min_u32 v238,v238,v243\n"
"v_mfma_f32_16x16x32_bf16 a[124:127],a[228:231],a[28:31],a[124:127]\n"
"v_mul_u32_u24 v239,1152,v239\n"
"v_lshl_add_u32 v248,v238,4,v239\n"
"v_mfma_f32_16x16x32_bf16 a[124:127],v[216:219],a[60:63],a[124:127]\n"
"v_subrev_u32 v248,2048,v248\n"
"v_add_u32 v238,192,v8\n"
"s_waitcnt vmcnt(24)\n"
"v_mfma_f32_16x16x32_bf16 v[128:131],v[220:223],a[32:35],v[128:131]\n"
"buffer_load_dwordx4 v[216:219],v9,s[56:59],s61 offen offset:3072 sc1\n"
"v_mfma_f32_16x16x32_bf16 v[128:131],a[232:235],a[0:3],v[128:131]\n"
"v_mul_u32_u24 v239,3641,v238\n"
"v_lshrrev_b32 v239,16,v239\n"
"v_mfma_f32_16x16x32_bf16 v[132:135],a[232:235],a[4:7],v[132:135]\n"
"v_mul_u32_u24 v243,18,v239\n"
"v_sub_u32 v238,v238,v243\n"
"v_mfma_f32_16x16x32_bf16 v[132:135],v[220:223],a[36:39],v[132:135]\n"
"s_add_u32 m0,s84,77824\n"
"s_add_u32 s73,s72,s82\n"
"v_mfma_f32_16x16x32_bf16 v[136:139],v[220:223],a[40:43],v[136:139]\n"
"buffer_load_dwordx4 v9,s[44:47],s73 offen offset:0 sc1 lds\n"
"v_mfma_f32_16x16x32_bf16 v[136:139],a[232:235],a[8:11],v[136:139]\n"
"v_add_u32 v238,s98,v238\n"
"v_subrev_u32 v243,18,v238\n"
"v_mfma_f32_16x16x32_bf16 v[140:143],a[232:235],a[12:15],v[140:143]\n"
"v_min_u32 v238,v238,v243\n"
"v_mul_u32_u24 v239,1152,v239\n"
"v_mfma_f32_16x16x32_bf16 v[140:143],v[220:223],a[44:47],v[140:143]\n"
"v_lshl_add_u32 v249,v238,4,v239\n"
"v_subrev_u32 v249,3072,v249\n"
"v_mfma_f32_16x16x32_bf16 a[128:131],v[220:223],a[48:51],a[128:131]\n"
"buffer_load_dwordx4 a[228:231],v9,s[56:59],s17 offen offset:3072 sc1\n"
"v_add_u32 v238,256,v8\n"
"v_mfma_f32_16x16x32_bf16 a[128:131],a[232:235],a[16:19],a[128:131]\n"
"v_mul_u32_u24 v239,3641,v238\n"
"v_lshrrev_b32 v239,16,v239\n"
"v_mfma_f32_16x16x32_bf16 a[132:135],a[232:235],a[20:23],a[132:135]\n"
"v_mul_u32_u24 v243,18,v239\n"
"v_sub_u32 v238,v238,v243\n"
"v_mfma_f32_16x16x32_bf16 a[132:135],v[220:223],a[52:55],a[132:135]\n"
"v_add_u32 v238,s98,v238\n"
"v_subrev_u32 v243,18,v238\n"
"v_mfma_f32_16x16x32_bf16 a[136:139],v[220:223],a[56:59],a[136:139]\n"
"buffer_load_dwordx4 v9,s[44:47],s73 offen offset:1024 sc1 lds\n"
"v_mfma_f32_16x16x32_bf16 a[136:139],a[232:235],a[24:27],a[136:139]\n"
"v_min_u32 v238,v238,v243\n"
"v_mul_u32_u24 v239,1152,v239\n"
"v_mfma_f32_16x16x32_bf16 a[140:143],a[232:235],a[28:31],a[140:143]\n"
"v_lshl_add_u32 v250,v238,4,v239\n"
"v_add_u32 v238,320,v8\n"
"v_mfma_f32_16x16x32_bf16 a[140:143],v[220:223],a[60:63],a[140:143]\n"
"s_add_u32 s61,s61,4096\n"
"v_mul_u32_u24 v239,3641,v238\n"
"s_waitcnt vmcnt(24)\n"
"v_mfma_f32_16x16x32_bf16 v[144:147],v[224:227],a[32:35],v[144:147]\n"
"buffer_load_dwordx4 v[220:223],v9,s[56:59],s61 offen offset:0 sc1\n"
"v_mfma_f32_16x16x32_bf16 v[144:147],a[236:239],a[0:3],v[144:147]\n"
"v_lshrrev_b32 v239,16,v239\n"
"v_mul_u32_u24 v243,18,v239\n"
"v_mfma_f32_16x16x32_bf16 v[148:151],a[236:239],a[4:7],v[148:151]\n"
"v_sub_u32 v238,v238,v243\n"
"v_add_u32 v238,s98,v238\n"
"v_mfma_f32_16x16x32_bf16 v[148:151],v[224:227],a[36:39],v[148:151]\n"
"v_subrev_u32 v243,18,v238\n"
"v_min_u32 v238,v238,v243\n"
"v_mfma_f32_16x16x32_bf16 v[152:155],v[224:227],a[40:43],v[152:155]\n"
"v_mul_u32_u24 v239,1152,v239\n"
"v_lshl_add_u32 v251,v238,4,v239\n"
"v_mfma_f32_16x16x32_bf16 v[152:155],a[236:239],a[8:11],v[152:155]\n"
"v_subrev_u32 v251,1024,v251\n"
"v_add_u32 v238,384,v8\n"
"v_mfma_f32_16x16x32_bf16 v[156:159],a[236:239],a[12:15],v[156:159]\n"
"v_mul_u32_u24 v239,3641,v238\n"
"v_lshrrev_b32 v239,16,v239\n"
"v_mfma_f32_16x16x32_bf16 v[156:159],v[224:227],a[44:47],v[156:159]\n"
"s_add_u32 s17,s17,4096\n"
"v_mul_u32_u24 v243,18,v239\n"
"v_mfma_f32_16x16x32_bf16 a[144:147],v[224:227],a[48:51],a[144:147]\n"
"L_pgs336_%=:\n"
"s_cbranch_execz L_pgs400_%=\n"
"buffer_load_dwordx4 a[232:235],v9,s[56:59],s17 offen offset:0 sc1\n"
"v_sub_u32 v238,v238,v243\n"
"v_mfma_f32_16x16x32_bf16 a[144:147],a[236:239],a[16:19],a[144:147]\n"
"v_add_u32 v238,s98,v238\n"
"v_subrev_u32 v243,18,v238\n"
"v_mfma_f32_16x16x32_bf16 a[148:151],a[236:239],a[20:23],a[148:151]\n"
"v_min_u32 v238,v238,v243\n"
"v_mul_u32_u24 v239,1152,v239\n"
"v_mfma_f32_16x16x32_bf16 a[148:151],v[224:227],a[52:55],a[148:151]\n"
"s_add_u32 s73,s72,s83\n"
"v_lshl_add_u32 v252,v238,4,v239\n"
"v_mfma_f32_16x16x32_bf16 a[152:155],v[224:227],a[56:59],a[152:155]\n"
"buffer_load_dwordx4 v9,s[44:47],s73 offen offset:2048 sc1 lds\n"
"v_mfma_f32_16x16x32_bf16 a[152:155],a[236:239],a[24:27],a[152:155]\n"
"v_subrev_u32 v252,2048,v252\n"
"v_add_u32 v238,448,v8\n"
"v_mfma_f32_16x16x32_bf16 a[156:159],a[236:239],a[28:31],a[156:159]\n"
"v_mul_u32_u24 v239,3641,v238\n"
"v_lshrrev_b32 v239,16,v239\n"
"v_mfma_f32_16x16x32_bf16 a[156:159],v[224:227],a[60:63],a[156:159]\n"
"v_mul_u32_u24 v243,18,v239\n"
"v_sub_u32 v238,v238,v243\n"
"s_waitcnt vmcnt(23)\n"
"v_mfma_f32_16x16x32_bf16 v[160:163],v[228:231],a[32:35],v[160:163]\n"
"buffer_load_dwordx4 v[224:227],v9,s[56:59],s61 offen offset:1024 sc1\n"
"v_mfma_f32_16x16x32_bf16 v[160:163],a[240:243],a[0:3],v[160:163]\n"
"v_add_u32 v238,s98,v238\n"
"v_subrev_u32 v243,18,v238\n"
"v_mfma_f32_16x16x32_bf16 v[164:167],a[240:243],a[4:7],v[164:167]\n"
"v_min_u32 v238,v238,v243\n"
"v_mul_u32_u24 v239,1152,v239\n"
"v_mfma_f32_16x16x32_bf16 v[164:167],v[228:231],a[36:39],v[164:167]\n"
"v_lshl_add_u32 v253,v238,4,v239\n"
"v_subrev_u32 v253,3072,v253\n"
"v_mfma_f32_16x16x32_bf16 v[168:171],v[228:231],a[40:43],v[168:171]\n"
"buffer_load_dwordx4 v9,s[44:47],s73 offen offset:3072 sc1 lds\n"
"v_mfma_f32_16x16x32_bf16 v[168:171],a[240:243],a[8:11],v[168:171]\n"
"v_add_u32 v238,512,v8\n"
"v_mul_u32_u24 v239,3641,v238\n"
"v_mfma_f32_16x16x32_bf16 v[172:175],a[240:243],a[12:15],v[172:175]\n"
"v_lshrrev_b32 v239,16,v239\n"
"v_mul_u32_u24 v243,18,v239\n"
"v_mfma_f32_16x16x32_bf16 v[172:175],v[228:231],a[44:47],v[172:175]\n"
"v_sub_u32 v238,v238,v243\n"
"v_add_u32 v238,s98,v238\n"
"v_mfma_f32_16x16x32_bf16 a[160:163],v[228:231],a[48:51],a[160:163]\n"
"buffer_load_dwordx4 a[236:239],v9,s[56:59],s17 offen offset:1024 sc1\n"
"v_subrev_u32 v243,18,v238\n"
"v_mfma_f32_16x16x32_bf16 a[160:163],a[240:243],a[16:19],a[160:163]\n"
"v_min_u32 v238,v238,v243\n"
"v_mul_u32_u24 v239,1152,v239\n"
"v_mfma_f32_16x16x32_bf16 a[164:167],a[240:243],a[20:23],a[164:167]\n"
"v_lshl_add_u32 v254,v238,4,v239\n"
"s_mov_b32 s77,0\n"
"v_mfma_f32_16x16x32_bf16 a[164:167],v[228:231],a[52:55],a[164:167]\n"
"s_mul_i32 s78,s36,663552\n"
"s_add_u32 s77,s77,s78\n"
"v_mfma_f32_16x16x32_bf16 a[168:171],v[228:231],a[56:59],a[168:171]\n"
"s_mul_i32 s78,s35,288\n"
"s_add_u32 s77,s77,s78\n"
"v_mfma_f32_16x16x32_bf16 a[168:171],a[240:243],a[24:27],a[168:171]\n"
"s_add_u32 s40,s26,s77\n"
"s_addc_u32 s41,s27,0\n"
"v_mfma_f32_16x16x32_bf16 a[172:175],a[240:243],a[28:31],a[172:175]\n"
"s_and_b32 s41,s41,0xffff\n"
"s_mov_b32 s42,662688\n"
"v_mfma_f32_16x16x32_bf16 a[172:175],v[228:231],a[60:63],a[172:175]\n"
"s_mov_b32 s43,0x20000\n"
"s_mul_i32 s86,s35,9\n"
"s_waitcnt vmcnt(21)\n"
"v_mfma_f32_16x16x32_bf16 v[176:179],a[208:211],a[32:35],v[176:179]\n"
"buffer_load_dwordx4 v[228:231],v9,s[56:59],s61 offen offset:2048 sc1\n"
"s_add_u32 m0,s84,110592\n"
"v_mfma_f32_16x16x32_bf16 v[176:179],a[244:247],a[0:3],v[176:179]\n"
"s_cmp_le_u32 s34,13\n"
"s_cselect_b32 s72,56,52\n"
"s_waitcnt vmcnt(21)\n"
"v_mfma_f32_16x16x32_bf16 v[192:195],a[248:251],a[0:3],v[192:195]\n"
"s_add_u32 s72,s72,s37\n"
"v_mfma_f32_16x16x32_bf16 v[192:195],a[212:215],a[32:35],v[192:195]\n"
"s_lshl_b32 s72,s72,13\n"
"s_add_u32 s73,s72,s80\n"
"v_mfma_f32_16x16x32_bf16 v[180:183],a[208:211],a[36:39],v[180:183]\n"
"buffer_load_dwordx4 v9,s[44:47],s73 offen offset:0 sc1 lds\n"
"v_mfma_f32_16x16x32_bf16 v[180:183],a[244:247],a[4:7],v[180:183]\n"
"ds_read_b128 a[32:35],v14 offset:0\n"
"v_mfma_f32_16x16x32_bf16 v[196:199],a[248:251],a[4:7],v[196:199]\n"
"ds_read_b128 a[0:3],v12 offset:36864\n"
"v_mfma_f32_16x16x32_bf16 v[196:199],a[212:215],a[36:39],v[196:199]\n"
"s_add_u32 s86,s86,1\n"
"s_lshr_b32 s86,s86,1\n"
"v_mfma_f32_16x16x32_bf16 v[184:187],a[208:211],a[40:43],v[184:187]\n"
"buffer_load_dwordx4 a[240:243],v9,s[56:59],s17 offen offset:2048 sc1\n"
"s_mov_b32 s77,47185920\n"
"v_mfma_f32_16x16x32_bf16 v[184:187],a[244:247],a[8:11],v[184:187]\n"
"ds_read_b128 a[36:39],v14 offset:1024\n"
"s_mul_i32 s78,s34,589824\n"
"v_mfma_f32_16x16x32_bf16 v[200:203],a[248:251],a[8:11],v[200:203]\n"
"ds_read_b128 a[4:7],v12 offset:37888\n"
"s_add_u32 s77,s77,s78\n"
"v_mfma_f32_16x16x32_bf16 v[200:203],a[212:215],a[40:43],v[200:203]\n"
"s_mul_i32 s78,s37,147456\n"
"s_add_u32 s77,s77,s78\n"
"v_mfma_f32_16x16x32_bf16 v[188:191],a[208:211],a[44:47],v[188:191]\n"
"buffer_load_dwordx4 v9,s[44:47],s73 offen offset:1024 sc1 lds\n"
"v_mfma_f32_16x16x32_bf16 v[188:191],a[244:247],a[12:15],v[188:191]\n"
"ds_read_b128 a[40:43],v14 offset:2048\n"
"s_mul_i32 s78,s36,18432\n"
"v_mfma_f32_16x16x32_bf16 v[204:207],a[248:251],a[12:15],v[204:207]\n"
"ds_read_b128 a[8:11],v12 offset:38912\n"
"s_add_u32 s77,s77,s78\n"
"v_mfma_f32_16x16x32_bf16 v[204:207],a[212:215],a[44:47],v[204:207]\n"
"s_add_u32 s52,s8,s77\n"
"s_addc_u32 s53,s9,0\n"
"v_mfma_f32_16x16x32_bf16 a[176:179],a[208:211],a[48:51],a[176:179]\n"
"s_and_b32 s53,s53,0xffff\n"
"s_mov_b32 s54,92160\n"
"v_mfma_f32_16x16x32_bf16 a[176:179],a[244:247],a[16:19],a[176:179]\n"
"ds_read_b128 a[44:47],v14 offset:3072\n"
"s_mov_b32 s55,0x20000\n"
"v_mfma_f32_16x16x32_bf16 a[192:195],a[248:251],a[16:19],a[192:195]\n"
"ds_read_b128 a[12:15],v12 offset:39936\n"
"v_mfma_f32_16x16x32_bf16 a[192:195],a[212:215],a[48:51],a[192:195]\n"
"s_add_u32 s73,s72,s81\n"
"v_mfma_f32_16x16x32_bf16 a[180:183],a[208:211],a[52:55],a[180:183]\n"
"buffer_load_dwordx4 v9,s[44:47],s73 offen offset:2048 sc1 lds\n"
"v_mfma_f32_16x16x32_bf16 a[180:183],a[244:247],a[20:23],a[180:183]\n"
"ds_read_b128 a[48:51],v255 offset:0\n"
"v_mfma_f32_16x16x32_bf16 a[196:199],a[248:251],a[20:23],a[196:199]\n"
"ds_read_b128 a[16:19],v12 offset:40960\n"
"v_mfma_f32_16x16x32_bf16 a[196:199],a[212:215],a[52:55],a[196:199]\n"
"v_mfma_f32_16x16x32_bf16 a[184:187],a[208:211],a[56:59],a[184:187]\n"
"v_mfma_f32_16x16x32_bf16 a[184:187],a[244:247],a[24:27],a[184:187]\n"
"ds_read_b128 a[52:55],v255 offset:1024\n"
"v_mfma_f32_16x16x32_bf16 a[200:203],a[248:251],a[24:27],a[200:203]\n"
"ds_read_b128 a[20:23],v12 offset:41984\n"
"v_mfma_f32_16x16x32_bf16 a[200:203],a[212:215],a[56:59],a[200:203]\n"
"v_mfma_f32_16x16x32_bf16 a[188:191],a[208:211],a[60:63],a[188:191]\n"
"v_mfma_f32_16x16x32_bf16 a[188:191],a[244:247],a[28:31],a[188:191]\n"
"ds_read_b128 a[56:59],v255 offset:2048\n"
"v_mfma_f32_16x16x32_bf16 a[204:207],a[248:251],a[28:31],a[204:207]\n"
"buffer_load_dwordx4 a[208:211],v9,s[56:59],s61 offen offset:3072 sc1\n"
"ds_read_b128 a[24:27],v12 offset:43008\n"
"v_mfma_f32_16x16x32_bf16 a[204:207],a[212:215],a[60:63],a[204:207]\n"
"s_waitcnt lgkmcnt(10)\n"
"s_waitcnt vmcnt(21)\n"
"v_mfma_f32_16x16x32_bf16 v[64:67],a[252:255],a[32:35],v[64:67]\n"
"v_mfma_f32_16x16x32_bf16 v[64:67],a[216:219],a[0:3],v[64:67]\n"
"s_add_u32 s61,s61,4096\n"
"ds_read_b128 a[60:63],v255 offset:3072\n"
"s_waitcnt vmcnt(20)\n"
"v_mfma_f32_16x16x32_bf16 v[80:83],a[220:223],a[0:3],v[80:83]\n"
"buffer_load_dwordx4 a[212:215],v9,s[56:59],s61 offen offset:0 sc1\n"
"ds_read_b128 a[28:31],v12 offset:44032\n"
"v_mfma_f32_16x16x32_bf16 v[80:83],v[208:211],a[32:35],v[80:83]\n"
"v_mfma_f32_16x16x32_bf16 v[68:71],a[252:255],a[36:39],v[68:71]\n"
"buffer_load_dwordx4 v9,s[44:47],s73 offen offset:3072 sc1 lds\n"
"v_mfma_f32_16x16x32_bf16 v[68:71],a[216:219],a[4:7],v[68:71]\n"
"v_mfma_f32_16x16x32_bf16 v[84:87],a[220:223],a[4:7],v[84:87]\n"
"buffer_load_dwordx4 a[244:247],v9,s[56:59],s17 offen offset:3072 sc1\n"
"v_mfma_f32_16x16x32_bf16 v[84:87],v[208:211],a[36:39],v[84:87]\n"
"s_waitcnt lgkmcnt(8)\n"
"v_mfma_f32_16x16x32_bf16 v[72:75],a[252:255],a[40:43],v[72:75]\n"
"v_mfma_f32_16x16x32_bf16 v[72:75],a[216:219],a[8:11],v[72:75]\n"
"s_add_u32 s17,s17,4096\n"
"v_mfma_f32_16x16x32_bf16 v[88:91],a[220:223],a[8:11],v[88:91]\n"
"buffer_load_dwordx4 a[248:251],v9,s[56:59],s17 offen offset:0 sc1\n"
"v_mfma_f32_16x16x32_bf16 v[88:91],v[208:211],a[40:43],v[88:91]\n"
"s_add_u32 m0,s84,114688\n"
"s_add_u32 s73,s72,s82\n"
"v_mfma_f32_16x16x32_bf16 v[76:79],a[252:255],a[44:47],v[76:79]\n"
"buffer_load_dwordx4 v9,s[44:47],s73 offen offset:0 sc1 lds\n"
"v_mfma_f32_16x16x32_bf16 v[76:79],a[216:219],a[12:15],v[76:79]\n"
"v_mfma_f32_16x16x32_bf16 v[92:95],a[220:223],a[12:15],v[92:95]\n"
"v_mfma_f32_16x16x32_bf16 v[92:95],v[208:211],a[44:47],v[92:95]\n"
"s_waitcnt lgkmcnt(4)\n"
"v_mfma_f32_16x16x32_bf16 a[64:67],a[252:255],a[48:51],a[64:67]\n"
"v_mfma_f32_16x16x32_bf16 a[64:67],a[216:219],a[16:19],a[64:67]\n"
"v_mfma_f32_16x16x32_bf16 a[80:83],a[220:223],a[16:19],a[80:83]\n"
"v_mfma_f32_16x16x32_bf16 a[80:83],v[208:211],a[48:51],a[80:83]\n"
"v_mfma_f32_16x16x32_bf16 a[68:71],a[252:255],a[52:55],a[68:71]\n"
"buffer_load_dwordx4 v9,s[44:47],s73 offen offset:1024 sc1 lds\n"
"v_mfma_f32_16x16x32_bf16 a[68:71],a[216:219],a[20:23],a[68:71]\n"
"v_mfma_f32_16x16x32_bf16 a[84:87],a[220:223],a[20:23],a[84:87]\n"
"v_mfma_f32_16x16x32_bf16 a[84:87],v[208:211],a[52:55],a[84:87]\n"
"s_waitcnt lgkmcnt(0)\n"
"v_mfma_f32_16x16x32_bf16 a[72:75],a[252:255],a[56:59],a[72:75]\n"
"v_mfma_f32_16x16x32_bf16 a[72:75],a[216:219],a[24:27],a[72:75]\n"
"v_mfma_f32_16x16x32_bf16 a[88:91],a[220:223],a[24:27],a[88:91]\n"
"v_mfma_f32_16x16x32_bf16 a[88:91],v[208:211],a[56:59],a[88:91]\n"
"v_mfma_f32_16x16x32_bf16 a[76:79],a[252:255],a[60:63],a[76:79]\n"
"s_cmp_le_u32 s34,12\n"
"s_cselect_b32 s61,52,48\n"
"v_mfma_f32_16x16x32_bf16 a[76:79],a[216:219],a[28:31],a[76:79]\n"
"s_add_u32 s61,s61,s37\n"
"s_mul_i32 s61,s61,9216\n"
"v_mfma_f32_16x16x32_bf16 a[92:95],a[220:223],a[28:31],a[92:95]\n"
"buffer_load_dwordx4 a[252:255],v9,s[56:59],s61 offen offset:0 sc1\n"
"v_mfma_f32_16x16x32_bf16 a[92:95],v[208:211],a[60:63],a[92:95]\n"
"s_waitcnt vmcnt(24)\n"
"v_mfma_f32_16x16x32_bf16 v[96:99],v[212:215],a[32:35],v[96:99]\n"
"v_mfma_f32_16x16x32_bf16 v[96:99],a[224:227],a[0:3],v[96:99]\n"
"v_mfma_f32_16x16x32_bf16 v[100:103],a[224:227],a[4:7],v[100:103]\n"
"buffer_load_dwordx4 v[208:211],v9,s[56:59],s61 offen offset:1024 sc1\n"
"v_mfma_f32_16x16x32_bf16 v[100:103],v[212:215],a[36:39],v[100:103]\n"
"s_add_u32 s73,s72,s83\n"
"v_mfma_f32_16x16x32_bf16 v[104:107],v[212:215],a[40:43],v[104:107]\n"
"s_cmp_le_u32 s34,13\n"
"s_cselect_b32 s17,56,52\n"
"buffer_load_dwordx4 v9,s[44:47],s73 offen offset:2048 sc1 lds\n"
"v_mfma_f32_16x16x32_bf16 v[104:107],a[224:227],a[8:11],v[104:107]\n"
"s_add_u32 s17,s17,s37\n"
"s_mul_i32 s17,s17,9216\n"
"v_mfma_f32_16x16x32_bf16 v[108:111],a[224:227],a[12:15],v[108:111]\n"
"buffer_load_dwordx4 a[216:219],v9,s[56:59],s17 offen offset:0 sc1\n"
"v_mfma_f32_16x16x32_bf16 v[108:111],v[212:215],a[44:47],v[108:111]\n"
"v_mfma_f32_16x16x32_bf16 a[96:99],v[212:215],a[48:51],a[96:99]\n"
"v_mfma_f32_16x16x32_bf16 a[96:99],a[224:227],a[16:19],a[96:99]\n"
"v_mfma_f32_16x16x32_bf16 a[100:103],a[224:227],a[20:23],a[100:103]\n"
"buffer_load_dwordx4 a[220:223],v9,s[56:59],s17 offen offset:1024 sc1\n"
"v_mfma_f32_16x16x32_bf16 a[100:103],v[212:215],a[52:55],a[100:103]\n"
"v_mfma_f32_16x16x32_bf16 a[104:107],v[212:215],a[56:59],a[104:107]\n"
"buffer_load_dwordx4 v9,s[44:47],s73 offen offset:3072 sc1 lds\n"
"v_mfma_f32_16x16x32_bf16 a[104:107],a[224:227],a[24:27],a[104:107]\n"
"v_mfma_f32_16x16x32_bf16 a[108:111],a[224:227],a[28:31],a[108:111]\n"
"v_mfma_f32_16x16x32_bf16 a[108:111],v[212:215],a[60:63],a[108:111]\n"
"s_waitcnt vmcnt(25)\n"
"v_mfma_f32_16x16x32_bf16 v[112:115],v[216:219],a[32:35],v[112:115]\n"
"buffer_load_dwordx4 v[212:215],v9,s[56:59],s61 offen offset:2048 sc1\n"
"v_mfma_f32_16x16x32_bf16 v[112:115],a[228:231],a[0:3],v[112:115]\n"
"v_mfma_f32_16x16x32_bf16 v[116:119],a[228:231],a[4:7],v[116:119]\n"
"v_mfma_f32_16x16x32_bf16 v[116:119],v[216:219],a[36:39],v[116:119]\n"
"v_mfma_f32_16x16x32_bf16 v[120:123],v[216:219],a[40:43],v[120:123]\n"
"v_mfma_f32_16x16x32_bf16 v[120:123],a[228:231],a[8:11],v[120:123]\n"
"v_mfma_f32_16x16x32_bf16 v[124:127],a[228:231],a[12:15],v[124:127]\n"
"v_mfma_f32_16x16x32_bf16 v[124:127],v[216:219],a[44:47],v[124:127]\n"
"v_mfma_f32_16x16x32_bf16 a[112:115],v[216:219],a[48:51],a[112:115]\n"
"buffer_load_dwordx4 a[224:227],v9,s[56:59],s17 offen offset:2048 sc1\n"
"s_add_u32 m0,s84,0\n"
"v_mfma_f32_16x16x32_bf16 a[112:115],a[228:231],a[16:19],a[112:115]\n"
"s_cmp_le_u32 s34,14\n"
"s_cselect_b32 s72,60,56\n"
"v_mfma_f32_16x16x32_bf16 a[116:119],a[228:231],a[20:23],a[116:119]\n"
"s_add_u32 s72,s72,s37\n"
"v_mfma_f32_16x16x32_bf16 a[116:119],v[216:219],a[52:55],a[116:119]\n"
"s_lshl_b32 s72,s72,13\n"
"s_add_u32 s73,s72,s80\n"
"v_mfma_f32_16x16x32_bf16 a[120:123],v[216:219],a[56:59],a[120:123]\n"
"buffer_load_dwordx4 v9,s[44:47],s73 offen offset:0 sc1 lds\n"
"v_mfma_f32_16x16x32_bf16 a[120:123],a[228:231],a[24:27],a[120:123]\n"
"v_mfma_f32_16x16x32_bf16 a[124:127],a[228:231],a[28:31],a[124:127]\n"
"v_mfma_f32_16x16x32_bf16 a[124:127],v[216:219],a[60:63],a[124:127]\n"
"s_waitcnt vmcnt(25)\n"
"v_mfma_f32_16x16x32_bf16 v[128:131],v[220:223],a[32:35],v[128:131]\n"
"buffer_load_dwordx4 v[216:219],v9,s[56:59],s61 offen offset:3072 sc1\n"
"v_mfma_f32_16x16x32_bf16 v[128:131],a[232:235],a[0:3],v[128:131]\n"
"v_mfma_f32_16x16x32_bf16 v[132:135],a[232:235],a[4:7],v[132:135]\n"
"v_mfma_f32_16x16x32_bf16 v[132:135],v[220:223],a[36:39],v[132:135]\n"
"v_mfma_f32_16x16x32_bf16 v[136:139],v[220:223],a[40:43],v[136:139]\n"
"buffer_load_dwordx4 v9,s[44:47],s73 offen offset:1024 sc1 lds\n"
"v_mfma_f32_16x16x32_bf16 v[136:139],a[232:235],a[8:11],v[136:139]\n"
"v_mfma_f32_16x16x32_bf16 v[140:143],a[232:235],a[12:15],v[140:143]\n"
"v_mfma_f32_16x16x32_bf16 v[140:143],v[220:223],a[44:47],v[140:143]\n"
"v_mfma_f32_16x16x32_bf16 a[128:131],v[220:223],a[48:51],a[128:131]\n"
"buffer_load_dwordx4 a[228:231],v9,s[56:59],s17 offen offset:3072 sc1\n"
"s_xor_b32 s75,s37,3\n"
"v_mfma_f32_16x16x32_bf16 a[128:131],a[232:235],a[16:19],a[128:131]\n"
"s_lshl_b32 s76,s75,12\n"
"s_lshl_b32 s75,s75,13\n"
"v_mfma_f32_16x16x32_bf16 a[132:135],a[232:235],a[20:23],a[132:135]\n"
"s_add_u32 s76,s76,147456\n"
"v_add_u32 v234,s75,v9\n"
"v_mfma_f32_16x16x32_bf16 a[132:135],v[220:223],a[52:55],a[132:135]\n"
"s_add_u32 s73,s72,s81\n"
"v_add_u32 v235,73728,v234\n"
"v_mfma_f32_16x16x32_bf16 a[136:139],v[220:223],a[56:59],a[136:139]\n"
"buffer_load_dwordx4 v9,s[44:47],s73 offen offset:2048 sc1 lds\n"
"v_mfma_f32_16x16x32_bf16 a[136:139],a[232:235],a[24:27],a[136:139]\n"
"v_add_u32 v236,s76,v9\n"
"s_xor_b32 s75,s37,1\n"
"v_mfma_f32_16x16x32_bf16 a[140:143],a[232:235],a[28:31],a[140:143]\n"
"s_lshl_b32 s76,s75,12\n"
"s_lshl_b32 s75,s75,13\n"
"v_mfma_f32_16x16x32_bf16 a[140:143],v[220:223],a[60:63],a[140:143]\n"
"s_add_u32 s61,s61,4096\n"
"s_add_u32 s76,s76,147456\n"
"s_waitcnt vmcnt(25)\n"
"v_mfma_f32_16x16x32_bf16 v[144:147],v[224:227],a[32:35],v[144:147]\n"
"buffer_load_dwordx4 v[220:223],v9,s[56:59],s61 offen offset:0 sc1\n"
"v_mfma_f32_16x16x32_bf16 v[144:147],a[236:239],a[0:3],v[144:147]\n"
"v_add_u32 v237,s75,v9\n"
"v_add_u32 v238,73728,v237\n"
"v_mfma_f32_16x16x32_bf16 v[148:151],a[236:239],a[4:7],v[148:151]\n"
"v_add_u32 v239,s76,v9\n"
"v_mfma_f32_16x16x32_bf16 v[148:151],v[224:227],a[36:39],v[148:151]\n"
"v_mfma_f32_16x16x32_bf16 v[152:155],v[224:227],a[40:43],v[152:155]\n"
"v_mfma_f32_16x16x32_bf16 v[152:155],a[236:239],a[8:11],v[152:155]\n"
"v_mfma_f32_16x16x32_bf16 v[156:159],a[236:239],a[12:15],v[156:159]\n"
"v_mfma_f32_16x16x32_bf16 v[156:159],v[224:227],a[44:47],v[156:159]\n"
"s_add_u32 s17,s17,4096\n"
"v_mfma_f32_16x16x32_bf16 a[144:147],v[224:227],a[48:51],a[144:147]\n"
"buffer_load_dwordx4 a[232:235],v9,s[56:59],s17 offen offset:0 sc1\n"
"v_mfma_f32_16x16x32_bf16 a[144:147],a[236:239],a[16:19],a[144:147]\n"
"v_mfma_f32_16x16x32_bf16 a[148:151],a[236:239],a[20:23],a[148:151]\n"
"v_mfma_f32_16x16x32_bf16 a[148:151],v[224:227],a[52:55],a[148:151]\n"
"v_mfma_f32_16x16x32_bf16 a[152:155],v[224:227],a[56:59],a[152:155]\n"
"buffer_load_dwordx4 v9,s[44:47],s73 offen offset:3072 sc1 lds\n"
"v_mfma_f32_16x16x32_bf16 a[152:155],a[236:239],a[24:27],a[152:155]\n"
"v_mfma_f32_16x16x32_bf16 a[156:159],a[236:239],a[28:31],a[156:159]\n"
"v_mfma_f32_16x16x32_bf16 a[156:159],v[224:227],a[60:63],a[156:159]\n"
"s_waitcnt vmcnt(25)\n"
"v_mfma_f32_16x16x32_bf16 v[160:163],v[228:231],a[32:35],v[160:163]\n"
"buffer_load_dwordx4 v[224:227],v9,s[56:59],s61 offen offset:1024 sc1\n"
"v_mfma_f32_16x16x32_bf16 v[160:163],a[240:243],a[0:3],v[160:163]\n"
"v_mfma_f32_16x16x32_bf16 v[164:167],a[240:243],a[4:7],v[164:167]\n"
"v_mfma_f32_16x16x32_bf16 v[164:167],v[228:231],a[36:39],v[164:167]\n"
"s_add_u32 m0,s84,4096\n"
"s_add_u32 s73,s72,s82\n"
"v_mfma_f32_16x16x32_bf16 v[168:171],v[228:231],a[40:43],v[168:171]\n"
"buffer_load_dwordx4 v9,s[44:47],s73 offen offset:0 sc1 lds\n"
"v_mfma_f32_16x16x32_bf16 v[168:171],a[240:243],a[8:11],v[168:171]\n"
"v_mfma_f32_16x16x32_bf16 v[172:175],a[240:243],a[12:15],v[172:175]\n"
"v_mfma_f32_16x16x32_bf16 v[172:175],v[228:231],a[44:47],v[172:175]\n"
"v_mfma_f32_16x16x32_bf16 a[160:163],v[228:231],a[48:51],a[160:163]\n"
"buffer_load_dwordx4 a[236:239],v9,s[56:59],s17 offen offset:1024 sc1\n"
"v_mfma_f32_16x16x32_bf16 a[160:163],a[240:243],a[16:19],a[160:163]\n"
"v_mfma_f32_16x16x32_bf16 a[164:167],a[240:243],a[20:23],a[164:167]\n"
"v_mfma_f32_16x16x32_bf16 a[164:167],v[228:231],a[52:55],a[164:167]\n"
"v_mfma_f32_16x16x32_bf16 a[168:171],v[228:231],a[56:59],a[168:171]\n"
"buffer_load_dwordx4 v9,s[44:47],s73 offen offset:1024 sc1 lds\n"
"v_mfma_f32_16x16x32_bf16 a[168:171],a[240:243],a[24:27],a[168:171]\n"
"v_mfma_f32_16x16x32_bf16 a[172:175],a[240:243],a[28:31],a[172:175]\n"
"v_mfma_f32_16x16x32_bf16 a[172:175],v[228:231],a[60:63],a[172:175]\n"
"s_waitcnt vmcnt(23)\n"
"v_mfma_f32_16x16x32_bf16 v[176:179],a[208:211],a[32:35],v[176:179]\n"
"buffer_load_dwordx4 v[228:231],v9,s[56:59],s61 offen offset:2048 sc1\n"
"v_mfma_f32_16x16x32_bf16 v[176:179],a[244:247],a[0:3],v[176:179]\n"
"s_waitcnt vmcnt(23)\n"
"v_mfma_f32_16x16x32_bf16 v[192:195],a[248:251],a[0:3],v[192:195]\n"
"v_mfma_f32_16x16x32_bf16 v[192:195],a[212:215],a[32:35],v[192:195]\n"
"s_add_u32 s73,s72,s83\n"
"v_mfma_f32_16x16x32_bf16 v[180:183],a[208:211],a[36:39],v[180:183]\n"
"buffer_load_dwordx4 v9,s[44:47],s73 offen offset:2048 sc1 lds\n"
"v_mfma_f32_16x16x32_bf16 v[180:183],a[244:247],a[4:7],v[180:183]\n"
"ds_read_b128 a[32:35],v13 offset:0\n"
"v_mfma_f32_16x16x32_bf16 v[196:199],a[248:251],a[4:7],v[196:199]\n"
"v_mfma_f32_16x16x32_bf16 v[196:199],a[212:215],a[36:39],v[196:199]\n"
"v_mfma_f32_16x16x32_bf16 v[184:187],a[208:211],a[40:43],v[184:187]\n"
"buffer_load_dwordx4 a[240:243],v9,s[56:59],s17 offen offset:2048 sc1\n"
"v_mfma_f32_16x16x32_bf16 v[184:187],a[244:247],a[8:11],v[184:187]\n"
"ds_read_b128 a[36:39],v13 offset:1024\n"
"v_mfma_f32_16x16x32_bf16 v[200:203],a[248:251],a[8:11],v[200:203]\n"
"v_mfma_f32_16x16x32_bf16 v[200:203],a[212:215],a[40:43],v[200:203]\n"
"v_mfma_f32_16x16x32_bf16 v[188:191],a[208:211],a[44:47],v[188:191]\n"
"buffer_load_dwordx4 v9,s[44:47],s73 offen offset:3072 sc1 lds\n"
"v_mfma_f32_16x16x32_bf16 v[188:191],a[244:247],a[12:15],v[188:191]\n"
"ds_read_b128 a[40:43],v13 offset:2048\n"
"v_mfma_f32_16x16x32_bf16 v[204:207],a[248:251],a[12:15],v[204:207]\n"
"v_mfma_f32_16x16x32_bf16 v[204:207],a[212:215],a[44:47],v[204:207]\n"
"v_mfma_f32_16x16x32_bf16 a[176:179],a[208:211],a[48:51],a[176:179]\n"
"v_mfma_f32_16x16x32_bf16 a[176:179],a[244:247],a[16:19],a[176:179]\n"
"ds_read_b128 a[44:47],v13 offset:3072\n"
"v_mfma_f32_16x16x32_bf16 a[192:195],a[248:251],a[16:19],a[192:195]\n"
"v_mfma_f32_16x16x32_bf16 a[192:195],a[212:215],a[48:51],a[192:195]\n"
"v_mfma_f32_16x16x32_bf16 a[180:183],a[208:211],a[52:55],a[180:183]\n"
"v_mfma_f32_16x16x32_bf16 a[180:183],a[244:247],a[20:23],a[180:183]\n"
"ds_read_b128 a[48:51],v13 offset:4096\n"
"v_mfma_f32_16x16x32_bf16 a[196:199],a[248:251],a[20:23],a[196:199]\n"
"v_mfma_f32_16x16x32_bf16 a[196:199],a[212:215],a[52:55],a[196:199]\n"
"v_mfma_f32_16x16x32_bf16 a[184:187],a[208:211],a[56:59],a[184:187]\n"
"v_mfma_f32_16x16x32_bf16 a[184:187],a[244:247],a[24:27],a[184:187]\n"
"ds_read_b128 a[52:55],v13 offset:5120\n"
"v_mfma_f32_16x16x32_bf16 a[200:203],a[248:251],a[24:27],a[200:203]\n"
"v_mfma_f32_16x16x32_bf16 a[200:203],a[212:215],a[56:59],a[200:203]\n"
"v_mfma_f32_16x16x32_bf16 a[188:191],a[208:211],a[60:63],a[188:191]\n"
"v_mfma_f32_16x16x32_bf16 a[188:191],a[244:247],a[28:31],a[188:191]\n"
"ds_read_b128 a[56:59],v13 offset:6144\n"
"v_mfma_f32_16x16x32_bf16 a[204:207],a[248:251],a[28:31],a[204:207]\n"
"buffer_load_dwordx4 a[208:211],v9,s[56:59],s61 offen offset:3072 sc1\n"
"v_mfma_f32_16x16x32_bf16 a[204:207],a[212:215],a[60:63],a[204:207]\n"
"s_waitcnt lgkmcnt(5)\n"
"s_waitcnt vmcnt(24)\n"
"v_mfma_f32_16x16x32_bf16 v[64:67],a[252:255],a[32:35],v[64:67]\n"
"s_waitcnt vmcnt(23)\n"
"v_mfma_f32_16x16x32_bf16 v[80:83],v[208:211],a[32:35],v[80:83]\n"
"s_add_u32 s61,s61,4096\n"
"ds_read_b128 a[60:63],v13 offset:7168\n"
"v_mfma_f32_16x16x32_bf16 v[68:71],a[252:255],a[36:39],v[68:71]\n"
"buffer_load_dwordx4 a[212:215],v9,s[56:59],s61 offen offset:0 sc1\n"
"v_mfma_f32_16x16x32_bf16 v[84:87],v[208:211],a[36:39],v[84:87]\n"
"s_waitcnt lgkmcnt(4)\n"
"v_mfma_f32_16x16x32_bf16 v[72:75],a[252:255],a[40:43],v[72:75]\n"
"v_mfma_f32_16x16x32_bf16 v[88:91],v[208:211],a[40:43],v[88:91]\n"
"v_mfma_f32_16x16x32_bf16 v[76:79],a[252:255],a[44:47],v[76:79]\n"
"buffer_load_dwordx4 a[244:247],v9,s[56:59],s17 offen offset:3072 sc1\n"
"v_mfma_f32_16x16x32_bf16 v[92:95],v[208:211],a[44:47],v[92:95]\n"
"s_waitcnt lgkmcnt(2)\n"
"v_mfma_f32_16x16x32_bf16 a[64:67],a[252:255],a[48:51],a[64:67]\n"
"v_mfma_f32_16x16x32_bf16 a[80:83],v[208:211],a[48:51],a[80:83]\n"
"s_add_u32 s17,s17,4096\n"
"v_mfma_f32_16x16x32_bf16 a[68:71],a[252:255],a[52:55],a[68:71]\n"
"buffer_load_dwordx4 a[248:251],v9,s[56:59],s17 offen offset:0 sc1\n"
"v_mfma_f32_16x16x32_bf16 a[84:87],v[208:211],a[52:55],a[84:87]\n"
"s_waitcnt lgkmcnt(0)\n"
"v_mfma_f32_16x16x32_bf16 a[72:75],a[252:255],a[56:59],a[72:75]\n"
"v_mfma_f32_16x16x32_bf16 a[88:91],v[208:211],a[56:59],a[88:91]\n"
"s_cmp_le_u32 s34,14\n"
"s_cselect_b32 s61,60,56\n"
"v_mfma_f32_16x16x32_bf16 a[76:79],a[252:255],a[60:63],a[76:79]\n"
"s_add_u32 s61,s61,s37\n"
"s_mul_i32 s61,s61,9216\n"
"v_mfma_f32_16x16x32_bf16 a[92:95],v[208:211],a[60:63],a[92:95]\n"
"buffer_load_dwordx4 a[252:255],v9,s[56:59],s61 offen offset:0 sc1\n"
"s_waitcnt vmcnt(22)\n"
"v_mfma_f32_16x16x32_bf16 v[96:99],v[212:215],a[32:35],v[96:99]\n"
"buffer_load_dwordx4 v[208:211],v9,s[56:59],s61 offen offset:1024 sc1\n"
"v_mfma_f32_16x16x32_bf16 v[100:103],v[212:215],a[36:39],v[100:103]\n"
"v_mfma_f32_16x16x32_bf16 v[104:107],v[212:215],a[40:43],v[104:107]\n"
"v_mfma_f32_16x16x32_bf16 v[108:111],v[212:215],a[44:47],v[108:111]\n"
"v_mfma_f32_16x16x32_bf16 a[96:99],v[212:215],a[48:51],a[96:99]\n"
"v_mfma_f32_16x16x32_bf16 a[100:103],v[212:215],a[52:55],a[100:103]\n"
"v_mfma_f32_16x16x32_bf16 a[104:107],v[212:215],a[56:59],a[104:107]\n"
"v_mfma_f32_16x16x32_bf16 a[108:111],v[212:215],a[60:63],a[108:111]\n"
"s_waitcnt vmcnt(20)\n"
"v_mfma_f32_16x16x32_bf16 v[112:115],v[216:219],a[32:35],v[112:115]\n"
"buffer_load_dwordx4 v[212:215],v9,s[56:59],s61 offen offset:2048 sc1\n"
"v_mfma_f32_16x16x32_bf16 v[116:119],v[216:219],a[36:39],v[116:119]\n"
"v_mfma_f32_16x16x32_bf16 v[120:123],v[216:219],a[40:43],v[120:123]\n"
"v_mfma_f32_16x16x32_bf16 v[124:127],v[216:219],a[44:47],v[124:127]\n"
"v_mfma_f32_16x16x32_bf16 a[112:115],v[216:219],a[48:51],a[112:115]\n"
"v_mfma_f32_16x16x32_bf16 a[116:119],v[216:219],a[52:55],a[116:119]\n"
"v_mfma_f32_16x16x32_bf16 a[120:123],v[216:219],a[56:59],a[120:123]\n"
"v_mfma_f32_16x16x32_bf16 a[124:127],v[216:219],a[60:63],a[124:127]\n"
"s_waitcnt vmcnt(17)\n"
"v_mfma_f32_16x16x32_bf16 v[128:131],v[220:223],a[32:35],v[128:131]\n"
"buffer_load_dwordx4 v[216:219],v9,s[56:59],s61 offen offset:3072 sc1\n"
"v_mfma_f32_16x16x32_bf16 v[132:135],v[220:223],a[36:39],v[132:135]\n"
"v_mfma_f32_16x16x32_bf16 v[136:139],v[220:223],a[40:43],v[136:139]\n"
"v_mfma_f32_16x16x32_bf16 v[140:143],v[220:223],a[44:47],v[140:143]\n"
"v_mfma_f32_16x16x32_bf16 a[128:131],v[220:223],a[48:51],a[128:131]\n"
"v_mfma_f32_16x16x32_bf16 a[132:135],v[220:223],a[52:55],a[132:135]\n"
"v_mfma_f32_16x16x32_bf16 a[136:139],v[220:223],a[56:59],a[136:139]\n"
"v_mfma_f32_16x16x32_bf16 a[140:143],v[220:223],a[60:63],a[140:143]\n"
"s_add_u32 s61,s61,4096\n"
"s_waitcnt vmcnt(15)\n"
"v_mfma_f32_16x16x32_bf16 v[144:147],v[224:227],a[32:35],v[144:147]\n"
"ds_read_b128 a[0:3],v13 offset:36864\n"
"buffer_load_dwordx4 v[220:223],v9,s[56:59],s61 offen offset:0 sc1\n"
"v_mfma_f32_16x16x32_bf16 v[148:151],v[224:227],a[36:39],v[148:151]\n"
"v_mfma_f32_16x16x32_bf16 v[152:155],v[224:227],a[40:43],v[152:155]\n"
"v_mfma_f32_16x16x32_bf16 v[156:159],v[224:227],a[44:47],v[156:159]\n"
"ds_read_b128 a[4:7],v13 offset:37888\n"
"v_mfma_f32_16x16x32_bf16 a[144:147],v[224:227],a[48:51],a[144:147]\n"
"v_mfma_f32_16x16x32_bf16 a[148:151],v[224:227],a[52:55],a[148:151]\n"
"v_mfma_f32_16x16x32_bf16 a[152:155],v[224:227],a[56:59],a[152:155]\n"
"ds_read_b128 a[8:11],v13 offset:38912\n"
"v_mfma_f32_16x16x32_bf16 a[156:159],v[224:227],a[60:63],a[156:159]\n"
"s_waitcnt vmcnt(12)\n"
"v_mfma_f32_16x16x32_bf16 v[160:163],v[228:231],a[32:35],v[160:163]\n"
"buffer_load_dwordx4 v[224:227],v9,s[56:59],s61 offen offset:1024 sc1\n"
"v_mfma_f32_16x16x32_bf16 v[164:167],v[228:231],a[36:39],v[164:167]\n"
"ds_read_b128 a[12:15],v13 offset:39936\n"
"v_mfma_f32_16x16x32_bf16 v[168:171],v[228:231],a[40:43],v[168:171]\n"
"v_mfma_f32_16x16x32_bf16 v[172:175],v[228:231],a[44:47],v[172:175]\n"
"v_mfma_f32_16x16x32_bf16 a[160:163],v[228:231],a[48:51],a[160:163]\n"
"ds_read_b128 a[16:19],v13 offset:40960\n"
"v_mfma_f32_16x16x32_bf16 a[164:167],v[228:231],a[52:55],a[164:167]\n"
"v_mfma_f32_16x16x32_bf16 a[168:171],v[228:231],a[56:59],a[168:171]\n"
"v_mfma_f32_16x16x32_bf16 a[172:175],v[228:231],a[60:63],a[172:175]\n"
"ds_read_b128 a[20:23],v13 offset:41984\n"
"s_waitcnt vmcnt(9)\n"
"v_mfma_f32_16x16x32_bf16 v[176:179],a[208:211],a[32:35],v[176:179]\n"
"buffer_load_dwordx4 v[228:231],v9,s[56:59],s61 offen offset:2048 sc1\n"
"v_mfma_f32_16x16x32_bf16 v[180:183],a[208:211],a[36:39],v[180:183]\n"
"v_mfma_f32_16x16x32_bf16 v[184:187],a[208:211],a[40:43],v[184:187]\n"
"ds_read_b128 a[24:27],v13 offset:43008\n"
"v_mfma_f32_16x16x32_bf16 v[188:191],a[208:211],a[44:47],v[188:191]\n"
"v_mfma_f32_16x16x32_bf16 a[176:179],a[208:211],a[48:51],a[176:179]\n"
"v_mfma_f32_16x16x32_bf16 a[180:183],a[208:211],a[52:55],a[180:183]\n"
"ds_read_b128 a[28:31],v13 offset:44032\n"
"v_mfma_f32_16x16x32_bf16 a[184:187],a[208:211],a[56:59],a[184:187]\n"
"v_mfma_f32_16x16x32_bf16 a[188:191],a[208:211],a[60:63],a[188:191]\n"
"s_waitcnt vmcnt(9)\n"
"v_mfma_f32_16x16x32_bf16 v[192:195],a[212:215],a[32:35],v[192:195]\n"
"buffer_load_dwordx4 a[208:211],v9,s[56:59],s61 offen offset:3072 sc1\n"
"v_mfma_f32_16x16x32_bf16 v[196:199],a[212:215],a[36:39],v[196:199]\n"
"v_mfma_f32_16x16x32_bf16 v[200:203],a[212:215],a[40:43],v[200:203]\n"
"v_mfma_f32_16x16x32_bf16 v[204:207],a[212:215],a[44:47],v[204:207]\n"
"v_mfma_f32_16x16x32_bf16 a[192:195],a[212:215],a[48:51],a[192:195]\n"
"v_mfma_f32_16x16x32_bf16 a[196:199],a[212:215],a[52:55],a[196:199]\n"
"v_mfma_f32_16x16x32_bf16 a[200:203],a[212:215],a[56:59],a[200:203]\n"
"v_mfma_f32_16x16x32_bf16 a[204:207],a[212:215],a[60:63],a[204:207]\n"
"s_add_u32 s61,s61,4096\n"
"s_waitcnt lgkmcnt(0)\n"
"v_mfma_f32_16x16x32_bf16 v[64:67],a[216:219],a[0:3],v[64:67]\n"
"buffer_load_dwordx4 a[212:215],v9,s[56:59],s61 offen offset:0 sc1\n"
"v_mfma_f32_16x16x32_bf16 v[68:71],a[216:219],a[4:7],v[68:71]\n"
"v_mfma_f32_16x16x32_bf16 v[72:75],a[216:219],a[8:11],v[72:75]\n"
"s_barrier\n"
"v_mfma_f32_16x16x32_bf16 v[76:79],a[216:219],a[12:15],v[76:79]\n"
"v_mfma_f32_16x16x32_bf16 a[64:67],a[216:219],a[16:19],a[64:67]\n"
"s_add_u32 m0,s60,36864\n"
"s_add_u32 s87,s86,0\n"
"s_add_u32 s87,s87,s37\n"
"s_sub_u32 s78,s87,18\n"
"s_min_u32 s87,s87,s78\n"
"s_mul_i32 s87,s87,36864\n"
"v_mfma_f32_16x16x32_bf16 a[68:71],a[216:219],a[20:23],a[68:71]\n"
"buffer_load_dwordx4 v246,s[40:43],s87 offen offset:0 lds\n"
"v_mfma_f32_16x16x32_bf16 a[72:75],a[216:219],a[24:27],a[72:75]\n"
"v_mfma_f32_16x16x32_bf16 a[76:79],a[216:219],a[28:31],a[76:79]\n"
"v_mfma_f32_16x16x32_bf16 v[80:83],a[220:223],a[0:3],v[80:83]\n"
"v_mfma_f32_16x16x32_bf16 v[84:87],a[220:223],a[4:7],v[84:87]\n"
"v_mfma_f32_16x16x32_bf16 v[88:91],a[220:223],a[8:11],v[88:91]\n"
"v_mfma_f32_16x16x32_bf16 v[92:95],a[220:223],a[12:15],v[92:95]\n"
"buffer_load_dwordx4 v247,s[40:43],s87 offen offset:1024 lds\n"
"v_mfma_f32_16x16x32_bf16 a[80:83],a[220:223],a[16:19],a[80:83]\n"
"v_mfma_f32_16x16x32_bf16 a[84:87],a[220:223],a[20:23],a[84:87]\n"
"v_mfma_f32_16x16x32_bf16 a[88:91],a[220:223],a[24:27],a[88:91]\n"
"v_mfma_f32_16x16x32_bf16 a[92:95],a[220:223],a[28:31],a[92:95]\n"
"v_mfma_f32_16x16x32_bf16 v[96:99],a[224:227],a[0:3],v[96:99]\n"
"v_mfma_f32_16x16x32_bf16 v[100:103],a[224:227],a[4:7],v[100:103]\n"
"buffer_load_dwordx4 v248,s[40:43],s87 offen offset:2048 lds\n"
"v_mfma_f32_16x16x32_bf16 v[104:107],a[224:227],a[8:11],v[104:107]\n"
"v_mfma_f32_16x16x32_bf16 v[108:111],a[224:227],a[12:15],v[108:111]\n"
"v_mfma_f32_16x16x32_bf16 a[96:99],a[224:227],a[16:19],a[96:99]\n"
"v_mfma_f32_16x16x32_bf16 a[100:103],a[224:227],a[20:23],a[100:103]\n"
"v_mfma_f32_16x16x32_bf16 a[104:107],a[224:227],a[24:27],a[104:107]\n"
"v_mfma_f32_16x16x32_bf16 a[108:111],a[224:227],a[28:31],a[108:111]\n"
"buffer_load_dwordx4 v249,s[40:43],s87 offen offset:3072 lds\n"
"v_mfma_f32_16x16x32_bf16 v[112:115],a[228:231],a[0:3],v[112:115]\n"
"v_mfma_f32_16x16x32_bf16 v[116:119],a[228:231],a[4:7],v[116:119]\n"
"v_mfma_f32_16x16x32_bf16 v[120:123],a[228:231],a[8:11],v[120:123]\n"
"v_mfma_f32_16x16x32_bf16 v[124:127],a[228:231],a[12:15],v[124:127]\n"
"v_mfma_f32_16x16x32_bf16 a[112:115],a[228:231],a[16:19],a[112:115]\n"
"s_add_u32 m0,s60,40960\n"
"v_mfma_f32_16x16x32_bf16 a[116:119],a[228:231],a[20:23],a[116:119]\n"
"buffer_load_dwordx4 v250,s[40:43],s87 offen offset:0 lds\n"
"v_mfma_f32_16x16x32_bf16 a[120:123],a[228:231],a[24:27],a[120:123]\n"
"v_mfma_f32_16x16x32_bf16 a[124:127],a[228:231],a[28:31],a[124:127]\n"
"v_mfma_f32_16x16x32_bf16 v[128:131],a[232:235],a[0:3],v[128:131]\n"
"v_mfma_f32_16x16x32_bf16 v[132:135],a[232:235],a[4:7],v[132:135]\n"
"v_mfma_f32_16x16x32_bf16 v[136:139],a[232:235],a[8:11],v[136:139]\n"
"v_mfma_f32_16x16x32_bf16 v[140:143],a[232:235],a[12:15],v[140:143]\n"
"buffer_load_dwordx4 v251,s[40:43],s87 offen offset:1024 lds\n"
"v_mfma_f32_16x16x32_bf16 a[128:131],a[232:235],a[16:19],a[128:131]\n"
"v_mfma_f32_16x16x32_bf16 a[132:135],a[232:235],a[20:23],a[132:135]\n"
"v_mfma_f32_16x16x32_bf16 a[136:139],a[232:235],a[24:27],a[136:139]\n"
"v_mfma_f32_16x16x32_bf16 a[140:143],a[232:235],a[28:31],a[140:143]\n"
"v_mfma_f32_16x16x32_bf16 v[144:147],a[236:239],a[0:3],v[144:147]\n"
"ds_read_b128 a[32:35],v12 offset:0\n"
"v_mfma_f32_16x16x32_bf16 v[148:151],a[236:239],a[4:7],v[148:151]\n"
"buffer_load_dwordx4 v252,s[40:43],s87 offen offset:2048 lds\n"
"v_mfma_f32_16x16x32_bf16 v[152:155],a[236:239],a[8:11],v[152:155]\n"
"v_mfma_f32_16x16x32_bf16 v[156:159],a[236:239],a[12:15],v[156:159]\n"
"ds_read_b128 a[36:39],v12 offset:1024\n"
"v_mfma_f32_16x16x32_bf16 a[144:147],a[236:239],a[16:19],a[144:147]\n"
"v_mfma_f32_16x16x32_bf16 a[148:151],a[236:239],a[20:23],a[148:151]\n"
"v_mfma_f32_16x16x32_bf16 a[152:155],a[236:239],a[24:27],a[152:155]\n"
"ds_read_b128 a[40:43],v12 offset:2048\n"
"v_mfma_f32_16x16x32_bf16 a[156:159],a[236:239],a[28:31],a[156:159]\n"
"buffer_load_dwordx4 v253,s[40:43],s87 offen offset:3072 lds\n"
"L_pgs352_%=:\n"
"s_cbranch_execz L_hrs_%=\n"
"v_mfma_f32_16x16x32_bf16 v[160:163],a[240:243],a[0:3],v[160:163]\n"
"v_mfma_f32_16x16x32_bf16 v[164:167],a[240:243],a[4:7],v[164:167]\n"
"ds_read_b128 a[44:47],v12 offset:3072\n"
"v_mfma_f32_16x16x32_bf16 v[168:171],a[240:243],a[8:11],v[168:171]\n"
"v_mfma_f32_16x16x32_bf16 v[172:175],a[240:243],a[12:15],v[172:175]\n"
"v_mfma_f32_16x16x32_bf16 a[160:163],a[240:243],a[16:19],a[160:163]\n"
"ds_read_b128 a[48:51],v12 offset:4096\n"
"s_add_u32 m0,s60,45056\n"
"v_mfma_f32_16x16x32_bf16 a[164:167],a[240:243],a[20:23],a[164:167]\n"
"buffer_load_dwordx4 v254,s[40:43],s87 offen offset:0 lds\n"
"v_mfma_f32_16x16x32_bf16 a[168:171],a[240:243],a[24:27],a[168:171]\n"
"v_mfma_f32_16x16x32_bf16 a[172:175],a[240:243],a[28:31],a[172:175]\n"
"ds_read_b128 a[52:55],v12 offset:5120\n"
"s_waitcnt vmcnt(19)\n"
"v_mfma_f32_16x16x32_bf16 v[176:179],a[244:247],a[0:3],v[176:179]\n"
"v_mfma_f32_16x16x32_bf16 v[180:183],a[244:247],a[4:7],v[180:183]\n"
"v_mfma_f32_16x16x32_bf16 v[184:187],a[244:247],a[8:11],v[184:187]\n"
"ds_read_b128 a[56:59],v12 offset:6144\n"
"s_add_u32 m0,s60,73728\n"
"s_add_u32 s87,s86,4\n"
"s_add_u32 s87,s87,s37\n"
"s_sub_u32 s78,s87,18\n"
"s_min_u32 s87,s87,s78\n"
"s_mul_i32 s87,s87,36864\n"
"v_mfma_f32_16x16x32_bf16 v[188:191],a[244:247],a[12:15],v[188:191]\n"
"buffer_load_dwordx4 v246,s[40:43],s87 offen offset:0 lds\n"
"v_mfma_f32_16x16x32_bf16 a[176:179],a[244:247],a[16:19],a[176:179]\n"
"v_mfma_f32_16x16x32_bf16 a[180:183],a[244:247],a[20:23],a[180:183]\n"
"ds_read_b128 a[60:63],v12 offset:7168\n"
"v_mfma_f32_16x16x32_bf16 a[184:187],a[244:247],a[24:27],a[184:187]\n"
"v_mfma_f32_16x16x32_bf16 a[188:191],a[244:247],a[28:31],a[188:191]\n"
"s_waitcnt vmcnt(19)\n"
"v_mfma_f32_16x16x32_bf16 v[192:195],a[248:251],a[0:3],v[192:195]\n"
"v_mfma_f32_16x16x32_bf16 v[196:199],a[248:251],a[4:7],v[196:199]\n"
"buffer_load_dwordx4 v247,s[40:43],s87 offen offset:1024 lds\n"
"v_mfma_f32_16x16x32_bf16 v[200:203],a[248:251],a[8:11],v[200:203]\n"
"v_mfma_f32_16x16x32_bf16 v[204:207],a[248:251],a[12:15],v[204:207]\n"
"v_mfma_f32_16x16x32_bf16 a[192:195],a[248:251],a[16:19],a[192:195]\n"
"v_mfma_f32_16x16x32_bf16 a[196:199],a[248:251],a[20:23],a[196:199]\n"
"v_mfma_f32_16x16x32_bf16 a[200:203],a[248:251],a[24:27],a[200:203]\n"
"v_mfma_f32_16x16x32_bf16 a[204:207],a[248:251],a[28:31],a[204:207]\n"
"buffer_load_dwordx4 v248,s[40:43],s87 offen offset:2048 lds\n"
"s_waitcnt lgkmcnt(0)\n"
"s_waitcnt vmcnt(20)\n"
"v_mfma_f32_16x16x32_bf16 a[72:75],a[252:255],a[56:59],a[72:75]\n"
"v_mfma_f32_16x16x32_bf16 a[76:79],a[252:255],a[60:63],a[76:79]\n"
"s_waitcnt vmcnt(19)\n"
"v_mfma_f32_16x16x32_bf16 a[88:91],v[208:211],a[56:59],a[88:91]\n"
"v_mfma_f32_16x16x32_bf16 a[92:95],v[208:211],a[60:63],a[92:95]\n"
"s_waitcnt vmcnt(18)\n"
"v_mfma_f32_16x16x32_bf16 a[104:107],v[212:215],a[56:59],a[104:107]\n"
"v_mfma_f32_16x16x32_bf16 a[108:111],v[212:215],a[60:63],a[108:111]\n"
"buffer_load_dwordx4 v249,s[40:43],s87 offen offset:3072 lds\n"
"s_waitcnt vmcnt(18)\n"
"v_mfma_f32_16x16x32_bf16 a[120:123],v[216:219],a[56:59],a[120:123]\n"
"v_mfma_f32_16x16x32_bf16 a[124:127],v[216:219],a[60:63],a[124:127]\n"
"ds_write_b128 v13,a[72:75] offset:36864\n"
"s_waitcnt vmcnt(17)\n"
"v_mfma_f32_16x16x32_bf16 a[136:139],v[220:223],a[56:59],a[136:139]\n"
"v_mfma_f32_16x16x32_bf16 a[140:143],v[220:223],a[60:63],a[140:143]\n"
"s_waitcnt vmcnt(16)\n"
"v_mfma_f32_16x16x32_bf16 a[152:155],v[224:227],a[56:59],a[152:155]\n"
"ds_write_b128 v13,a[76:79] offset:37888\n"
"s_add_u32 m0,s60,77824\n"
"v_mfma_f32_16x16x32_bf16 a[156:159],v[224:227],a[60:63],a[156:159]\n"
"buffer_load_dwordx4 v250,s[40:43],s87 offen offset:0 lds\n"
"s_waitcnt vmcnt(16)\n"
"v_mfma_f32_16x16x32_bf16 a[168:171],v[228:231],a[56:59],a[168:171]\n"
"v_mfma_f32_16x16x32_bf16 a[172:175],v[228:231],a[60:63],a[172:175]\n"
"ds_write_b128 v13,a[88:91] offset:38912\n"
"s_waitcnt vmcnt(15)\n"
"v_mfma_f32_16x16x32_bf16 a[184:187],a[208:211],a[56:59],a[184:187]\n"
"v_mfma_f32_16x16x32_bf16 a[188:191],a[208:211],a[60:63],a[188:191]\n"
"s_waitcnt vmcnt(14)\n"
"v_mfma_f32_16x16x32_bf16 a[200:203],a[212:215],a[56:59],a[200:203]\n"
"ds_write_b128 v13,a[92:95] offset:39936\n"
"v_mfma_f32_16x16x32_bf16 a[204:207],a[212:215],a[60:63],a[204:207]\n"
"buffer_load_dwordx4 v251,s[40:43],s87 offen offset:1024 lds\n"
"v_mfma_f32_16x16x32_bf16 v[64:67],a[252:255],a[32:35],v[64:67]\n"
"v_mfma_f32_16x16x32_bf16 v[68:71],a[252:255],a[36:39],v[68:71]\n"
"ds_write_b128 v13,a[104:107] offset:40960\n"
"v_mfma_f32_16x16x32_bf16 v[80:83],v[208:211],a[32:35],v[80:83]\n"
"v_mfma_f32_16x16x32_bf16 v[84:87],v[208:211],a[36:39],v[84:87]\n"
"v_mfma_f32_16x16x32_bf16 v[96:99],v[212:215],a[32:35],v[96:99]\n"
"ds_write_b128 v13,a[108:111] offset:41984\n"
"v_mfma_f32_16x16x32_bf16 v[100:103],v[212:215],a[36:39],v[100:103]\n"
"buffer_load_dwordx4 v252,s[40:43],s87 offen offset:2048 lds\n"
"v_mfma_f32_16x16x32_bf16 v[112:115],v[216:219],a[32:35],v[112:115]\n"
"v_mfma_f32_16x16x32_bf16 v[116:119],v[216:219],a[36:39],v[116:119]\n"
"ds_write_b128 v13,a[120:123] offset:43008\n"
"v_mfma_f32_16x16x32_bf16 v[128:131],v[220:223],a[32:35],v[128:131]\n"
"v_mfma_f32_16x16x32_bf16 v[132:135],v[220:223],a[36:39],v[132:135]\n"
"v_mfma_f32_16x16x32_bf16 v[144:147],v[224:227],a[32:35],v[144:147]\n"
"ds_write_b128 v13,a[124:127] offset:44032\n"
"v_mfma_f32_16x16x32_bf16 v[148:151],v[224:227],a[36:39],v[148:151]\n"
"buffer_load_dwordx4 v253,s[40:43],s87 offen offset:3072 lds\n"
"v_mfma_f32_16x16x32_bf16 v[160:163],v[228:231],a[32:35],v[160:163]\n"
"v_mfma_f32_16x16x32_bf16 v[164:167],v[228:231],a[36:39],v[164:167]\n"
"ds_write_b128 v12,a[136:139] offset:0\n"
"v_mfma_f32_16x16x32_bf16 v[176:179],a[208:211],a[32:35],v[176:179]\n"
"v_mfma_f32_16x16x32_bf16 v[180:183],a[208:211],a[36:39],v[180:183]\n"
"v_mfma_f32_16x16x32_bf16 v[192:195],a[212:215],a[32:35],v[192:195]\n"
"ds_write_b128 v12,a[140:143] offset:1024\n"
"s_add_u32 m0,s60,81920\n"
"v_mfma_f32_16x16x32_bf16 v[196:199],a[212:215],a[36:39],v[196:199]\n"
"buffer_load_dwordx4 v254,s[40:43],s87 offen offset:0 lds\n"
"v_mfma_f32_16x16x32_bf16 a[64:67],a[252:255],a[48:51],a[64:67]\n"
"v_mfma_f32_16x16x32_bf16 a[68:71],a[252:255],a[52:55],a[68:71]\n"
"ds_write_b128 v12,a[152:155] offset:2048\n"
"v_mfma_f32_16x16x32_bf16 a[80:83],v[208:211],a[48:51],a[80:83]\n"
"v_mfma_f32_16x16x32_bf16 a[84:87],v[208:211],a[52:55],a[84:87]\n"
"v_mfma_f32_16x16x32_bf16 a[96:99],v[212:215],a[48:51],a[96:99]\n"
"ds_write_b128 v12,a[156:159] offset:3072\n"
"v_mfma_f32_16x16x32_bf16 a[100:103],v[212:215],a[52:55],a[100:103]\n"
"v_mfma_f32_16x16x32_bf16 a[112:115],v[216:219],a[48:51],a[112:115]\n"
"v_mfma_f32_16x16x32_bf16 a[116:119],v[216:219],a[52:55],a[116:119]\n"
"ds_write_b128 v12,a[168:171] offset:4096\n"
"v_mfma_f32_16x16x32_bf16 a[128:131],v[220:223],a[48:51],a[128:131]\n"
"v_mfma_f32_16x16x32_bf16 a[132:135],v[220:223],a[52:55],a[132:135]\n"
"v_mfma_f32_16x16x32_bf16 a[144:147],v[224:227],a[48:51],a[144:147]\n"
"ds_write_b128 v12,a[172:175] offset:5120\n"
"v_mfma_f32_16x16x32_bf16 a[148:151],v[224:227],a[52:55],a[148:151]\n"
"v_mfma_f32_16x16x32_bf16 a[160:163],v[228:231],a[48:51],a[160:163]\n"
"v_mfma_f32_16x16x32_bf16 a[164:167],v[228:231],a[52:55],a[164:167]\n"
"ds_write_b128 v12,a[184:187] offset:6144\n"
"v_mfma_f32_16x16x32_bf16 a[176:179],a[208:211],a[48:51],a[176:179]\n"
"v_mfma_f32_16x16x32_bf16 a[180:183],a[208:211],a[52:55],a[180:183]\n"
"v_mfma_f32_16x16x32_bf16 a[192:195],a[212:215],a[48:51],a[192:195]\n"
"ds_write_b128 v12,a[188:191] offset:7168\n"
"v_mfma_f32_16x16x32_bf16 a[196:199],a[212:215],a[52:55],a[196:199]\n"
"v_mfma_f32_16x16x32_bf16 v[72:75],a[252:255],a[40:43],v[72:75]\n"
"v_mfma_f32_16x16x32_bf16 v[76:79],a[252:255],a[44:47],v[76:79]\n"
"ds_write_b128 v14,a[200:203] offset:0\n"
"v_mfma_f32_16x16x32_bf16 v[88:91],v[208:211],a[40:43],v[88:91]\n"
"v_mfma_f32_16x16x32_bf16 v[92:95],v[208:211],a[44:47],v[92:95]\n"
"v_mfma_f32_16x16x32_bf16 v[104:107],v[212:215],a[40:43],v[104:107]\n"
"ds_write_b128 v14,a[204:207] offset:1024\n"
"v_mfma_f32_16x16x32_bf16 v[108:111],v[212:215],a[44:47],v[108:111]\n"
"v_mfma_f32_16x16x32_bf16 v[120:123],v[216:219],a[40:43],v[120:123]\n"
"v_mfma_f32_16x16x32_bf16 v[124:127],v[216:219],a[44:47],v[124:127]\n"
"v_mfma_f32_16x16x32_bf16 v[136:139],v[220:223],a[40:43],v[136:139]\n"
"v_mfma_f32_16x16x32_bf16 v[140:143],v[220:223],a[44:47],v[140:143]\n"
"v_mfma_f32_16x16x32_bf16 v[152:155],v[224:227],a[40:43],v[152:155]\n"
"v_mfma_f32_16x16x32_bf16 v[156:159],v[224:227],a[44:47],v[156:159]\n"
"v_mfma_f32_16x16x32_bf16 v[168:171],v[228:231],a[40:43],v[168:171]\n"
"v_mfma_f32_16x16x32_bf16 v[172:175],v[228:231],a[44:47],v[172:175]\n"
"v_mfma_f32_16x16x32_bf16 v[184:187],a[208:211],a[40:43],v[184:187]\n"
"v_mfma_f32_16x16x32_bf16 v[188:191],a[208:211],a[44:47],v[188:191]\n"
"v_mfma_f32_16x16x32_bf16 v[200:203],a[212:215],a[40:43],v[200:203]\n"
"v_mfma_f32_16x16x32_bf16 v[204:207],a[212:215],a[44:47],v[204:207]\n"
"s_waitcnt lgkmcnt(0)\n"
"s_barrier\n"
"ds_read_b128 v[208:211],v235 offset:36864\n"
"ds_read_b128 v[212:215],v235 offset:37888\n"
"ds_read_b128 v[216:219],v235 offset:38912\n"
"ds_read_b128 v[220:223],v235 offset:39936\n"
"ds_read_b128 v[224:227],v235 offset:40960\n"
"ds_read_b128 v[52:55],v235 offset:41984\n"
"ds_read_b128 v[56:59],v235 offset:43008\n"
"ds_read_b128 v[60:63],v235 offset:44032\n"
"s_waitcnt lgkmcnt(7)\n"
"v_pk_add_f32 v[64:65],v[64:65],v[208:209]\n"
"v_pk_add_f32 v[66:67],v[66:67],v[210:211]\n"
"ds_write_b128 v235,a[64:67] offset:36864\n"
"ds_read_b128 v[208:211],v234 offset:0\n"
"s_waitcnt lgkmcnt(8)\n"
"v_pk_add_f32 v[68:69],v[68:69],v[212:213]\n"
"v_pk_add_f32 v[70:71],v[70:71],v[214:215]\n"
"ds_write_b128 v235,a[68:71] offset:37888\n"
"ds_read_b128 v[212:215],v234 offset:1024\n"
"s_waitcnt lgkmcnt(9)\n"
"v_pk_add_f32 v[80:81],v[80:81],v[216:217]\n"
"v_pk_add_f32 v[82:83],v[82:83],v[218:219]\n"
"ds_write_b128 v235,a[80:83] offset:38912\n"
"ds_read_b128 v[216:219],v234 offset:2048\n"
"s_waitcnt lgkmcnt(10)\n"
"v_pk_add_f32 v[84:85],v[84:85],v[220:221]\n"
"v_pk_add_f32 v[86:87],v[86:87],v[222:223]\n"
"ds_write_b128 v235,a[84:87] offset:39936\n"
"s_add_u32 s76,s70,4096\n"
"buffer_load_dwordx4 a[228:231],v9,s[88:91],s70 offen offset:0\n"
"ds_read_b128 v[220:223],v234 offset:3072\n"
"s_waitcnt lgkmcnt(11)\n"
"v_pk_add_f32 v[96:97],v[96:97],v[224:225]\n"
"v_pk_add_f32 v[98:99],v[98:99],v[226:227]\n"
"ds_write_b128 v235,a[96:99] offset:40960\n"
"ds_read_b128 v[224:227],v234 offset:4096\n"
"s_waitcnt lgkmcnt(12)\n"
"v_pk_add_f32 v[100:101],v[100:101],v[52:53]\n"
"v_pk_add_f32 v[102:103],v[102:103],v[54:55]\n"
"ds_write_b128 v235,a[100:103] offset:41984\n"
"ds_read_b128 v[52:55],v234 offset:5120\n"
"s_waitcnt lgkmcnt(13)\n"
"v_pk_add_f32 v[112:113],v[112:113],v[56:57]\n"
"v_pk_add_f32 v[114:115],v[114:115],v[58:59]\n"
"ds_write_b128 v235,a[112:115] offset:43008\n"
"buffer_load_dwordx4 a[232:235],v9,s[88:91],s70 offen offset:1024\n"
"ds_read_b128 v[56:59],v234 offset:6144\n"
"s_waitcnt lgkmcnt(14)\n"
"v_pk_add_f32 v[116:117],v[116:117],v[60:61]\n"
"v_pk_add_f32 v[118:119],v[118:119],v[62:63]\n"
"ds_write_b128 v235,a[116:119] offset:44032\n"
"ds_read_b128 v[60:63],v234 offset:7168\n"
"s_waitcnt lgkmcnt(14)\n"
"v_pk_add_f32 v[128:129],v[128:129],v[208:209]\n"
"v_pk_add_f32 v[130:131],v[130:131],v[210:211]\n"
"ds_write_b128 v234,a[128:131] offset:0\n"
"ds_read_b128 v[208:211],v236 offset:0\n"
"s_waitcnt lgkmcnt(14)\n"
"v_pk_add_f32 v[132:133],v[132:133],v[212:213]\n"
"v_pk_add_f32 v[134:135],v[134:135],v[214:215]\n"
"ds_write_b128 v234,a[132:135] offset:1024\n"
"buffer_load_dwordx4 a[236:239],v9,s[88:91],s70 offen offset:2048\n"
"ds_read_b128 v[212:215],v236 offset:1024\n"
"s_waitcnt lgkmcnt(14)\n"
"v_pk_add_f32 v[144:145],v[144:145],v[216:217]\n"
"v_pk_add_f32 v[146:147],v[146:147],v[218:219]\n"
"ds_write_b128 v234,a[144:147] offset:2048\n"
"s_waitcnt lgkmcnt(13)\n"
"v_pk_add_f32 v[148:149],v[148:149],v[220:221]\n"
"v_pk_add_f32 v[150:151],v[150:151],v[222:223]\n"
"ds_write_b128 v234,a[148:151] offset:3072\n"
"s_waitcnt lgkmcnt(12)\n"
"v_pk_add_f32 v[160:161],v[160:161],v[224:225]\n"
"v_pk_add_f32 v[162:163],v[162:163],v[226:227]\n"
"ds_write_b128 v234,a[160:163] offset:4096\n"
"buffer_load_dwordx4 a[240:243],v9,s[88:91],s70 offen offset:3072\n"
"s_waitcnt lgkmcnt(11)\n"
"v_pk_add_f32 v[164:165],v[164:165],v[52:53]\n"
"v_pk_add_f32 v[166:167],v[166:167],v[54:55]\n"
"ds_write_b128 v234,a[164:167] offset:5120\n"
"s_waitcnt lgkmcnt(10)\n"
"v_pk_add_f32 v[176:177],v[176:177],v[56:57]\n"
"v_pk_add_f32 v[178:179],v[178:179],v[58:59]\n"
"ds_write_b128 v234,a[176:179] offset:6144\n"
"s_waitcnt lgkmcnt(9)\n"
"v_pk_add_f32 v[180:181],v[180:181],v[60:61]\n"
"v_pk_add_f32 v[182:183],v[182:183],v[62:63]\n"
"ds_write_b128 v234,a[180:183] offset:7168\n"
"buffer_load_dwordx4 a[244:247],v9,s[88:91],s76 offen offset:0\n"
"s_waitcnt lgkmcnt(8)\n"
"v_pk_add_f32 v[192:193],v[192:193],v[208:209]\n"
"v_pk_add_f32 v[194:195],v[194:195],v[210:211]\n"
"ds_write_b128 v236,a[192:195] offset:0\n"
"s_waitcnt lgkmcnt(7)\n"
"v_pk_add_f32 v[196:197],v[196:197],v[212:213]\n"
"v_pk_add_f32 v[198:199],v[198:199],v[214:215]\n"
"ds_write_b128 v236,a[196:199] offset:1024\n"
"s_waitcnt lgkmcnt(0)\n"
"s_barrier\n"
"ds_read_b128 v[208:211],v238 offset:36864\n"
"ds_read_b128 v[212:215],v238 offset:37888\n"
"ds_read_b128 v[216:219],v238 offset:38912\n"
"ds_read_b128 v[220:223],v238 offset:39936\n"
"ds_read_b128 v[224:227],v238 offset:40960\n"
"ds_read_b128 v[52:55],v238 offset:41984\n"
"ds_read_b128 v[56:59],v238 offset:43008\n"
"ds_read_b128 v[60:63],v238 offset:44032\n"
"s_waitcnt lgkmcnt(7)\n"
"v_pk_add_f32 v[64:65],v[64:65],v[208:209]\n"
"v_pk_add_f32 v[66:67],v[66:67],v[210:211]\n"
"ds_write_b128 v238,v[72:75] offset:36864\n"
"s_mov_b32 s77,7351552\n"
"s_mul_i32 s78,s37,576\n"
"s_add_u32 s77,s77,s78\n"
"buffer_load_dwordx4 a[208:211],v244,s[88:91],s77 offen offset:0 sc1\n"
"ds_read_b128 v[208:211],v237 offset:0\n"
"s_waitcnt lgkmcnt(8)\n"
"v_pk_add_f32 v[68:69],v[68:69],v[212:213]\n"
"v_pk_add_f32 v[70:71],v[70:71],v[214:215]\n"
"ds_write_b128 v238,v[76:79] offset:37888\n"
"ds_read_b128 v[212:215],v237 offset:1024\n"
"s_waitcnt lgkmcnt(9)\n"
"v_pk_add_f32 v[80:81],v[80:81],v[216:217]\n"
"v_pk_add_f32 v[82:83],v[82:83],v[218:219]\n"
"ds_write_b128 v238,v[88:91] offset:38912\n"
"ds_read_b128 v[216:219],v237 offset:2048\n"
"s_waitcnt lgkmcnt(10)\n"
"v_pk_add_f32 v[84:85],v[84:85],v[220:221]\n"
"v_pk_add_f32 v[86:87],v[86:87],v[222:223]\n"
"ds_write_b128 v238,v[92:95] offset:39936\n"
"buffer_load_dwordx4 a[212:215],v244,s[88:91],s77 offen offset:64 sc1\n"
"ds_read_b128 v[220:223],v237 offset:3072\n"
"s_waitcnt lgkmcnt(11)\n"
"v_pk_add_f32 v[96:97],v[96:97],v[224:225]\n"
"v_pk_add_f32 v[98:99],v[98:99],v[226:227]\n"
"ds_write_b128 v238,v[104:107] offset:40960\n"
"ds_read_b128 v[224:227],v237 offset:4096\n"
"s_waitcnt lgkmcnt(12)\n"
"v_pk_add_f32 v[100:101],v[100:101],v[52:53]\n"
"v_pk_add_f32 v[102:103],v[102:103],v[54:55]\n"
"ds_write_b128 v238,v[108:111] offset:41984\n"
"ds_read_b128 v[52:55],v237 offset:5120\n"
"s_waitcnt lgkmcnt(13)\n"
"v_pk_add_f32 v[112:113],v[112:113],v[56:57]\n"
"v_pk_add_f32 v[114:115],v[114:115],v[58:59]\n"
"ds_write_b128 v238,v[120:123] offset:43008\n"
"buffer_load_dwordx4 a[216:219],v244,s[88:91],s77 offen offset:256 sc1\n"
"ds_read_b128 v[56:59],v237 offset:6144\n"
"s_waitcnt lgkmcnt(14)\n"
"v_pk_add_f32 v[116:117],v[116:117],v[60:61]\n"
"v_pk_add_f32 v[118:119],v[118:119],v[62:63]\n"
"ds_write_b128 v238,v[124:127] offset:44032\n"
"ds_read_b128 v[60:63],v237 offset:7168\n"
"s_waitcnt lgkmcnt(14)\n"
"v_pk_add_f32 v[128:129],v[128:129],v[208:209]\n"
"v_pk_add_f32 v[130:131],v[130:131],v[210:211]\n"
"ds_write_b128 v237,v[136:139] offset:0\n"
"ds_read_b128 v[208:211],v239 offset:0\n"
"s_waitcnt lgkmcnt(14)\n"
"v_pk_add_f32 v[132:133],v[132:133],v[212:213]\n"
"v_pk_add_f32 v[134:135],v[134:135],v[214:215]\n"
"ds_write_b128 v237,v[140:143] offset:1024\n"
"buffer_load_dwordx4 a[220:223],v244,s[88:91],s77 offen offset:320 sc1\n"
"ds_read_b128 v[212:215],v239 offset:1024\n"
"s_waitcnt lgkmcnt(14)\n"
"v_pk_add_f32 v[144:145],v[144:145],v[216:217]\n"
"v_pk_add_f32 v[146:147],v[146:147],v[218:219]\n"
"ds_write_b128 v237,v[152:155] offset:2048\n"
"s_waitcnt lgkmcnt(13)\n"
"v_pk_add_f32 v[148:149],v[148:149],v[220:221]\n"
"v_pk_add_f32 v[150:151],v[150:151],v[222:223]\n"
"ds_write_b128 v237,v[156:159] offset:3072\n"
"s_waitcnt lgkmcnt(12)\n"
"v_pk_add_f32 v[160:161],v[160:161],v[224:225]\n"
"v_pk_add_f32 v[162:163],v[162:163],v[226:227]\n"
"ds_write_b128 v237,v[168:171] offset:4096\n"
"buffer_load_dwordx4 a[224:227],v245,s[88:91],s77 offen offset:0 sc1\n"
"s_waitcnt lgkmcnt(11)\n"
"v_pk_add_f32 v[164:165],v[164:165],v[52:53]\n"
"v_pk_add_f32 v[166:167],v[166:167],v[54:55]\n"
"ds_write_b128 v237,v[172:175] offset:5120\n"
"s_waitcnt lgkmcnt(10)\n"
"v_pk_add_f32 v[176:177],v[176:177],v[56:57]\n"
"v_pk_add_f32 v[178:179],v[178:179],v[58:59]\n"
"ds_write_b128 v237,v[184:187] offset:6144\n"
"s_waitcnt lgkmcnt(9)\n"
"v_pk_add_f32 v[180:181],v[180:181],v[60:61]\n"
"v_pk_add_f32 v[182:183],v[182:183],v[62:63]\n"
"ds_write_b128 v237,v[188:191] offset:7168\n"
"s_waitcnt lgkmcnt(8)\n"
"v_pk_add_f32 v[192:193],v[192:193],v[208:209]\n"
"v_pk_add_f32 v[194:195],v[194:195],v[210:211]\n"
"ds_write_b128 v239,v[200:203] offset:0\n"
"s_waitcnt lgkmcnt(7)\n"
"v_pk_add_f32 v[196:197],v[196:197],v[212:213]\n"
"v_pk_add_f32 v[198:199],v[198:199],v[214:215]\n"
"ds_write_b128 v239,v[204:207] offset:1024\n"
"s_waitcnt vmcnt(19) lgkmcnt(0)\n"
"s_barrier\n"
"ds_read_b128 v[208:211],v14 offset:0\n"
"ds_read_b128 v[212:215],v14 offset:1024\n"
"ds_read_b128 v[216:219],v13 offset:36864\n"
"ds_read_b128 v[220:223],v13 offset:37888\n"
"ds_read_b128 v[224:227],v13 offset:38912\n"
"ds_read_b128 v[52:55],v13 offset:39936\n"
"ds_read_b128 v[56:59],v13 offset:40960\n"
"ds_read_b128 v[60:63],v13 offset:41984\n"
"v_lshlrev_b32 v232,16,v232\n"
"v_lshlrev_b32 v233,16,v233\n"
"s_lshl_b32 s75,s36,2\n"
"s_add_u32 s75,s75,s35\n"
"s_mul_i32 s75,s75,9\n"
"s_add_u32 s75,s75,1\n"
"s_lshr_b32 s75,s75,1\n"
"s_mov_b32 s77,47185920\n"
"s_mul_i32 s78,s34,589824\n"
"s_add_u32 s77,s77,s78\n"
"s_mul_i32 s78,s37,147456\n"
"s_add_u32 s77,s77,s78\n"
"s_mul_i32 s78,s75,1024\n"
"s_add_u32 s77,s77,s78\n"
"s_add_u32 s72,s77,73728\n"
"s_mul_i32 s75,s98,-2556\n"
"s_add_u32 s75,s75,4096\n"
"s_add_u32 s75,s75,s77\n"
"s_add_u32 s76,s75,73728\n"
"s_waitcnt lgkmcnt(7)\n"
"v_pk_add_f32 v[192:193],v[192:193],v[208:209]\n"
"v_pk_add_f32 v[194:195],v[194:195],v[210:211]\n"
"v_lshlrev_b32 v200,16,v48\n"
"v_lshlrev_b32 v202,16,v49\n"
"v_and_b32 v201,s62,v48\n"
"v_and_b32 v203,s62,v49\n"
"v_pk_add_f32 v[192:193],v[192:193],v[232:233] op_sel_hi:[1,0]\n"
"v_pk_add_f32 v[194:195],v[194:195],v[232:233] op_sel_hi:[1,0]\n"
"v_pk_mul_f32 v[192:193],v[192:193],v[200:201]\n"
"v_pk_mul_f32 v[194:195],v[194:195],v[202:203]\n"
"v_cvt_pk_bf16_f32 v48,v192,v193\n"
"v_cvt_pk_bf16_f32 v49,v194,v195\n"
"buffer_store_dwordx2 v[48:49],v9,s[88:91],s75 offen sc1\n"
"ds_read_b128 v[208:211],v13 offset:43008\n"
"s_waitcnt lgkmcnt(7)\n"
"v_pk_add_f32 v[196:197],v[196:197],v[212:213]\n"
"v_pk_add_f32 v[198:199],v[198:199],v[214:215]\n"
"v_lshlrev_b32 v204,16,v50\n"
"v_lshlrev_b32 v206,16,v51\n"
"v_and_b32 v205,s62,v50\n"
"v_and_b32 v207,s62,v51\n"
"v_pk_add_f32 v[196:197],v[196:197],v[232:233] op_sel:[0,1]\n"
"v_pk_add_f32 v[198:199],v[198:199],v[232:233] op_sel:[0,1]\n"
"v_pk_mul_f32 v[196:197],v[196:197],v[204:205]\n"
"v_pk_mul_f32 v[198:199],v[198:199],v[206:207]\n"
"v_cvt_pk_bf16_f32 v50,v196,v197\n"
"v_cvt_pk_bf16_f32 v51,v198,v199\n"
"buffer_store_dwordx2 v[50:51],v9,s[88:91],s76 offen sc1\n"
"ds_read_b128 v[212:215],v13 offset:44032\n"
"s_waitcnt lgkmcnt(7)\n"
"v_pk_add_f32 v[64:65],v[64:65],v[216:217]\n"
"v_pk_add_f32 v[66:67],v[66:67],v[218:219]\n"
"v_lshlrev_b32 v72,16,v16\n"
"v_lshlrev_b32 v74,16,v17\n"
"v_and_b32 v73,s62,v16\n"
"v_and_b32 v75,s62,v17\n"
"v_pk_add_f32 v[64:65],v[64:65],v[232:233] op_sel_hi:[1,0]\n"
"v_pk_add_f32 v[66:67],v[66:67],v[232:233] op_sel_hi:[1,0]\n"
"v_pk_mul_f32 v[64:65],v[64:65],v[72:73]\n"
"v_pk_mul_f32 v[66:67],v[66:67],v[74:75]\n"
"v_cvt_pk_bf16_f32 v16,v64,v65\n"
"v_cvt_pk_bf16_f32 v17,v66,v67\n"
"ds_read_b128 v[216:219],v12 offset:0\n"
"s_waitcnt lgkmcnt(7)\n"
"v_pk_add_f32 v[68:69],v[68:69],v[220:221]\n"
"v_pk_add_f32 v[70:71],v[70:71],v[222:223]\n"
"v_lshlrev_b32 v76,16,v20\n"
"v_lshlrev_b32 v78,16,v21\n"
"v_and_b32 v77,s62,v20\n"
"v_and_b32 v79,s62,v21\n"
"v_pk_add_f32 v[68:69],v[68:69],v[232:233] op_sel:[0,1]\n"
"v_pk_add_f32 v[70:71],v[70:71],v[232:233] op_sel:[0,1]\n"
"v_pk_mul_f32 v[68:69],v[68:69],v[76:77]\n"
"v_pk_mul_f32 v[70:71],v[70:71],v[78:79]\n"
"v_cvt_pk_bf16_f32 v20,v68,v69\n"
"v_cvt_pk_bf16_f32 v21,v70,v71\n"
"ds_read_b128 v[220:223],v12 offset:1024\n"
"s_waitcnt lgkmcnt(7)\n"
"v_pk_add_f32 v[80:81],v[80:81],v[224:225]\n"
"v_pk_add_f32 v[82:83],v[82:83],v[226:227]\n"
"v_lshlrev_b32 v88,16,v18\n"
"v_lshlrev_b32 v90,16,v19\n"
"v_and_b32 v89,s62,v18\n"
"v_and_b32 v91,s62,v19\n"
"v_pk_add_f32 v[80:81],v[80:81],v[232:233] op_sel_hi:[1,0]\n"
"v_pk_add_f32 v[82:83],v[82:83],v[232:233] op_sel_hi:[1,0]\n"
"v_pk_mul_f32 v[80:81],v[80:81],v[88:89]\n"
"v_pk_mul_f32 v[82:83],v[82:83],v[90:91]\n"
"v_cvt_pk_bf16_f32 v18,v80,v81\n"
"v_cvt_pk_bf16_f32 v19,v82,v83\n"
"buffer_store_dwordx4 v[16:19],v9,s[88:91],s77 offen offset:0 sc1\n"
"ds_read_b128 v[224:227],v12 offset:2048\n"
"s_waitcnt lgkmcnt(7)\n"
"v_pk_add_f32 v[84:85],v[84:85],v[52:53]\n"
"v_pk_add_f32 v[86:87],v[86:87],v[54:55]\n"
"v_lshlrev_b32 v92,16,v22\n"
"v_lshlrev_b32 v94,16,v23\n"
"v_and_b32 v93,s62,v22\n"
"v_and_b32 v95,s62,v23\n"
"v_pk_add_f32 v[84:85],v[84:85],v[232:233] op_sel:[0,1]\n"
"v_pk_add_f32 v[86:87],v[86:87],v[232:233] op_sel:[0,1]\n"
"v_pk_mul_f32 v[84:85],v[84:85],v[92:93]\n"
"v_pk_mul_f32 v[86:87],v[86:87],v[94:95]\n"
"v_cvt_pk_bf16_f32 v22,v84,v85\n"
"v_cvt_pk_bf16_f32 v23,v86,v87\n"
"buffer_store_dwordx4 v[20:23],v9,s[88:91],s72 offen offset:0 sc1\n"
"ds_read_b128 v[52:55],v12 offset:3072\n"
"s_waitcnt lgkmcnt(7)\n"
"v_pk_add_f32 v[96:97],v[96:97],v[56:57]\n"
"v_pk_add_f32 v[98:99],v[98:99],v[58:59]\n"
"v_lshlrev_b32 v104,16,v24\n"
"v_lshlrev_b32 v106,16,v25\n"
"v_and_b32 v105,s62,v24\n"
"v_and_b32 v107,s62,v25\n"
"v_pk_add_f32 v[96:97],v[96:97],v[232:233] op_sel_hi:[1,0]\n"
"v_pk_add_f32 v[98:99],v[98:99],v[232:233] op_sel_hi:[1,0]\n"
"v_pk_mul_f32 v[96:97],v[96:97],v[104:105]\n"
"v_pk_mul_f32 v[98:99],v[98:99],v[106:107]\n"
"v_cvt_pk_bf16_f32 v24,v96,v97\n"
"v_cvt_pk_bf16_f32 v25,v98,v99\n"
"ds_read_b128 v[56:59],v12 offset:4096\n"
"s_waitcnt lgkmcnt(7)\n"
"v_pk_add_f32 v[100:101],v[100:101],v[60:61]\n"
"v_pk_add_f32 v[102:103],v[102:103],v[62:63]\n"
"v_lshlrev_b32 v108,16,v28\n"
"v_lshlrev_b32 v110,16,v29\n"
"v_and_b32 v109,s62,v28\n"
"v_and_b32 v111,s62,v29\n"
"v_pk_add_f32 v[100:101],v[100:101],v[232:233] op_sel:[0,1]\n"
"v_pk_add_f32 v[102:103],v[102:103],v[232:233] op_sel:[0,1]\n"
"v_pk_mul_f32 v[100:101],v[100:101],v[108:109]\n"
"v_pk_mul_f32 v[102:103],v[102:103],v[110:111]\n"
"v_cvt_pk_bf16_f32 v28,v100,v101\n"
"v_cvt_pk_bf16_f32 v29,v102,v103\n"
"ds_read_b128 v[60:63],v12 offset:5120\n"
"s_waitcnt lgkmcnt(7)\n"
"v_pk_add_f32 v[112:113],v[112:113],v[208:209]\n"
"v_pk_add_f32 v[114:115],v[114:115],v[210:211]\n"
"v_lshlrev_b32 v120,16,v26\n"
"v_lshlrev_b32 v122,16,v27\n"
"v_and_b32 v121,s62,v26\n"
"v_and_b32 v123,s62,v27\n"
"v_pk_add_f32 v[112:113],v[112:113],v[232:233] op_sel_hi:[1,0]\n"
"v_pk_add_f32 v[114:115],v[114:115],v[232:233] op_sel_hi:[1,0]\n"
"v_pk_mul_f32 v[112:113],v[112:113],v[120:121]\n"
"v_pk_mul_f32 v[114:115],v[114:115],v[122:123]\n"
"v_cvt_pk_bf16_f32 v26,v112,v113\n"
"v_cvt_pk_bf16_f32 v27,v114,v115\n"
"buffer_store_dwordx4 v[24:27],v9,s[88:91],s77 offen offset:1024 sc1\n"
"ds_read_b128 v[208:211],v12 offset:6144\n"
"s_waitcnt lgkmcnt(7)\n"
"v_pk_add_f32 v[116:117],v[116:117],v[212:213]\n"
"v_pk_add_f32 v[118:119],v[118:119],v[214:215]\n"
"v_lshlrev_b32 v124,16,v30\n"
"v_lshlrev_b32 v126,16,v31\n"
"v_and_b32 v125,s62,v30\n"
"v_and_b32 v127,s62,v31\n"
"v_pk_add_f32 v[116:117],v[116:117],v[232:233] op_sel:[0,1]\n"
"v_pk_add_f32 v[118:119],v[118:119],v[232:233] op_sel:[0,1]\n"
"v_pk_mul_f32 v[116:117],v[116:117],v[124:125]\n"
"v_pk_mul_f32 v[118:119],v[118:119],v[126:127]\n"
"v_cvt_pk_bf16_f32 v30,v116,v117\n"
"v_cvt_pk_bf16_f32 v31,v118,v119\n"
"buffer_store_dwordx4 v[28:31],v9,s[88:91],s72 offen offset:1024 sc1\n"
"ds_read_b128 v[212:215],v12 offset:7168\n"
"s_waitcnt lgkmcnt(7)\n"
"v_pk_add_f32 v[128:129],v[128:129],v[216:217]\n"
"v_pk_add_f32 v[130:131],v[130:131],v[218:219]\n"
"v_lshlrev_b32 v136,16,v32\n"
"v_lshlrev_b32 v138,16,v33\n"
"v_and_b32 v137,s62,v32\n"
"v_and_b32 v139,s62,v33\n"
"v_pk_add_f32 v[128:129],v[128:129],v[232:233] op_sel_hi:[1,0]\n"
"v_pk_add_f32 v[130:131],v[130:131],v[232:233] op_sel_hi:[1,0]\n"
"v_pk_mul_f32 v[128:129],v[128:129],v[136:137]\n"
"v_pk_mul_f32 v[130:131],v[130:131],v[138:139]\n"
"v_cvt_pk_bf16_f32 v32,v128,v129\n"
"v_cvt_pk_bf16_f32 v33,v130,v131\n"
"s_waitcnt lgkmcnt(6)\n"
"v_pk_add_f32 v[132:133],v[132:133],v[220:221]\n"
"v_pk_add_f32 v[134:135],v[134:135],v[222:223]\n"
"v_lshlrev_b32 v140,16,v36\n"
"v_lshlrev_b32 v142,16,v37\n"
"v_and_b32 v141,s62,v36\n"
"v_and_b32 v143,s62,v37\n"
"v_pk_add_f32 v[132:133],v[132:133],v[232:233] op_sel:[0,1]\n"
"v_pk_add_f32 v[134:135],v[134:135],v[232:233] op_sel:[0,1]\n"
"v_pk_mul_f32 v[132:133],v[132:133],v[140:141]\n"
"v_pk_mul_f32 v[134:135],v[134:135],v[142:143]\n"
"v_cvt_pk_bf16_f32 v36,v132,v133\n"
"v_cvt_pk_bf16_f32 v37,v134,v135\n"
"s_waitcnt lgkmcnt(5)\n"
"v_pk_add_f32 v[144:145],v[144:145],v[224:225]\n"
"v_pk_add_f32 v[146:147],v[146:147],v[226:227]\n"
"v_lshlrev_b32 v152,16,v34\n"
"v_lshlrev_b32 v154,16,v35\n"
"v_and_b32 v153,s62,v34\n"
"v_and_b32 v155,s62,v35\n"
"v_pk_add_f32 v[144:145],v[144:145],v[232:233] op_sel_hi:[1,0]\n"
"v_pk_add_f32 v[146:147],v[146:147],v[232:233] op_sel_hi:[1,0]\n"
"v_pk_mul_f32 v[144:145],v[144:145],v[152:153]\n"
"v_pk_mul_f32 v[146:147],v[146:147],v[154:155]\n"
"v_cvt_pk_bf16_f32 v34,v144,v145\n"
"v_cvt_pk_bf16_f32 v35,v146,v147\n"
"buffer_store_dwordx4 v[32:35],v9,s[88:91],s77 offen offset:2048 sc1\n"
"ds_read_b64_tr_b16 a[0:1],v240 offset:36864\n"
"ds_read_b64_tr_b16 a[2:3],v240 offset:41472\n"
"ds_read_b64_tr_b16 a[4:5],v240 offset:36896\n"
"ds_read_b64_tr_b16 a[6:7],v240 offset:41504\n"
"ds_read_b64_tr_b16 a[8:9],v240 offset:36928\n"
"ds_read_b64_tr_b16 a[10:11],v240 offset:41536\n"
"ds_read_b64_tr_b16 a[12:13],v240 offset:36960\n"
"ds_read_b64_tr_b16 a[14:15],v240 offset:41568\n"
"ds_read_b64_tr_b16 a[16:17],v240 offset:36992\n"
"ds_read_b64_tr_b16 a[18:19],v240 offset:41600\n"
"ds_read_b64_tr_b16 a[20:21],v240 offset:37024\n"
"ds_read_b64_tr_b16 a[22:23],v240 offset:41632\n"
"ds_read_b64_tr_b16 a[24:25],v240 offset:37056\n"
"ds_read_b64_tr_b16 a[26:27],v240 offset:41664\n"
"ds_read_b64_tr_b16 a[28:29],v240 offset:37088\n"
"ds_read_b64_tr_b16 a[30:31],v240 offset:41696\n"
"L_pgs368_%=:\n"
"s_cbranch_execz L_hrs_%=\n"
"ds_read_b64_tr_b16 a[32:33],v240 offset:37120\n"
"ds_read_b64_tr_b16 a[34:35],v240 offset:41728\n"
"s_waitcnt lgkmcnt(15)\n"
"v_pk_add_f32 v[148:149],v[148:149],v[52:53]\n"
"v_pk_add_f32 v[150:151],v[150:151],v[54:55]\n"
"v_lshlrev_b32 v156,16,v38\n"
"v_lshlrev_b32 v158,16,v39\n"
"v_and_b32 v157,s62,v38\n"
"v_and_b32 v159,s62,v39\n"
"v_pk_add_f32 v[148:149],v[148:149],v[232:233] op_sel:[0,1]\n"
"v_pk_add_f32 v[150:151],v[150:151],v[232:233] op_sel:[0,1]\n"
"v_pk_mul_f32 v[148:149],v[148:149],v[156:157]\n"
"v_pk_mul_f32 v[150:151],v[150:151],v[158:159]\n"
"v_cvt_pk_bf16_f32 v38,v148,v149\n"
"v_cvt_pk_bf16_f32 v39,v150,v151\n"
"buffer_store_dwordx4 v[36:39],v9,s[88:91],s72 offen offset:2048 sc1\n"
"v_pk_add_f32 v[160:161],v[160:161],v[56:57]\n"
"v_pk_add_f32 v[162:163],v[162:163],v[58:59]\n"
"v_lshlrev_b32 v168,16,v40\n"
"v_lshlrev_b32 v170,16,v41\n"
"v_and_b32 v169,s62,v40\n"
"v_and_b32 v171,s62,v41\n"
"v_pk_add_f32 v[160:161],v[160:161],v[232:233] op_sel_hi:[1,0]\n"
"v_pk_add_f32 v[162:163],v[162:163],v[232:233] op_sel_hi:[1,0]\n"
"v_pk_mul_f32 v[160:161],v[160:161],v[168:169]\n"
"v_pk_mul_f32 v[162:163],v[162:163],v[170:171]\n"
"v_cvt_pk_bf16_f32 v40,v160,v161\n"
"v_cvt_pk_bf16_f32 v41,v162,v163\n"
"v_pk_add_f32 v[164:165],v[164:165],v[60:61]\n"
"v_pk_add_f32 v[166:167],v[166:167],v[62:63]\n"
"v_lshlrev_b32 v172,16,v44\n"
"v_lshlrev_b32 v174,16,v45\n"
"v_and_b32 v173,s62,v44\n"
"v_and_b32 v175,s62,v45\n"
"v_pk_add_f32 v[164:165],v[164:165],v[232:233] op_sel:[0,1]\n"
"v_pk_add_f32 v[166:167],v[166:167],v[232:233] op_sel:[0,1]\n"
"v_pk_mul_f32 v[164:165],v[164:165],v[172:173]\n"
"v_pk_mul_f32 v[166:167],v[166:167],v[174:175]\n"
"v_cvt_pk_bf16_f32 v44,v164,v165\n"
"v_cvt_pk_bf16_f32 v45,v166,v167\n"
"v_pk_add_f32 v[176:177],v[176:177],v[208:209]\n"
"v_pk_add_f32 v[178:179],v[178:179],v[210:211]\n"
"v_lshlrev_b32 v184,16,v42\n"
"v_lshlrev_b32 v186,16,v43\n"
"v_and_b32 v185,s62,v42\n"
"v_and_b32 v187,s62,v43\n"
"v_pk_add_f32 v[176:177],v[176:177],v[232:233] op_sel_hi:[1,0]\n"
"v_pk_add_f32 v[178:179],v[178:179],v[232:233] op_sel_hi:[1,0]\n"
"v_pk_mul_f32 v[176:177],v[176:177],v[184:185]\n"
"v_pk_mul_f32 v[178:179],v[178:179],v[186:187]\n"
"v_cvt_pk_bf16_f32 v42,v176,v177\n"
"v_cvt_pk_bf16_f32 v43,v178,v179\n"
"buffer_store_dwordx4 v[40:43],v9,s[88:91],s77 offen offset:3072 sc1\n"
"v_pk_add_f32 v[180:181],v[180:181],v[212:213]\n"
"v_pk_add_f32 v[182:183],v[182:183],v[214:215]\n"
"v_lshlrev_b32 v188,16,v46\n"
"v_lshlrev_b32 v190,16,v47\n"
"v_and_b32 v189,s62,v46\n"
"v_and_b32 v191,s62,v47\n"
"v_pk_add_f32 v[180:181],v[180:181],v[232:233] op_sel:[0,1]\n"
"v_pk_add_f32 v[182:183],v[182:183],v[232:233] op_sel:[0,1]\n"
"v_pk_mul_f32 v[180:181],v[180:181],v[188:189]\n"
"v_pk_mul_f32 v[182:183],v[182:183],v[190:191]\n"
"v_cvt_pk_bf16_f32 v46,v180,v181\n"
"v_cvt_pk_bf16_f32 v47,v182,v183\n"
"buffer_store_dwordx4 v[44:47],v9,s[88:91],s72 offen offset:3072 sc1\n"
"s_waitcnt lgkmcnt(0)\n"
"s_barrier\n"
"v_mfma_f32_16x16x32_bf16 v[64:67],a[0:3],v[16:19],0\n"
"ds_read_b64_tr_b16 a[36:37],v240 offset:46080\n"
"ds_read_b64_tr_b16 a[38:39],v240 offset:50688\n"
"s_add_u32 m0,s60,110592\n"
"s_add_u32 s87,s86,8\n"
"s_add_u32 s87,s87,s37\n"
"s_sub_u32 s78,s87,18\n"
"s_min_u32 s87,s87,s78\n"
"s_mul_i32 s87,s87,36864\n"
"v_mfma_f32_16x16x32_bf16 v[68:71],a[0:3],v[20:23],0\n"
"buffer_load_dwordx4 v246,s[40:43],s87 offen offset:0 lds\n"
"v_mfma_f32_16x16x32_bf16 v[72:75],a[4:7],v[16:19],0\n"
"ds_read_b64_tr_b16 a[40:41],v240 offset:46112\n"
"ds_read_b64_tr_b16 a[42:43],v240 offset:50720\n"
"v_mfma_f32_16x16x32_bf16 v[76:79],a[4:7],v[20:23],0\n"
"v_mfma_f32_16x16x32_bf16 v[80:83],a[8:11],v[16:19],0\n"
"ds_read_b64_tr_b16 a[44:45],v240 offset:46144\n"
"ds_read_b64_tr_b16 a[46:47],v240 offset:50752\n"
"v_mfma_f32_16x16x32_bf16 v[84:87],a[8:11],v[20:23],0\n"
"buffer_load_dwordx4 v247,s[40:43],s87 offen offset:1024 lds\n"
"v_mfma_f32_16x16x32_bf16 v[88:91],a[12:15],v[16:19],0\n"
"ds_read_b64_tr_b16 a[48:49],v240 offset:46176\n"
"ds_read_b64_tr_b16 a[50:51],v240 offset:50784\n"
"v_mfma_f32_16x16x32_bf16 v[92:95],a[12:15],v[20:23],0\n"
"v_mfma_f32_16x16x32_bf16 v[96:99],a[16:19],v[16:19],0\n"
"ds_read_b64_tr_b16 a[52:53],v240 offset:46208\n"
"ds_read_b64_tr_b16 a[54:55],v240 offset:50816\n"
"v_mfma_f32_16x16x32_bf16 v[100:103],a[16:19],v[20:23],0\n"
"buffer_load_dwordx4 v248,s[40:43],s87 offen offset:2048 lds\n"
"v_mfma_f32_16x16x32_bf16 v[104:107],a[20:23],v[16:19],0\n"
"ds_read_b64_tr_b16 a[56:57],v240 offset:46240\n"
"ds_read_b64_tr_b16 a[58:59],v240 offset:50848\n"
"v_mfma_f32_16x16x32_bf16 v[108:111],a[20:23],v[20:23],0\n"
"v_mfma_f32_16x16x32_bf16 v[112:115],a[24:27],v[16:19],0\n"
"ds_read_b64_tr_b16 a[60:61],v240 offset:46272\n"
"ds_read_b64_tr_b16 a[62:63],v240 offset:50880\n"
"v_mfma_f32_16x16x32_bf16 v[116:119],a[24:27],v[20:23],0\n"
"buffer_load_dwordx4 v249,s[40:43],s87 offen offset:3072 lds\n"
"v_mfma_f32_16x16x32_bf16 v[120:123],a[28:31],v[16:19],0\n"
"ds_read_b64_tr_b16 a[64:65],v240 offset:46304\n"
"ds_read_b64_tr_b16 a[66:67],v240 offset:50912\n"
"v_mfma_f32_16x16x32_bf16 v[124:127],a[28:31],v[20:23],0\n"
"s_add_u32 m0,s60,114688\n"
"v_mfma_f32_16x16x32_bf16 v[128:131],a[32:35],v[16:19],0\n"
"ds_read_b64_tr_b16 a[68:69],v240 offset:46336\n"
"ds_read_b64_tr_b16 a[70:71],v240 offset:50944\n"
"v_mfma_f32_16x16x32_bf16 v[132:135],a[32:35],v[20:23],0\n"
"buffer_load_dwordx4 v250,s[40:43],s87 offen offset:0 lds\n"
"s_waitcnt lgkmcnt(12)\n"
"v_mfma_f32_16x16x32_bf16 v[64:67],a[36:39],v[24:27],v[64:67]\n"
"ds_read_b64_tr_b16 a[0:1],v241 offset:0\n"
"ds_read_b64_tr_b16 a[2:3],v241 offset:4608\n"
"v_mfma_f32_16x16x32_bf16 v[68:71],a[36:39],v[28:31],v[68:71]\n"
"v_mfma_f32_16x16x32_bf16 v[72:75],a[40:43],v[24:27],v[72:75]\n"
"ds_read_b64_tr_b16 a[4:5],v241 offset:32\n"
"ds_read_b64_tr_b16 a[6:7],v241 offset:4640\n"
"v_mfma_f32_16x16x32_bf16 v[76:79],a[40:43],v[28:31],v[76:79]\n"
"buffer_load_dwordx4 v251,s[40:43],s87 offen offset:1024 lds\n"
"v_mfma_f32_16x16x32_bf16 v[80:83],a[44:47],v[24:27],v[80:83]\n"
"ds_read_b64_tr_b16 a[8:9],v241 offset:64\n"
"ds_read_b64_tr_b16 a[10:11],v241 offset:4672\n"
"v_mfma_f32_16x16x32_bf16 v[84:87],a[44:47],v[28:31],v[84:87]\n"
"s_waitcnt lgkmcnt(12)\n"
"v_mfma_f32_16x16x32_bf16 v[88:91],a[48:51],v[24:27],v[88:91]\n"
"ds_read_b64_tr_b16 a[12:13],v241 offset:96\n"
"ds_read_b64_tr_b16 a[14:15],v241 offset:4704\n"
"v_mfma_f32_16x16x32_bf16 v[92:95],a[48:51],v[28:31],v[92:95]\n"
"buffer_load_dwordx4 v252,s[40:43],s87 offen offset:2048 lds\n"
"v_mfma_f32_16x16x32_bf16 v[96:99],a[52:55],v[24:27],v[96:99]\n"
"ds_read_b64_tr_b16 a[16:17],v241 offset:128\n"
"ds_read_b64_tr_b16 a[18:19],v241 offset:4736\n"
"v_mfma_f32_16x16x32_bf16 v[100:103],a[52:55],v[28:31],v[100:103]\n"
"v_mfma_f32_16x16x32_bf16 v[104:107],a[56:59],v[24:27],v[104:107]\n"
"ds_read_b64_tr_b16 a[20:21],v241 offset:160\n"
"ds_read_b64_tr_b16 a[22:23],v241 offset:4768\n"
"v_mfma_f32_16x16x32_bf16 v[108:111],a[56:59],v[28:31],v[108:111]\n"
"buffer_load_dwordx4 v253,s[40:43],s87 offen offset:3072 lds\n"
"s_waitcnt lgkmcnt(12)\n"
"v_mfma_f32_16x16x32_bf16 v[112:115],a[60:63],v[24:27],v[112:115]\n"
"ds_read_b64_tr_b16 a[24:25],v241 offset:192\n"
"ds_read_b64_tr_b16 a[26:27],v241 offset:4800\n"
"v_mfma_f32_16x16x32_bf16 v[116:119],a[60:63],v[28:31],v[116:119]\n"
"s_add_u32 s72,s86,4\n"
"s_sub_u32 s13,s72,18\n"
"s_min_u32 s72,s72,s13\n"
"s_lshl_b32 s72,s72,10\n"
"v_mfma_f32_16x16x32_bf16 v[120:123],a[64:67],v[24:27],v[120:123]\n"
"ds_read_b64_tr_b16 a[28:29],v241 offset:224\n"
"ds_read_b64_tr_b16 a[30:31],v241 offset:4832\n"
"v_mfma_f32_16x16x32_bf16 v[124:127],a[64:67],v[28:31],v[124:127]\n"
"buffer_load_dwordx4 a[72:75],v9,s[52:55],s72 offen sc1\n"
"v_mfma_f32_16x16x32_bf16 v[128:131],a[68:71],v[24:27],v[128:131]\n"
"ds_read_b64_tr_b16 a[32:33],v241 offset:256\n"
"ds_read_b64_tr_b16 a[34:35],v241 offset:4864\n"
"v_mfma_f32_16x16x32_bf16 v[132:135],a[68:71],v[28:31],v[132:135]\n"
"s_add_u32 s72,s72,73728\n"
"s_waitcnt lgkmcnt(12)\n"
"v_mfma_f32_16x16x32_bf16 v[64:67],a[0:3],v[32:35],v[64:67]\n"
"ds_read_b64_tr_b16 a[36:37],v241 offset:9216\n"
"ds_read_b64_tr_b16 a[38:39],v241 offset:13824\n"
"v_mfma_f32_16x16x32_bf16 v[68:71],a[0:3],v[36:39],v[68:71]\n"
"buffer_load_dwordx4 a[76:79],v9,s[52:55],s72 offen sc1\n"
"v_mfma_f32_16x16x32_bf16 v[72:75],a[4:7],v[32:35],v[72:75]\n"
"ds_read_b64_tr_b16 a[40:41],v241 offset:9248\n"
"ds_read_b64_tr_b16 a[42:43],v241 offset:13856\n"
"v_mfma_f32_16x16x32_bf16 v[76:79],a[4:7],v[36:39],v[76:79]\n"
"s_add_u32 s72,s86,5\n"
"s_sub_u32 s13,s72,18\n"
"s_min_u32 s72,s72,s13\n"
"s_lshl_b32 s72,s72,10\n"
"v_mfma_f32_16x16x32_bf16 v[80:83],a[8:11],v[32:35],v[80:83]\n"
"ds_read_b64_tr_b16 a[44:45],v241 offset:9280\n"
"ds_read_b64_tr_b16 a[46:47],v241 offset:13888\n"
"v_mfma_f32_16x16x32_bf16 v[84:87],a[8:11],v[36:39],v[84:87]\n"
"buffer_load_dwordx4 a[80:83],v9,s[52:55],s72 offen sc1\n"
"s_waitcnt lgkmcnt(12)\n"
"v_mfma_f32_16x16x32_bf16 v[88:91],a[12:15],v[32:35],v[88:91]\n"
"ds_read_b64_tr_b16 a[48:49],v241 offset:9312\n"
"ds_read_b64_tr_b16 a[50:51],v241 offset:13920\n"
"v_mfma_f32_16x16x32_bf16 v[92:95],a[12:15],v[36:39],v[92:95]\n"
"s_add_u32 s72,s72,73728\n"
"v_mfma_f32_16x16x32_bf16 v[96:99],a[16:19],v[32:35],v[96:99]\n"
"ds_read_b64_tr_b16 a[52:53],v241 offset:9344\n"
"ds_read_b64_tr_b16 a[54:55],v241 offset:13952\n"
"v_mfma_f32_16x16x32_bf16 v[100:103],a[16:19],v[36:39],v[100:103]\n"
"buffer_load_dwordx4 a[84:87],v9,s[52:55],s72 offen sc1\n"
"v_mfma_f32_16x16x32_bf16 v[104:107],a[20:23],v[32:35],v[104:107]\n"
"ds_read_b64_tr_b16 a[56:57],v241 offset:9376\n"
"ds_read_b64_tr_b16 a[58:59],v241 offset:13984\n"
"v_mfma_f32_16x16x32_bf16 v[108:111],a[20:23],v[36:39],v[108:111]\n"
"s_add_u32 s72,s86,6\n"
"s_sub_u32 s13,s72,18\n"
"s_min_u32 s72,s72,s13\n"
"s_lshl_b32 s72,s72,10\n"
"s_waitcnt lgkmcnt(12)\n"
"v_mfma_f32_16x16x32_bf16 v[112:115],a[24:27],v[32:35],v[112:115]\n"
"ds_read_b64_tr_b16 a[60:61],v241 offset:9408\n"
"ds_read_b64_tr_b16 a[62:63],v241 offset:14016\n"
"v_mfma_f32_16x16x32_bf16 v[116:119],a[24:27],v[36:39],v[116:119]\n"
"buffer_load_dwordx4 a[88:91],v9,s[52:55],s72 offen sc1\n"
"v_mfma_f32_16x16x32_bf16 v[120:123],a[28:31],v[32:35],v[120:123]\n"
"ds_read_b64_tr_b16 a[64:65],v241 offset:9440\n"
"ds_read_b64_tr_b16 a[66:67],v241 offset:14048\n"
"v_mfma_f32_16x16x32_bf16 v[124:127],a[28:31],v[36:39],v[124:127]\n"
"s_add_u32 s72,s72,73728\n"
"v_mfma_f32_16x16x32_bf16 v[128:131],a[32:35],v[32:35],v[128:131]\n"
"ds_read_b64_tr_b16 a[68:69],v241 offset:9472\n"
"ds_read_b64_tr_b16 a[70:71],v241 offset:14080\n"
"v_mfma_f32_16x16x32_bf16 v[132:135],a[32:35],v[36:39],v[132:135]\n"
"buffer_load_dwordx4 a[92:95],v9,s[52:55],s72 offen sc1\n"
"s_waitcnt lgkmcnt(12)\n"
"v_mfma_f32_16x16x32_bf16 v[64:67],a[36:39],v[40:43],v[64:67]\n"
"v_mfma_f32_16x16x32_bf16 v[68:71],a[36:39],v[44:47],v[68:71]\n"
"v_mfma_f32_16x16x32_bf16 v[72:75],a[40:43],v[40:43],v[72:75]\n"
"v_mfma_f32_16x16x32_bf16 v[76:79],a[40:43],v[44:47],v[76:79]\n"
"s_waitcnt vmcnt(34) lgkmcnt(0)\n"
"s_barrier\n"
"v_mfma_f32_16x16x32_bf16 v[80:83],a[44:47],v[40:43],v[80:83]\n"
"ds_read_b64_tr_b16 a[0:1],v241 offset:18432\n"
"ds_read_b64_tr_b16 a[2:3],v241 offset:23040\n"
"v_mfma_f32_16x16x32_bf16 v[84:87],a[44:47],v[44:47],v[84:87]\n"
"ds_read_b64_tr_b16 a[4:5],v241 offset:27648\n"
"ds_read_b64_tr_b16 a[6:7],v241 offset:32256\n"
"s_add_u32 s72,s86,7\n"
"s_sub_u32 s13,s72,18\n"
"s_min_u32 s72,s72,s13\n"
"s_lshl_b32 s72,s72,10\n"
"v_mfma_f32_16x16x32_bf16 v[88:91],a[48:51],v[40:43],v[88:91]\n"
"ds_read_b64_tr_b16 a[8:9],v241 offset:36864\n"
"ds_read_b64_tr_b16 a[10:11],v241 offset:41472\n"
"v_mfma_f32_16x16x32_bf16 v[92:95],a[48:51],v[44:47],v[92:95]\n"
"buffer_load_dwordx4 a[96:99],v9,s[52:55],s72 offen sc1\n"
"v_mfma_f32_16x16x32_bf16 v[96:99],a[52:55],v[40:43],v[96:99]\n"
"ds_read_b64_tr_b16 a[12:13],v241 offset:46080\n"
"ds_read_b64_tr_b16 a[14:15],v241 offset:50688\n"
"s_add_u32 s72,s72,73728\n"
"v_mfma_f32_16x16x32_bf16 v[100:103],a[52:55],v[44:47],v[100:103]\n"
"buffer_load_dwordx4 a[100:103],v9,s[52:55],s72 offen sc1\n"
"v_mfma_f32_16x16x32_bf16 v[104:107],a[56:59],v[40:43],v[104:107]\n"
"ds_read_b64_tr_b16 a[16:17],v241 offset:18464\n"
"ds_read_b64_tr_b16 a[18:19],v241 offset:23072\n"
"v_mfma_f32_16x16x32_bf16 v[108:111],a[56:59],v[44:47],v[108:111]\n"
"v_mfma_f32_16x16x32_bf16 v[112:115],a[60:63],v[40:43],v[112:115]\n"
"ds_read_b64_tr_b16 a[20:21],v241 offset:18496\n"
"ds_read_b64_tr_b16 a[22:23],v241 offset:23104\n"
"v_mfma_f32_16x16x32_bf16 v[116:119],a[60:63],v[44:47],v[116:119]\n"
"ds_read_b64_tr_b16 a[24:25],v241 offset:18528\n"
"ds_read_b64_tr_b16 a[26:27],v241 offset:23136\n"
"s_add_u32 m0,s60,118784\n"
"v_mfma_f32_16x16x32_bf16 v[120:123],a[64:67],v[40:43],v[120:123]\n"
"ds_read_b64_tr_b16 a[28:29],v241 offset:18560\n"
"ds_read_b64_tr_b16 a[30:31],v241 offset:23168\n"
"v_mfma_f32_16x16x32_bf16 v[124:127],a[64:67],v[44:47],v[124:127]\n"
"buffer_load_dwordx4 v254,s[40:43],s87 offen offset:0 lds\n"
"s_add_u32 s72,s86,8\n"
"s_sub_u32 s13,s72,18\n"
"s_min_u32 s72,s72,s13\n"
"s_lshl_b32 s72,s72,10\n"
"v_mfma_f32_16x16x32_bf16 v[128:131],a[68:71],v[40:43],v[128:131]\n"
"ds_read_b64_tr_b16 a[32:33],v241 offset:18592\n"
"ds_read_b64_tr_b16 a[34:35],v241 offset:23200\n"
"v_mfma_f32_16x16x32_bf16 v[132:135],a[68:71],v[44:47],v[132:135]\n"
"buffer_load_dwordx4 a[104:107],v9,s[52:55],s72 offen sc1\n"
"s_waitcnt lgkmcnt(12) vmcnt(4)\n"
"v_mfma_f32_16x16x32_bf16 v[208:211],a[0:3],a[72:75],v[64:67]\n"
"ds_read_b64_tr_b16 a[36:37],v241 offset:18624\n"
"ds_read_b64_tr_b16 a[38:39],v241 offset:23232\n"
"v_mfma_f32_16x16x32_bf16 v[212:215],a[0:3],a[76:79],v[68:71]\n"
"s_add_u32 s72,s72,73728\n"
"s_nop 1\n"
"v_mfma_f32_16x16x32_bf16 v[136:139],a[4:7],a[80:83],v[208:211]\n"
"ds_read_b64_tr_b16 a[40:41],v241 offset:18656\n"
"ds_read_b64_tr_b16 a[42:43],v241 offset:23264\n"
"v_mfma_f32_16x16x32_bf16 v[140:143],a[4:7],a[84:87],v[212:215]\n"
"buffer_load_dwordx4 a[108:111],v9,s[52:55],s72 offen sc1\n"
"v_mfma_f32_16x16x32_bf16 v[136:139],a[8:11],a[88:91],v[136:139]\n"
"ds_read_b64_tr_b16 a[44:45],v241 offset:18688\n"
"ds_read_b64_tr_b16 a[46:47],v241 offset:23296\n"
"v_mfma_f32_16x16x32_bf16 v[140:143],a[8:11],a[92:95],v[140:143]\n"
"s_waitcnt lgkmcnt(12) vmcnt(3)\n"
"v_mfma_f32_16x16x32_bf16 v[136:139],a[12:15],a[96:99],v[136:139]\n"
"ds_read_b64_tr_b16 a[48:49],v241 offset:27680\n"
"ds_read_b64_tr_b16 a[50:51],v241 offset:32288\n"
"v_mfma_f32_16x16x32_bf16 v[140:143],a[12:15],a[100:103],v[140:143]\n"
"s_add_u32 s72,s86,9\n"
"s_sub_u32 s13,s72,18\n"
"s_min_u32 s72,s72,s13\n"
"s_lshl_b32 s72,s72,10\n"
"v_cmp_u_f32 vcc,v208,v212\n"
"s_cbranch_vccnz L_fze4_0_%=\n"
"L_fre4_0_%=:\n"
"v_mfma_f32_16x16x32_bf16 v[72:75],a[16:19],a[72:75],v[72:75]\n"
"ds_read_b64_tr_b16 a[52:53],v241 offset:27712\n"
"ds_read_b64_tr_b16 a[54:55],v241 offset:32320\n"
"v_mfma_f32_16x16x32_bf16 v[76:79],a[16:19],a[76:79],v[76:79]\n"
"buffer_load_dwordx4 a[112:115],v9,s[52:55],s72 offen sc1\n"
"v_mfma_f32_16x16x32_bf16 v[80:83],a[20:23],a[72:75],v[80:83]\n"
"ds_read_b64_tr_b16 a[56:57],v241 offset:27744\n"
"ds_read_b64_tr_b16 a[58:59],v241 offset:32352\n"
"s_add_u32 s72,s72,73728\n"
"v_mfma_f32_16x16x32_bf16 v[84:87],a[20:23],a[76:79],v[84:87]\n"
"buffer_load_dwordx4 a[116:119],v9,s[52:55],s72 offen sc1\n"
"s_waitcnt lgkmcnt(12)\n"
"v_mfma_f32_16x16x32_bf16 v[88:91],a[24:27],a[72:75],v[88:91]\n"
"ds_read_b64_tr_b16 a[60:61],v241 offset:27776\n"
"ds_read_b64_tr_b16 a[62:63],v241 offset:32384\n"
"v_mfma_f32_16x16x32_bf16 v[92:95],a[24:27],a[76:79],v[92:95]\n"
"s_add_u32 s72,s86,10\n"
"s_sub_u32 s13,s72,18\n"
"s_min_u32 s72,s72,s13\n"
"s_lshl_b32 s72,s72,10\n"
"v_cmp_u_f32 vcc,v136,v140\n"
"s_cbranch_vccnz L_fze4_1_%=\n"
"L_fre4_1_%=:\n"
"v_mfma_f32_16x16x32_bf16 v[96:99],a[28:31],a[72:75],v[96:99]\n"
"ds_read_b64_tr_b16 a[64:65],v241 offset:27808\n"
"ds_read_b64_tr_b16 a[66:67],v241 offset:32416\n"
"v_mfma_f32_16x16x32_bf16 v[100:103],a[28:31],a[76:79],v[100:103]\n"
"buffer_load_dwordx4 a[120:123],v9,s[52:55],s72 offen sc1\n"
"v_mfma_f32_16x16x32_bf16 v[104:107],a[32:35],a[72:75],v[104:107]\n"
"ds_read_b64_tr_b16 a[68:69],v241 offset:27840\n"
"ds_read_b64_tr_b16 a[70:71],v241 offset:32448\n"
"v_mfma_f32_16x16x32_bf16 v[108:111],a[32:35],a[76:79],v[108:111]\n"
"s_add_u32 s72,s72,73728\n"
"s_waitcnt lgkmcnt(12)\n"
"v_mfma_f32_16x16x32_bf16 v[112:115],a[36:39],a[72:75],v[112:115]\n"
"ds_read_b64_tr_b16 a[0:1],v241 offset:27872\n"
"ds_read_b64_tr_b16 a[2:3],v241 offset:32480\n"
"v_mfma_f32_16x16x32_bf16 v[116:119],a[36:39],a[76:79],v[116:119]\n"
"buffer_load_dwordx4 a[124:127],v9,s[52:55],s72 offen sc1\n"
"v_mfma_f32_16x16x32_bf16 v[120:123],a[40:43],a[72:75],v[120:123]\n"
"ds_read_b64_tr_b16 a[4:5],v241 offset:27904\n"
"ds_read_b64_tr_b16 a[6:7],v241 offset:32512\n"
"v_mfma_f32_16x16x32_bf16 v[124:127],a[40:43],a[76:79],v[124:127]\n"
"s_add_u32 s72,s86,11\n"
"s_sub_u32 s13,s72,18\n"
"s_min_u32 s72,s72,s13\n"
"s_lshl_b32 s72,s72,10\n"
"v_mfma_f32_16x16x32_bf16 v[128:131],a[44:47],a[72:75],v[128:131]\n"
"ds_read_b64_tr_b16 a[8:9],v241 offset:36896\n"
"ds_read_b64_tr_b16 a[10:11],v241 offset:41504\n"
"v_mfma_f32_16x16x32_bf16 v[132:135],a[44:47],a[76:79],v[132:135]\n"
"buffer_load_dwordx4 a[128:131],v9,s[52:55],s72 offen sc1\n"
"s_waitcnt lgkmcnt(12)\n"
"v_mfma_f32_16x16x32_bf16 v[72:75],a[48:51],a[80:83],v[72:75]\n"
"ds_read_b64_tr_b16 a[12:13],v241 offset:36928\n"
"ds_read_b64_tr_b16 a[14:15],v241 offset:41536\n"
"v_mfma_f32_16x16x32_bf16 v[76:79],a[48:51],a[84:87],v[76:79]\n"
"s_add_u32 s72,s72,73728\n"
"v_mfma_f32_16x16x32_bf16 v[80:83],a[52:55],a[80:83],v[80:83]\n"
"ds_read_b64_tr_b16 a[16:17],v241 offset:36960\n"
"ds_read_b64_tr_b16 a[18:19],v241 offset:41568\n"
"v_mfma_f32_16x16x32_bf16 v[84:87],a[52:55],a[84:87],v[84:87]\n"
"buffer_load_dwordx4 a[132:135],v9,s[52:55],s72 offen sc1\n"
"v_mfma_f32_16x16x32_bf16 v[88:91],a[56:59],a[80:83],v[88:91]\n"
"ds_read_b64_tr_b16 a[20:21],v241 offset:36992\n"
"ds_read_b64_tr_b16 a[22:23],v241 offset:41600\n"
"v_mfma_f32_16x16x32_bf16 v[92:95],a[56:59],a[84:87],v[92:95]\n"
"s_add_u32 m0,s60,0\n"
"s_add_u32 s87,s86,12\n"
"s_add_u32 s87,s87,s37\n"
"s_sub_u32 s78,s87,18\n"
"s_min_u32 s87,s87,s78\n"
"s_mul_i32 s87,s87,36864\n"
"s_waitcnt lgkmcnt(12)\n"
"v_mfma_f32_16x16x32_bf16 v[96:99],a[60:63],a[80:83],v[96:99]\n"
"ds_read_b64_tr_b16 a[24:25],v241 offset:37024\n"
"ds_read_b64_tr_b16 a[26:27],v241 offset:41632\n"
"v_mfma_f32_16x16x32_bf16 v[100:103],a[60:63],a[84:87],v[100:103]\n"
"buffer_load_dwordx4 v246,s[40:43],s87 offen offset:0 lds\n"
"v_mfma_f32_16x16x32_bf16 v[104:107],a[64:67],a[80:83],v[104:107]\n"
"ds_read_b64_tr_b16 a[28:29],v241 offset:37056\n"
"ds_read_b64_tr_b16 a[30:31],v241 offset:41664\n"
"v_mfma_f32_16x16x32_bf16 v[108:111],a[64:67],a[84:87],v[108:111]\n"
"v_mfma_f32_16x16x32_bf16 v[112:115],a[68:71],a[80:83],v[112:115]\n"
"ds_read_b64_tr_b16 a[32:33],v241 offset:37088\n"
"ds_read_b64_tr_b16 a[34:35],v241 offset:41696\n"
"v_mfma_f32_16x16x32_bf16 v[116:119],a[68:71],a[84:87],v[116:119]\n"
"buffer_load_dwordx4 v247,s[40:43],s87 offen offset:1024 lds\n"
"s_waitcnt lgkmcnt(12)\n"
"v_mfma_f32_16x16x32_bf16 v[120:123],a[0:3],a[80:83],v[120:123]\n"
"ds_read_b64_tr_b16 a[36:37],v241 offset:37120\n"
"ds_read_b64_tr_b16 a[38:39],v241 offset:41728\n"
"v_mfma_f32_16x16x32_bf16 v[124:127],a[0:3],a[84:87],v[124:127]\n"
"v_mfma_f32_16x16x32_bf16 v[128:131],a[4:7],a[80:83],v[128:131]\n"
"ds_read_b64_tr_b16 a[40:41],v241 offset:46112\n"
"ds_read_b64_tr_b16 a[42:43],v241 offset:50720\n"
"v_mfma_f32_16x16x32_bf16 v[132:135],a[4:7],a[84:87],v[132:135]\n"
"buffer_load_dwordx4 v248,s[40:43],s87 offen offset:2048 lds\n"
"v_mfma_f32_16x16x32_bf16 v[72:75],a[8:11],a[88:91],v[72:75]\n"
"ds_read_b64_tr_b16 a[44:45],v241 offset:46144\n"
"ds_read_b64_tr_b16 a[46:47],v241 offset:50752\n"
"v_mfma_f32_16x16x32_bf16 v[76:79],a[8:11],a[92:95],v[76:79]\n"
"s_waitcnt lgkmcnt(12)\n"
"v_mfma_f32_16x16x32_bf16 v[80:83],a[12:15],a[88:91],v[80:83]\n"
"ds_read_b64_tr_b16 a[48:49],v241 offset:46176\n"
"ds_read_b64_tr_b16 a[50:51],v241 offset:50784\n"
"v_mfma_f32_16x16x32_bf16 v[84:87],a[12:15],a[92:95],v[84:87]\n"
"buffer_load_dwordx4 v249,s[40:43],s87 offen offset:3072 lds\n"
"v_mfma_f32_16x16x32_bf16 v[88:91],a[16:19],a[88:91],v[88:91]\n"
"ds_read_b64_tr_b16 a[52:53],v241 offset:46208\n"
"ds_read_b64_tr_b16 a[54:55],v241 offset:50816\n"
"v_mfma_f32_16x16x32_bf16 v[92:95],a[16:19],a[92:95],v[92:95]\n"
"s_add_u32 m0,s60,4096\n"
"v_mfma_f32_16x16x32_bf16 v[96:99],a[20:23],a[88:91],v[96:99]\n"
"ds_read_b64_tr_b16 a[56:57],v241 offset:46240\n"
"ds_read_b64_tr_b16 a[58:59],v241 offset:50848\n"
"v_mfma_f32_16x16x32_bf16 v[100:103],a[20:23],a[92:95],v[100:103]\n"
"buffer_load_dwordx4 v250,s[40:43],s87 offen offset:0 lds\n"
"s_waitcnt lgkmcnt(12)\n"
"v_mfma_f32_16x16x32_bf16 v[104:107],a[24:27],a[88:91],v[104:107]\n"
"ds_read_b64_tr_b16 a[60:61],v241 offset:46272\n"
"ds_read_b64_tr_b16 a[62:63],v241 offset:50880\n"
"v_mfma_f32_16x16x32_bf16 v[108:111],a[24:27],a[92:95],v[108:111]\n"
"v_mfma_f32_16x16x32_bf16 v[112:115],a[28:31],a[88:91],v[112:115]\n"
"ds_read_b64_tr_b16 a[64:65],v241 offset:46304\n"
"ds_read_b64_tr_b16 a[66:67],v241 offset:50912\n"
"v_mfma_f32_16x16x32_bf16 v[116:119],a[28:31],a[92:95],v[116:119]\n"
"buffer_load_dwordx4 v251,s[40:43],s87 offen offset:1024 lds\n"
"v_mfma_f32_16x16x32_bf16 v[120:123],a[32:35],a[88:91],v[120:123]\n"
"ds_read_b64_tr_b16 a[68:69],v241 offset:46336\n"
"ds_read_b64_tr_b16 a[70:71],v241 offset:50944\n"
"v_mfma_f32_16x16x32_bf16 v[124:127],a[32:35],a[92:95],v[124:127]\n"
"s_waitcnt lgkmcnt(12)\n"
"v_mfma_f32_16x16x32_bf16 v[128:131],a[36:39],a[88:91],v[128:131]\n"
"v_mfma_f32_16x16x32_bf16 v[132:135],a[36:39],a[92:95],v[132:135]\n"
"buffer_load_dwordx4 v252,s[40:43],s87 offen offset:2048 lds\n"
"v_mfma_f32_16x16x32_bf16 v[72:75],a[40:43],a[96:99],v[72:75]\n"
"v_mfma_f32_16x16x32_bf16 v[76:79],a[40:43],a[100:103],v[76:79]\n"
"s_waitcnt vmcnt(15) lgkmcnt(0)\n"
"s_barrier\n"
"v_mfma_f32_16x16x32_bf16 v[80:83],a[44:47],a[96:99],v[80:83]\n"
"ds_read_b64_tr_b16 a[0:1],v242 offset:0\n"
"ds_read_b64_tr_b16 a[2:3],v242 offset:4608\n"
"v_mfma_f32_16x16x32_bf16 v[84:87],a[44:47],a[100:103],v[84:87]\n"
"ds_read_b64_tr_b16 a[4:5],v242 offset:9216\n"
"ds_read_b64_tr_b16 a[6:7],v242 offset:13824\n"
"v_mfma_f32_16x16x32_bf16 v[88:91],a[48:51],a[96:99],v[88:91]\n"
"ds_read_b64_tr_b16 a[8:9],v242 offset:18432\n"
"ds_read_b64_tr_b16 a[10:11],v242 offset:23040\n"
"v_mfma_f32_16x16x32_bf16 v[92:95],a[48:51],a[100:103],v[92:95]\n"
"buffer_load_dwordx4 v253,s[40:43],s87 offen offset:3072 lds\n"
"v_mfma_f32_16x16x32_bf16 v[96:99],a[52:55],a[96:99],v[96:99]\n"
"ds_read_b64_tr_b16 a[12:13],v242 offset:27648\n"
"ds_read_b64_tr_b16 a[14:15],v242 offset:32256\n"
"s_add_u32 m0,s60,8192\n"
"v_mfma_f32_16x16x32_bf16 v[100:103],a[52:55],a[100:103],v[100:103]\n"
"buffer_load_dwordx4 v254,s[40:43],s87 offen offset:0 lds\n"
"v_mfma_f32_16x16x32_bf16 v[104:107],a[56:59],a[96:99],v[104:107]\n"
"ds_read_b64_tr_b16 a[16:17],v242 offset:32\n"
"ds_read_b64_tr_b16 a[18:19],v242 offset:4640\n"
"v_mfma_f32_16x16x32_bf16 v[108:111],a[56:59],a[100:103],v[108:111]\n"
"v_mfma_f32_16x16x32_bf16 v[112:115],a[60:63],a[96:99],v[112:115]\n"
"ds_read_b64_tr_b16 a[20:21],v242 offset:64\n"
"ds_read_b64_tr_b16 a[22:23],v242 offset:4672\n"
"v_mfma_f32_16x16x32_bf16 v[116:119],a[60:63],a[100:103],v[116:119]\n"
"ds_read_b64_tr_b16 a[24:25],v242 offset:96\n"
"ds_read_b64_tr_b16 a[26:27],v242 offset:4704\n"
"s_add_u32 m0,s60,36864\n"
"s_add_u32 s87,s86,16\n"
"s_add_u32 s87,s87,s37\n"
"s_sub_u32 s78,s87,18\n"
"s_min_u32 s87,s87,s78\n"
"s_mul_i32 s87,s87,36864\n"
"s_cmp_lt_u32 s37,2\n"
"s_cselect_b32 s87,s87,0x3ffffff0\n"
"v_mfma_f32_16x16x32_bf16 v[120:123],a[64:67],a[96:99],v[120:123]\n"
"ds_read_b64_tr_b16 a[28:29],v242 offset:128\n"
"ds_read_b64_tr_b16 a[30:31],v242 offset:4736\n"
"v_mfma_f32_16x16x32_bf16 v[124:127],a[64:67],a[100:103],v[124:127]\n"
"buffer_load_dwordx4 v246,s[40:43],s87 offen offset:0 lds\n"
"v_mfma_f32_16x16x32_bf16 v[128:131],a[68:71],a[96:99],v[128:131]\n"
"ds_read_b64_tr_b16 a[32:33],v242 offset:160\n"
"ds_read_b64_tr_b16 a[34:35],v242 offset:4768\n"
"v_mfma_f32_16x16x32_bf16 v[132:135],a[68:71],a[100:103],v[132:135]\n"
"buffer_load_dwordx4 v247,s[40:43],s87 offen offset:1024 lds\n"
"s_waitcnt lgkmcnt(12) vmcnt(13)\n"
"v_mfma_f32_16x16x32_bf16 v[64:67],a[0:3],a[104:107],v[136:139]\n"
"ds_read_b64_tr_b16 a[36:37],v242 offset:192\n"
"ds_read_b64_tr_b16 a[38:39],v242 offset:4800\n"
"v_mfma_f32_16x16x32_bf16 v[68:71],a[0:3],a[108:111],v[140:143]\n"
"s_nop 1\n"
"v_mfma_f32_16x16x32_bf16 v[208:211],a[4:7],a[112:115],v[64:67]\n"
"ds_read_b64_tr_b16 a[40:41],v242 offset:224\n"
"ds_read_b64_tr_b16 a[42:43],v242 offset:4832\n"
"v_mfma_f32_16x16x32_bf16 v[212:215],a[4:7],a[116:119],v[68:71]\n"
"buffer_load_dwordx4 v248,s[40:43],s87 offen offset:2048 lds\n"
"v_mfma_f32_16x16x32_bf16 v[208:211],a[8:11],a[120:123],v[208:211]\n"
"ds_read_b64_tr_b16 a[44:45],v242 offset:256\n"
"ds_read_b64_tr_b16 a[46:47],v242 offset:4864\n"
"v_mfma_f32_16x16x32_bf16 v[212:215],a[8:11],a[124:127],v[212:215]\n"
"s_waitcnt lgkmcnt(12) vmcnt(12)\n"
"v_mfma_f32_16x16x32_bf16 v[208:211],a[12:15],a[128:131],v[208:211]\n"
"ds_read_b64_tr_b16 a[48:49],v242 offset:9248\n"
"ds_read_b64_tr_b16 a[50:51],v242 offset:13856\n"
"v_mfma_f32_16x16x32_bf16 v[212:215],a[12:15],a[132:135],v[212:215]\n"
"v_cmp_u_f32 vcc,v64,v68\n"
"s_cbranch_vccnz L_fze8_0_%=\n"
"L_fre8_0_%=:\n"
"v_mfma_f32_16x16x32_bf16 v[72:75],a[16:19],a[104:107],v[72:75]\n"
"ds_read_b64_tr_b16 a[52:53],v242 offset:9280\n"
"ds_read_b64_tr_b16 a[54:55],v242 offset:13888\n"
"v_mfma_f32_16x16x32_bf16 v[76:79],a[16:19],a[108:111],v[76:79]\n"
"buffer_load_dwordx4 v249,s[40:43],s87 offen offset:3072 lds\n"
"v_mfma_f32_16x16x32_bf16 v[80:83],a[20:23],a[104:107],v[80:83]\n"
"ds_read_b64_tr_b16 a[56:57],v242 offset:9312\n"
"ds_read_b64_tr_b16 a[58:59],v242 offset:13920\n"
"s_add_u32 m0,s60,40960\n"
"v_mfma_f32_16x16x32_bf16 v[84:87],a[20:23],a[108:111],v[84:87]\n"
"buffer_load_dwordx4 v250,s[40:43],s87 offen offset:0 lds\n"
"s_waitcnt lgkmcnt(12)\n"
"v_mfma_f32_16x16x32_bf16 v[88:91],a[24:27],a[104:107],v[88:91]\n"
"ds_read_b64_tr_b16 a[60:61],v242 offset:9344\n"
"ds_read_b64_tr_b16 a[62:63],v242 offset:13952\n"
"v_mfma_f32_16x16x32_bf16 v[92:95],a[24:27],a[108:111],v[92:95]\n"
"v_cmp_u_f32 vcc,v208,v212\n"
"s_cbranch_vccnz L_fze8_1_%=\n"
"L_fre8_1_%=:\n"
"v_mfma_f32_16x16x32_bf16 v[96:99],a[28:31],a[104:107],v[96:99]\n"
"ds_read_b64_tr_b16 a[64:65],v242 offset:9376\n"
"ds_read_b64_tr_b16 a[66:67],v242 offset:13984\n"
"v_mfma_f32_16x16x32_bf16 v[100:103],a[28:31],a[108:111],v[100:103]\n"
"buffer_load_dwordx4 v251,s[40:43],s87 offen offset:1024 lds\n"
"v_mfma_f32_16x16x32_bf16 v[104:107],a[32:35],a[104:107],v[104:107]\n"
"ds_read_b64_tr_b16 a[68:69],v242 offset:9408\n"
"ds_read_b64_tr_b16 a[70:71],v242 offset:14016\n"
"v_mfma_f32_16x16x32_bf16 v[108:111],a[32:35],a[108:111],v[108:111]\n"
"s_waitcnt lgkmcnt(12)\n"
"v_mfma_f32_16x16x32_bf16 v[112:115],a[36:39],a[104:107],v[112:115]\n"
"ds_read_b64_tr_b16 a[0:1],v242 offset:9440\n"
"ds_read_b64_tr_b16 a[2:3],v242 offset:14048\n"
"v_mfma_f32_16x16x32_bf16 v[116:119],a[36:39],a[108:111],v[116:119]\n"
"buffer_load_dwordx4 v252,s[40:43],s87 offen offset:2048 lds\n"
"v_mfma_f32_16x16x32_bf16 v[120:123],a[40:43],a[104:107],v[120:123]\n"
"ds_read_b64_tr_b16 a[4:5],v242 offset:9472\n"
"ds_read_b64_tr_b16 a[6:7],v242 offset:14080\n"
"v_mfma_f32_16x16x32_bf16 v[124:127],a[40:43],a[108:111],v[124:127]\n"
"v_mfma_f32_16x16x32_bf16 v[128:131],a[44:47],a[104:107],v[128:131]\n"
"ds_read_b64_tr_b16 a[8:9],v242 offset:18464\n"
"ds_read_b64_tr_b16 a[10:11],v242 offset:23072\n"
"v_mfma_f32_16x16x32_bf16 v[132:135],a[44:47],a[108:111],v[132:135]\n"
"buffer_load_dwordx4 v253,s[40:43],s87 offen offset:3072 lds\n"
"s_waitcnt lgkmcnt(12)\n"
"v_mfma_f32_16x16x32_bf16 v[72:75],a[48:51],a[112:115],v[72:75]\n"
"ds_read_b64_tr_b16 a[12:13],v242 offset:18496\n"
"ds_read_b64_tr_b16 a[14:15],v242 offset:23104\n"
"v_mfma_f32_16x16x32_bf16 v[76:79],a[48:51],a[116:119],v[76:79]\n"
"s_add_u32 m0,s60,45056\n"
"v_mfma_f32_16x16x32_bf16 v[80:83],a[52:55],a[112:115],v[80:83]\n"
"ds_read_b64_tr_b16 a[16:17],v242 offset:18528\n"
"ds_read_b64_tr_b16 a[18:19],v242 offset:23136\n"
"v_mfma_f32_16x16x32_bf16 v[84:87],a[52:55],a[116:119],v[84:87]\n"
"buffer_load_dwordx4 v254,s[40:43],s87 offen offset:0 lds\n"
"v_mfma_f32_16x16x32_bf16 v[88:91],a[56:59],a[112:115],v[88:91]\n"
"ds_read_b64_tr_b16 a[20:21],v242 offset:18560\n"
"ds_read_b64_tr_b16 a[22:23],v242 offset:23168\n"
"v_mfma_f32_16x16x32_bf16 v[92:95],a[56:59],a[116:119],v[92:95]\n"
"s_add_u32 s72,s86,12\n"
"s_sub_u32 s13,s72,18\n"
"s_min_u32 s72,s72,s13\n"
"s_lshl_b32 s72,s72,10\n"
"s_waitcnt lgkmcnt(12)\n"
"L_pgs384_%=:\n"
"s_cbranch_execz L_hrs_%=\n"
"v_mfma_f32_16x16x32_bf16 v[96:99],a[60:63],a[112:115],v[96:99]\n"
"ds_read_b64_tr_b16 a[24:25],v242 offset:18592\n"
"ds_read_b64_tr_b16 a[26:27],v242 offset:23200\n"
"v_mfma_f32_16x16x32_bf16 v[100:103],a[60:63],a[116:119],v[100:103]\n"
"buffer_load_dwordx4 a[136:139],v9,s[52:55],s72 offen sc1\n"
"v_mfma_f32_16x16x32_bf16 v[104:107],a[64:67],a[112:115],v[104:107]\n"
"ds_read_b64_tr_b16 a[28:29],v242 offset:18624\n"
"ds_read_b64_tr_b16 a[30:31],v242 offset:23232\n"
"v_mfma_f32_16x16x32_bf16 v[108:111],a[64:67],a[116:119],v[108:111]\n"
"s_add_u32 s72,s72,73728\n"
"v_mfma_f32_16x16x32_bf16 v[112:115],a[68:71],a[112:115],v[112:115]\n"
"ds_read_b64_tr_b16 a[32:33],v242 offset:18656\n"
"ds_read_b64_tr_b16 a[34:35],v242 offset:23264\n"
"v_mfma_f32_16x16x32_bf16 v[116:119],a[68:71],a[116:119],v[116:119]\n"
"buffer_load_dwordx4 a[140:143],v9,s[52:55],s72 offen sc1\n"
"s_waitcnt lgkmcnt(12)\n"
"v_mfma_f32_16x16x32_bf16 v[120:123],a[0:3],a[112:115],v[120:123]\n"
"ds_read_b64_tr_b16 a[36:37],v242 offset:18688\n"
"ds_read_b64_tr_b16 a[38:39],v242 offset:23296\n"
"v_mfma_f32_16x16x32_bf16 v[124:127],a[0:3],a[116:119],v[124:127]\n"
"s_add_u32 s72,s86,13\n"
"s_sub_u32 s13,s72,18\n"
"s_min_u32 s72,s72,s13\n"
"s_lshl_b32 s72,s72,10\n"
"v_mfma_f32_16x16x32_bf16 v[128:131],a[4:7],a[112:115],v[128:131]\n"
"ds_read_b64_tr_b16 a[40:41],v242 offset:27680\n"
"ds_read_b64_tr_b16 a[42:43],v242 offset:32288\n"
"v_mfma_f32_16x16x32_bf16 v[132:135],a[4:7],a[116:119],v[132:135]\n"
"buffer_load_dwordx4 a[144:147],v9,s[52:55],s72 offen sc1\n"
"v_mfma_f32_16x16x32_bf16 v[72:75],a[8:11],a[120:123],v[72:75]\n"
"ds_read_b64_tr_b16 a[44:45],v242 offset:27712\n"
"ds_read_b64_tr_b16 a[46:47],v242 offset:32320\n"
"v_mfma_f32_16x16x32_bf16 v[76:79],a[8:11],a[124:127],v[76:79]\n"
"s_add_u32 s72,s72,73728\n"
"s_waitcnt lgkmcnt(12)\n"
"v_mfma_f32_16x16x32_bf16 v[80:83],a[12:15],a[120:123],v[80:83]\n"
"ds_read_b64_tr_b16 a[48:49],v242 offset:27744\n"
"ds_read_b64_tr_b16 a[50:51],v242 offset:32352\n"
"v_mfma_f32_16x16x32_bf16 v[84:87],a[12:15],a[124:127],v[84:87]\n"
"buffer_load_dwordx4 a[148:151],v9,s[52:55],s72 offen sc1\n"
"v_mfma_f32_16x16x32_bf16 v[88:91],a[16:19],a[120:123],v[88:91]\n"
"ds_read_b64_tr_b16 a[52:53],v242 offset:27776\n"
"ds_read_b64_tr_b16 a[54:55],v242 offset:32384\n"
"v_mfma_f32_16x16x32_bf16 v[92:95],a[16:19],a[124:127],v[92:95]\n"
"s_add_u32 s72,s86,14\n"
"s_sub_u32 s13,s72,18\n"
"s_min_u32 s72,s72,s13\n"
"s_lshl_b32 s72,s72,10\n"
"v_mfma_f32_16x16x32_bf16 v[96:99],a[20:23],a[120:123],v[96:99]\n"
"ds_read_b64_tr_b16 a[56:57],v242 offset:27808\n"
"ds_read_b64_tr_b16 a[58:59],v242 offset:32416\n"
"v_mfma_f32_16x16x32_bf16 v[100:103],a[20:23],a[124:127],v[100:103]\n"
"buffer_load_dwordx4 a[152:155],v9,s[52:55],s72 offen sc1\n"
"s_waitcnt lgkmcnt(12)\n"
"v_mfma_f32_16x16x32_bf16 v[104:107],a[24:27],a[120:123],v[104:107]\n"
"ds_read_b64_tr_b16 a[60:61],v242 offset:27840\n"
"ds_read_b64_tr_b16 a[62:63],v242 offset:32448\n"
"v_mfma_f32_16x16x32_bf16 v[108:111],a[24:27],a[124:127],v[108:111]\n"
"s_add_u32 s72,s72,73728\n"
"v_mfma_f32_16x16x32_bf16 v[112:115],a[28:31],a[120:123],v[112:115]\n"
"ds_read_b64_tr_b16 a[64:65],v242 offset:27872\n"
"ds_read_b64_tr_b16 a[66:67],v242 offset:32480\n"
"v_mfma_f32_16x16x32_bf16 v[116:119],a[28:31],a[124:127],v[116:119]\n"
"buffer_load_dwordx4 a[156:159],v9,s[52:55],s72 offen sc1\n"
"v_mfma_f32_16x16x32_bf16 v[120:123],a[32:35],a[120:123],v[120:123]\n"
"ds_read_b64_tr_b16 a[68:69],v242 offset:27904\n"
"ds_read_b64_tr_b16 a[70:71],v242 offset:32512\n"
"v_mfma_f32_16x16x32_bf16 v[124:127],a[32:35],a[124:127],v[124:127]\n"
"s_add_u32 s72,s86,15\n"
"s_sub_u32 s13,s72,18\n"
"s_min_u32 s72,s72,s13\n"
"s_lshl_b32 s72,s72,10\n"
"s_waitcnt lgkmcnt(12)\n"
"v_mfma_f32_16x16x32_bf16 v[128:131],a[36:39],a[120:123],v[128:131]\n"
"v_mfma_f32_16x16x32_bf16 v[132:135],a[36:39],a[124:127],v[132:135]\n"
"buffer_load_dwordx4 a[160:163],v9,s[52:55],s72 offen sc1\n"
"v_mfma_f32_16x16x32_bf16 v[72:75],a[40:43],a[128:131],v[72:75]\n"
"v_mfma_f32_16x16x32_bf16 v[76:79],a[40:43],a[132:135],v[76:79]\n"
"s_waitcnt vmcnt(7) lgkmcnt(0)\n"
"s_barrier\n"
"v_mfma_f32_16x16x32_bf16 v[80:83],a[44:47],a[128:131],v[80:83]\n"
"ds_read_b64_tr_b16 a[0:1],v240 offset:0\n"
"ds_read_b64_tr_b16 a[2:3],v240 offset:4608\n"
"v_mfma_f32_16x16x32_bf16 v[84:87],a[44:47],a[132:135],v[84:87]\n"
"ds_read_b64_tr_b16 a[4:5],v240 offset:9216\n"
"ds_read_b64_tr_b16 a[6:7],v240 offset:13824\n"
"s_add_u32 s72,s72,73728\n"
"v_mfma_f32_16x16x32_bf16 v[88:91],a[48:51],a[128:131],v[88:91]\n"
"ds_read_b64_tr_b16 a[8:9],v240 offset:18432\n"
"ds_read_b64_tr_b16 a[10:11],v240 offset:23040\n"
"v_mfma_f32_16x16x32_bf16 v[92:95],a[48:51],a[132:135],v[92:95]\n"
"buffer_load_dwordx4 a[164:167],v9,s[52:55],s72 offen sc1\n"
"v_mfma_f32_16x16x32_bf16 v[96:99],a[52:55],a[128:131],v[96:99]\n"
"ds_read_b64_tr_b16 a[12:13],v240 offset:27648\n"
"ds_read_b64_tr_b16 a[14:15],v240 offset:32256\n"
"s_add_u32 s72,s86,16\n"
"s_sub_u32 s13,s72,18\n"
"s_min_u32 s72,s72,s13\n"
"s_lshl_b32 s72,s72,10\n"
"v_mfma_f32_16x16x32_bf16 v[100:103],a[52:55],a[132:135],v[100:103]\n"
"buffer_load_dwordx4 a[168:171],v9,s[52:55],s72 offen sc1\n"
"v_mfma_f32_16x16x32_bf16 v[104:107],a[56:59],a[128:131],v[104:107]\n"
"ds_read_b64_tr_b16 a[16:17],v240 offset:36864\n"
"ds_read_b64_tr_b16 a[18:19],v240 offset:41472\n"
"v_mfma_f32_16x16x32_bf16 v[108:111],a[56:59],a[132:135],v[108:111]\n"
"v_mfma_f32_16x16x32_bf16 v[112:115],a[60:63],a[128:131],v[112:115]\n"
"ds_read_b64_tr_b16 a[20:21],v240 offset:46080\n"
"ds_read_b64_tr_b16 a[22:23],v240 offset:50688\n"
"v_mfma_f32_16x16x32_bf16 v[116:119],a[60:63],a[132:135],v[116:119]\n"
"ds_read_b64_tr_b16 a[24:25],v240 offset:32\n"
"ds_read_b64_tr_b16 a[26:27],v240 offset:4640\n"
"s_add_u32 s72,s72,73728\n"
"v_mfma_f32_16x16x32_bf16 v[120:123],a[64:67],a[128:131],v[120:123]\n"
"ds_read_b64_tr_b16 a[28:29],v240 offset:9248\n"
"ds_read_b64_tr_b16 a[30:31],v240 offset:13856\n"
"v_mfma_f32_16x16x32_bf16 v[124:127],a[64:67],a[132:135],v[124:127]\n"
"buffer_load_dwordx4 a[172:175],v9,s[52:55],s72 offen sc1\n"
"v_mfma_f32_16x16x32_bf16 v[128:131],a[68:71],a[128:131],v[128:131]\n"
"ds_read_b64_tr_b16 a[32:33],v240 offset:18464\n"
"ds_read_b64_tr_b16 a[34:35],v240 offset:23072\n"
"s_add_u32 s72,s86,17\n"
"s_sub_u32 s13,s72,18\n"
"s_min_u32 s72,s72,s13\n"
"s_lshl_b32 s72,s72,10\n"
"v_mfma_f32_16x16x32_bf16 v[132:135],a[68:71],a[132:135],v[132:135]\n"
"buffer_load_dwordx4 a[176:179],v9,s[52:55],s72 offen sc1\n"
"s_waitcnt lgkmcnt(12) vmcnt(5)\n"
"v_mfma_f32_16x16x32_bf16 v[136:139],a[0:3],a[136:139],v[208:211]\n"
"ds_read_b64_tr_b16 a[36:37],v240 offset:27680\n"
"ds_read_b64_tr_b16 a[38:39],v240 offset:32288\n"
"v_mfma_f32_16x16x32_bf16 v[140:143],a[0:3],a[140:143],v[212:215]\n"
"s_add_u32 s72,s72,73728\n"
"s_nop 1\n"
"v_mfma_f32_16x16x32_bf16 v[64:67],a[4:7],a[144:147],v[136:139]\n"
"ds_read_b64_tr_b16 a[40:41],v240 offset:36896\n"
"ds_read_b64_tr_b16 a[42:43],v240 offset:41504\n"
"v_mfma_f32_16x16x32_bf16 v[68:71],a[4:7],a[148:151],v[140:143]\n"
"buffer_load_dwordx4 a[180:183],v9,s[52:55],s72 offen sc1\n"
"v_mfma_f32_16x16x32_bf16 v[64:67],a[8:11],a[152:155],v[64:67]\n"
"ds_read_b64_tr_b16 a[44:45],v240 offset:46112\n"
"ds_read_b64_tr_b16 a[46:47],v240 offset:50720\n"
"v_mfma_f32_16x16x32_bf16 v[68:71],a[8:11],a[156:159],v[68:71]\n"
"v_and_b32 v234,7,v8\n"
"v_bfe_u32 v235,v8,3,1\n"
"s_waitcnt lgkmcnt(12) vmcnt(0)\n"
"v_mfma_f32_16x16x32_bf16 v[64:67],a[12:15],a[160:163],v[64:67]\n"
"ds_read_b64_tr_b16 a[48:49],v240 offset:64\n"
"ds_read_b64_tr_b16 a[50:51],v240 offset:4672\n"
"v_mfma_f32_16x16x32_bf16 v[68:71],a[12:15],a[164:167],v[68:71]\n"
"v_mul_u32_u24 v235,36864,v235\n"
"v_lshl_add_u32 v234,v234,4,v235\n"
"v_mfma_f32_16x16x32_bf16 v[64:67],a[16:19],a[168:171],v[64:67]\n"
"ds_read_b64_tr_b16 a[52:53],v240 offset:96\n"
"ds_read_b64_tr_b16 a[54:55],v240 offset:4704\n"
"v_mfma_f32_16x16x32_bf16 v[68:71],a[16:19],a[172:175],v[68:71]\n"
"v_lshrrev_b32 v235,4,v8\n"
"v_lshl_add_u32 v238,v235,7,v234\n"
"v_mfma_f32_16x16x32_bf16 v[64:67],a[20:23],a[176:179],v[64:67]\n"
"ds_read_b64_tr_b16 a[56:57],v240 offset:9280\n"
"ds_read_b64_tr_b16 a[58:59],v240 offset:13888\n"
"v_mfma_f32_16x16x32_bf16 v[68:71],a[20:23],a[180:183],v[68:71]\n"
"v_and_b32 v235,1,v235\n"
"v_lshl_add_u32 v234,v235,7,v234\n"
"s_nop 7\n"
"v_cmp_u_f32 vcc,v64,v68\n"
"s_cbranch_vccnz L_fze12_0_%=\n"
"L_fre12_0_%=:\n"
"s_waitcnt lgkmcnt(12)\n"
"v_mfma_f32_16x16x32_bf16 v[72:75],a[24:27],a[136:139],v[72:75]\n"
"ds_read_b64_tr_b16 a[60:61],v240 offset:9312\n"
"ds_read_b64_tr_b16 a[62:63],v240 offset:13920\n"
"v_mfma_f32_16x16x32_bf16 v[76:79],a[24:27],a[140:143],v[76:79]\n"
"v_lshrrev_b32 v235,5,v8\n"
"v_mul_u32_u24 v235,73728,v235\n"
"v_mfma_f32_16x16x32_bf16 v[72:75],a[28:31],a[144:147],v[72:75]\n"
"ds_read_b64_tr_b16 a[64:65],v240 offset:18496\n"
"ds_read_b64_tr_b16 a[66:67],v240 offset:23104\n"
"v_mfma_f32_16x16x32_bf16 v[76:79],a[28:31],a[148:151],v[76:79]\n"
"v_add_u32 v239,v234,v235\n"
"s_mov_b32 s77,37748736\n"
"v_mfma_f32_16x16x32_bf16 v[72:75],a[32:35],a[152:155],v[72:75]\n"
"ds_read_b64_tr_b16 a[68:69],v240 offset:18528\n"
"ds_read_b64_tr_b16 a[70:71],v240 offset:23136\n"
"v_mfma_f32_16x16x32_bf16 v[76:79],a[32:35],a[156:159],v[76:79]\n"
"s_mul_i32 s78,s34,589824\n"
"s_add_u32 s77,s77,s78\n"
"s_waitcnt lgkmcnt(12)\n"
"v_mfma_f32_16x16x32_bf16 v[72:75],a[36:39],a[160:163],v[72:75]\n"
"ds_read_b64_tr_b16 a[0:1],v240 offset:27712\n"
"ds_read_b64_tr_b16 a[2:3],v240 offset:32320\n"
"v_mfma_f32_16x16x32_bf16 v[76:79],a[36:39],a[164:167],v[76:79]\n"
"s_mul_i32 s78,s37,147456\n"
"s_add_u32 s77,s77,s78\n"
"v_mfma_f32_16x16x32_bf16 v[72:75],a[40:43],a[168:171],v[72:75]\n"
"ds_read_b64_tr_b16 a[4:5],v240 offset:27744\n"
"ds_read_b64_tr_b16 a[6:7],v240 offset:32352\n"
"v_mfma_f32_16x16x32_bf16 v[76:79],a[40:43],a[172:175],v[76:79]\n"
"s_mul_i32 s78,s35,9216\n"
"s_add_u32 s77,s77,s78\n"
"v_mfma_f32_16x16x32_bf16 v[72:75],a[44:47],a[176:179],v[72:75]\n"
"ds_read_b64_tr_b16 a[8:9],v240 offset:36928\n"
"ds_read_b64_tr_b16 a[10:11],v240 offset:41536\n"
"v_mfma_f32_16x16x32_bf16 v[76:79],a[44:47],a[180:183],v[76:79]\n"
"s_mul_i32 s78,s36,1024\n"
"s_add_u32 s77,s77,s78\n"
"s_waitcnt lgkmcnt(12)\n"
"v_mfma_f32_16x16x32_bf16 v[80:83],a[48:51],a[136:139],v[80:83]\n"
"ds_read_b64_tr_b16 a[12:13],v240 offset:36960\n"
"ds_read_b64_tr_b16 a[14:15],v240 offset:41568\n"
"v_mfma_f32_16x16x32_bf16 v[84:87],a[48:51],a[140:143],v[84:87]\n"
"s_mov_b32 s75,37756928\n"
"s_mul_i32 s78,s34,589824\n"
"v_mfma_f32_16x16x32_bf16 v[88:91],a[52:55],a[136:139],v[88:91]\n"
"ds_read_b64_tr_b16 a[16:17],v240 offset:46144\n"
"ds_read_b64_tr_b16 a[18:19],v240 offset:50752\n"
"v_mfma_f32_16x16x32_bf16 v[92:95],a[52:55],a[140:143],v[92:95]\n"
"s_add_u32 s75,s75,s78\n"
"s_mul_i32 s78,s37,147456\n"
"v_mfma_f32_16x16x32_bf16 v[80:83],a[56:59],a[144:147],v[80:83]\n"
"ds_read_b64_tr_b16 a[20:21],v240 offset:46176\n"
"ds_read_b64_tr_b16 a[22:23],v240 offset:50784\n"
"v_mfma_f32_16x16x32_bf16 v[84:87],a[56:59],a[148:151],v[84:87]\n"
"s_add_u32 s75,s75,s78\n"
"s_mul_i32 s78,s35,9216\n"
"s_waitcnt lgkmcnt(12)\n"
"v_mfma_f32_16x16x32_bf16 v[88:91],a[60:63],a[144:147],v[88:91]\n"
"ds_read_b64_tr_b16 a[24:25],v240 offset:128\n"
"ds_read_b64_tr_b16 a[26:27],v240 offset:4736\n"
"v_mfma_f32_16x16x32_bf16 v[92:95],a[60:63],a[148:151],v[92:95]\n"
"s_add_u32 s75,s75,s78\n"
"s_mul_i32 s78,s36,256\n"
"v_mfma_f32_16x16x32_bf16 v[80:83],a[64:67],a[152:155],v[80:83]\n"
"ds_read_b64_tr_b16 a[28:29],v240 offset:160\n"
"ds_read_b64_tr_b16 a[30:31],v240 offset:4768\n"
"v_mfma_f32_16x16x32_bf16 v[84:87],a[64:67],a[156:159],v[84:87]\n"
"s_add_u32 s75,s75,s78\n"
"v_cvt_pk_bf16_f32 v16,v64,v65\n"
"v_cvt_pk_bf16_f32 v17,v66,v67\n"
"v_mfma_f32_16x16x32_bf16 v[88:91],a[68:71],a[152:155],v[88:91]\n"
"ds_read_b64_tr_b16 a[32:33],v240 offset:9344\n"
"ds_read_b64_tr_b16 a[34:35],v240 offset:13952\n"
"v_cvt_pk_bf16_f32 v18,v72,v73\n"
"v_mfma_f32_16x16x32_bf16 v[92:95],a[68:71],a[156:159],v[92:95]\n"
"v_cvt_pk_bf16_f32 v19,v74,v75\n"
"s_add_u32 s76,s77,0\n"
"buffer_store_dwordx4 v[16:19],v238,s[88:91],s76 offen offset:0 sc1\n"
"s_waitcnt lgkmcnt(12)\n"
"v_mfma_f32_16x16x32_bf16 v[80:83],a[0:3],a[160:163],v[80:83]\n"
"ds_read_b64_tr_b16 a[36:37],v240 offset:9376\n"
"ds_read_b64_tr_b16 a[38:39],v240 offset:13984\n"
"v_cvt_pk_bf16_f32 v20,v68,v69\n"
"v_mfma_f32_16x16x32_bf16 v[84:87],a[0:3],a[164:167],v[84:87]\n"
"v_cvt_pk_bf16_f32 v21,v70,v71\n"
"v_cvt_pk_bf16_f32 v22,v76,v77\n"
"v_cvt_pk_bf16_f32 v23,v78,v79\n"
"v_mfma_f32_16x16x32_bf16 v[88:91],a[4:7],a[160:163],v[88:91]\n"
"ds_read_b64_tr_b16 a[40:41],v240 offset:18560\n"
"ds_read_b64_tr_b16 a[42:43],v240 offset:23168\n"
"s_add_u32 s76,s77,73728\n"
"v_mfma_f32_16x16x32_bf16 v[92:95],a[4:7],a[164:167],v[92:95]\n"
"buffer_store_dwordx4 v[20:23],v238,s[88:91],s76 offen offset:0 sc1\n"
"v_mfma_f32_16x16x32_bf16 v[80:83],a[8:11],a[168:171],v[80:83]\n"
"ds_read_b64_tr_b16 a[44:45],v240 offset:18592\n"
"ds_read_b64_tr_b16 a[46:47],v240 offset:23200\n"
"v_mfma_f32_16x16x32_bf16 v[84:87],a[8:11],a[172:175],v[84:87]\n"
"s_waitcnt lgkmcnt(12)\n"
"v_mfma_f32_16x16x32_bf16 v[88:91],a[12:15],a[168:171],v[88:91]\n"
"ds_read_b64_tr_b16 a[48:49],v240 offset:27776\n"
"ds_read_b64_tr_b16 a[50:51],v240 offset:32384\n"
"v_mfma_f32_16x16x32_bf16 v[92:95],a[12:15],a[172:175],v[92:95]\n"
"v_mfma_f32_16x16x32_bf16 v[80:83],a[16:19],a[176:179],v[80:83]\n"
"ds_read_b64_tr_b16 a[52:53],v240 offset:27808\n"
"ds_read_b64_tr_b16 a[54:55],v240 offset:32416\n"
"v_mfma_f32_16x16x32_bf16 v[84:87],a[16:19],a[180:183],v[84:87]\n"
"v_mfma_f32_16x16x32_bf16 v[88:91],a[20:23],a[176:179],v[88:91]\n"
"ds_read_b64_tr_b16 a[56:57],v240 offset:36992\n"
"ds_read_b64_tr_b16 a[58:59],v240 offset:41600\n"
"v_mfma_f32_16x16x32_bf16 v[92:95],a[20:23],a[180:183],v[92:95]\n"
"s_waitcnt lgkmcnt(12)\n"
"v_mfma_f32_16x16x32_bf16 v[96:99],a[24:27],a[136:139],v[96:99]\n"
"ds_read_b64_tr_b16 a[60:61],v240 offset:37024\n"
"ds_read_b64_tr_b16 a[62:63],v240 offset:41632\n"
"v_mfma_f32_16x16x32_bf16 v[100:103],a[24:27],a[140:143],v[100:103]\n"
"v_mfma_f32_16x16x32_bf16 v[104:107],a[28:31],a[136:139],v[104:107]\n"
"ds_read_b64_tr_b16 a[64:65],v240 offset:46208\n"
"ds_read_b64_tr_b16 a[66:67],v240 offset:50816\n"
"v_mfma_f32_16x16x32_bf16 v[108:111],a[28:31],a[140:143],v[108:111]\n"
"v_mfma_f32_16x16x32_bf16 v[96:99],a[32:35],a[144:147],v[96:99]\n"
"ds_read_b64_tr_b16 a[68:69],v240 offset:46240\n"
"ds_read_b64_tr_b16 a[70:71],v240 offset:50848\n"
"v_cvt_pk_bf16_f32 v24,v80,v81\n"
"v_mfma_f32_16x16x32_bf16 v[100:103],a[32:35],a[148:151],v[100:103]\n"
"v_cvt_pk_bf16_f32 v25,v82,v83\n"
"v_cvt_pk_bf16_f32 v26,v88,v89\n"
"v_cvt_pk_bf16_f32 v27,v90,v91\n"
"s_waitcnt lgkmcnt(12)\n"
"v_mfma_f32_16x16x32_bf16 v[104:107],a[36:39],a[144:147],v[104:107]\n"
"ds_read_b64_tr_b16 a[0:1],v240 offset:192\n"
"ds_read_b64_tr_b16 a[2:3],v240 offset:4800\n"
"s_add_u32 s76,s77,0\n"
"v_mfma_f32_16x16x32_bf16 v[108:111],a[36:39],a[148:151],v[108:111]\n"
"buffer_store_dwordx4 v[24:27],v238,s[88:91],s76 offen offset:512 sc1\n"
"v_cvt_pk_bf16_f32 v28,v84,v85\n"
"v_cvt_pk_bf16_f32 v29,v86,v87\n"
"v_mfma_f32_16x16x32_bf16 v[96:99],a[40:43],a[152:155],v[96:99]\n"
"ds_read_b64_tr_b16 a[4:5],v240 offset:224\n"
"ds_read_b64_tr_b16 a[6:7],v240 offset:4832\n"
"v_cvt_pk_bf16_f32 v30,v92,v93\n"
"v_mfma_f32_16x16x32_bf16 v[100:103],a[40:43],a[156:159],v[100:103]\n"
"v_cvt_pk_bf16_f32 v31,v94,v95\n"
"s_add_u32 s76,s77,73728\n"
"buffer_store_dwordx4 v[28:31],v238,s[88:91],s76 offen offset:512 sc1\n"
"v_mfma_f32_16x16x32_bf16 v[104:107],a[44:47],a[152:155],v[104:107]\n"
"ds_read_b64_tr_b16 a[8:9],v240 offset:9408\n"
"ds_read_b64_tr_b16 a[10:11],v240 offset:14016\n"
"v_mfma_f32_16x16x32_bf16 v[108:111],a[44:47],a[156:159],v[108:111]\n"
"s_waitcnt lgkmcnt(12)\n"
"v_mfma_f32_16x16x32_bf16 v[96:99],a[48:51],a[160:163],v[96:99]\n"
"ds_read_b64_tr_b16 a[12:13],v240 offset:9440\n"
"ds_read_b64_tr_b16 a[14:15],v240 offset:14048\n"
"v_mfma_f32_16x16x32_bf16 v[100:103],a[48:51],a[164:167],v[100:103]\n"
"v_mfma_f32_16x16x32_bf16 v[104:107],a[52:55],a[160:163],v[104:107]\n"
"ds_read_b64_tr_b16 a[16:17],v240 offset:18624\n"
"ds_read_b64_tr_b16 a[18:19],v240 offset:23232\n"
"v_mfma_f32_16x16x32_bf16 v[108:111],a[52:55],a[164:167],v[108:111]\n"
"v_mfma_f32_16x16x32_bf16 v[96:99],a[56:59],a[168:171],v[96:99]\n"
"ds_read_b64_tr_b16 a[20:21],v240 offset:18656\n"
"ds_read_b64_tr_b16 a[22:23],v240 offset:23264\n"
"v_mfma_f32_16x16x32_bf16 v[100:103],a[56:59],a[172:175],v[100:103]\n"
"s_waitcnt lgkmcnt(12)\n"
"v_mfma_f32_16x16x32_bf16 v[104:107],a[60:63],a[168:171],v[104:107]\n"
"ds_read_b64_tr_b16 a[24:25],v240 offset:27840\n"
"ds_read_b64_tr_b16 a[26:27],v240 offset:32448\n"
"v_mfma_f32_16x16x32_bf16 v[108:111],a[60:63],a[172:175],v[108:111]\n"
"v_mfma_f32_16x16x32_bf16 v[96:99],a[64:67],a[176:179],v[96:99]\n"
"ds_read_b64_tr_b16 a[28:29],v240 offset:27872\n"
"ds_read_b64_tr_b16 a[30:31],v240 offset:32480\n"
"v_mfma_f32_16x16x32_bf16 v[100:103],a[64:67],a[180:183],v[100:103]\n"
"v_mfma_f32_16x16x32_bf16 v[104:107],a[68:71],a[176:179],v[104:107]\n"
"ds_read_b64_tr_b16 a[32:33],v240 offset:37056\n"
"ds_read_b64_tr_b16 a[34:35],v240 offset:41664\n"
"v_mfma_f32_16x16x32_bf16 v[108:111],a[68:71],a[180:183],v[108:111]\n"
"s_waitcnt lgkmcnt(12)\n"
"v_mfma_f32_16x16x32_bf16 v[112:115],a[0:3],a[136:139],v[112:115]\n"
"ds_read_b64_tr_b16 a[36:37],v240 offset:37088\n"
"ds_read_b64_tr_b16 a[38:39],v240 offset:41696\n"
"v_mfma_f32_16x16x32_bf16 v[116:119],a[0:3],a[140:143],v[116:119]\n"
"v_mfma_f32_16x16x32_bf16 v[120:123],a[4:7],a[136:139],v[120:123]\n"
"ds_read_b64_tr_b16 a[40:41],v240 offset:46272\n"
"ds_read_b64_tr_b16 a[42:43],v240 offset:50880\n"
"v_mfma_f32_16x16x32_bf16 v[124:127],a[4:7],a[140:143],v[124:127]\n"
"v_mfma_f32_16x16x32_bf16 v[112:115],a[8:11],a[144:147],v[112:115]\n"
"ds_read_b64_tr_b16 a[44:45],v240 offset:46304\n"
"ds_read_b64_tr_b16 a[46:47],v240 offset:50912\n"
"v_cvt_pk_bf16_f32 v32,v96,v97\n"
"v_mfma_f32_16x16x32_bf16 v[116:119],a[8:11],a[148:151],v[116:119]\n"
"v_cvt_pk_bf16_f32 v33,v98,v99\n"
"v_cvt_pk_bf16_f32 v34,v104,v105\n"
"v_cvt_pk_bf16_f32 v35,v106,v107\n"
"s_waitcnt lgkmcnt(12)\n"
"v_mfma_f32_16x16x32_bf16 v[120:123],a[12:15],a[144:147],v[120:123]\n"
"ds_read_b64_tr_b16 a[48:49],v240 offset:256\n"
"ds_read_b64_tr_b16 a[50:51],v240 offset:4864\n"
"s_add_u32 s76,s77,4096\n"
"v_mfma_f32_16x16x32_bf16 v[124:127],a[12:15],a[148:151],v[124:127]\n"
"buffer_store_dwordx4 v[32:35],v238,s[88:91],s76 offen offset:0 sc1\n"
"v_cvt_pk_bf16_f32 v36,v100,v101\n"
"v_cvt_pk_bf16_f32 v37,v102,v103\n"
"v_mfma_f32_16x16x32_bf16 v[112:115],a[16:19],a[152:155],v[112:115]\n"
"ds_read_b64_tr_b16 a[52:53],v240 offset:9472\n"
"ds_read_b64_tr_b16 a[54:55],v240 offset:14080\n"
"v_cvt_pk_bf16_f32 v38,v108,v109\n"
"v_mfma_f32_16x16x32_bf16 v[116:119],a[16:19],a[156:159],v[116:119]\n"
"v_cvt_pk_bf16_f32 v39,v110,v111\n"
"s_add_u32 s76,s77,77824\n"
"buffer_store_dwordx4 v[36:39],v238,s[88:91],s76 offen offset:0 sc1\n"
"v_mfma_f32_16x16x32_bf16 v[120:123],a[20:23],a[152:155],v[120:123]\n"
"ds_read_b64_tr_b16 a[56:57],v240 offset:18688\n"
"ds_read_b64_tr_b16 a[58:59],v240 offset:23296\n"
"v_mfma_f32_16x16x32_bf16 v[124:127],a[20:23],a[156:159],v[124:127]\n"
"s_waitcnt lgkmcnt(12)\n"
"v_mfma_f32_16x16x32_bf16 v[112:115],a[24:27],a[160:163],v[112:115]\n"
"ds_read_b64_tr_b16 a[60:61],v240 offset:27904\n"
"ds_read_b64_tr_b16 a[62:63],v240 offset:32512\n"
"v_mfma_f32_16x16x32_bf16 v[116:119],a[24:27],a[164:167],v[116:119]\n"
"v_mfma_f32_16x16x32_bf16 v[120:123],a[28:31],a[160:163],v[120:123]\n"
"ds_read_b64_tr_b16 a[64:65],v240 offset:37120\n"
"ds_read_b64_tr_b16 a[66:67],v240 offset:41728\n"
"v_mfma_f32_16x16x32_bf16 v[124:127],a[28:31],a[164:167],v[124:127]\n"
"v_mfma_f32_16x16x32_bf16 v[112:115],a[32:35],a[168:171],v[112:115]\n"
"ds_read_b64_tr_b16 a[68:69],v240 offset:46336\n"
"ds_read_b64_tr_b16 a[70:71],v240 offset:50944\n"
"v_mfma_f32_16x16x32_bf16 v[116:119],a[32:35],a[172:175],v[116:119]\n"
"s_waitcnt lgkmcnt(12)\n"
"v_mfma_f32_16x16x32_bf16 v[120:123],a[36:39],a[168:171],v[120:123]\n"
"v_mfma_f32_16x16x32_bf16 v[124:127],a[36:39],a[172:175],v[124:127]\n"
"v_mfma_f32_16x16x32_bf16 v[112:115],a[40:43],a[176:179],v[112:115]\n"
"v_mfma_f32_16x16x32_bf16 v[116:119],a[40:43],a[180:183],v[116:119]\n"
"v_mfma_f32_16x16x32_bf16 v[120:123],a[44:47],a[176:179],v[120:123]\n"
"v_mfma_f32_16x16x32_bf16 v[124:127],a[44:47],a[180:183],v[124:127]\n"
"s_waitcnt lgkmcnt(6)\n"
"v_mfma_f32_16x16x32_bf16 v[128:131],a[48:51],a[136:139],v[128:131]\n"
"v_mfma_f32_16x16x32_bf16 v[132:135],a[48:51],a[140:143],v[132:135]\n"
"v_mfma_f32_16x16x32_bf16 v[128:131],a[52:55],a[144:147],v[128:131]\n"
"v_mfma_f32_16x16x32_bf16 v[132:135],a[52:55],a[148:151],v[132:135]\n"
"v_mfma_f32_16x16x32_bf16 v[128:131],a[56:59],a[152:155],v[128:131]\n"
"v_cvt_pk_bf16_f32 v40,v112,v113\n"
"v_cvt_pk_bf16_f32 v41,v114,v115\n"
"v_cvt_pk_bf16_f32 v42,v120,v121\n"
"v_mfma_f32_16x16x32_bf16 v[132:135],a[56:59],a[156:159],v[132:135]\n"
"v_cvt_pk_bf16_f32 v43,v122,v123\n"
"s_add_u32 s76,s77,4096\n"
"buffer_store_dwordx4 v[40:43],v238,s[88:91],s76 offen offset:512 sc1\n"
"s_waitcnt lgkmcnt(0)\n"
"v_mfma_f32_16x16x32_bf16 v[128:131],a[60:63],a[160:163],v[128:131]\n"
"v_cvt_pk_bf16_f32 v44,v116,v117\n"
"v_cvt_pk_bf16_f32 v45,v118,v119\n"
"v_cvt_pk_bf16_f32 v46,v124,v125\n"
"v_mfma_f32_16x16x32_bf16 v[132:135],a[60:63],a[164:167],v[132:135]\n"
"v_cvt_pk_bf16_f32 v47,v126,v127\n"
"s_add_u32 s76,s77,77824\n"
"buffer_store_dwordx4 v[44:47],v238,s[88:91],s76 offen offset:512 sc1\n"
"v_mfma_f32_16x16x32_bf16 v[128:131],a[64:67],a[168:171],v[128:131]\n"
"v_mfma_f32_16x16x32_bf16 v[132:135],a[64:67],a[172:175],v[132:135]\n"
"v_mfma_f32_16x16x32_bf16 v[128:131],a[68:71],a[176:179],v[128:131]\n"
"v_mfma_f32_16x16x32_bf16 v[132:135],a[68:71],a[180:183],v[132:135]\n"
"s_nop 7\n"
"v_cvt_pk_bf16_f32 v48,v128,v129\n"
"v_cvt_pk_bf16_f32 v49,v130,v131\n"
"v_cvt_pk_bf16_f32 v50,v132,v133\n"
"v_cvt_pk_bf16_f32 v51,v134,v135\n"
"s_nop 0\n"
"v_permlane32_swap_b32 v48,v50\n"
"v_permlane32_swap_b32 v49,v51\n"
"buffer_store_dwordx4 v[48:51],v239,s[88:91],s75 offen sc1\n"
"v_accvgpr_read_b32 v144,a208\n"
"v_accvgpr_read_b32 v145,a209\n"
"v_accvgpr_read_b32 v146,a210\n"
"v_accvgpr_read_b32 v147,a211\n"
"v_accvgpr_read_b32 v148,a212\n"
"v_accvgpr_read_b32 v149,a213\n"
"v_accvgpr_read_b32 v150,a214\n"
"v_accvgpr_read_b32 v151,a215\n"
"v_accvgpr_read_b32 v152,a216\n"
"v_accvgpr_read_b32 v153,a217\n"
"v_accvgpr_read_b32 v154,a218\n"
"v_accvgpr_read_b32 v155,a219\n"
"v_accvgpr_read_b32 v156,a220\n"
"v_accvgpr_read_b32 v157,a221\n"
"v_accvgpr_read_b32 v158,a222\n"
"v_accvgpr_read_b32 v159,a223\n"
"v_accvgpr_read_b32 v160,a224\n"
"v_accvgpr_read_b32 v161,a225\n"
"v_accvgpr_read_b32 v162,a226\n"
"v_accvgpr_read_b32 v163,a227\n"
"v_accvgpr_read_b32 v164,a228\n"
"v_accvgpr_read_b32 v165,a229\n"
"v_accvgpr_read_b32 v166,a230\n"
"v_accvgpr_read_b32 v167,a231\n"
"v_accvgpr_read_b32 v168,a232\n"
"v_accvgpr_read_b32 v169,a233\n"
"v_accvgpr_read_b32 v170,a234\n"
"v_accvgpr_read_b32 v171,a235\n"
"v_accvgpr_read_b32 v172,a236\n"
"v_accvgpr_read_b32 v173,a237\n"
"v_accvgpr_read_b32 v174,a238\n"
"v_accvgpr_read_b32 v175,a239\n"
"v_accvgpr_read_b32 v176,a240\n"
"v_accvgpr_read_b32 v177,a241\n"
"v_accvgpr_read_b32 v178,a242\n"
"v_accvgpr_read_b32 v179,a243\n"
"v_accvgpr_read_b32 v180,a244\n"
"v_accvgpr_read_b32 v181,a245\n"
"v_accvgpr_read_b32 v182,a246\n"
"v_accvgpr_read_b32 v183,a247\n"
"s_mov_b32 s75,0\n"
"s_mul_i32 s78,s34,147456\n"
"s_add_u32 s75,s75,s78\n"
"s_mul_i32 s78,s36,36864\n"
"s_add_u32 s75,s75,s78\n"
"s_mul_i32 s78,s35,9216\n"
"s_add_u32 s75,s75,s78\n"
"s_mul_i32 s78,s37,288\n"
"s_add_u32 s75,s75,s78\n"
"s_add_u32 s48,s28,s75\n"
"s_addc_u32 s49,s29,0\n"
"s_and_b32 s49,s49,0xffff\n"
"s_mov_b32 s50,8352\n"
"s_mov_b32 s51,0x20000\n"
"v_and_b32 v234,7,v8\n"
"v_mul_u32_u24 v234,1152,v234\n"
"v_lshrrev_b32 v235,1,v244\n"
"v_add_u32 v238,v234,v235\n"
"v_lshrrev_b32 v235,1,v245\n"
"v_add_u32 v239,v234,v235\n"
"s_mov_b32 s77,37748736\n"
"s_mul_i32 s78,s34,589824\n"
"s_add_u32 s77,s77,s78\n"
"s_mul_i32 s78,s36,147456\n"
"s_add_u32 s77,s77,s78\n"
"s_mul_i32 s78,s35,36864\n"
"s_add_u32 s77,s77,s78\n"
"s_mul_i32 s78,s37,9216\n"
"s_add_u32 s77,s77,s78\n"
"s_add_u32 s76,s77,4096\n"
"s_add_u32 s75,s77,8192\n"
"s_mov_b32 s78,0xffff0000\n"
"s_sleep 24\n"
"L_pe_%=:\n"
"buffer_load_dwordx4 v[48:51],v9,s[88:91],s75 offen sc1\n"
"buffer_load_dwordx4 v[16:19],v9,s[88:91],s77 offen offset:0 sc1\n"
"buffer_load_dwordx4 v[20:23],v9,s[88:91],s77 offen offset:1024 sc1\n"
"buffer_load_dwordx4 v[24:27],v9,s[88:91],s77 offen offset:2048 sc1\n"
"buffer_load_dwordx4 v[28:31],v9,s[88:91],s77 offen offset:3072 sc1\n"
"buffer_load_dwordx4 v[32:35],v9,s[88:91],s76 offen offset:0 sc1\n"
"buffer_load_dwordx4 v[36:39],v9,s[88:91],s76 offen offset:1024 sc1\n"
"buffer_load_dwordx4 v[40:43],v9,s[88:91],s76 offen offset:2048 sc1\n"
"buffer_load_dwordx4 v[44:47],v9,s[88:91],s76 offen offset:3072 sc1\n"
"v_mov_b32 v230,0\n"
"s_waitcnt vmcnt(8)\n"
"v_lshlrev_b32 v84,16,v48\n"
"v_and_b32 v85,s78,v48\n"
"v_lshlrev_b32 v86,16,v49\n"
"v_and_b32 v87,s78,v49\n"
"v_lshlrev_b32 v88,16,v50\n"
"v_and_b32 v89,s78,v50\n"
"v_lshlrev_b32 v90,16,v51\n"
"v_and_b32 v91,s78,v51\n"
"s_nop 0\n"
"v_permlane32_swap_b32 v84,v88\n"
"v_permlane32_swap_b32 v85,v89\n"
"v_permlane32_swap_b32 v86,v90\n"
"v_permlane32_swap_b32 v87,v91\n"
"v_pk_add_f32 v[84:85],v[84:85],v[88:89]\n"
"v_pk_add_f32 v[86:87],v[86:87],v[90:91]\n"
"v_mov_b32 v88,v84\n"
"v_mov_b32 v89,v85\n"
"v_mov_b32 v90,v86\n"
"v_mov_b32 v91,v87\n"
"v_permlane16_swap_b32 v84,v88\n"
"v_permlane16_swap_b32 v85,v89\n"
"v_permlane16_swap_b32 v86,v90\n"
"v_permlane16_swap_b32 v87,v91\n"
"v_pk_add_f32 v[80:81],v[84:85],v[88:89]\n"
"v_add_f32 v82,v86,v90\n"
"v_add_f32 v83,v87,v91\n"
"v_cmp_u_f32 vcc,v80,v82\n"
"s_cbranch_vccnz L_rt7_%=\n"
"L_rb7_%=:\n"
"v_fmac_f32 v230,v80,v81\n"
"v_fmac_f32 v230,v82,v83\n"
"v_pk_fma_f32 v[80:81],v[160:161],v[80:81],v[180:181]\n"
"v_pk_fma_f32 v[82:83],v[162:163],v[82:83],v[182:183]\n"
"v_cvt_pk_bf16_f32 v192,v80,v81\n"
"v_cvt_pk_bf16_f32 v193,v82,v83\n"
"s_waitcnt vmcnt(6)\n"
"v_lshlrev_b32 v64,16,v16\n"
"v_and_b32 v65,s78,v16\n"
"v_lshlrev_b32 v66,16,v17\n"
"v_and_b32 v67,s78,v17\n"
"v_lshlrev_b32 v68,16,v18\n"
"v_and_b32 v69,s78,v18\n"
"v_lshlrev_b32 v70,16,v19\n"
"v_and_b32 v71,s78,v19\n"
"v_lshlrev_b32 v84,16,v20\n"
"v_and_b32 v85,s78,v20\n"
"v_lshlrev_b32 v86,16,v21\n"
"v_and_b32 v87,s78,v21\n"
"v_lshlrev_b32 v88,16,v22\n"
"v_and_b32 v89,s78,v22\n"
"v_lshlrev_b32 v90,16,v23\n"
"v_and_b32 v91,s78,v23\n"
"v_pk_add_f32 v[64:65],v[64:65],v[84:85]\n"
"v_pk_add_f32 v[66:67],v[66:67],v[86:87]\n"
"v_pk_add_f32 v[68:69],v[68:69],v[88:89]\n"
"v_pk_add_f32 v[70:71],v[70:71],v[90:91]\n"
"s_waitcnt vmcnt(4)\n"
"v_lshlrev_b32 v92,16,v24\n"
"v_and_b32 v93,s78,v24\n"
"v_lshlrev_b32 v94,16,v25\n"
"v_and_b32 v95,s78,v25\n"
"v_lshlrev_b32 v96,16,v26\n"
"v_and_b32 v97,s78,v26\n"
"L_pgs400_%=:\n"
"s_cbranch_execz L_hrs_%=\n"
"v_lshlrev_b32 v98,16,v27\n"
"v_and_b32 v99,s78,v27\n"
"v_lshlrev_b32 v100,16,v28\n"
"v_and_b32 v101,s78,v28\n"
"v_lshlrev_b32 v102,16,v29\n"
"v_and_b32 v103,s78,v29\n"
"v_lshlrev_b32 v104,16,v30\n"
"v_and_b32 v105,s78,v30\n"
"v_lshlrev_b32 v106,16,v31\n"
"v_and_b32 v107,s78,v31\n"
"v_pk_add_f32 v[92:93],v[92:93],v[100:101]\n"
"v_pk_add_f32 v[94:95],v[94:95],v[102:103]\n"
"v_pk_add_f32 v[96:97],v[96:97],v[104:105]\n"
"v_pk_add_f32 v[98:99],v[98:99],v[106:107]\n"
"v_pk_add_f32 v[64:65],v[64:65],v[92:93]\n"
"v_pk_add_f32 v[66:67],v[66:67],v[94:95]\n"
"v_pk_add_f32 v[68:69],v[68:69],v[96:97]\n"
"v_pk_add_f32 v[70:71],v[70:71],v[98:99]\n"
"v_fmac_f32 v230,v64,v65\n"
"v_fmac_f32 v230,v66,v67\n"
"v_fmac_f32 v230,v68,v69\n"
"v_fmac_f32 v230,v70,v71\n"
"v_pk_fma_f32 v[64:65],v[144:145],v[64:65],v[164:165]\n"
"v_pk_fma_f32 v[66:67],v[146:147],v[66:67],v[166:167]\n"
"v_pk_fma_f32 v[68:69],v[148:149],v[68:69],v[168:169]\n"
"v_pk_fma_f32 v[70:71],v[150:151],v[70:71],v[170:171]\n"
"v_cvt_pk_bf16_f32 v184,v64,v65\n"
"v_cvt_pk_bf16_f32 v185,v66,v67\n"
"v_cvt_pk_bf16_f32 v186,v68,v69\n"
"v_cvt_pk_bf16_f32 v187,v70,v71\n"
"s_waitcnt vmcnt(2)\n"
"v_lshlrev_b32 v72,16,v32\n"
"v_and_b32 v73,s78,v32\n"
"v_lshlrev_b32 v74,16,v33\n"
"v_and_b32 v75,s78,v33\n"
"v_lshlrev_b32 v76,16,v34\n"
"v_and_b32 v77,s78,v34\n"
"v_lshlrev_b32 v78,16,v35\n"
"v_and_b32 v79,s78,v35\n"
"v_lshlrev_b32 v84,16,v36\n"
"v_and_b32 v85,s78,v36\n"
"v_lshlrev_b32 v86,16,v37\n"
"v_and_b32 v87,s78,v37\n"
"v_lshlrev_b32 v88,16,v38\n"
"v_and_b32 v89,s78,v38\n"
"v_lshlrev_b32 v90,16,v39\n"
"v_and_b32 v91,s78,v39\n"
"v_pk_add_f32 v[72:73],v[72:73],v[84:85]\n"
"v_pk_add_f32 v[74:75],v[74:75],v[86:87]\n"
"v_pk_add_f32 v[76:77],v[76:77],v[88:89]\n"
"v_pk_add_f32 v[78:79],v[78:79],v[90:91]\n"
"s_waitcnt vmcnt(0)\n"
"v_lshlrev_b32 v92,16,v40\n"
"v_and_b32 v93,s78,v40\n"
"v_lshlrev_b32 v94,16,v41\n"
"v_and_b32 v95,s78,v41\n"
"v_lshlrev_b32 v96,16,v42\n"
"v_and_b32 v97,s78,v42\n"
"v_lshlrev_b32 v98,16,v43\n"
"v_and_b32 v99,s78,v43\n"
"v_lshlrev_b32 v100,16,v44\n"
"v_and_b32 v101,s78,v44\n"
"v_lshlrev_b32 v102,16,v45\n"
"v_and_b32 v103,s78,v45\n"
"v_lshlrev_b32 v104,16,v46\n"
"v_and_b32 v105,s78,v46\n"
"v_lshlrev_b32 v106,16,v47\n"
"v_and_b32 v107,s78,v47\n"
"v_pk_add_f32 v[92:93],v[92:93],v[100:101]\n"
"v_pk_add_f32 v[94:95],v[94:95],v[102:103]\n"
"v_pk_add_f32 v[96:97],v[96:97],v[104:105]\n"
"v_pk_add_f32 v[98:99],v[98:99],v[106:107]\n"
"v_pk_add_f32 v[72:73],v[72:73],v[92:93]\n"
"v_pk_add_f32 v[74:75],v[74:75],v[94:95]\n"
"v_pk_add_f32 v[76:77],v[76:77],v[96:97]\n"
"v_pk_add_f32 v[78:79],v[78:79],v[98:99]\n"
"v_fmac_f32 v230,v72,v73\n"
"v_fmac_f32 v230,v74,v75\n"
"v_fmac_f32 v230,v76,v77\n"
"v_fmac_f32 v230,v78,v79\n"
"v_pk_fma_f32 v[72:73],v[152:153],v[72:73],v[172:173]\n"
"v_pk_fma_f32 v[74:75],v[154:155],v[74:75],v[174:175]\n"
"v_pk_fma_f32 v[76:77],v[156:157],v[76:77],v[176:177]\n"
"v_pk_fma_f32 v[78:79],v[158:159],v[78:79],v[178:179]\n"
"v_cvt_pk_bf16_f32 v188,v72,v73\n"
"v_cvt_pk_bf16_f32 v189,v74,v75\n"
"v_cvt_pk_bf16_f32 v190,v76,v77\n"
"v_cvt_pk_bf16_f32 v191,v78,v79\n"
"v_cmp_u_f32 vcc,v230,v230\n"
"s_cbranch_vccnz L_rt8_%=\n"
"L_rb8_%=:\n"
"buffer_store_dwordx2 v[184:185],v238,s[48:51],0 offen offset:0\n"
"buffer_store_dwordx2 v[186:187],v238,s[48:51],0 offen offset:32\n"
"buffer_store_dwordx2 v[188:189],v238,s[48:51],0 offen offset:128\n"
"buffer_store_dwordx2 v[190:191],v238,s[48:51],0 offen offset:160\n"
"buffer_store_dwordx2 v[192:193],v239,s[48:51],0 offen offset:0\n"
"s_mov_b32 s76,0x7ffffff0\n"
"s_cmp_eq_u32 s39,0\n"
"s_cselect_b32 s75,262144,s76\n"
"v_mov_b32 v234,s38\n"
"buffer_store_dword v234,v11,s[88:91],s75 offen sc1\n"
"s_endpgm\n"
"L_stub0_%=:\n"
"s_mov_b32 s67,0\n"
"s_branch L_rare0_%=\n"
"L_stub9_%=:\n"
"s_mov_b32 s67,9\n"
"L_rare0_%=:\n"
"s_nop 15\n"
"v_max3_f32 v235,v48,v49,v50\n"
"v_max3_f32 v235,v235,v51,v52\n"
"v_max3_f32 v235,v235,v53,v54\n"
"v_max3_f32 v235,v235,v55,v16\n"
"v_max3_f32 v235,v235,v17,v18\n"
"v_max3_f32 v235,v235,v19,v20\n"
"v_max3_f32 v235,v235,v21,v22\n"
"v_max_f32 v235,v235,v23\n"
"v_mov_b32 v236,v235\n"
"s_nop 1\n"
"v_permlane32_swap_b32 v235,v236\n"
"v_max_f32 v235,v235,v236\n"
"v_mov_b32 v236,v235\n"
"s_nop 1\n"
"v_permlane16_swap_b32 v235,v236\n"
"v_max_f32 v235,v235,v236\n"
"v_add_f32 v235,s70,v235\n"
"v_max_f32 v235,0,v235\n"
"v_mul_f32 v236,-0.5,v235\n"
"v_exp_f32 v236,v236\n"
"v_sub_f32 v48,v48,v235\n"
"v_sub_f32 v49,v49,v235\n"
"v_sub_f32 v50,v50,v235\n"
"v_sub_f32 v51,v51,v235\n"
"v_sub_f32 v52,v52,v235\n"
"v_sub_f32 v53,v53,v235\n"
"v_sub_f32 v54,v54,v235\n"
"v_sub_f32 v55,v55,v235\n"
"v_sub_f32 v16,v16,v235\n"
"v_sub_f32 v17,v17,v235\n"
"v_sub_f32 v18,v18,v235\n"
"v_sub_f32 v19,v19,v235\n"
"v_sub_f32 v20,v20,v235\n"
"v_sub_f32 v21,v21,v235\n"
"v_sub_f32 v22,v22,v235\n"
"v_sub_f32 v23,v23,v235\n"
"v_mul_f32_dpp v96,v96,v236 quad_perm:[0,1,2,3] row_mask:0x5 bank_mask:0xf\n"
"v_mul_f32_dpp v97,v97,v236 quad_perm:[0,1,2,3] row_mask:0x5 bank_mask:0xf\n"
"s_nop 1\n"
"v_mul_f32_dpp v96,v96,v236 quad_perm:[0,1,2,3] row_mask:0x5 bank_mask:0xf\n"
"v_mul_f32_dpp v97,v97,v236 quad_perm:[0,1,2,3] row_mask:0x5 bank_mask:0xf\n"
"s_nop 1\n"
"s_call_b64 s[18:19],L_rs0_%=\n"
"v_max3_f32 v235,v56,v57,v58\n"
"v_max3_f32 v235,v235,v59,v60\n"
"v_max3_f32 v235,v235,v61,v62\n"
"v_max3_f32 v235,v235,v63,v24\n"
"v_max3_f32 v235,v235,v25,v26\n"
"v_max3_f32 v235,v235,v27,v28\n"
"v_max3_f32 v235,v235,v29,v30\n"
"v_max_f32 v235,v235,v31\n"
"v_mov_b32 v236,v235\n"
"s_nop 1\n"
"v_permlane32_swap_b32 v235,v236\n"
"v_max_f32 v235,v235,v236\n"
"v_mov_b32 v236,v235\n"
"s_nop 1\n"
"v_permlane16_swap_b32 v235,v236\n"
"v_max_f32 v235,v235,v236\n"
"v_add_f32 v235,s70,v235\n"
"v_max_f32 v235,0,v235\n"
"v_mul_f32 v236,-0.5,v235\n"
"v_exp_f32 v236,v236\n"
"v_sub_f32 v56,v56,v235\n"
"v_sub_f32 v57,v57,v235\n"
"v_sub_f32 v58,v58,v235\n"
"v_sub_f32 v59,v59,v235\n"
"v_sub_f32 v60,v60,v235\n"
"v_sub_f32 v61,v61,v235\n"
"v_sub_f32 v62,v62,v235\n"
"v_sub_f32 v63,v63,v235\n"
"v_sub_f32 v24,v24,v235\n"
"v_sub_f32 v25,v25,v235\n"
"v_sub_f32 v26,v26,v235\n"
"v_sub_f32 v27,v27,v235\n"
"v_sub_f32 v28,v28,v235\n"
"v_sub_f32 v29,v29,v235\n"
"v_sub_f32 v30,v30,v235\n"
"v_sub_f32 v31,v31,v235\n"
"v_mul_f32_dpp v98,v98,v236 quad_perm:[0,1,2,3] row_mask:0x5 bank_mask:0xf\n"
"v_mul_f32_dpp v99,v99,v236 quad_perm:[0,1,2,3] row_mask:0x5 bank_mask:0xf\n"
"s_nop 1\n"
"v_mul_f32_dpp v98,v98,v236 quad_perm:[0,1,2,3] row_mask:0x5 bank_mask:0xf\n"
"v_mul_f32_dpp v99,v99,v236 quad_perm:[0,1,2,3] row_mask:0x5 bank_mask:0xf\n"
"s_nop 1\n"
"s_call_b64 s[18:19],L_rs1_%=\n"
"v_max3_f32 v235,v64,v65,v66\n"
"v_max3_f32 v235,v235,v67,v68\n"
"v_max3_f32 v235,v235,v69,v70\n"
"v_max3_f32 v235,v235,v71,v32\n"
"v_max3_f32 v235,v235,v33,v34\n"
"v_max3_f32 v235,v235,v35,v36\n"
"v_max3_f32 v235,v235,v37,v38\n"
"v_max_f32 v235,v235,v39\n"
"v_mov_b32 v236,v235\n"
"s_nop 1\n"
"v_permlane32_swap_b32 v235,v236\n"
"v_max_f32 v235,v235,v236\n"
"v_mov_b32 v236,v235\n"
"s_nop 1\n"
"v_permlane16_swap_b32 v235,v236\n"
"v_max_f32 v235,v235,v236\n"
"v_add_f32 v235,s70,v235\n"
"v_max_f32 v235,0,v235\n"
"v_mul_f32 v236,-0.5,v235\n"
"v_exp_f32 v236,v236\n"
"v_sub_f32 v64,v64,v235\n"
"v_sub_f32 v65,v65,v235\n"
"v_sub_f32 v66,v66,v235\n"
"v_sub_f32 v67,v67,v235\n"
"v_sub_f32 v68,v68,v235\n"
"v_sub_f32 v69,v69,v235\n"
"v_sub_f32 v70,v70,v235\n"
"v_sub_f32 v71,v71,v235\n"
"v_sub_f32 v32,v32,v235\n"
"v_sub_f32 v33,v33,v235\n"
"v_sub_f32 v34,v34,v235\n"
"v_sub_f32 v35,v35,v235\n"
"v_sub_f32 v36,v36,v235\n"
"v_sub_f32 v37,v37,v235\n"
"v_sub_f32 v38,v38,v235\n"
"v_sub_f32 v39,v39,v235\n"
"v_mul_f32_dpp v96,v96,v236 quad_perm:[0,1,2,3] row_mask:0xa bank_mask:0xf\n"
"v_mul_f32_dpp v97,v97,v236 quad_perm:[0,1,2,3] row_mask:0xa bank_mask:0xf\n"
"s_nop 1\n"
"v_mul_f32_dpp v96,v96,v236 quad_perm:[0,1,2,3] row_mask:0xa bank_mask:0xf\n"
"v_mul_f32_dpp v97,v97,v236 quad_perm:[0,1,2,3] row_mask:0xa bank_mask:0xf\n"
"s_nop 1\n"
"s_call_b64 s[18:19],L_rs2_%=\n"
"v_max3_f32 v235,v72,v73,v74\n"
"v_max3_f32 v235,v235,v75,v76\n"
"v_max3_f32 v235,v235,v77,v78\n"
"v_max3_f32 v235,v235,v79,v40\n"
"v_max3_f32 v235,v235,v41,v42\n"
"v_max3_f32 v235,v235,v43,v44\n"
"v_max3_f32 v235,v235,v45,v46\n"
"v_max_f32 v235,v235,v47\n"
"v_mov_b32 v236,v235\n"
"s_nop 1\n"
"v_permlane32_swap_b32 v235,v236\n"
"v_max_f32 v235,v235,v236\n"
"v_mov_b32 v236,v235\n"
"s_nop 1\n"
"v_permlane16_swap_b32 v235,v236\n"
"v_max_f32 v235,v235,v236\n"
"v_add_f32 v235,s70,v235\n"
"v_max_f32 v235,0,v235\n"
"v_mul_f32 v236,-0.5,v235\n"
"v_exp_f32 v236,v236\n"
"v_sub_f32 v72,v72,v235\n"
"v_sub_f32 v73,v73,v235\n"
"v_sub_f32 v74,v74,v235\n"
"v_sub_f32 v75,v75,v235\n"
"v_sub_f32 v76,v76,v235\n"
"v_sub_f32 v77,v77,v235\n"
"v_sub_f32 v78,v78,v235\n"
"v_sub_f32 v79,v79,v235\n"
"v_sub_f32 v40,v40,v235\n"
"v_sub_f32 v41,v41,v235\n"
"v_sub_f32 v42,v42,v235\n"
"v_sub_f32 v43,v43,v235\n"
"v_sub_f32 v44,v44,v235\n"
"v_sub_f32 v45,v45,v235\n"
"v_sub_f32 v46,v46,v235\n"
"v_sub_f32 v47,v47,v235\n"
"v_mul_f32_dpp v98,v98,v236 quad_perm:[0,1,2,3] row_mask:0xa bank_mask:0xf\n"
"v_mul_f32_dpp v99,v99,v236 quad_perm:[0,1,2,3] row_mask:0xa bank_mask:0xf\n"
"s_nop 1\n"
"v_mul_f32_dpp v98,v98,v236 quad_perm:[0,1,2,3] row_mask:0xa bank_mask:0xf\n"
"v_mul_f32_dpp v99,v99,v236 quad_perm:[0,1,2,3] row_mask:0xa bank_mask:0xf\n"
"s_nop 1\n"
"s_call_b64 s[18:19],L_rs3_%=\n"
"v_mov_b32 v100,v96\n"
"v_mov_b32 v101,v97\n"
"v_mov_b32 v102,v98\n"
"v_mov_b32 v103,v99\n"
"v_exp_f32 v208,v48\n"
"v_exp_f32 v209,v49\n"
"v_exp_f32 v210,v50\n"
"v_cvt_pk_bf16_f32 v128,v208,v209\n"
"v_exp_f32 v211,v51\n"
"v_exp_f32 v212,v52\n"
"v_cvt_pk_bf16_f32 v129,v210,v211\n"
"v_exp_f32 v213,v53\n"
"v_exp_f32 v214,v54\n"
"v_cvt_pk_bf16_f32 v130,v212,v213\n"
"v_exp_f32 v215,v55\n"
"v_exp_f32 v208,v56\n"
"v_cvt_pk_bf16_f32 v131,v214,v215\n"
"v_exp_f32 v209,v57\n"
"v_exp_f32 v210,v58\n"
"v_cvt_pk_bf16_f32 v132,v208,v209\n"
"v_exp_f32 v211,v59\n"
"v_exp_f32 v212,v60\n"
"v_cvt_pk_bf16_f32 v133,v210,v211\n"
"v_exp_f32 v213,v61\n"
"v_exp_f32 v214,v62\n"
"v_cvt_pk_bf16_f32 v134,v212,v213\n"
"v_exp_f32 v215,v63\n"
"v_exp_f32 v208,v64\n"
"v_cvt_pk_bf16_f32 v135,v214,v215\n"
"v_exp_f32 v209,v65\n"
"v_exp_f32 v210,v66\n"
"v_cvt_pk_bf16_f32 v136,v208,v209\n"
"v_exp_f32 v211,v67\n"
"v_exp_f32 v212,v68\n"
"v_cvt_pk_bf16_f32 v137,v210,v211\n"
"v_exp_f32 v213,v69\n"
"v_exp_f32 v214,v70\n"
"v_cvt_pk_bf16_f32 v138,v212,v213\n"
"v_exp_f32 v215,v71\n"
"v_exp_f32 v208,v72\n"
"v_cvt_pk_bf16_f32 v139,v214,v215\n"
"v_exp_f32 v209,v73\n"
"v_exp_f32 v210,v74\n"
"v_cvt_pk_bf16_f32 v140,v208,v209\n"
"v_exp_f32 v211,v75\n"
"v_exp_f32 v212,v76\n"
"v_cvt_pk_bf16_f32 v141,v210,v211\n"
"v_exp_f32 v213,v77\n"
"v_exp_f32 v214,v78\n"
"v_cvt_pk_bf16_f32 v142,v212,v213\n"
"v_exp_f32 v215,v79\n"
"s_nop 0\n"
"v_cvt_pk_bf16_f32 v143,v214,v215\n"
"s_nop 1\n"
"v_smfmac_f32_16x16x64_bf16 v[100:103],a[240:243],v[128:135],v104\n"
"v_smfmac_f32_16x16x64_bf16 v[100:103],a[244:247],v[136:143],v104\n"
"s_nop 7\n"
"s_cmp_eq_u32 s67,0\n"
"s_cbranch_scc1 L_ret0_%=\n"
"s_branch L_ret9_%=\n"
"L_stub1_%=:\n"
"s_mov_b32 s67,1\n"
"s_branch L_rare1_%=\n"
"L_stub10_%=:\n"
"s_mov_b32 s67,10\n"
"L_rare1_%=:\n"
"s_nop 15\n"
"v_max3_f32 v235,v16,v17,v18\n"
"v_max3_f32 v235,v235,v19,v20\n"
"v_max3_f32 v235,v235,v21,v22\n"
"v_max3_f32 v235,v235,v23,v48\n"
"v_max3_f32 v235,v235,v49,v50\n"
"v_max3_f32 v235,v235,v51,v52\n"
"v_max3_f32 v235,v235,v53,v54\n"
"v_max_f32 v235,v235,v55\n"
"v_mov_b32 v236,v235\n"
"s_nop 1\n"
"v_permlane32_swap_b32 v235,v236\n"
"v_max_f32 v235,v235,v236\n"
"v_mov_b32 v236,v235\n"
"s_nop 1\n"
"v_permlane16_swap_b32 v235,v236\n"
"v_max_f32 v235,v235,v236\n"
"v_add_f32 v235,s70,v235\n"
"v_max_f32 v235,0,v235\n"
"v_mul_f32 v236,-0.5,v235\n"
"v_exp_f32 v236,v236\n"
"v_sub_f32 v16,v16,v235\n"
"v_sub_f32 v17,v17,v235\n"
"v_sub_f32 v18,v18,v235\n"
"v_sub_f32 v19,v19,v235\n"
"v_sub_f32 v20,v20,v235\n"
"v_sub_f32 v21,v21,v235\n"
"v_sub_f32 v22,v22,v235\n"
"v_sub_f32 v23,v23,v235\n"
"v_sub_f32 v48,v48,v235\n"
"v_sub_f32 v49,v49,v235\n"
"v_sub_f32 v50,v50,v235\n"
"v_sub_f32 v51,v51,v235\n"
"v_sub_f32 v52,v52,v235\n"
"v_sub_f32 v53,v53,v235\n"
"v_sub_f32 v54,v54,v235\n"
"v_sub_f32 v55,v55,v235\n"
"v_mul_f32_dpp v100,v100,v236 quad_perm:[0,1,2,3] row_mask:0x5 bank_mask:0xf\n"
"v_mul_f32_dpp v101,v101,v236 quad_perm:[0,1,2,3] row_mask:0x5 bank_mask:0xf\n"
"s_nop 1\n"
"v_mul_f32_dpp v100,v100,v236 quad_perm:[0,1,2,3] row_mask:0x5 bank_mask:0xf\n"
"v_mul_f32_dpp v101,v101,v236 quad_perm:[0,1,2,3] row_mask:0x5 bank_mask:0xf\n"
"s_nop 1\n"
"s_call_b64 s[18:19],L_rs0_%=\n"
"v_max3_f32 v235,v24,v25,v26\n"
"v_max3_f32 v235,v235,v27,v28\n"
"v_max3_f32 v235,v235,v29,v30\n"
"v_max3_f32 v235,v235,v31,v56\n"
"v_max3_f32 v235,v235,v57,v58\n"
"v_max3_f32 v235,v235,v59,v60\n"
"v_max3_f32 v235,v235,v61,v62\n"
"v_max_f32 v235,v235,v63\n"
"v_mov_b32 v236,v235\n"
"s_nop 1\n"
"v_permlane32_swap_b32 v235,v236\n"
"v_max_f32 v235,v235,v236\n"
"v_mov_b32 v236,v235\n"
"s_nop 1\n"
"v_permlane16_swap_b32 v235,v236\n"
"v_max_f32 v235,v235,v236\n"
"v_add_f32 v235,s70,v235\n"
"v_max_f32 v235,0,v235\n"
"v_mul_f32 v236,-0.5,v235\n"
"v_exp_f32 v236,v236\n"
"v_sub_f32 v24,v24,v235\n"
"v_sub_f32 v25,v25,v235\n"
"v_sub_f32 v26,v26,v235\n"
"v_sub_f32 v27,v27,v235\n"
"v_sub_f32 v28,v28,v235\n"
"v_sub_f32 v29,v29,v235\n"
"v_sub_f32 v30,v30,v235\n"
"v_sub_f32 v31,v31,v235\n"
"v_sub_f32 v56,v56,v235\n"
"v_sub_f32 v57,v57,v235\n"
"v_sub_f32 v58,v58,v235\n"
"v_sub_f32 v59,v59,v235\n"
"v_sub_f32 v60,v60,v235\n"
"v_sub_f32 v61,v61,v235\n"
"v_sub_f32 v62,v62,v235\n"
"v_sub_f32 v63,v63,v235\n"
"v_mul_f32_dpp v102,v102,v236 quad_perm:[0,1,2,3] row_mask:0x5 bank_mask:0xf\n"
"v_mul_f32_dpp v103,v103,v236 quad_perm:[0,1,2,3] row_mask:0x5 bank_mask:0xf\n"
"s_nop 1\n"
"v_mul_f32_dpp v102,v102,v236 quad_perm:[0,1,2,3] row_mask:0x5 bank_mask:0xf\n"
"v_mul_f32_dpp v103,v103,v236 quad_perm:[0,1,2,3] row_mask:0x5 bank_mask:0xf\n"
"s_nop 1\n"
"s_call_b64 s[18:19],L_rs1_%=\n"
"v_max3_f32 v235,v32,v33,v34\n"
"v_max3_f32 v235,v235,v35,v36\n"
"v_max3_f32 v235,v235,v37,v38\n"
"v_max3_f32 v235,v235,v39,v64\n"
"v_max3_f32 v235,v235,v65,v66\n"
"v_max3_f32 v235,v235,v67,v68\n"
"v_max3_f32 v235,v235,v69,v70\n"
"v_max_f32 v235,v235,v71\n"
"v_mov_b32 v236,v235\n"
"s_nop 1\n"
"v_permlane32_swap_b32 v235,v236\n"
"v_max_f32 v235,v235,v236\n"
"v_mov_b32 v236,v235\n"
"s_nop 1\n"
"v_permlane16_swap_b32 v235,v236\n"
"v_max_f32 v235,v235,v236\n"
"v_add_f32 v235,s70,v235\n"
"v_max_f32 v235,0,v235\n"
"v_mul_f32 v236,-0.5,v235\n"
"v_exp_f32 v236,v236\n"
"v_sub_f32 v32,v32,v235\n"
"v_sub_f32 v33,v33,v235\n"
"v_sub_f32 v34,v34,v235\n"
"v_sub_f32 v35,v35,v235\n"
"v_sub_f32 v36,v36,v235\n"
"v_sub_f32 v37,v37,v235\n"
"v_sub_f32 v38,v38,v235\n"
"v_sub_f32 v39,v39,v235\n"
"v_sub_f32 v64,v64,v235\n"
"v_sub_f32 v65,v65,v235\n"
"v_sub_f32 v66,v66,v235\n"
"v_sub_f32 v67,v67,v235\n"
"v_sub_f32 v68,v68,v235\n"
"v_sub_f32 v69,v69,v235\n"
"v_sub_f32 v70,v70,v235\n"
"v_sub_f32 v71,v71,v235\n"
"v_mul_f32_dpp v100,v100,v236 quad_perm:[0,1,2,3] row_mask:0xa bank_mask:0xf\n"
"v_mul_f32_dpp v101,v101,v236 quad_perm:[0,1,2,3] row_mask:0xa bank_mask:0xf\n"
"s_nop 1\n"
"v_mul_f32_dpp v100,v100,v236 quad_perm:[0,1,2,3] row_mask:0xa bank_mask:0xf\n"
"v_mul_f32_dpp v101,v101,v236 quad_perm:[0,1,2,3] row_mask:0xa bank_mask:0xf\n"
"s_nop 1\n"
"s_call_b64 s[18:19],L_rs2_%=\n"
"v_max3_f32 v235,v40,v41,v42\n"
"v_max3_f32 v235,v235,v43,v44\n"
"v_max3_f32 v235,v235,v45,v46\n"
"v_max3_f32 v235,v235,v47,v72\n"
"v_max3_f32 v235,v235,v73,v74\n"
"v_max3_f32 v235,v235,v75,v76\n"
"v_max3_f32 v235,v235,v77,v78\n"
"v_max_f32 v235,v235,v79\n"
"v_mov_b32 v236,v235\n"
"s_nop 1\n"
"v_permlane32_swap_b32 v235,v236\n"
"v_max_f32 v235,v235,v236\n"
"v_mov_b32 v236,v235\n"
"s_nop 1\n"
"v_permlane16_swap_b32 v235,v236\n"
"v_max_f32 v235,v235,v236\n"
"v_add_f32 v235,s70,v235\n"
"v_max_f32 v235,0,v235\n"
"v_mul_f32 v236,-0.5,v235\n"
"v_exp_f32 v236,v236\n"
"v_sub_f32 v40,v40,v235\n"
"v_sub_f32 v41,v41,v235\n"
"v_sub_f32 v42,v42,v235\n"
"v_sub_f32 v43,v43,v235\n"
"v_sub_f32 v44,v44,v235\n"
"v_sub_f32 v45,v45,v235\n"
"v_sub_f32 v46,v46,v235\n"
"v_sub_f32 v47,v47,v235\n"
"v_sub_f32 v72,v72,v235\n"
"v_sub_f32 v73,v73,v235\n"
"v_sub_f32 v74,v74,v235\n"
"v_sub_f32 v75,v75,v235\n"
"v_sub_f32 v76,v76,v235\n"
"v_sub_f32 v77,v77,v235\n"
"v_sub_f32 v78,v78,v235\n"
"v_sub_f32 v79,v79,v235\n"
"v_mul_f32_dpp v102,v102,v236 quad_perm:[0,1,2,3] row_mask:0xa bank_mask:0xf\n"
"v_mul_f32_dpp v103,v103,v236 quad_perm:[0,1,2,3] row_mask:0xa bank_mask:0xf\n"
"s_nop 1\n"
"v_mul_f32_dpp v102,v102,v236 quad_perm:[0,1,2,3] row_mask:0xa bank_mask:0xf\n"
"v_mul_f32_dpp v103,v103,v236 quad_perm:[0,1,2,3] row_mask:0xa bank_mask:0xf\n"
"s_nop 1\n"
"s_call_b64 s[18:19],L_rs3_%=\n"
"v_mov_b32 v96,v100\n"
"v_mov_b32 v97,v101\n"
"v_mov_b32 v98,v102\n"
"v_mov_b32 v99,v103\n"
"v_exp_f32 v208,v16\n"
"v_exp_f32 v209,v17\n"
"v_exp_f32 v210,v18\n"
"v_cvt_pk_bf16_f32 v112,v208,v209\n"
"v_exp_f32 v211,v19\n"
"v_exp_f32 v212,v20\n"
"v_cvt_pk_bf16_f32 v113,v210,v211\n"
"v_exp_f32 v213,v21\n"
"v_exp_f32 v214,v22\n"
"v_cvt_pk_bf16_f32 v114,v212,v213\n"
"v_exp_f32 v215,v23\n"
"v_exp_f32 v208,v24\n"
"v_cvt_pk_bf16_f32 v115,v214,v215\n"
"v_exp_f32 v209,v25\n"
"v_exp_f32 v210,v26\n"
"v_cvt_pk_bf16_f32 v116,v208,v209\n"
"v_exp_f32 v211,v27\n"
"v_exp_f32 v212,v28\n"
"v_cvt_pk_bf16_f32 v117,v210,v211\n"
"v_exp_f32 v213,v29\n"
"v_exp_f32 v214,v30\n"
"v_cvt_pk_bf16_f32 v118,v212,v213\n"
"v_exp_f32 v215,v31\n"
"v_exp_f32 v208,v32\n"
"v_cvt_pk_bf16_f32 v119,v214,v215\n"
"v_exp_f32 v209,v33\n"
"v_exp_f32 v210,v34\n"
"v_cvt_pk_bf16_f32 v120,v208,v209\n"
"v_exp_f32 v211,v35\n"
"v_exp_f32 v212,v36\n"
"v_cvt_pk_bf16_f32 v121,v210,v211\n"
"v_exp_f32 v213,v37\n"
"v_exp_f32 v214,v38\n"
"v_cvt_pk_bf16_f32 v122,v212,v213\n"
"v_exp_f32 v215,v39\n"
"v_exp_f32 v208,v40\n"
"v_cvt_pk_bf16_f32 v123,v214,v215\n"
"v_exp_f32 v209,v41\n"
"v_exp_f32 v210,v42\n"
"v_cvt_pk_bf16_f32 v124,v208,v209\n"
"v_exp_f32 v211,v43\n"
"v_exp_f32 v212,v44\n"
"v_cvt_pk_bf16_f32 v125,v210,v211\n"
"v_exp_f32 v213,v45\n"
"v_exp_f32 v214,v46\n"
"v_cvt_pk_bf16_f32 v126,v212,v213\n"
"v_exp_f32 v215,v47\n"
"s_nop 0\n"
"v_cvt_pk_bf16_f32 v127,v214,v215\n"
"s_nop 1\n"
"v_smfmac_f32_16x16x64_bf16 v[96:99],a[240:243],v[112:119],v104\n"
"v_smfmac_f32_16x16x64_bf16 v[96:99],a[244:247],v[120:127],v104\n"
"s_nop 7\n"
"s_cmp_eq_u32 s67,1\n"
"s_cbranch_scc1 L_reu1_%=\n"
"s_branch L_reu10_%=\n"
"L_stub3_%=:\n"
"s_mov_b32 s67,3\n"
"L_rare2_%=:\n"
"s_nop 15\n"
"v_max3_f32 v235,v48,v49,v50\n"
"v_max3_f32 v235,v235,v51,v52\n"
"v_max3_f32 v235,v235,v53,v54\n"
"v_max3_f32 v235,v235,v55,v16\n"
"v_max3_f32 v235,v235,v17,v18\n"
"v_max3_f32 v235,v235,v19,v20\n"
"v_max3_f32 v235,v235,v21,v22\n"
"v_max3_f32 v235,v235,v23,v32\n"
"v_max3_f32 v235,v235,v33,v34\n"
"v_max3_f32 v235,v235,v35,v36\n"
"v_max3_f32 v235,v235,v37,v38\n"
"v_max_f32 v235,v235,v39\n"
"v_mov_b32 v236,v235\n"
"s_nop 1\n"
"v_permlane32_swap_b32 v235,v236\n"
"v_max_f32 v235,v235,v236\n"
"v_mov_b32 v236,v235\n"
"s_nop 1\n"
"v_permlane16_swap_b32 v235,v236\n"
"v_max_f32 v235,v235,v236\n"
"v_add_f32 v235,s70,v235\n"
"v_max_f32 v235,0,v235\n"
"v_mul_f32 v236,-0.5,v235\n"
"v_exp_f32 v236,v236\n"
"v_sub_f32 v48,v48,v235\n"
"v_sub_f32 v49,v49,v235\n"
"v_sub_f32 v50,v50,v235\n"
"v_sub_f32 v51,v51,v235\n"
"v_sub_f32 v52,v52,v235\n"
"v_sub_f32 v53,v53,v235\n"
"v_sub_f32 v54,v54,v235\n"
"v_sub_f32 v55,v55,v235\n"
"v_sub_f32 v16,v16,v235\n"
"v_sub_f32 v17,v17,v235\n"
"v_sub_f32 v18,v18,v235\n"
"v_sub_f32 v19,v19,v235\n"
"v_sub_f32 v20,v20,v235\n"
"v_sub_f32 v21,v21,v235\n"
"v_sub_f32 v22,v22,v235\n"
"v_sub_f32 v23,v23,v235\n"
"v_sub_f32 v32,v32,v235\n"
"v_sub_f32 v33,v33,v235\n"
"v_sub_f32 v34,v34,v235\n"
"v_sub_f32 v35,v35,v235\n"
"v_sub_f32 v36,v36,v235\n"
"v_sub_f32 v37,v37,v235\n"
"v_sub_f32 v38,v38,v235\n"
"v_sub_f32 v39,v39,v235\n"
"v_mul_f32_dpp v96,v96,v236 quad_perm:[0,1,2,3] row_mask:0x5 bank_mask:0xf\n"
"v_mul_f32_dpp v97,v97,v236 quad_perm:[0,1,2,3] row_mask:0x5 bank_mask:0xf\n"
"s_nop 1\n"
"v_mul_f32_dpp v96,v96,v236 quad_perm:[0,1,2,3] row_mask:0x5 bank_mask:0xf\n"
"v_mul_f32_dpp v97,v97,v236 quad_perm:[0,1,2,3] row_mask:0x5 bank_mask:0xf\n"
"s_nop 1\n"
"s_call_b64 s[18:19],L_rs0_%=\n"
"v_max3_f32 v235,v56,v57,v58\n"
"v_max3_f32 v235,v235,v59,v60\n"
"v_max3_f32 v235,v235,v61,v62\n"
"v_max3_f32 v235,v235,v63,v24\n"
"v_max3_f32 v235,v235,v25,v26\n"
"v_max3_f32 v235,v235,v27,v28\n"
"v_max3_f32 v235,v235,v29,v30\n"
"v_max3_f32 v235,v235,v31,v40\n"
"v_max3_f32 v235,v235,v41,v42\n"
"v_max3_f32 v235,v235,v43,v44\n"
"v_max3_f32 v235,v235,v45,v46\n"
"v_max_f32 v235,v235,v47\n"
"v_mov_b32 v236,v235\n"
"s_nop 1\n"
"v_permlane32_swap_b32 v235,v236\n"
"v_max_f32 v235,v235,v236\n"
"v_mov_b32 v236,v235\n"
"s_nop 1\n"
"v_permlane16_swap_b32 v235,v236\n"
"v_max_f32 v235,v235,v236\n"
"v_add_f32 v235,s70,v235\n"
"v_max_f32 v235,0,v235\n"
"v_mul_f32 v236,-0.5,v235\n"
"v_exp_f32 v236,v236\n"
"v_sub_f32 v56,v56,v235\n"
"v_sub_f32 v57,v57,v235\n"
"v_sub_f32 v58,v58,v235\n"
"v_sub_f32 v59,v59,v235\n"
"v_sub_f32 v60,v60,v235\n"
"v_sub_f32 v61,v61,v235\n"
"v_sub_f32 v62,v62,v235\n"
"v_sub_f32 v63,v63,v235\n"
"v_sub_f32 v24,v24,v235\n"
"v_sub_f32 v25,v25,v235\n"
"v_sub_f32 v26,v26,v235\n"
"v_sub_f32 v27,v27,v235\n"
"v_sub_f32 v28,v28,v235\n"
"v_sub_f32 v29,v29,v235\n"
"v_sub_f32 v30,v30,v235\n"
"v_sub_f32 v31,v31,v235\n"
"v_sub_f32 v40,v40,v235\n"
"v_sub_f32 v41,v41,v235\n"
"v_sub_f32 v42,v42,v235\n"
"v_sub_f32 v43,v43,v235\n"
"v_sub_f32 v44,v44,v235\n"
"v_sub_f32 v45,v45,v235\n"
"v_sub_f32 v46,v46,v235\n"
"v_sub_f32 v47,v47,v235\n"
"v_mul_f32_dpp v98,v98,v236 quad_perm:[0,1,2,3] row_mask:0x5 bank_mask:0xf\n"
"v_mul_f32_dpp v99,v99,v236 quad_perm:[0,1,2,3] row_mask:0x5 bank_mask:0xf\n"
"s_nop 1\n"
"v_mul_f32_dpp v98,v98,v236 quad_perm:[0,1,2,3] row_mask:0x5 bank_mask:0xf\n"
"v_mul_f32_dpp v99,v99,v236 quad_perm:[0,1,2,3] row_mask:0x5 bank_mask:0xf\n"
"s_nop 1\n"
"s_call_b64 s[18:19],L_rs1_%=\n"
"v_max3_f32 v235,v64,v65,v66\n"
"v_max3_f32 v235,v235,v67,v68\n"
"v_max3_f32 v235,v235,v69,v70\n"
"v_max_f32 v235,v235,v71\n"
"v_mov_b32 v236,v235\n"
"s_nop 1\n"
"v_permlane32_swap_b32 v235,v236\n"
"v_max_f32 v235,v235,v236\n"
"v_mov_b32 v236,v235\n"
"s_nop 1\n"
"v_permlane16_swap_b32 v235,v236\n"
"v_max_f32 v235,v235,v236\n"
"v_add_f32 v235,s70,v235\n"
"v_max_f32 v235,0,v235\n"
"v_mul_f32 v236,-0.5,v235\n"
"v_exp_f32 v236,v236\n"
"v_sub_f32 v64,v64,v235\n"
"v_sub_f32 v65,v65,v235\n"
"v_sub_f32 v66,v66,v235\n"
"v_sub_f32 v67,v67,v235\n"
"v_sub_f32 v68,v68,v235\n"
"v_sub_f32 v69,v69,v235\n"
"v_sub_f32 v70,v70,v235\n"
"v_sub_f32 v71,v71,v235\n"
"v_mul_f32_dpp v96,v96,v236 quad_perm:[0,1,2,3] row_mask:0xa bank_mask:0xf\n"
"v_mul_f32_dpp v97,v97,v236 quad_perm:[0,1,2,3] row_mask:0xa bank_mask:0xf\n"
"s_nop 1\n"
"v_mul_f32_dpp v96,v96,v236 quad_perm:[0,1,2,3] row_mask:0xa bank_mask:0xf\n"
"v_mul_f32_dpp v97,v97,v236 quad_perm:[0,1,2,3] row_mask:0xa bank_mask:0xf\n"
"s_nop 1\n"
"s_call_b64 s[18:19],L_rs2_%=\n"
"v_max3_f32 v235,v72,v73,v74\n"
"v_max3_f32 v235,v235,v75,v76\n"
"v_max3_f32 v235,v235,v77,v78\n"
"v_max_f32 v235,v235,v79\n"
"v_mov_b32 v236,v235\n"
"s_nop 1\n"
"v_permlane32_swap_b32 v235,v236\n"
"v_max_f32 v235,v235,v236\n"
"v_mov_b32 v236,v235\n"
"s_nop 1\n"
"v_permlane16_swap_b32 v235,v236\n"
"v_max_f32 v235,v235,v236\n"
"v_add_f32 v235,s70,v235\n"
"v_max_f32 v235,0,v235\n"
"v_mul_f32 v236,-0.5,v235\n"
"v_exp_f32 v236,v236\n"
"v_sub_f32 v72,v72,v235\n"
"v_sub_f32 v73,v73,v235\n"
"v_sub_f32 v74,v74,v235\n"
"v_sub_f32 v75,v75,v235\n"
"v_sub_f32 v76,v76,v235\n"
"v_sub_f32 v77,v77,v235\n"
"v_sub_f32 v78,v78,v235\n"
"v_sub_f32 v79,v79,v235\n"
"v_mul_f32_dpp v98,v98,v236 quad_perm:[0,1,2,3] row_mask:0xa bank_mask:0xf\n"
"v_mul_f32_dpp v99,v99,v236 quad_perm:[0,1,2,3] row_mask:0xa bank_mask:0xf\n"
"s_nop 1\n"
"v_mul_f32_dpp v98,v98,v236 quad_perm:[0,1,2,3] row_mask:0xa bank_mask:0xf\n"
"v_mul_f32_dpp v99,v99,v236 quad_perm:[0,1,2,3] row_mask:0xa bank_mask:0xf\n"
"s_nop 1\n"
"s_call_b64 s[18:19],L_rs3_%=\n"
"v_mov_b32 v100,v96\n"
"v_mov_b32 v101,v97\n"
"v_mov_b32 v102,v98\n"
"v_mov_b32 v103,v99\n"
"v_exp_f32 v208,v48\n"
"v_exp_f32 v209,v49\n"
"v_exp_f32 v210,v50\n"
"v_cvt_pk_bf16_f32 v128,v208,v209\n"
"v_exp_f32 v211,v51\n"
"v_exp_f32 v212,v52\n"
"v_cvt_pk_bf16_f32 v129,v210,v211\n"
"v_exp_f32 v213,v53\n"
"v_exp_f32 v214,v54\n"
"v_cvt_pk_bf16_f32 v130,v212,v213\n"
"v_exp_f32 v215,v55\n"
"v_exp_f32 v208,v56\n"
"v_cvt_pk_bf16_f32 v131,v214,v215\n"
"v_exp_f32 v209,v57\n"
"v_exp_f32 v210,v58\n"
"v_cvt_pk_bf16_f32 v132,v208,v209\n"
"v_exp_f32 v211,v59\n"
"v_exp_f32 v212,v60\n"
"v_cvt_pk_bf16_f32 v133,v210,v211\n"
"v_exp_f32 v213,v61\n"
"v_exp_f32 v214,v62\n"
"v_cvt_pk_bf16_f32 v134,v212,v213\n"
"v_exp_f32 v215,v63\n"
"v_exp_f32 v208,v64\n"
"v_cvt_pk_bf16_f32 v135,v214,v215\n"
"v_exp_f32 v209,v65\n"
"v_exp_f32 v210,v66\n"
"v_cvt_pk_bf16_f32 v136,v208,v209\n"
"v_exp_f32 v211,v67\n"
"v_exp_f32 v212,v68\n"
"v_cvt_pk_bf16_f32 v137,v210,v211\n"
"v_exp_f32 v213,v69\n"
"v_exp_f32 v214,v70\n"
"v_cvt_pk_bf16_f32 v138,v212,v213\n"
"v_exp_f32 v215,v71\n"
"v_exp_f32 v208,v72\n"
"v_cvt_pk_bf16_f32 v139,v214,v215\n"
"v_exp_f32 v209,v73\n"
"v_exp_f32 v210,v74\n"
"v_cvt_pk_bf16_f32 v140,v208,v209\n"
"v_exp_f32 v211,v75\n"
"v_exp_f32 v212,v76\n"
"v_cvt_pk_bf16_f32 v141,v210,v211\n"
"v_exp_f32 v213,v77\n"
"v_exp_f32 v214,v78\n"
"v_cvt_pk_bf16_f32 v142,v212,v213\n"
"v_exp_f32 v215,v79\n"
"s_nop 0\n"
"v_cvt_pk_bf16_f32 v143,v214,v215\n"
"s_nop 1\n"
"v_smfmac_f32_16x16x64_bf16 v[100:103],a[240:243],v[128:135],v104\n"
"v_smfmac_f32_16x16x64_bf16 v[100:103],a[244:247],v[136:143],v104\n"
"s_nop 7\n"
"s_branch L_ret3_%=\n"
"L_stub4_%=:\n"
"s_mov_b32 s67,4\n"
"L_rare3_%=:\n"
"s_nop 15\n"
"v_max3_f32 v235,v16,v17,v18\n"
"v_max3_f32 v235,v235,v19,v20\n"
"v_max3_f32 v235,v235,v21,v22\n"
"v_max3_f32 v235,v235,v23,v32\n"
"v_max3_f32 v235,v235,v33,v34\n"
"v_max3_f32 v235,v235,v35,v36\n"
"v_max3_f32 v235,v235,v37,v38\n"
"v_max3_f32 v235,v235,v39,v48\n"
"v_max3_f32 v235,v235,v49,v50\n"
"v_max3_f32 v235,v235,v51,v52\n"
"v_max3_f32 v235,v235,v53,v54\n"
"v_max3_f32 v235,v235,v55,v64\n"
"v_max3_f32 v235,v235,v65,v66\n"
"v_max3_f32 v235,v235,v67,v68\n"
"v_max3_f32 v235,v235,v69,v70\n"
"v_max_f32 v235,v235,v71\n"
"v_mov_b32 v236,v235\n"
"s_nop 1\n"
"v_permlane32_swap_b32 v235,v236\n"
"v_max_f32 v235,v235,v236\n"
"v_mov_b32 v236,v235\n"
"s_nop 1\n"
"v_permlane16_swap_b32 v235,v236\n"
"v_max_f32 v235,v235,v236\n"
"v_add_f32 v235,s70,v235\n"
"v_max_f32 v235,0,v235\n"
"v_mul_f32 v236,-0.5,v235\n"
"v_exp_f32 v236,v236\n"
"v_sub_f32 v16,v16,v235\n"
"v_sub_f32 v17,v17,v235\n"
"v_sub_f32 v18,v18,v235\n"
"v_sub_f32 v19,v19,v235\n"
"v_sub_f32 v20,v20,v235\n"
"v_sub_f32 v21,v21,v235\n"
"v_sub_f32 v22,v22,v235\n"
"v_sub_f32 v23,v23,v235\n"
"v_sub_f32 v32,v32,v235\n"
"v_sub_f32 v33,v33,v235\n"
"v_sub_f32 v34,v34,v235\n"
"v_sub_f32 v35,v35,v235\n"
"v_sub_f32 v36,v36,v235\n"
"v_sub_f32 v37,v37,v235\n"
"v_sub_f32 v38,v38,v235\n"
"v_sub_f32 v39,v39,v235\n"
"v_sub_f32 v48,v48,v235\n"
"v_sub_f32 v49,v49,v235\n"
"v_sub_f32 v50,v50,v235\n"
"v_sub_f32 v51,v51,v235\n"
"v_sub_f32 v52,v52,v235\n"
"v_sub_f32 v53,v53,v235\n"
"v_sub_f32 v54,v54,v235\n"
"v_sub_f32 v55,v55,v235\n"
"v_sub_f32 v64,v64,v235\n"
"v_sub_f32 v65,v65,v235\n"
"v_sub_f32 v66,v66,v235\n"
"v_sub_f32 v67,v67,v235\n"
"v_sub_f32 v68,v68,v235\n"
"v_sub_f32 v69,v69,v235\n"
"v_sub_f32 v70,v70,v235\n"
"v_sub_f32 v71,v71,v235\n"
"v_mul_f32_dpp v100,v100,v236 quad_perm:[0,1,2,3] row_mask:0x5 bank_mask:0xf\n"
"v_mul_f32_dpp v101,v101,v236 quad_perm:[0,1,2,3] row_mask:0x5 bank_mask:0xf\n"
"s_nop 1\n"
"v_mul_f32_dpp v100,v100,v236 quad_perm:[0,1,2,3] row_mask:0x5 bank_mask:0xf\n"
"v_mul_f32_dpp v101,v101,v236 quad_perm:[0,1,2,3] row_mask:0x5 bank_mask:0xf\n"
"s_nop 1\n"
"s_call_b64 s[18:19],L_rs0_%=\n"
"v_max3_f32 v235,v24,v25,v26\n"
"v_max3_f32 v235,v235,v27,v28\n"
"v_max3_f32 v235,v235,v29,v30\n"
"v_max3_f32 v235,v235,v31,v40\n"
"v_max3_f32 v235,v235,v41,v42\n"
"v_max3_f32 v235,v235,v43,v44\n"
"v_max3_f32 v235,v235,v45,v46\n"
"v_max3_f32 v235,v235,v47,v56\n"
"v_max3_f32 v235,v235,v57,v58\n"
"v_max3_f32 v235,v235,v59,v60\n"
"v_max3_f32 v235,v235,v61,v62\n"
"v_max3_f32 v235,v235,v63,v72\n"
"v_max3_f32 v235,v235,v73,v74\n"
"v_max3_f32 v235,v235,v75,v76\n"
"v_max3_f32 v235,v235,v77,v78\n"
"v_max_f32 v235,v235,v79\n"
"v_mov_b32 v236,v235\n"
"s_nop 1\n"
"v_permlane32_swap_b32 v235,v236\n"
"v_max_f32 v235,v235,v236\n"
"v_mov_b32 v236,v235\n"
"s_nop 1\n"
"v_permlane16_swap_b32 v235,v236\n"
"v_max_f32 v235,v235,v236\n"
"v_add_f32 v235,s70,v235\n"
"v_max_f32 v235,0,v235\n"
"v_mul_f32 v236,-0.5,v235\n"
"v_exp_f32 v236,v236\n"
"v_sub_f32 v24,v24,v235\n"
"v_sub_f32 v25,v25,v235\n"
"v_sub_f32 v26,v26,v235\n"
"v_sub_f32 v27,v27,v235\n"
"v_sub_f32 v28,v28,v235\n"
"v_sub_f32 v29,v29,v235\n"
"v_sub_f32 v30,v30,v235\n"
"v_sub_f32 v31,v31,v235\n"
"v_sub_f32 v40,v40,v235\n"
"v_sub_f32 v41,v41,v235\n"
"v_sub_f32 v42,v42,v235\n"
"v_sub_f32 v43,v43,v235\n"
"v_sub_f32 v44,v44,v235\n"
"v_sub_f32 v45,v45,v235\n"
"v_sub_f32 v46,v46,v235\n"
"v_sub_f32 v47,v47,v235\n"
"v_sub_f32 v56,v56,v235\n"
"v_sub_f32 v57,v57,v235\n"
"v_sub_f32 v58,v58,v235\n"
"v_sub_f32 v59,v59,v235\n"
"v_sub_f32 v60,v60,v235\n"
"v_sub_f32 v61,v61,v235\n"
"v_sub_f32 v62,v62,v235\n"
"v_sub_f32 v63,v63,v235\n"
"v_sub_f32 v72,v72,v235\n"
"v_sub_f32 v73,v73,v235\n"
"v_sub_f32 v74,v74,v235\n"
"v_sub_f32 v75,v75,v235\n"
"v_sub_f32 v76,v76,v235\n"
"v_sub_f32 v77,v77,v235\n"
"v_sub_f32 v78,v78,v235\n"
"v_sub_f32 v79,v79,v235\n"
"v_mul_f32_dpp v102,v102,v236 quad_perm:[0,1,2,3] row_mask:0x5 bank_mask:0xf\n"
"v_mul_f32_dpp v103,v103,v236 quad_perm:[0,1,2,3] row_mask:0x5 bank_mask:0xf\n"
"s_nop 1\n"
"v_mul_f32_dpp v102,v102,v236 quad_perm:[0,1,2,3] row_mask:0x5 bank_mask:0xf\n"
"v_mul_f32_dpp v103,v103,v236 quad_perm:[0,1,2,3] row_mask:0x5 bank_mask:0xf\n"
"s_nop 1\n"
"s_call_b64 s[18:19],L_rs1_%=\n"
"v_mov_b32 v96,v100\n"
"v_mov_b32 v97,v101\n"
"v_mov_b32 v98,v102\n"
"v_mov_b32 v99,v103\n"
"v_exp_f32 v208,v16\n"
"v_exp_f32 v209,v17\n"
"v_exp_f32 v210,v18\n"
"v_cvt_pk_bf16_f32 v112,v208,v209\n"
"v_exp_f32 v211,v19\n"
"v_exp_f32 v212,v20\n"
"v_cvt_pk_bf16_f32 v113,v210,v211\n"
"v_exp_f32 v213,v21\n"
"v_exp_f32 v214,v22\n"
"v_cvt_pk_bf16_f32 v114,v212,v213\n"
"v_exp_f32 v215,v23\n"
"v_exp_f32 v208,v24\n"
"v_cvt_pk_bf16_f32 v115,v214,v215\n"
"v_exp_f32 v209,v25\n"
"v_exp_f32 v210,v26\n"
"v_cvt_pk_bf16_f32 v116,v208,v209\n"
"v_exp_f32 v211,v27\n"
"v_exp_f32 v212,v28\n"
"v_cvt_pk_bf16_f32 v117,v210,v211\n"
"v_exp_f32 v213,v29\n"
"v_exp_f32 v214,v30\n"
"v_cvt_pk_bf16_f32 v118,v212,v213\n"
"v_exp_f32 v215,v31\n"
"v_exp_f32 v208,v32\n"
"v_cvt_pk_bf16_f32 v119,v214,v215\n"
"v_exp_f32 v209,v33\n"
"v_exp_f32 v210,v34\n"
"v_cvt_pk_bf16_f32 v120,v208,v209\n"
"v_exp_f32 v211,v35\n"
"v_exp_f32 v212,v36\n"
"v_cvt_pk_bf16_f32 v121,v210,v211\n"
"v_exp_f32 v213,v37\n"
"v_exp_f32 v214,v38\n"
"v_cvt_pk_bf16_f32 v122,v212,v213\n"
"v_exp_f32 v215,v39\n"
"v_exp_f32 v208,v40\n"
"v_cvt_pk_bf16_f32 v123,v214,v215\n"
"v_exp_f32 v209,v41\n"
"v_exp_f32 v210,v42\n"
"v_cvt_pk_bf16_f32 v124,v208,v209\n"
"v_exp_f32 v211,v43\n"
"v_exp_f32 v212,v44\n"
"v_cvt_pk_bf16_f32 v125,v210,v211\n"
"v_exp_f32 v213,v45\n"
"v_exp_f32 v214,v46\n"
"v_cvt_pk_bf16_f32 v126,v212,v213\n"
"v_exp_f32 v215,v47\n"
"s_nop 0\n"
"v_cvt_pk_bf16_f32 v127,v214,v215\n"
"s_nop 1\n"
"v_smfmac_f32_16x16x64_bf16 v[96:99],a[240:243],v[112:119],v104\n"
"v_smfmac_f32_16x16x64_bf16 v[96:99],a[240:243],v[120:127],v104\n"
"s_nop 7\n"
"s_branch L_ret4_%=\n"
"L_stub5_%=:\n"
"s_mov_b32 s67,5\n"
"L_rare4_%=:\n"
"s_nop 15\n"
"v_max3_f32 v235,v48,v49,v50\n"
"v_max3_f32 v235,v235,v51,v52\n"
"v_max3_f32 v235,v235,v53,v54\n"
"v_max3_f32 v235,v235,v55,v64\n"
"v_max3_f32 v235,v235,v65,v66\n"
"v_max3_f32 v235,v235,v67,v68\n"
"v_max3_f32 v235,v235,v69,v70\n"
"v_max3_f32 v235,v235,v71,v16\n"
"v_max3_f32 v235,v235,v17,v18\n"
"v_max3_f32 v235,v235,v19,v20\n"
"v_max3_f32 v235,v235,v21,v22\n"
"v_max3_f32 v235,v235,v23,v32\n"
"v_max3_f32 v235,v235,v33,v34\n"
"v_max3_f32 v235,v235,v35,v36\n"
"v_max3_f32 v235,v235,v37,v38\n"
"v_max_f32 v235,v235,v39\n"
"v_mov_b32 v236,v235\n"
"s_nop 1\n"
"v_permlane32_swap_b32 v235,v236\n"
"v_max_f32 v235,v235,v236\n"
"v_mov_b32 v236,v235\n"
"s_nop 1\n"
"v_permlane16_swap_b32 v235,v236\n"
"v_max_f32 v235,v235,v236\n"
"v_add_f32 v235,s70,v235\n"
"v_max_f32 v235,0,v235\n"
"v_mul_f32 v236,-0.5,v235\n"
"v_exp_f32 v236,v236\n"
"v_sub_f32 v48,v48,v235\n"
"v_sub_f32 v49,v49,v235\n"
"v_sub_f32 v50,v50,v235\n"
"v_sub_f32 v51,v51,v235\n"
"v_sub_f32 v52,v52,v235\n"
"v_sub_f32 v53,v53,v235\n"
"v_sub_f32 v54,v54,v235\n"
"v_sub_f32 v55,v55,v235\n"
"v_sub_f32 v64,v64,v235\n"
"v_sub_f32 v65,v65,v235\n"
"v_sub_f32 v66,v66,v235\n"
"v_sub_f32 v67,v67,v235\n"
"v_sub_f32 v68,v68,v235\n"
"v_sub_f32 v69,v69,v235\n"
"v_sub_f32 v70,v70,v235\n"
"v_sub_f32 v71,v71,v235\n"
"v_sub_f32 v16,v16,v235\n"
"v_sub_f32 v17,v17,v235\n"
"v_sub_f32 v18,v18,v235\n"
"v_sub_f32 v19,v19,v235\n"
"v_sub_f32 v20,v20,v235\n"
"v_sub_f32 v21,v21,v235\n"
"v_sub_f32 v22,v22,v235\n"
"v_sub_f32 v23,v23,v235\n"
"v_sub_f32 v32,v32,v235\n"
"v_sub_f32 v33,v33,v235\n"
"v_sub_f32 v34,v34,v235\n"
"v_sub_f32 v35,v35,v235\n"
"v_sub_f32 v36,v36,v235\n"
"v_sub_f32 v37,v37,v235\n"
"v_sub_f32 v38,v38,v235\n"
"v_sub_f32 v39,v39,v235\n"
"v_mul_f32_dpp v96,v96,v236 quad_perm:[0,1,2,3] row_mask:0x5 bank_mask:0xf\n"
"v_mul_f32_dpp v97,v97,v236 quad_perm:[0,1,2,3] row_mask:0x5 bank_mask:0xf\n"
"s_nop 1\n"
"v_mul_f32_dpp v96,v96,v236 quad_perm:[0,1,2,3] row_mask:0x5 bank_mask:0xf\n"
"v_mul_f32_dpp v97,v97,v236 quad_perm:[0,1,2,3] row_mask:0x5 bank_mask:0xf\n"
"s_nop 1\n"
"s_call_b64 s[18:19],L_rs0_%=\n"
"v_max3_f32 v235,v56,v57,v58\n"
"v_max3_f32 v235,v235,v59,v60\n"
"v_max3_f32 v235,v235,v61,v62\n"
"v_max3_f32 v235,v235,v63,v72\n"
"v_max3_f32 v235,v235,v73,v74\n"
"v_max3_f32 v235,v235,v75,v76\n"
"v_max3_f32 v235,v235,v77,v78\n"
"v_max3_f32 v235,v235,v79,v24\n"
"v_max3_f32 v235,v235,v25,v26\n"
"v_max3_f32 v235,v235,v27,v28\n"
"v_max3_f32 v235,v235,v29,v30\n"
"v_max3_f32 v235,v235,v31,v40\n"
"v_max3_f32 v235,v235,v41,v42\n"
"v_max3_f32 v235,v235,v43,v44\n"
"v_max3_f32 v235,v235,v45,v46\n"
"v_max_f32 v235,v235,v47\n"
"v_mov_b32 v236,v235\n"
"s_nop 1\n"
"v_permlane32_swap_b32 v235,v236\n"
"v_max_f32 v235,v235,v236\n"
"v_mov_b32 v236,v235\n"
"s_nop 1\n"
"v_permlane16_swap_b32 v235,v236\n"
"v_max_f32 v235,v235,v236\n"
"v_add_f32 v235,s70,v235\n"
"v_max_f32 v235,0,v235\n"
"v_mul_f32 v236,-0.5,v235\n"
"v_exp_f32 v236,v236\n"
"v_sub_f32 v56,v56,v235\n"
"v_sub_f32 v57,v57,v235\n"
"v_sub_f32 v58,v58,v235\n"
"v_sub_f32 v59,v59,v235\n"
"v_sub_f32 v60,v60,v235\n"
"v_sub_f32 v61,v61,v235\n"
"v_sub_f32 v62,v62,v235\n"
"v_sub_f32 v63,v63,v235\n"
"v_sub_f32 v72,v72,v235\n"
"v_sub_f32 v73,v73,v235\n"
"v_sub_f32 v74,v74,v235\n"
"v_sub_f32 v75,v75,v235\n"
"v_sub_f32 v76,v76,v235\n"
"v_sub_f32 v77,v77,v235\n"
"v_sub_f32 v78,v78,v235\n"
"v_sub_f32 v79,v79,v235\n"
"v_sub_f32 v24,v24,v235\n"
"v_sub_f32 v25,v25,v235\n"
"v_sub_f32 v26,v26,v235\n"
"v_sub_f32 v27,v27,v235\n"
"v_sub_f32 v28,v28,v235\n"
"v_sub_f32 v29,v29,v235\n"
"v_sub_f32 v30,v30,v235\n"
"v_sub_f32 v31,v31,v235\n"
"v_sub_f32 v40,v40,v235\n"
"v_sub_f32 v41,v41,v235\n"
"v_sub_f32 v42,v42,v235\n"
"v_sub_f32 v43,v43,v235\n"
"v_sub_f32 v44,v44,v235\n"
"v_sub_f32 v45,v45,v235\n"
"v_sub_f32 v46,v46,v235\n"
"v_sub_f32 v47,v47,v235\n"
"v_mul_f32_dpp v98,v98,v236 quad_perm:[0,1,2,3] row_mask:0x5 bank_mask:0xf\n"
"v_mul_f32_dpp v99,v99,v236 quad_perm:[0,1,2,3] row_mask:0x5 bank_mask:0xf\n"
"s_nop 1\n"
"v_mul_f32_dpp v98,v98,v236 quad_perm:[0,1,2,3] row_mask:0x5 bank_mask:0xf\n"
"v_mul_f32_dpp v99,v99,v236 quad_perm:[0,1,2,3] row_mask:0x5 bank_mask:0xf\n"
"s_nop 1\n"
"s_call_b64 s[18:19],L_rs1_%=\n"
"v_mov_b32 v100,v96\n"
"v_mov_b32 v101,v97\n"
"v_mov_b32 v102,v98\n"
"v_mov_b32 v103,v99\n"
"v_exp_f32 v208,v48\n"
"v_exp_f32 v209,v49\n"
"v_exp_f32 v210,v50\n"
"v_cvt_pk_bf16_f32 v128,v208,v209\n"
"v_exp_f32 v211,v51\n"
"v_exp_f32 v212,v52\n"
"v_cvt_pk_bf16_f32 v129,v210,v211\n"
"v_exp_f32 v213,v53\n"
"v_exp_f32 v214,v54\n"
"v_cvt_pk_bf16_f32 v130,v212,v213\n"
"v_exp_f32 v215,v55\n"
"v_exp_f32 v208,v56\n"
"v_cvt_pk_bf16_f32 v131,v214,v215\n"
"v_exp_f32 v209,v57\n"
"v_exp_f32 v210,v58\n"
"v_cvt_pk_bf16_f32 v132,v208,v209\n"
"v_exp_f32 v211,v59\n"
"v_exp_f32 v212,v60\n"
"v_cvt_pk_bf16_f32 v133,v210,v211\n"
"v_exp_f32 v213,v61\n"
"v_exp_f32 v214,v62\n"
"v_cvt_pk_bf16_f32 v134,v212,v213\n"
"v_exp_f32 v215,v63\n"
"v_exp_f32 v208,v64\n"
"v_cvt_pk_bf16_f32 v135,v214,v215\n"
"v_exp_f32 v209,v65\n"
"v_exp_f32 v210,v66\n"
"v_cvt_pk_bf16_f32 v136,v208,v209\n"
"v_exp_f32 v211,v67\n"
"v_exp_f32 v212,v68\n"
"v_cvt_pk_bf16_f32 v137,v210,v211\n"
"v_exp_f32 v213,v69\n"
"v_exp_f32 v214,v70\n"
"v_cvt_pk_bf16_f32 v138,v212,v213\n"
"v_exp_f32 v215,v71\n"
"v_exp_f32 v208,v72\n"
"v_cvt_pk_bf16_f32 v139,v214,v215\n"
"v_exp_f32 v209,v73\n"
"v_exp_f32 v210,v74\n"
"v_cvt_pk_bf16_f32 v140,v208,v209\n"
"v_exp_f32 v211,v75\n"
"v_exp_f32 v212,v76\n"
"v_cvt_pk_bf16_f32 v141,v210,v211\n"
"v_exp_f32 v213,v77\n"
"v_exp_f32 v214,v78\n"
"v_cvt_pk_bf16_f32 v142,v212,v213\n"
"v_exp_f32 v215,v79\n"
"s_nop 0\n"
"v_cvt_pk_bf16_f32 v143,v214,v215\n"
"s_nop 1\n"
"v_smfmac_f32_16x16x64_bf16 v[100:103],a[240:243],v[128:135],v104\n"
"v_smfmac_f32_16x16x64_bf16 v[100:103],a[240:243],v[136:143],v104\n"
"s_nop 7\n"
"s_branch L_ret5_%=\n"
"L_stub6_%=:\n"
"s_mov_b32 s67,6\n"
"L_rare5_%=:\n"
"s_nop 15\n"
"v_max3_f32 v235,v16,v17,v18\n"
"v_max3_f32 v235,v235,v19,v20\n"
"v_max3_f32 v235,v235,v21,v22\n"
"v_max3_f32 v235,v235,v23,v32\n"
"v_max3_f32 v235,v235,v33,v34\n"
"v_max3_f32 v235,v235,v35,v36\n"
"v_max3_f32 v235,v235,v37,v38\n"
"v_max_f32 v235,v235,v39\n"
"v_mov_b32 v236,v235\n"
"s_nop 1\n"
"v_permlane32_swap_b32 v235,v236\n"
"v_max_f32 v235,v235,v236\n"
"v_mov_b32 v236,v235\n"
"s_nop 1\n"
"v_permlane16_swap_b32 v235,v236\n"
"v_max_f32 v235,v235,v236\n"
"v_add_f32 v235,s70,v235\n"
"v_max_f32 v235,0,v235\n"
"v_mul_f32 v236,-0.5,v235\n"
"v_exp_f32 v236,v236\n"
"v_sub_f32 v16,v16,v235\n"
"v_sub_f32 v17,v17,v235\n"
"v_sub_f32 v18,v18,v235\n"
"v_sub_f32 v19,v19,v235\n"
"v_sub_f32 v20,v20,v235\n"
"v_sub_f32 v21,v21,v235\n"
"v_sub_f32 v22,v22,v235\n"
"v_sub_f32 v23,v23,v235\n"
"v_sub_f32 v32,v32,v235\n"
"v_sub_f32 v33,v33,v235\n"
"v_sub_f32 v34,v34,v235\n"
"v_sub_f32 v35,v35,v235\n"
"v_sub_f32 v36,v36,v235\n"
"v_sub_f32 v37,v37,v235\n"
"v_sub_f32 v38,v38,v235\n"
"v_sub_f32 v39,v39,v235\n"
"v_mul_f32_dpp v100,v100,v236 quad_perm:[0,1,2,3] row_mask:0x5 bank_mask:0xf\n"
"v_mul_f32_dpp v101,v101,v236 quad_perm:[0,1,2,3] row_mask:0x5 bank_mask:0xf\n"
"s_nop 1\n"
"v_mul_f32_dpp v100,v100,v236 quad_perm:[0,1,2,3] row_mask:0x5 bank_mask:0xf\n"
"v_mul_f32_dpp v101,v101,v236 quad_perm:[0,1,2,3] row_mask:0x5 bank_mask:0xf\n"
"s_nop 1\n"
"s_call_b64 s[18:19],L_rs0_%=\n"
"v_max3_f32 v235,v24,v25,v26\n"
"v_max3_f32 v235,v235,v27,v28\n"
"v_max3_f32 v235,v235,v29,v30\n"
"v_max3_f32 v235,v235,v31,v40\n"
"v_max3_f32 v235,v235,v41,v42\n"
"v_max3_f32 v235,v235,v43,v44\n"
"v_max3_f32 v235,v235,v45,v46\n"
"v_max_f32 v235,v235,v47\n"
"v_mov_b32 v236,v235\n"
"s_nop 1\n"
"v_permlane32_swap_b32 v235,v236\n"
"v_max_f32 v235,v235,v236\n"
"v_mov_b32 v236,v235\n"
"s_nop 1\n"
"v_permlane16_swap_b32 v235,v236\n"
"v_max_f32 v235,v235,v236\n"
"v_add_f32 v235,s70,v235\n"
"v_max_f32 v235,0,v235\n"
"v_mul_f32 v236,-0.5,v235\n"
"v_exp_f32 v236,v236\n"
"v_sub_f32 v24,v24,v235\n"
"v_sub_f32 v25,v25,v235\n"
"v_sub_f32 v26,v26,v235\n"
"v_sub_f32 v27,v27,v235\n"
"v_sub_f32 v28,v28,v235\n"
"v_sub_f32 v29,v29,v235\n"
"v_sub_f32 v30,v30,v235\n"
"v_sub_f32 v31,v31,v235\n"
"v_sub_f32 v40,v40,v235\n"
"v_sub_f32 v41,v41,v235\n"
"v_sub_f32 v42,v42,v235\n"
"v_sub_f32 v43,v43,v235\n"
"v_sub_f32 v44,v44,v235\n"
"v_sub_f32 v45,v45,v235\n"
"v_sub_f32 v46,v46,v235\n"
"v_sub_f32 v47,v47,v235\n"
"v_mul_f32_dpp v102,v102,v236 quad_perm:[0,1,2,3] row_mask:0x5 bank_mask:0xf\n"
"v_mul_f32_dpp v103,v103,v236 quad_perm:[0,1,2,3] row_mask:0x5 bank_mask:0xf\n"
"s_nop 1\n"
"v_mul_f32_dpp v102,v102,v236 quad_perm:[0,1,2,3] row_mask:0x5 bank_mask:0xf\n"
"v_mul_f32_dpp v103,v103,v236 quad_perm:[0,1,2,3] row_mask:0x5 bank_mask:0xf\n"
"s_nop 1\n"
"s_call_b64 s[18:19],L_rs1_%=\n"
"v_max3_f32 v235,v48,v49,v50\n"
"v_max3_f32 v235,v235,v51,v52\n"
"v_max3_f32 v235,v235,v53,v54\n"
"v_max3_f32 v235,v235,v55,v64\n"
"v_max3_f32 v235,v235,v65,v66\n"
"v_max3_f32 v235,v235,v67,v68\n"
"v_max3_f32 v235,v235,v69,v70\n"
"v_max_f32 v235,v235,v71\n"
"v_mov_b32 v236,v235\n"
"s_nop 1\n"
"v_permlane32_swap_b32 v235,v236\n"
"v_max_f32 v235,v235,v236\n"
"v_mov_b32 v236,v235\n"
"s_nop 1\n"
"v_permlane16_swap_b32 v235,v236\n"
"v_max_f32 v235,v235,v236\n"
"v_add_f32 v235,s70,v235\n"
"v_max_f32 v235,0,v235\n"
"v_mul_f32 v236,-0.5,v235\n"
"v_exp_f32 v236,v236\n"
"v_sub_f32 v48,v48,v235\n"
"v_sub_f32 v49,v49,v235\n"
"v_sub_f32 v50,v50,v235\n"
"v_sub_f32 v51,v51,v235\n"
"v_sub_f32 v52,v52,v235\n"
"v_sub_f32 v53,v53,v235\n"
"v_sub_f32 v54,v54,v235\n"
"v_sub_f32 v55,v55,v235\n"
"v_sub_f32 v64,v64,v235\n"
"v_sub_f32 v65,v65,v235\n"
"v_sub_f32 v66,v66,v235\n"
"v_sub_f32 v67,v67,v235\n"
"v_sub_f32 v68,v68,v235\n"
"v_sub_f32 v69,v69,v235\n"
"v_sub_f32 v70,v70,v235\n"
"v_sub_f32 v71,v71,v235\n"
"v_mul_f32_dpp v100,v100,v236 quad_perm:[0,1,2,3] row_mask:0xa bank_mask:0xf\n"
"v_mul_f32_dpp v101,v101,v236 quad_perm:[0,1,2,3] row_mask:0xa bank_mask:0xf\n"
"s_nop 1\n"
"v_mul_f32_dpp v100,v100,v236 quad_perm:[0,1,2,3] row_mask:0xa bank_mask:0xf\n"
"v_mul_f32_dpp v101,v101,v236 quad_perm:[0,1,2,3] row_mask:0xa bank_mask:0xf\n"
"s_nop 1\n"
"s_call_b64 s[18:19],L_rs2_%=\n"
"v_max3_f32 v235,v56,v57,v58\n"
"v_max3_f32 v235,v235,v59,v60\n"
"v_max3_f32 v235,v235,v61,v62\n"
"v_max3_f32 v235,v235,v63,v72\n"
"v_max3_f32 v235,v235,v73,v74\n"
"v_max3_f32 v235,v235,v75,v76\n"
"v_max3_f32 v235,v235,v77,v78\n"
"v_max_f32 v235,v235,v79\n"
"v_mov_b32 v236,v235\n"
"s_nop 1\n"
"v_permlane32_swap_b32 v235,v236\n"
"v_max_f32 v235,v235,v236\n"
"v_mov_b32 v236,v235\n"
"s_nop 1\n"
"v_permlane16_swap_b32 v235,v236\n"
"v_max_f32 v235,v235,v236\n"
"v_add_f32 v235,s70,v235\n"
"v_max_f32 v235,0,v235\n"
"v_mul_f32 v236,-0.5,v235\n"
"v_exp_f32 v236,v236\n"
"v_sub_f32 v56,v56,v235\n"
"v_sub_f32 v57,v57,v235\n"
"v_sub_f32 v58,v58,v235\n"
"v_sub_f32 v59,v59,v235\n"
"v_sub_f32 v60,v60,v235\n"
"v_sub_f32 v61,v61,v235\n"
"v_sub_f32 v62,v62,v235\n"
"v_sub_f32 v63,v63,v235\n"
"v_sub_f32 v72,v72,v235\n"
"v_sub_f32 v73,v73,v235\n"
"v_sub_f32 v74,v74,v235\n"
"v_sub_f32 v75,v75,v235\n"
"v_sub_f32 v76,v76,v235\n"
"v_sub_f32 v77,v77,v235\n"
"v_sub_f32 v78,v78,v235\n"
"v_sub_f32 v79,v79,v235\n"
"v_mul_f32_dpp v102,v102,v236 quad_perm:[0,1,2,3] row_mask:0xa bank_mask:0xf\n"
"v_mul_f32_dpp v103,v103,v236 quad_perm:[0,1,2,3] row_mask:0xa bank_mask:0xf\n"
"s_nop 1\n"
"v_mul_f32_dpp v102,v102,v236 quad_perm:[0,1,2,3] row_mask:0xa bank_mask:0xf\n"
"v_mul_f32_dpp v103,v103,v236 quad_perm:[0,1,2,3] row_mask:0xa bank_mask:0xf\n"
"s_nop 1\n"
"s_call_b64 s[18:19],L_rs3_%=\n"
"v_mov_b32 v96,v100\n"
"v_mov_b32 v97,v101\n"
"v_mov_b32 v98,v102\n"
"v_mov_b32 v99,v103\n"
"v_exp_f32 v208,v16\n"
"v_exp_f32 v209,v17\n"
"v_exp_f32 v210,v18\n"
"v_cvt_pk_bf16_f32 v112,v208,v209\n"
"v_exp_f32 v211,v19\n"
"v_exp_f32 v212,v20\n"
"v_cvt_pk_bf16_f32 v113,v210,v211\n"
"v_exp_f32 v213,v21\n"
"v_exp_f32 v214,v22\n"
"v_cvt_pk_bf16_f32 v114,v212,v213\n"
"v_exp_f32 v215,v23\n"
"v_exp_f32 v208,v24\n"
"v_cvt_pk_bf16_f32 v115,v214,v215\n"
"v_exp_f32 v209,v25\n"
"v_exp_f32 v210,v26\n"
"v_cvt_pk_bf16_f32 v116,v208,v209\n"
"v_exp_f32 v211,v27\n"
"v_exp_f32 v212,v28\n"
"v_cvt_pk_bf16_f32 v117,v210,v211\n"
"v_exp_f32 v213,v29\n"
"v_exp_f32 v214,v30\n"
"v_cvt_pk_bf16_f32 v118,v212,v213\n"
"v_exp_f32 v215,v31\n"
"v_exp_f32 v208,v32\n"
"v_cvt_pk_bf16_f32 v119,v214,v215\n"
"v_exp_f32 v209,v33\n"
"v_exp_f32 v210,v34\n"
"v_cvt_pk_bf16_f32 v120,v208,v209\n"
"v_exp_f32 v211,v35\n"
"v_exp_f32 v212,v36\n"
"v_cvt_pk_bf16_f32 v121,v210,v211\n"
"v_exp_f32 v213,v37\n"
"v_exp_f32 v214,v38\n"
"v_cvt_pk_bf16_f32 v122,v212,v213\n"
"v_exp_f32 v215,v39\n"
"v_exp_f32 v208,v40\n"
"v_cvt_pk_bf16_f32 v123,v214,v215\n"
"v_exp_f32 v209,v41\n"
"v_exp_f32 v210,v42\n"
"v_cvt_pk_bf16_f32 v124,v208,v209\n"
"v_exp_f32 v211,v43\n"
"v_exp_f32 v212,v44\n"
"v_cvt_pk_bf16_f32 v125,v210,v211\n"
"v_exp_f32 v213,v45\n"
"v_exp_f32 v214,v46\n"
"v_cvt_pk_bf16_f32 v126,v212,v213\n"
"v_exp_f32 v215,v47\n"
"s_nop 0\n"
"v_cvt_pk_bf16_f32 v127,v214,v215\n"
"s_nop 1\n"
"v_smfmac_f32_16x16x64_bf16 v[96:99],a[240:243],v[112:119],v104\n"
"v_smfmac_f32_16x16x64_bf16 v[96:99],a[240:243],v[120:127],v104\n"
"s_nop 7\n"
"s_branch L_ret6_%=\n"
"L_stub7_%=:\n"
"s_mov_b32 s67,7\n"
"L_rare6_%=:\n"
"s_nop 15\n"
"v_max3_f32 v235,v48,v49,v50\n"
"v_max3_f32 v235,v235,v51,v52\n"
"v_max3_f32 v235,v235,v53,v54\n"
"v_max3_f32 v235,v235,v55,v64\n"
"v_max3_f32 v235,v235,v65,v66\n"
"v_max3_f32 v235,v235,v67,v68\n"
"v_max3_f32 v235,v235,v69,v70\n"
"v_max3_f32 v235,v235,v71,v16\n"
"v_max3_f32 v235,v235,v17,v18\n"
"v_max3_f32 v235,v235,v19,v20\n"
"v_max3_f32 v235,v235,v21,v22\n"
"v_max3_f32 v235,v235,v23,v32\n"
"v_max3_f32 v235,v235,v33,v34\n"
"v_max3_f32 v235,v235,v35,v36\n"
"v_max3_f32 v235,v235,v37,v38\n"
"v_max_f32 v235,v235,v39\n"
"v_mov_b32 v236,v235\n"
"s_nop 1\n"
"v_permlane32_swap_b32 v235,v236\n"
"v_max_f32 v235,v235,v236\n"
"v_mov_b32 v236,v235\n"
"s_nop 1\n"
"v_permlane16_swap_b32 v235,v236\n"
"v_max_f32 v235,v235,v236\n"
"v_add_f32 v235,s70,v235\n"
"v_max_f32 v235,0,v235\n"
"v_mul_f32 v236,-0.5,v235\n"
"v_exp_f32 v236,v236\n"
"v_sub_f32 v48,v48,v235\n"
"v_sub_f32 v49,v49,v235\n"
"v_sub_f32 v50,v50,v235\n"
"v_sub_f32 v51,v51,v235\n"
"v_sub_f32 v52,v52,v235\n"
"v_sub_f32 v53,v53,v235\n"
"v_sub_f32 v54,v54,v235\n"
"v_sub_f32 v55,v55,v235\n"
"v_sub_f32 v64,v64,v235\n"
"v_sub_f32 v65,v65,v235\n"
"v_sub_f32 v66,v66,v235\n"
"v_sub_f32 v67,v67,v235\n"
"v_sub_f32 v68,v68,v235\n"
"v_sub_f32 v69,v69,v235\n"
"v_sub_f32 v70,v70,v235\n"
"v_sub_f32 v71,v71,v235\n"
"v_sub_f32 v16,v16,v235\n"
"v_sub_f32 v17,v17,v235\n"
"v_sub_f32 v18,v18,v235\n"
"v_sub_f32 v19,v19,v235\n"
"v_sub_f32 v20,v20,v235\n"
"v_sub_f32 v21,v21,v235\n"
"v_sub_f32 v22,v22,v235\n"
"v_sub_f32 v23,v23,v235\n"
"v_sub_f32 v32,v32,v235\n"
"v_sub_f32 v33,v33,v235\n"
"v_sub_f32 v34,v34,v235\n"
"v_sub_f32 v35,v35,v235\n"
"v_sub_f32 v36,v36,v235\n"
"v_sub_f32 v37,v37,v235\n"
"v_sub_f32 v38,v38,v235\n"
"v_sub_f32 v39,v39,v235\n"
"v_mul_f32_dpp v96,v96,v236 quad_perm:[0,1,2,3] row_mask:0xa bank_mask:0xf\n"
"v_mul_f32_dpp v97,v97,v236 quad_perm:[0,1,2,3] row_mask:0xa bank_mask:0xf\n"
"s_nop 1\n"
"v_mul_f32_dpp v96,v96,v236 quad_perm:[0,1,2,3] row_mask:0xa bank_mask:0xf\n"
"v_mul_f32_dpp v97,v97,v236 quad_perm:[0,1,2,3] row_mask:0xa bank_mask:0xf\n"
"s_nop 1\n"
"s_call_b64 s[18:19],L_rs2_%=\n"
"v_max3_f32 v235,v56,v57,v58\n"
"v_max3_f32 v235,v235,v59,v60\n"
"v_max3_f32 v235,v235,v61,v62\n"
"v_max3_f32 v235,v235,v63,v72\n"
"v_max3_f32 v235,v235,v73,v74\n"
"v_max3_f32 v235,v235,v75,v76\n"
"v_max3_f32 v235,v235,v77,v78\n"
"v_max3_f32 v235,v235,v79,v24\n"
"v_max3_f32 v235,v235,v25,v26\n"
"v_max3_f32 v235,v235,v27,v28\n"
"v_max3_f32 v235,v235,v29,v30\n"
"v_max3_f32 v235,v235,v31,v40\n"
"v_max3_f32 v235,v235,v41,v42\n"
"v_max3_f32 v235,v235,v43,v44\n"
"v_max3_f32 v235,v235,v45,v46\n"
"v_max_f32 v235,v235,v47\n"
"v_mov_b32 v236,v235\n"
"s_nop 1\n"
"v_permlane32_swap_b32 v235,v236\n"
"v_max_f32 v235,v235,v236\n"
"v_mov_b32 v236,v235\n"
"s_nop 1\n"
"v_permlane16_swap_b32 v235,v236\n"
"v_max_f32 v235,v235,v236\n"
"v_add_f32 v235,s70,v235\n"
"v_max_f32 v235,0,v235\n"
"v_mul_f32 v236,-0.5,v235\n"
"v_exp_f32 v236,v236\n"
"v_sub_f32 v56,v56,v235\n"
"v_sub_f32 v57,v57,v235\n"
"v_sub_f32 v58,v58,v235\n"
"v_sub_f32 v59,v59,v235\n"
"v_sub_f32 v60,v60,v235\n"
"v_sub_f32 v61,v61,v235\n"
"v_sub_f32 v62,v62,v235\n"
"v_sub_f32 v63,v63,v235\n"
"v_sub_f32 v72,v72,v235\n"
"v_sub_f32 v73,v73,v235\n"
"v_sub_f32 v74,v74,v235\n"
"v_sub_f32 v75,v75,v235\n"
"v_sub_f32 v76,v76,v235\n"
"v_sub_f32 v77,v77,v235\n"
"v_sub_f32 v78,v78,v235\n"
"v_sub_f32 v79,v79,v235\n"
"v_sub_f32 v24,v24,v235\n"
"v_sub_f32 v25,v25,v235\n"
"v_sub_f32 v26,v26,v235\n"
"v_sub_f32 v27,v27,v235\n"
"v_sub_f32 v28,v28,v235\n"
"v_sub_f32 v29,v29,v235\n"
"v_sub_f32 v30,v30,v235\n"
"v_sub_f32 v31,v31,v235\n"
"v_sub_f32 v40,v40,v235\n"
"v_sub_f32 v41,v41,v235\n"
"v_sub_f32 v42,v42,v235\n"
"v_sub_f32 v43,v43,v235\n"
"v_sub_f32 v44,v44,v235\n"
"v_sub_f32 v45,v45,v235\n"
"v_sub_f32 v46,v46,v235\n"
"v_sub_f32 v47,v47,v235\n"
"v_mul_f32_dpp v98,v98,v236 quad_perm:[0,1,2,3] row_mask:0xa bank_mask:0xf\n"
"v_mul_f32_dpp v99,v99,v236 quad_perm:[0,1,2,3] row_mask:0xa bank_mask:0xf\n"
"s_nop 1\n"
"v_mul_f32_dpp v98,v98,v236 quad_perm:[0,1,2,3] row_mask:0xa bank_mask:0xf\n"
"v_mul_f32_dpp v99,v99,v236 quad_perm:[0,1,2,3] row_mask:0xa bank_mask:0xf\n"
"s_nop 1\n"
"s_call_b64 s[18:19],L_rs3_%=\n"
"v_mov_b32 v100,v96\n"
"v_mov_b32 v101,v97\n"
"v_mov_b32 v102,v98\n"
"v_mov_b32 v103,v99\n"
"v_exp_f32 v208,v48\n"
"v_exp_f32 v209,v49\n"
"v_exp_f32 v210,v50\n"
"v_cvt_pk_bf16_f32 v128,v208,v209\n"
"v_exp_f32 v211,v51\n"
"v_exp_f32 v212,v52\n"
"v_cvt_pk_bf16_f32 v129,v210,v211\n"
"v_exp_f32 v213,v53\n"
"v_exp_f32 v214,v54\n"
"v_cvt_pk_bf16_f32 v130,v212,v213\n"
"v_exp_f32 v215,v55\n"
"v_exp_f32 v208,v56\n"
"v_cvt_pk_bf16_f32 v131,v214,v215\n"
"v_exp_f32 v209,v57\n"
"v_exp_f32 v210,v58\n"
"v_cvt_pk_bf16_f32 v132,v208,v209\n"
"v_exp_f32 v211,v59\n"
"v_exp_f32 v212,v60\n"
"v_cvt_pk_bf16_f32 v133,v210,v211\n"
"v_exp_f32 v213,v61\n"
"v_exp_f32 v214,v62\n"
"v_cvt_pk_bf16_f32 v134,v212,v213\n"
"v_exp_f32 v215,v63\n"
"v_exp_f32 v208,v64\n"
"v_cvt_pk_bf16_f32 v135,v214,v215\n"
"v_exp_f32 v209,v65\n"
"v_exp_f32 v210,v66\n"
"v_cvt_pk_bf16_f32 v136,v208,v209\n"
"v_exp_f32 v211,v67\n"
"v_exp_f32 v212,v68\n"
"v_cvt_pk_bf16_f32 v137,v210,v211\n"
"v_exp_f32 v213,v69\n"
"v_exp_f32 v214,v70\n"
"v_cvt_pk_bf16_f32 v138,v212,v213\n"
"v_exp_f32 v215,v71\n"
"v_exp_f32 v208,v72\n"
"v_cvt_pk_bf16_f32 v139,v214,v215\n"
"v_exp_f32 v209,v73\n"
"v_exp_f32 v210,v74\n"
"v_cvt_pk_bf16_f32 v140,v208,v209\n"
"v_exp_f32 v211,v75\n"
"v_exp_f32 v212,v76\n"
"v_cvt_pk_bf16_f32 v141,v210,v211\n"
"v_exp_f32 v213,v77\n"
"v_exp_f32 v214,v78\n"
"v_cvt_pk_bf16_f32 v142,v212,v213\n"
"v_exp_f32 v215,v79\n"
"s_nop 0\n"
"v_cvt_pk_bf16_f32 v143,v214,v215\n"
"s_nop 1\n"
"v_smfmac_f32_16x16x64_bf16 v[100:103],a[244:247],v[128:135],v104\n"
"v_smfmac_f32_16x16x64_bf16 v[100:103],a[244:247],v[136:143],v104\n"
"s_nop 7\n"
"s_branch L_ret7_%=\n"
"L_stub8_%=:\n"
"s_mov_b32 s67,8\n"
"L_rare7_%=:\n"
"s_nop 15\n"
"v_max3_f32 v235,v16,v17,v18\n"
"v_max3_f32 v235,v235,v19,v20\n"
"v_max3_f32 v235,v235,v21,v22\n"
"v_max3_f32 v235,v235,v23,v32\n"
"v_max3_f32 v235,v235,v33,v34\n"
"v_max3_f32 v235,v235,v35,v36\n"
"v_max3_f32 v235,v235,v37,v38\n"
"v_max3_f32 v235,v235,v39,v48\n"
"v_max3_f32 v235,v235,v49,v50\n"
"v_max3_f32 v235,v235,v51,v52\n"
"v_max3_f32 v235,v235,v53,v54\n"
"v_max3_f32 v235,v235,v55,v64\n"
"v_max3_f32 v235,v235,v65,v66\n"
"v_max3_f32 v235,v235,v67,v68\n"
"v_max3_f32 v235,v235,v69,v70\n"
"v_max_f32 v235,v235,v71\n"
"v_mov_b32 v236,v235\n"
"s_nop 1\n"
"v_permlane32_swap_b32 v235,v236\n"
"v_max_f32 v235,v235,v236\n"
"v_mov_b32 v236,v235\n"
"s_nop 1\n"
"v_permlane16_swap_b32 v235,v236\n"
"v_max_f32 v235,v235,v236\n"
"v_add_f32 v235,s70,v235\n"
"v_max_f32 v235,0,v235\n"
"v_mul_f32 v236,-0.5,v235\n"
"v_exp_f32 v236,v236\n"
"v_sub_f32 v16,v16,v235\n"
"v_sub_f32 v17,v17,v235\n"
"v_sub_f32 v18,v18,v235\n"
"v_sub_f32 v19,v19,v235\n"
"v_sub_f32 v20,v20,v235\n"
"v_sub_f32 v21,v21,v235\n"
"v_sub_f32 v22,v22,v235\n"
"v_sub_f32 v23,v23,v235\n"
"v_sub_f32 v32,v32,v235\n"
"v_sub_f32 v33,v33,v235\n"
"v_sub_f32 v34,v34,v235\n"
"v_sub_f32 v35,v35,v235\n"
"v_sub_f32 v36,v36,v235\n"
"v_sub_f32 v37,v37,v235\n"
"v_sub_f32 v38,v38,v235\n"
"v_sub_f32 v39,v39,v235\n"
"v_sub_f32 v48,v48,v235\n"
"v_sub_f32 v49,v49,v235\n"
"v_sub_f32 v50,v50,v235\n"
"v_sub_f32 v51,v51,v235\n"
"v_sub_f32 v52,v52,v235\n"
"v_sub_f32 v53,v53,v235\n"
"v_sub_f32 v54,v54,v235\n"
"v_sub_f32 v55,v55,v235\n"
"v_sub_f32 v64,v64,v235\n"
"v_sub_f32 v65,v65,v235\n"
"v_sub_f32 v66,v66,v235\n"
"v_sub_f32 v67,v67,v235\n"
"v_sub_f32 v68,v68,v235\n"
"v_sub_f32 v69,v69,v235\n"
"v_sub_f32 v70,v70,v235\n"
"v_sub_f32 v71,v71,v235\n"
"v_mul_f32_dpp v100,v100,v236 quad_perm:[0,1,2,3] row_mask:0xa bank_mask:0xf\n"
"v_mul_f32_dpp v101,v101,v236 quad_perm:[0,1,2,3] row_mask:0xa bank_mask:0xf\n"
"s_nop 1\n"
"v_mul_f32_dpp v100,v100,v236 quad_perm:[0,1,2,3] row_mask:0xa bank_mask:0xf\n"
"v_mul_f32_dpp v101,v101,v236 quad_perm:[0,1,2,3] row_mask:0xa bank_mask:0xf\n"
"s_nop 1\n"
"s_call_b64 s[18:19],L_rs2_%=\n"
"v_max3_f32 v235,v24,v25,v26\n"
"v_max3_f32 v235,v235,v27,v28\n"
"v_max3_f32 v235,v235,v29,v30\n"
"v_max3_f32 v235,v235,v31,v40\n"
"v_max3_f32 v235,v235,v41,v42\n"
"v_max3_f32 v235,v235,v43,v44\n"
"v_max3_f32 v235,v235,v45,v46\n"
"v_max3_f32 v235,v235,v47,v56\n"
"v_max3_f32 v235,v235,v57,v58\n"
"v_max3_f32 v235,v235,v59,v60\n"
"v_max3_f32 v235,v235,v61,v62\n"
"v_max3_f32 v235,v235,v63,v72\n"
"v_max3_f32 v235,v235,v73,v74\n"
"v_max3_f32 v235,v235,v75,v76\n"
"v_max3_f32 v235,v235,v77,v78\n"
"v_max_f32 v235,v235,v79\n"
"v_mov_b32 v236,v235\n"
"s_nop 1\n"
"v_permlane32_swap_b32 v235,v236\n"
"v_max_f32 v235,v235,v236\n"
"v_mov_b32 v236,v235\n"
"s_nop 1\n"
"v_permlane16_swap_b32 v235,v236\n"
"v_max_f32 v235,v235,v236\n"
"v_add_f32 v235,s70,v235\n"
"v_max_f32 v235,0,v235\n"
"v_mul_f32 v236,-0.5,v235\n"
"v_exp_f32 v236,v236\n"
"v_sub_f32 v24,v24,v235\n"
"v_sub_f32 v25,v25,v235\n"
"v_sub_f32 v26,v26,v235\n"
"v_sub_f32 v27,v27,v235\n"
"v_sub_f32 v28,v28,v235\n"
"v_sub_f32 v29,v29,v235\n"
"v_sub_f32 v30,v30,v235\n"
"v_sub_f32 v31,v31,v235\n"
"v_sub_f32 v40,v40,v235\n"
"v_sub_f32 v41,v41,v235\n"
"v_sub_f32 v42,v42,v235\n"
"v_sub_f32 v43,v43,v235\n"
"v_sub_f32 v44,v44,v235\n"
"v_sub_f32 v45,v45,v235\n"
"v_sub_f32 v46,v46,v235\n"
"v_sub_f32 v47,v47,v235\n"
"v_sub_f32 v56,v56,v235\n"
"v_sub_f32 v57,v57,v235\n"
"v_sub_f32 v58,v58,v235\n"
"v_sub_f32 v59,v59,v235\n"
"v_sub_f32 v60,v60,v235\n"
"v_sub_f32 v61,v61,v235\n"
"v_sub_f32 v62,v62,v235\n"
"v_sub_f32 v63,v63,v235\n"
"v_sub_f32 v72,v72,v235\n"
"v_sub_f32 v73,v73,v235\n"
"v_sub_f32 v74,v74,v235\n"
"v_sub_f32 v75,v75,v235\n"
"v_sub_f32 v76,v76,v235\n"
"v_sub_f32 v77,v77,v235\n"
"v_sub_f32 v78,v78,v235\n"
"v_sub_f32 v79,v79,v235\n"
"v_mul_f32_dpp v102,v102,v236 quad_perm:[0,1,2,3] row_mask:0xa bank_mask:0xf\n"
"v_mul_f32_dpp v103,v103,v236 quad_perm:[0,1,2,3] row_mask:0xa bank_mask:0xf\n"
"s_nop 1\n"
"v_mul_f32_dpp v102,v102,v236 quad_perm:[0,1,2,3] row_mask:0xa bank_mask:0xf\n"
"v_mul_f32_dpp v103,v103,v236 quad_perm:[0,1,2,3] row_mask:0xa bank_mask:0xf\n"
"s_nop 1\n"
"s_call_b64 s[18:19],L_rs3_%=\n"
"v_mov_b32 v96,v100\n"
"v_mov_b32 v97,v101\n"
"v_mov_b32 v98,v102\n"
"v_mov_b32 v99,v103\n"
"v_exp_f32 v208,v16\n"
"v_exp_f32 v209,v17\n"
"v_exp_f32 v210,v18\n"
"v_cvt_pk_bf16_f32 v112,v208,v209\n"
"v_exp_f32 v211,v19\n"
"v_exp_f32 v212,v20\n"
"v_cvt_pk_bf16_f32 v113,v210,v211\n"
"v_exp_f32 v213,v21\n"
"v_exp_f32 v214,v22\n"
"v_cvt_pk_bf16_f32 v114,v212,v213\n"
"v_exp_f32 v215,v23\n"
"v_exp_f32 v208,v24\n"
"v_cvt_pk_bf16_f32 v115,v214,v215\n"
"v_exp_f32 v209,v25\n"
"v_exp_f32 v210,v26\n"
"v_cvt_pk_bf16_f32 v116,v208,v209\n"
"v_exp_f32 v211,v27\n"
"v_exp_f32 v212,v28\n"
"v_cvt_pk_bf16_f32 v117,v210,v211\n"
"v_exp_f32 v213,v29\n"
"v_exp_f32 v214,v30\n"
"v_cvt_pk_bf16_f32 v118,v212,v213\n"
"v_exp_f32 v215,v31\n"
"v_exp_f32 v208,v32\n"
"v_cvt_pk_bf16_f32 v119,v214,v215\n"
"v_exp_f32 v209,v33\n"
"v_exp_f32 v210,v34\n"
"v_cvt_pk_bf16_f32 v120,v208,v209\n"
"v_exp_f32 v211,v35\n"
"v_exp_f32 v212,v36\n"
"v_cvt_pk_bf16_f32 v121,v210,v211\n"
"v_exp_f32 v213,v37\n"
"v_exp_f32 v214,v38\n"
"v_cvt_pk_bf16_f32 v122,v212,v213\n"
"v_exp_f32 v215,v39\n"
"v_exp_f32 v208,v40\n"
"v_cvt_pk_bf16_f32 v123,v214,v215\n"
"v_exp_f32 v209,v41\n"
"v_exp_f32 v210,v42\n"
"v_cvt_pk_bf16_f32 v124,v208,v209\n"
"v_exp_f32 v211,v43\n"
"v_exp_f32 v212,v44\n"
"v_cvt_pk_bf16_f32 v125,v210,v211\n"
"v_exp_f32 v213,v45\n"
"v_exp_f32 v214,v46\n"
"v_cvt_pk_bf16_f32 v126,v212,v213\n"
"v_exp_f32 v215,v47\n"
"s_nop 0\n"
"v_cvt_pk_bf16_f32 v127,v214,v215\n"
"s_nop 1\n"
"v_smfmac_f32_16x16x64_bf16 v[96:99],a[244:247],v[112:119],v104\n"
"v_smfmac_f32_16x16x64_bf16 v[96:99],a[244:247],v[120:127],v104\n"
"s_nop 7\n"
"s_branch L_ret8_%=\n"
"L_stub2_%=:\n"
"s_mov_b32 s67,2\n"
"L_rare8_%=:\n"
"s_nop 15\n"
"v_max3_f32 v235,v48,v49,v50\n"
"v_max3_f32 v235,v235,v51,v52\n"
"v_max3_f32 v235,v235,v53,v54\n"
"v_max3_f32 v235,v235,v55,v64\n"
"v_max3_f32 v235,v235,v65,v66\n"
"v_max3_f32 v235,v235,v67,v68\n"
"v_max3_f32 v235,v235,v69,v70\n"
"v_max_f32 v235,v235,v71\n"
"v_mov_b32 v236,v235\n"
"s_nop 1\n"
"v_permlane32_swap_b32 v235,v236\n"
"v_max_f32 v235,v235,v236\n"
"v_mov_b32 v236,v235\n"
"s_nop 1\n"
"v_permlane16_swap_b32 v235,v236\n"
"v_max_f32 v235,v235,v236\n"
"v_add_f32 v235,s70,v235\n"
"v_max_f32 v235,0,v235\n"
"v_mul_f32 v236,-0.5,v235\n"
"v_exp_f32 v236,v236\n"
"v_sub_f32 v48,v48,v235\n"
"v_sub_f32 v49,v49,v235\n"
"v_sub_f32 v50,v50,v235\n"
"v_sub_f32 v51,v51,v235\n"
"v_sub_f32 v52,v52,v235\n"
"v_sub_f32 v53,v53,v235\n"
"v_sub_f32 v54,v54,v235\n"
"v_sub_f32 v55,v55,v235\n"
"v_sub_f32 v64,v64,v235\n"
"v_sub_f32 v65,v65,v235\n"
"v_sub_f32 v66,v66,v235\n"
"v_sub_f32 v67,v67,v235\n"
"v_sub_f32 v68,v68,v235\n"
"v_sub_f32 v69,v69,v235\n"
"v_sub_f32 v70,v70,v235\n"
"v_sub_f32 v71,v71,v235\n"
"v_mul_f32_dpp v96,v96,v236 quad_perm:[0,1,2,3] row_mask:0xa bank_mask:0xf\n"
"v_mul_f32_dpp v97,v97,v236 quad_perm:[0,1,2,3] row_mask:0xa bank_mask:0xf\n"
"s_nop 1\n"
"v_mul_f32_dpp v96,v96,v236 quad_perm:[0,1,2,3] row_mask:0xa bank_mask:0xf\n"
"v_mul_f32_dpp v97,v97,v236 quad_perm:[0,1,2,3] row_mask:0xa bank_mask:0xf\n"
"s_nop 1\n"
"s_call_b64 s[18:19],L_rs2_%=\n"
"v_max3_f32 v235,v56,v57,v58\n"
"v_max3_f32 v235,v235,v59,v60\n"
"v_max3_f32 v235,v235,v61,v62\n"
"v_max3_f32 v235,v235,v63,v72\n"
"v_max3_f32 v235,v235,v73,v74\n"
"v_max3_f32 v235,v235,v75,v76\n"
"v_max3_f32 v235,v235,v77,v78\n"
"v_max_f32 v235,v235,v79\n"
"v_mov_b32 v236,v235\n"
"s_nop 1\n"
"v_permlane32_swap_b32 v235,v236\n"
"v_max_f32 v235,v235,v236\n"
"v_mov_b32 v236,v235\n"
"s_nop 1\n"
"v_permlane16_swap_b32 v235,v236\n"
"v_max_f32 v235,v235,v236\n"
"v_add_f32 v235,s70,v235\n"
"v_max_f32 v235,0,v235\n"
"v_mul_f32 v236,-0.5,v235\n"
"v_exp_f32 v236,v236\n"
"v_sub_f32 v56,v56,v235\n"
"v_sub_f32 v57,v57,v235\n"
"v_sub_f32 v58,v58,v235\n"
"v_sub_f32 v59,v59,v235\n"
"v_sub_f32 v60,v60,v235\n"
"v_sub_f32 v61,v61,v235\n"
"v_sub_f32 v62,v62,v235\n"
"v_sub_f32 v63,v63,v235\n"
"v_sub_f32 v72,v72,v235\n"
"v_sub_f32 v73,v73,v235\n"
"v_sub_f32 v74,v74,v235\n"
"v_sub_f32 v75,v75,v235\n"
"v_sub_f32 v76,v76,v235\n"
"v_sub_f32 v77,v77,v235\n"
"v_sub_f32 v78,v78,v235\n"
"v_sub_f32 v79,v79,v235\n"
"v_mul_f32_dpp v98,v98,v236 quad_perm:[0,1,2,3] row_mask:0xa bank_mask:0xf\n"
"v_mul_f32_dpp v99,v99,v236 quad_perm:[0,1,2,3] row_mask:0xa bank_mask:0xf\n"
"s_nop 1\n"
"v_mul_f32_dpp v98,v98,v236 quad_perm:[0,1,2,3] row_mask:0xa bank_mask:0xf\n"
"v_mul_f32_dpp v99,v99,v236 quad_perm:[0,1,2,3] row_mask:0xa bank_mask:0xf\n"
"s_nop 1\n"
"s_call_b64 s[18:19],L_rs3_%=\n"
"v_mov_b32 v100,v96\n"
"v_mov_b32 v101,v97\n"
"v_mov_b32 v102,v98\n"
"v_mov_b32 v103,v99\n"
"v_exp_f32 v208,v48\n"
"v_exp_f32 v209,v49\n"
"v_exp_f32 v210,v50\n"
"v_cvt_pk_bf16_f32 v128,v208,v209\n"
"v_exp_f32 v211,v51\n"
"v_exp_f32 v212,v52\n"
"v_cvt_pk_bf16_f32 v129,v210,v211\n"
"v_exp_f32 v213,v53\n"
"v_exp_f32 v214,v54\n"
"v_cvt_pk_bf16_f32 v130,v212,v213\n"
"v_exp_f32 v215,v55\n"
"v_exp_f32 v208,v56\n"
"v_cvt_pk_bf16_f32 v131,v214,v215\n"
"v_exp_f32 v209,v57\n"
"v_exp_f32 v210,v58\n"
"v_cvt_pk_bf16_f32 v132,v208,v209\n"
"v_exp_f32 v211,v59\n"
"v_exp_f32 v212,v60\n"
"v_cvt_pk_bf16_f32 v133,v210,v211\n"
"v_exp_f32 v213,v61\n"
"v_exp_f32 v214,v62\n"
"v_cvt_pk_bf16_f32 v134,v212,v213\n"
"v_exp_f32 v215,v63\n"
"v_exp_f32 v208,v64\n"
"v_cvt_pk_bf16_f32 v135,v214,v215\n"
"v_exp_f32 v209,v65\n"
"v_exp_f32 v210,v66\n"
"v_cvt_pk_bf16_f32 v136,v208,v209\n"
"v_exp_f32 v211,v67\n"
"v_exp_f32 v212,v68\n"
"v_cvt_pk_bf16_f32 v137,v210,v211\n"
"v_exp_f32 v213,v69\n"
"v_exp_f32 v214,v70\n"
"v_cvt_pk_bf16_f32 v138,v212,v213\n"
"v_exp_f32 v215,v71\n"
"v_exp_f32 v208,v72\n"
"v_cvt_pk_bf16_f32 v139,v214,v215\n"
"v_exp_f32 v209,v73\n"
"v_exp_f32 v210,v74\n"
"v_cvt_pk_bf16_f32 v140,v208,v209\n"
"v_exp_f32 v211,v75\n"
"v_exp_f32 v212,v76\n"
"v_cvt_pk_bf16_f32 v141,v210,v211\n"
"v_exp_f32 v213,v77\n"
"v_exp_f32 v214,v78\n"
"v_cvt_pk_bf16_f32 v142,v212,v213\n"
"v_exp_f32 v215,v79\n"
"s_nop 0\n"
"v_cvt_pk_bf16_f32 v143,v214,v215\n"
"s_nop 1\n"
"v_smfmac_f32_16x16x64_bf16 v[100:103],a[244:247],v[128:135],v104\n"
"v_smfmac_f32_16x16x64_bf16 v[100:103],a[244:247],v[136:143],v104\n"
"s_nop 7\n"
"s_branch L_ret2_%=\n"
"L_reu1_%=:\n"
"s_cmp_lg_u32 s63,8\n"
"s_branch L_ret1_%=\n"
"L_reu10_%=:\n"
"s_cmp_lg_u32 s63,3\n"
"s_branch L_ret10_%=\n"
"L_rs0_%=:\n"
"v_sub_f32 v80,v80,v235\n"
"v_sub_f32 v81,v81,v235\n"
"v_sub_f32 v82,v82,v235\n"
"v_sub_f32 v83,v83,v235\n"
"v_accvgpr_read_b32 v237,a0\n"
"v_accvgpr_read_b32 v238,a1\n"
"v_mul_f32 v237,v237,v236\n"
"v_mul_f32 v238,v238,v236\n"
"v_mul_f32 v237,v237,v236\n"
"v_mul_f32 v238,v238,v236\n"
"v_accvgpr_write_b32 a0,v237\n"
"v_accvgpr_write_b32 a1,v238\n"
"v_accvgpr_read_b32 v237,a2\n"
"v_accvgpr_read_b32 v238,a3\n"
"v_mul_f32 v237,v237,v236\n"
"v_mul_f32 v238,v238,v236\n"
"v_mul_f32 v237,v237,v236\n"
"v_mul_f32 v238,v238,v236\n"
"v_accvgpr_write_b32 a2,v237\n"
"v_accvgpr_write_b32 a3,v238\n"
"v_accvgpr_read_b32 v237,a4\n"
"v_accvgpr_read_b32 v238,a5\n"
"v_mul_f32 v237,v237,v236\n"
"v_mul_f32 v238,v238,v236\n"
"v_mul_f32 v237,v237,v236\n"
"v_mul_f32 v238,v238,v236\n"
"v_accvgpr_write_b32 a4,v237\n"
"v_accvgpr_write_b32 a5,v238\n"
"v_accvgpr_read_b32 v237,a6\n"
"v_accvgpr_read_b32 v238,a7\n"
"v_mul_f32 v237,v237,v236\n"
"v_mul_f32 v238,v238,v236\n"
"v_mul_f32 v237,v237,v236\n"
"v_mul_f32 v238,v238,v236\n"
"v_accvgpr_write_b32 a6,v237\n"
"v_accvgpr_write_b32 a7,v238\n"
"v_accvgpr_read_b32 v237,a32\n"
"v_accvgpr_read_b32 v238,a33\n"
"v_mul_f32 v237,v237,v236\n"
"v_mul_f32 v238,v238,v236\n"
"v_mul_f32 v237,v237,v236\n"
"v_mul_f32 v238,v238,v236\n"
"v_accvgpr_write_b32 a32,v237\n"
"v_accvgpr_write_b32 a33,v238\n"
"v_accvgpr_read_b32 v237,a34\n"
"v_accvgpr_read_b32 v238,a35\n"
"v_mul_f32 v237,v237,v236\n"
"v_mul_f32 v238,v238,v236\n"
"v_mul_f32 v237,v237,v236\n"
"v_mul_f32 v238,v238,v236\n"
"v_accvgpr_write_b32 a34,v237\n"
"v_accvgpr_write_b32 a35,v238\n"
"v_accvgpr_read_b32 v237,a36\n"
"v_accvgpr_read_b32 v238,a37\n"
"v_mul_f32 v237,v237,v236\n"
"v_mul_f32 v238,v238,v236\n"
"v_mul_f32 v237,v237,v236\n"
"v_mul_f32 v238,v238,v236\n"
"v_accvgpr_write_b32 a36,v237\n"
"v_accvgpr_write_b32 a37,v238\n"
"v_accvgpr_read_b32 v237,a38\n"
"v_accvgpr_read_b32 v238,a39\n"
"v_mul_f32 v237,v237,v236\n"
"v_mul_f32 v238,v238,v236\n"
"v_mul_f32 v237,v237,v236\n"
"v_mul_f32 v238,v238,v236\n"
"v_accvgpr_write_b32 a38,v237\n"
"v_accvgpr_write_b32 a39,v238\n"
"v_accvgpr_read_b32 v237,a64\n"
"v_accvgpr_read_b32 v238,a65\n"
"v_mul_f32 v237,v237,v236\n"
"v_mul_f32 v238,v238,v236\n"
"v_mul_f32 v237,v237,v236\n"
"v_mul_f32 v238,v238,v236\n"
"v_accvgpr_write_b32 a64,v237\n"
"v_accvgpr_write_b32 a65,v238\n"
"v_accvgpr_read_b32 v237,a66\n"
"v_accvgpr_read_b32 v238,a67\n"
"v_mul_f32 v237,v237,v236\n"
"v_mul_f32 v238,v238,v236\n"
"v_mul_f32 v237,v237,v236\n"
"v_mul_f32 v238,v238,v236\n"
"v_accvgpr_write_b32 a66,v237\n"
"v_accvgpr_write_b32 a67,v238\n"
"v_accvgpr_read_b32 v237,a68\n"
"v_accvgpr_read_b32 v238,a69\n"
"v_mul_f32 v237,v237,v236\n"
"v_mul_f32 v238,v238,v236\n"
"v_mul_f32 v237,v237,v236\n"
"v_mul_f32 v238,v238,v236\n"
"v_accvgpr_write_b32 a68,v237\n"
"v_accvgpr_write_b32 a69,v238\n"
"v_accvgpr_read_b32 v237,a70\n"
"v_accvgpr_read_b32 v238,a71\n"
"v_mul_f32 v237,v237,v236\n"
"v_mul_f32 v238,v238,v236\n"
"v_mul_f32 v237,v237,v236\n"
"v_mul_f32 v238,v238,v236\n"
"v_accvgpr_write_b32 a70,v237\n"
"v_accvgpr_write_b32 a71,v238\n"
"v_accvgpr_read_b32 v237,a96\n"
"v_accvgpr_read_b32 v238,a97\n"
"v_mul_f32 v237,v237,v236\n"
"v_mul_f32 v238,v238,v236\n"
"v_mul_f32 v237,v237,v236\n"
"v_mul_f32 v238,v238,v236\n"
"v_accvgpr_write_b32 a96,v237\n"
"v_accvgpr_write_b32 a97,v238\n"
"v_accvgpr_read_b32 v237,a98\n"
"v_accvgpr_read_b32 v238,a99\n"
"v_mul_f32 v237,v237,v236\n"
"v_mul_f32 v238,v238,v236\n"
"v_mul_f32 v237,v237,v236\n"
"v_mul_f32 v238,v238,v236\n"
"v_accvgpr_write_b32 a98,v237\n"
"v_accvgpr_write_b32 a99,v238\n"
"v_accvgpr_read_b32 v237,a100\n"
"v_accvgpr_read_b32 v238,a101\n"
"v_mul_f32 v237,v237,v236\n"
"v_mul_f32 v238,v238,v236\n"
"v_mul_f32 v237,v237,v236\n"
"v_mul_f32 v238,v238,v236\n"
"v_accvgpr_write_b32 a100,v237\n"
"v_accvgpr_write_b32 a101,v238\n"
"v_accvgpr_read_b32 v237,a102\n"
"v_accvgpr_read_b32 v238,a103\n"
"v_mul_f32 v237,v237,v236\n"
"v_mul_f32 v238,v238,v236\n"
"v_mul_f32 v237,v237,v236\n"
"v_mul_f32 v238,v238,v236\n"
"v_accvgpr_write_b32 a102,v237\n"
"v_accvgpr_write_b32 a103,v238\n"
"s_setpc_b64 s[18:19]\n"
"L_rs1_%=:\n"
"v_sub_f32 v84,v84,v235\n"
"v_sub_f32 v85,v85,v235\n"
"v_sub_f32 v86,v86,v235\n"
"v_sub_f32 v87,v87,v235\n"
"v_accvgpr_read_b32 v237,a8\n"
"v_accvgpr_read_b32 v238,a9\n"
"v_mul_f32 v237,v237,v236\n"
"v_mul_f32 v238,v238,v236\n"
"v_mul_f32 v237,v237,v236\n"
"v_mul_f32 v238,v238,v236\n"
"v_accvgpr_write_b32 a8,v237\n"
"v_accvgpr_write_b32 a9,v238\n"
"v_accvgpr_read_b32 v237,a10\n"
"v_accvgpr_read_b32 v238,a11\n"
"v_mul_f32 v237,v237,v236\n"
"v_mul_f32 v238,v238,v236\n"
"v_mul_f32 v237,v237,v236\n"
"v_mul_f32 v238,v238,v236\n"
"v_accvgpr_write_b32 a10,v237\n"
"v_accvgpr_write_b32 a11,v238\n"
"v_accvgpr_read_b32 v237,a12\n"
"v_accvgpr_read_b32 v238,a13\n"
"v_mul_f32 v237,v237,v236\n"
"v_mul_f32 v238,v238,v236\n"
"v_mul_f32 v237,v237,v236\n"
"v_mul_f32 v238,v238,v236\n"
"v_accvgpr_write_b32 a12,v237\n"
"v_accvgpr_write_b32 a13,v238\n"
"v_accvgpr_read_b32 v237,a14\n"
"v_accvgpr_read_b32 v238,a15\n"
"v_mul_f32 v237,v237,v236\n"
"v_mul_f32 v238,v238,v236\n"
"v_mul_f32 v237,v237,v236\n"
"v_mul_f32 v238,v238,v236\n"
"v_accvgpr_write_b32 a14,v237\n"
"v_accvgpr_write_b32 a15,v238\n"
"v_accvgpr_read_b32 v237,a40\n"
"v_accvgpr_read_b32 v238,a41\n"
"v_mul_f32 v237,v237,v236\n"
"v_mul_f32 v238,v238,v236\n"
"v_mul_f32 v237,v237,v236\n"
"v_mul_f32 v238,v238,v236\n"
"v_accvgpr_write_b32 a40,v237\n"
"v_accvgpr_write_b32 a41,v238\n"
"v_accvgpr_read_b32 v237,a42\n"
"v_accvgpr_read_b32 v238,a43\n"
"v_mul_f32 v237,v237,v236\n"
"v_mul_f32 v238,v238,v236\n"
"v_mul_f32 v237,v237,v236\n"
"v_mul_f32 v238,v238,v236\n"
"v_accvgpr_write_b32 a42,v237\n"
"v_accvgpr_write_b32 a43,v238\n"
"v_accvgpr_read_b32 v237,a44\n"
"v_accvgpr_read_b32 v238,a45\n"
"v_mul_f32 v237,v237,v236\n"
"v_mul_f32 v238,v238,v236\n"
"v_mul_f32 v237,v237,v236\n"
"v_mul_f32 v238,v238,v236\n"
"v_accvgpr_write_b32 a44,v237\n"
"v_accvgpr_write_b32 a45,v238\n"
"v_accvgpr_read_b32 v237,a46\n"
"v_accvgpr_read_b32 v238,a47\n"
"v_mul_f32 v237,v237,v236\n"
"v_mul_f32 v238,v238,v236\n"
"v_mul_f32 v237,v237,v236\n"
"v_mul_f32 v238,v238,v236\n"
"v_accvgpr_write_b32 a46,v237\n"
"v_accvgpr_write_b32 a47,v238\n"
"v_accvgpr_read_b32 v237,a72\n"
"v_accvgpr_read_b32 v238,a73\n"
"v_mul_f32 v237,v237,v236\n"
"v_mul_f32 v238,v238,v236\n"
"v_mul_f32 v237,v237,v236\n"
"v_mul_f32 v238,v238,v236\n"
"v_accvgpr_write_b32 a72,v237\n"
"v_accvgpr_write_b32 a73,v238\n"
"v_accvgpr_read_b32 v237,a74\n"
"v_accvgpr_read_b32 v238,a75\n"
"v_mul_f32 v237,v237,v236\n"
"v_mul_f32 v238,v238,v236\n"
"v_mul_f32 v237,v237,v236\n"
"v_mul_f32 v238,v238,v236\n"
"v_accvgpr_write_b32 a74,v237\n"
"v_accvgpr_write_b32 a75,v238\n"
"v_accvgpr_read_b32 v237,a76\n"
"v_accvgpr_read_b32 v238,a77\n"
"v_mul_f32 v237,v237,v236\n"
"v_mul_f32 v238,v238,v236\n"
"v_mul_f32 v237,v237,v236\n"
"v_mul_f32 v238,v238,v236\n"
"v_accvgpr_write_b32 a76,v237\n"
"v_accvgpr_write_b32 a77,v238\n"
"v_accvgpr_read_b32 v237,a78\n"
"v_accvgpr_read_b32 v238,a79\n"
"v_mul_f32 v237,v237,v236\n"
"v_mul_f32 v238,v238,v236\n"
"v_mul_f32 v237,v237,v236\n"
"v_mul_f32 v238,v238,v236\n"
"v_accvgpr_write_b32 a78,v237\n"
"v_accvgpr_write_b32 a79,v238\n"
"v_accvgpr_read_b32 v237,a104\n"
"v_accvgpr_read_b32 v238,a105\n"
"v_mul_f32 v237,v237,v236\n"
"v_mul_f32 v238,v238,v236\n"
"v_mul_f32 v237,v237,v236\n"
"v_mul_f32 v238,v238,v236\n"
"v_accvgpr_write_b32 a104,v237\n"
"v_accvgpr_write_b32 a105,v238\n"
"v_accvgpr_read_b32 v237,a106\n"
"v_accvgpr_read_b32 v238,a107\n"
"v_mul_f32 v237,v237,v236\n"
"v_mul_f32 v238,v238,v236\n"
"v_mul_f32 v237,v237,v236\n"
"v_mul_f32 v238,v238,v236\n"
"v_accvgpr_write_b32 a106,v237\n"
"v_accvgpr_write_b32 a107,v238\n"
"v_accvgpr_read_b32 v237,a108\n"
"v_accvgpr_read_b32 v238,a109\n"
"v_mul_f32 v237,v237,v236\n"
"v_mul_f32 v238,v238,v236\n"
"v_mul_f32 v237,v237,v236\n"
"v_mul_f32 v238,v238,v236\n"
"v_accvgpr_write_b32 a108,v237\n"
"v_accvgpr_write_b32 a109,v238\n"
"v_accvgpr_read_b32 v237,a110\n"
"v_accvgpr_read_b32 v238,a111\n"
"v_mul_f32 v237,v237,v236\n"
"v_mul_f32 v238,v238,v236\n"
"v_mul_f32 v237,v237,v236\n"
"v_mul_f32 v238,v238,v236\n"
"v_accvgpr_write_b32 a110,v237\n"
"v_accvgpr_write_b32 a111,v238\n"
"s_setpc_b64 s[18:19]\n"
"L_rs2_%=:\n"
"v_sub_f32 v88,v88,v235\n"
"v_sub_f32 v89,v89,v235\n"
"v_sub_f32 v90,v90,v235\n"
"v_sub_f32 v91,v91,v235\n"
"v_accvgpr_read_b32 v237,a16\n"
"v_accvgpr_read_b32 v238,a17\n"
"v_mul_f32 v237,v237,v236\n"
"v_mul_f32 v238,v238,v236\n"
"v_mul_f32 v237,v237,v236\n"
"v_mul_f32 v238,v238,v236\n"
"v_accvgpr_write_b32 a16,v237\n"
"v_accvgpr_write_b32 a17,v238\n"
"v_accvgpr_read_b32 v237,a18\n"
"v_accvgpr_read_b32 v238,a19\n"
"v_mul_f32 v237,v237,v236\n"
"v_mul_f32 v238,v238,v236\n"
"v_mul_f32 v237,v237,v236\n"
"v_mul_f32 v238,v238,v236\n"
"v_accvgpr_write_b32 a18,v237\n"
"v_accvgpr_write_b32 a19,v238\n"
"v_accvgpr_read_b32 v237,a20\n"
"v_accvgpr_read_b32 v238,a21\n"
"v_mul_f32 v237,v237,v236\n"
"v_mul_f32 v238,v238,v236\n"
"v_mul_f32 v237,v237,v236\n"
"v_mul_f32 v238,v238,v236\n"
"v_accvgpr_write_b32 a20,v237\n"
"v_accvgpr_write_b32 a21,v238\n"
"v_accvgpr_read_b32 v237,a22\n"
"v_accvgpr_read_b32 v238,a23\n"
"v_mul_f32 v237,v237,v236\n"
"v_mul_f32 v238,v238,v236\n"
"v_mul_f32 v237,v237,v236\n"
"v_mul_f32 v238,v238,v236\n"
"v_accvgpr_write_b32 a22,v237\n"
"v_accvgpr_write_b32 a23,v238\n"
"v_accvgpr_read_b32 v237,a48\n"
"v_accvgpr_read_b32 v238,a49\n"
"v_mul_f32 v237,v237,v236\n"
"v_mul_f32 v238,v238,v236\n"
"v_mul_f32 v237,v237,v236\n"
"v_mul_f32 v238,v238,v236\n"
"v_accvgpr_write_b32 a48,v237\n"
"v_accvgpr_write_b32 a49,v238\n"
"v_accvgpr_read_b32 v237,a50\n"
"v_accvgpr_read_b32 v238,a51\n"
"v_mul_f32 v237,v237,v236\n"
"v_mul_f32 v238,v238,v236\n"
"v_mul_f32 v237,v237,v236\n"
"v_mul_f32 v238,v238,v236\n"
"v_accvgpr_write_b32 a50,v237\n"
"v_accvgpr_write_b32 a51,v238\n"
"v_accvgpr_read_b32 v237,a52\n"
"v_accvgpr_read_b32 v238,a53\n"
"v_mul_f32 v237,v237,v236\n"
"v_mul_f32 v238,v238,v236\n"
"v_mul_f32 v237,v237,v236\n"
"v_mul_f32 v238,v238,v236\n"
"v_accvgpr_write_b32 a52,v237\n"
"v_accvgpr_write_b32 a53,v238\n"
"v_accvgpr_read_b32 v237,a54\n"
"v_accvgpr_read_b32 v238,a55\n"
"v_mul_f32 v237,v237,v236\n"
"v_mul_f32 v238,v238,v236\n"
"v_mul_f32 v237,v237,v236\n"
"v_mul_f32 v238,v238,v236\n"
"v_accvgpr_write_b32 a54,v237\n"
"v_accvgpr_write_b32 a55,v238\n"
"v_accvgpr_read_b32 v237,a80\n"
"v_accvgpr_read_b32 v238,a81\n"
"v_mul_f32 v237,v237,v236\n"
"v_mul_f32 v238,v238,v236\n"
"v_mul_f32 v237,v237,v236\n"
"v_mul_f32 v238,v238,v236\n"
"v_accvgpr_write_b32 a80,v237\n"
"v_accvgpr_write_b32 a81,v238\n"
"v_accvgpr_read_b32 v237,a82\n"
"v_accvgpr_read_b32 v238,a83\n"
"v_mul_f32 v237,v237,v236\n"
"v_mul_f32 v238,v238,v236\n"
"v_mul_f32 v237,v237,v236\n"
"v_mul_f32 v238,v238,v236\n"
"v_accvgpr_write_b32 a82,v237\n"
"v_accvgpr_write_b32 a83,v238\n"
"v_accvgpr_read_b32 v237,a84\n"
"v_accvgpr_read_b32 v238,a85\n"
"v_mul_f32 v237,v237,v236\n"
"v_mul_f32 v238,v238,v236\n"
"v_mul_f32 v237,v237,v236\n"
"v_mul_f32 v238,v238,v236\n"
"v_accvgpr_write_b32 a84,v237\n"
"v_accvgpr_write_b32 a85,v238\n"
"v_accvgpr_read_b32 v237,a86\n"
"v_accvgpr_read_b32 v238,a87\n"
"v_mul_f32 v237,v237,v236\n"
"v_mul_f32 v238,v238,v236\n"
"v_mul_f32 v237,v237,v236\n"
"v_mul_f32 v238,v238,v236\n"
"v_accvgpr_write_b32 a86,v237\n"
"v_accvgpr_write_b32 a87,v238\n"
"v_accvgpr_read_b32 v237,a112\n"
"v_accvgpr_read_b32 v238,a113\n"
"v_mul_f32 v237,v237,v236\n"
"v_mul_f32 v238,v238,v236\n"
"v_mul_f32 v237,v237,v236\n"
"v_mul_f32 v238,v238,v236\n"
"v_accvgpr_write_b32 a112,v237\n"
"v_accvgpr_write_b32 a113,v238\n"
"v_accvgpr_read_b32 v237,a114\n"
"v_accvgpr_read_b32 v238,a115\n"
"v_mul_f32 v237,v237,v236\n"
"v_mul_f32 v238,v238,v236\n"
"v_mul_f32 v237,v237,v236\n"
"v_mul_f32 v238,v238,v236\n"
"v_accvgpr_write_b32 a114,v237\n"
"v_accvgpr_write_b32 a115,v238\n"
"v_accvgpr_read_b32 v237,a116\n"
"v_accvgpr_read_b32 v238,a117\n"
"v_mul_f32 v237,v237,v236\n"
"v_mul_f32 v238,v238,v236\n"
"v_mul_f32 v237,v237,v236\n"
"v_mul_f32 v238,v238,v236\n"
"v_accvgpr_write_b32 a116,v237\n"
"v_accvgpr_write_b32 a117,v238\n"
"v_accvgpr_read_b32 v237,a118\n"
"v_accvgpr_read_b32 v238,a119\n"
"v_mul_f32 v237,v237,v236\n"
"v_mul_f32 v238,v238,v236\n"
"v_mul_f32 v237,v237,v236\n"
"v_mul_f32 v238,v238,v236\n"
"v_accvgpr_write_b32 a118,v237\n"
"v_accvgpr_write_b32 a119,v238\n"
"s_setpc_b64 s[18:19]\n"
"L_rs3_%=:\n"
"v_sub_f32 v92,v92,v235\n"
"v_sub_f32 v93,v93,v235\n"
"v_sub_f32 v94,v94,v235\n"
"v_sub_f32 v95,v95,v235\n"
"v_accvgpr_read_b32 v237,a24\n"
"v_accvgpr_read_b32 v238,a25\n"
"v_mul_f32 v237,v237,v236\n"
"v_mul_f32 v238,v238,v236\n"
"v_mul_f32 v237,v237,v236\n"
"v_mul_f32 v238,v238,v236\n"
"v_accvgpr_write_b32 a24,v237\n"
"v_accvgpr_write_b32 a25,v238\n"
"v_accvgpr_read_b32 v237,a26\n"
"v_accvgpr_read_b32 v238,a27\n"
"v_mul_f32 v237,v237,v236\n"
"v_mul_f32 v238,v238,v236\n"
"v_mul_f32 v237,v237,v236\n"
"v_mul_f32 v238,v238,v236\n"
"v_accvgpr_write_b32 a26,v237\n"
"v_accvgpr_write_b32 a27,v238\n"
"v_accvgpr_read_b32 v237,a28\n"
"v_accvgpr_read_b32 v238,a29\n"
"v_mul_f32 v237,v237,v236\n"
"v_mul_f32 v238,v238,v236\n"
"v_mul_f32 v237,v237,v236\n"
"v_mul_f32 v238,v238,v236\n"
"v_accvgpr_write_b32 a28,v237\n"
"v_accvgpr_write_b32 a29,v238\n"
"v_accvgpr_read_b32 v237,a30\n"
"v_accvgpr_read_b32 v238,a31\n"
"v_mul_f32 v237,v237,v236\n"
"v_mul_f32 v238,v238,v236\n"
"v_mul_f32 v237,v237,v236\n"
"v_mul_f32 v238,v238,v236\n"
"v_accvgpr_write_b32 a30,v237\n"
"v_accvgpr_write_b32 a31,v238\n"
"v_accvgpr_read_b32 v237,a56\n"
"v_accvgpr_read_b32 v238,a57\n"
"v_mul_f32 v237,v237,v236\n"
"v_mul_f32 v238,v238,v236\n"
"v_mul_f32 v237,v237,v236\n"
"v_mul_f32 v238,v238,v236\n"
"v_accvgpr_write_b32 a56,v237\n"
"v_accvgpr_write_b32 a57,v238\n"
"v_accvgpr_read_b32 v237,a58\n"
"v_accvgpr_read_b32 v238,a59\n"
"v_mul_f32 v237,v237,v236\n"
"v_mul_f32 v238,v238,v236\n"
"v_mul_f32 v237,v237,v236\n"
"v_mul_f32 v238,v238,v236\n"
"v_accvgpr_write_b32 a58,v237\n"
"v_accvgpr_write_b32 a59,v238\n"
"v_accvgpr_read_b32 v237,a60\n"
"v_accvgpr_read_b32 v238,a61\n"
"v_mul_f32 v237,v237,v236\n"
"v_mul_f32 v238,v238,v236\n"
"v_mul_f32 v237,v237,v236\n"
"v_mul_f32 v238,v238,v236\n"
"v_accvgpr_write_b32 a60,v237\n"
"v_accvgpr_write_b32 a61,v238\n"
"v_accvgpr_read_b32 v237,a62\n"
"v_accvgpr_read_b32 v238,a63\n"
"v_mul_f32 v237,v237,v236\n"
"v_mul_f32 v238,v238,v236\n"
"v_mul_f32 v237,v237,v236\n"
"v_mul_f32 v238,v238,v236\n"
"v_accvgpr_write_b32 a62,v237\n"
"v_accvgpr_write_b32 a63,v238\n"
"v_accvgpr_read_b32 v237,a88\n"
"v_accvgpr_read_b32 v238,a89\n"
"v_mul_f32 v237,v237,v236\n"
"v_mul_f32 v238,v238,v236\n"
"v_mul_f32 v237,v237,v236\n"
"v_mul_f32 v238,v238,v236\n"
"v_accvgpr_write_b32 a88,v237\n"
"v_accvgpr_write_b32 a89,v238\n"
"v_accvgpr_read_b32 v237,a90\n"
"v_accvgpr_read_b32 v238,a91\n"
"v_mul_f32 v237,v237,v236\n"
"v_mul_f32 v238,v238,v236\n"
"v_mul_f32 v237,v237,v236\n"
"v_mul_f32 v238,v238,v236\n"
"v_accvgpr_write_b32 a90,v237\n"
"v_accvgpr_write_b32 a91,v238\n"
"v_accvgpr_read_b32 v237,a92\n"
"v_accvgpr_read_b32 v238,a93\n"
"v_mul_f32 v237,v237,v236\n"
"v_mul_f32 v238,v238,v236\n"
"v_mul_f32 v237,v237,v236\n"
"v_mul_f32 v238,v238,v236\n"
"v_accvgpr_write_b32 a92,v237\n"
"v_accvgpr_write_b32 a93,v238\n"
"v_accvgpr_read_b32 v237,a94\n"
"v_accvgpr_read_b32 v238,a95\n"
"v_mul_f32 v237,v237,v236\n"
"v_mul_f32 v238,v238,v236\n"
"v_mul_f32 v237,v237,v236\n"
"v_mul_f32 v238,v238,v236\n"
"v_accvgpr_write_b32 a94,v237\n"
"v_accvgpr_write_b32 a95,v238\n"
"v_accvgpr_read_b32 v237,a120\n"
"v_accvgpr_read_b32 v238,a121\n"
"v_mul_f32 v237,v237,v236\n"
"v_mul_f32 v238,v238,v236\n"
"v_mul_f32 v237,v237,v236\n"
"v_mul_f32 v238,v238,v236\n"
"v_accvgpr_write_b32 a120,v237\n"
"v_accvgpr_write_b32 a121,v238\n"
"v_accvgpr_read_b32 v237,a122\n"
"v_accvgpr_read_b32 v238,a123\n"
"v_mul_f32 v237,v237,v236\n"
"v_mul_f32 v238,v238,v236\n"
"v_mul_f32 v237,v237,v236\n"
"v_mul_f32 v238,v238,v236\n"
"v_accvgpr_write_b32 a122,v237\n"
"v_accvgpr_write_b32 a123,v238\n"
"v_accvgpr_read_b32 v237,a124\n"
"v_accvgpr_read_b32 v238,a125\n"
"v_mul_f32 v237,v237,v236\n"
"v_mul_f32 v238,v238,v236\n"
"v_mul_f32 v237,v237,v236\n"
"v_mul_f32 v238,v238,v236\n"
"v_accvgpr_write_b32 a124,v237\n"
"v_accvgpr_write_b32 a125,v238\n"
"v_accvgpr_read_b32 v237,a126\n"
"v_accvgpr_read_b32 v238,a127\n"
"v_mul_f32 v237,v237,v236\n"
"v_mul_f32 v238,v238,v236\n"
"v_mul_f32 v237,v237,v236\n"
"v_mul_f32 v238,v238,v236\n"
"v_accvgpr_write_b32 a126,v237\n"
"v_accvgpr_write_b32 a127,v238\n"
"s_setpc_b64 s[18:19]\n"
"L_mg_%=:\n"
"s_add_u32 s69,s62,0\n"
"buffer_load_dwordx4 v[160:163],v9,s[88:91],s69 offen sc1\n"
"s_add_u32 s69,s62,4096\n"
"buffer_load_dwordx4 v[164:167],v9,s[88:91],s69 offen sc1\n"
"s_mov_b32 s69,73728\n"
"buffer_load_dwordx4 v[168:171],v142,s[88:91],s69 offen sc1\n"
"s_add_u32 s69,s62,9216\n"
"buffer_load_dwordx4 v[172:175],v9,s[88:91],s69 offen sc1\n"
"s_add_u32 s69,s62,13312\n"
"buffer_load_dwordx4 v[176:179],v9,s[88:91],s69 offen sc1\n"
"s_mov_b32 s69,82944\n"
"buffer_load_dwordx4 v[180:183],v142,s[88:91],s69 offen sc1\n"
"s_add_u32 s69,s62,18432\n"
"buffer_load_dwordx4 v[184:187],v9,s[88:91],s69 offen sc1\n"
"s_add_u32 s69,s62,22528\n"
"buffer_load_dwordx4 v[188:191],v9,s[88:91],s69 offen sc1\n"
"s_mov_b32 s69,92160\n"
"buffer_load_dwordx4 v[192:195],v142,s[88:91],s69 offen sc1\n"
"buffer_load_dwordx4 v[196:199],v140,s[88:91],s76 offen sc1\n"
"s_waitcnt vmcnt(0)\n"
"s_branch L_mc_%=\n"
"L_cl_%=:\n"
"s_mov_b32 s79,0x40000\n"
"L_cm_%=:\n"
"buffer_load_dwordx4 v[218:221],v9,s[88:91],s63 offen sc1\n"
"s_sub_u32 s79,s79,1\n"
"s_waitcnt vmcnt(0)\n"
"v_min3_u32 v216,v218,v219,s38\n"
"v_max3_u32 v217,v218,v219,s38\n"
"v_min3_u32 v216,v220,v221,v216\n"
"v_max3_u32 v217,v220,v221,v217\n"
"v_cmp_ne_u32 vcc,v216,v217\n"
"s_cbranch_scc1 L_co_%=\n"
"s_cbranch_vccnz L_cm_%=\n"
"s_branch L_co_%=\n"
"L_ap1_%=:\n"
"s_mov_b32 s79,0x40000\n"
"L_pl1_%=:\n"
"buffer_load_dword v222,v223,s[88:91],s15 offen sc1\n"
"s_sub_u32 s79,s79,1\n"
"s_waitcnt vmcnt(0)\n"
"v_cmp_ne_u32 vcc,s38,v222\n"
"s_cbranch_scc1 L_pd1_%=\n"
"s_cbranch_vccnz L_pl1_%=\n"
"L_pd1_%=:\n"
"s_branch L_aq1_%=\n"
"L_ap2_%=:\n"
"s_mov_b32 s79,0x40000\n"
"L_pl2_%=:\n"
"buffer_load_dword v222,v223,s[88:91],s15 offen sc1\n"
"s_sub_u32 s79,s79,1\n"
"s_waitcnt vmcnt(0)\n"
"v_cmp_ne_u32 vcc,s38,v222\n"
"s_cbranch_scc1 L_pd2_%=\n"
"s_cbranch_vccnz L_pl2_%=\n"
"L_pd2_%=:\n"
"s_branch L_aq2_%=\n"
"L_pbr_%=:\n"
"buffer_load_dwordx4 v[48:51],v9,s[88:91],s75 offen sc1\n"
"buffer_load_dwordx4 v[16:19],v9,s[88:91],s77 offen offset:0 sc1\n"
"buffer_load_dwordx4 v[20:23],v9,s[88:91],s77 offen offset:1024 sc1\n"
"buffer_load_dwordx4 v[24:27],v9,s[88:91],s77 offen offset:2048 sc1\n"
"buffer_load_dwordx4 v[28:31],v9,s[88:91],s77 offen offset:3072 sc1\n"
"buffer_load_dwordx4 v[32:35],v9,s[88:91],s76 offen offset:0 sc1\n"
"buffer_load_dwordx4 v[36:39],v9,s[88:91],s76 offen offset:1024 sc1\n"
"buffer_load_dwordx4 v[40:43],v9,s[88:91],s76 offen offset:2048 sc1\n"
"buffer_load_dwordx4 v[44:47],v9,s[88:91],s76 offen offset:3072 sc1\n"
"s_waitcnt vmcnt(0)\n"
"s_branch L_pb_%=\n"
"L_rt3_%=:\n"
"s_cmp_eq_u32 s63,0\n"
"s_cbranch_scc1 L_rb3_%=\n"
"s_sub_u32 s63,s63,1\n"
"s_sleep 0\n"
"s_branch L_pbr_%=\n"
"L_rt4_%=:\n"
"s_cmp_eq_u32 s63,0\n"
"s_cbranch_scc1 L_rb4_%=\n"
"s_sub_u32 s63,s63,1\n"
"s_sleep 0\n"
"s_branch L_pbr_%=\n"
"L_fxc0_%=:\n"
"s_cmp_eq_u32 s63,0\n"
"s_cbranch_scc1 L_fyc0_%=\n"
"s_sub_u32 s63,s63,1\n"
"s_mov_b32 s17,0\n"
"buffer_load_dwordx4 a[0:3],v9,s[52:55],s17 offen offset:0 sc1\n"
"s_mov_b32 s17,16384\n"
"buffer_load_dwordx4 a[4:7],v9,s[52:55],s17 offen offset:2048 sc1\n"
"s_mov_b32 s17,0\n"
"buffer_load_dwordx4 a[8:11],v9,s[52:55],s17 offen offset:1024 sc1\n"
"s_mov_b32 s17,16384\n"
"buffer_load_dwordx4 a[12:15],v9,s[52:55],s17 offen offset:3072 sc1\n"
"s_mov_b32 s17,0\n"
"buffer_load_dwordx4 a[16:19],v9,s[52:55],s17 offen offset:2048 sc1\n"
"s_mov_b32 s17,20480\n"
"buffer_load_dwordx4 a[20:23],v9,s[52:55],s17 offen offset:0 sc1\n"
"s_mov_b32 s17,0\n"
"buffer_load_dwordx4 a[24:27],v9,s[52:55],s17 offen offset:3072 sc1\n"
"s_mov_b32 s17,20480\n"
"buffer_load_dwordx4 a[28:31],v9,s[52:55],s17 offen offset:1024 sc1\n"
"s_waitcnt vmcnt(0)\n"
"v_mfma_f32_16x16x32_bf16 v[64:67],a[144:147],a[0:3],0\n"
"v_mfma_f32_16x16x32_bf16 v[68:71],a[144:147],a[4:7],0\n"
"s_nop 5\n"
"v_mfma_f32_16x16x32_bf16 v[24:27],a[148:151],a[8:11],v[64:67]\n"
"v_mfma_f32_16x16x32_bf16 v[28:31],a[148:151],a[12:15],v[68:71]\n"
"v_mfma_f32_16x16x32_bf16 v[24:27],a[152:155],a[16:19],v[24:27]\n"
"v_mfma_f32_16x16x32_bf16 v[28:31],a[152:155],a[20:23],v[28:31]\n"
"v_mfma_f32_16x16x32_bf16 v[24:27],a[156:159],a[24:27],v[24:27]\n"
"v_mfma_f32_16x16x32_bf16 v[28:31],a[156:159],a[28:31],v[28:31]\n"
"s_nop 7\n"
"v_cmp_u_f32 vcc,v24,v28\n"
"s_cbranch_vccnz L_fxc0_%=\n"
"L_fyc0_%=:\n"
"s_setpc_b64 s[18:19]\n"
"L_fzc0_0_%=:\n"
"s_call_b64 s[18:19],L_fxc0_%=\n"
"s_branch L_frc0_0_%=\n"
"L_fzc0_1_%=:\n"
"s_call_b64 s[18:19],L_fxc0_%=\n"
"s_branch L_frc0_1_%=\n"
"L_fxc4_%=:\n"
"s_cmp_eq_u32 s63,0\n"
"s_cbranch_scc1 L_fyc4_%=\n"
"s_sub_u32 s63,s63,1\n"
"s_mov_b32 s17,4096\n"
"buffer_load_dwordx4 a[32:35],v9,s[52:55],s17 offen offset:0 sc1\n"
"s_mov_b32 s17,20480\n"
"buffer_load_dwordx4 a[36:39],v9,s[52:55],s17 offen offset:2048 sc1\n"
"s_mov_b32 s17,4096\n"
"buffer_load_dwordx4 a[40:43],v9,s[52:55],s17 offen offset:1024 sc1\n"
"s_mov_b32 s17,20480\n"
"buffer_load_dwordx4 a[44:47],v9,s[52:55],s17 offen offset:3072 sc1\n"
"s_mov_b32 s17,4096\n"
"buffer_load_dwordx4 a[48:51],v9,s[52:55],s17 offen offset:2048 sc1\n"
"s_mov_b32 s17,24576\n"
"buffer_load_dwordx4 a[52:55],v9,s[52:55],s17 offen offset:0 sc1\n"
"s_mov_b32 s17,4096\n"
"buffer_load_dwordx4 a[56:59],v9,s[52:55],s17 offen offset:3072 sc1\n"
"s_mov_b32 s17,24576\n"
"buffer_load_dwordx4 a[60:63],v9,s[52:55],s17 offen offset:1024 sc1\n"
"s_waitcnt vmcnt(0)\n"
"v_mfma_f32_16x16x32_bf16 v[16:19],a[144:147],a[32:35],v[24:27]\n"
"v_mfma_f32_16x16x32_bf16 v[20:23],a[144:147],a[36:39],v[28:31]\n"
"s_nop 5\n"
"v_mfma_f32_16x16x32_bf16 v[64:67],a[148:151],a[40:43],v[16:19]\n"
"v_mfma_f32_16x16x32_bf16 v[68:71],a[148:151],a[44:47],v[20:23]\n"
"v_mfma_f32_16x16x32_bf16 v[64:67],a[152:155],a[48:51],v[64:67]\n"
"v_mfma_f32_16x16x32_bf16 v[68:71],a[152:155],a[52:55],v[68:71]\n"
"v_mfma_f32_16x16x32_bf16 v[64:67],a[156:159],a[56:59],v[64:67]\n"
"v_mfma_f32_16x16x32_bf16 v[68:71],a[156:159],a[60:63],v[68:71]\n"
"s_nop 7\n"
"v_cmp_u_f32 vcc,v64,v68\n"
"s_cbranch_vccnz L_fxc4_%=\n"
"L_fyc4_%=:\n"
"s_setpc_b64 s[18:19]\n"
"L_fzc4_0_%=:\n"
"s_call_b64 s[18:19],L_fxc4_%=\n"
"s_branch L_frc4_0_%=\n"
"L_fzc4_1_%=:\n"
"s_call_b64 s[18:19],L_fxc4_%=\n"
"s_branch L_frc4_1_%=\n"
"L_fxc8_%=:\n"
"s_cmp_eq_u32 s63,0\n"
"s_cbranch_scc1 L_fyc8_%=\n"
"s_sub_u32 s63,s63,1\n"
"s_mov_b32 s17,8192\n"
"buffer_load_dwordx4 a[64:67],v9,s[52:55],s17 offen offset:0 sc1\n"
"s_mov_b32 s17,24576\n"
"buffer_load_dwordx4 a[68:71],v9,s[52:55],s17 offen offset:2048 sc1\n"
"s_mov_b32 s17,8192\n"
"buffer_load_dwordx4 a[72:75],v9,s[52:55],s17 offen offset:1024 sc1\n"
"s_mov_b32 s17,24576\n"
"buffer_load_dwordx4 a[76:79],v9,s[52:55],s17 offen offset:3072 sc1\n"
"s_mov_b32 s17,8192\n"
"buffer_load_dwordx4 a[80:83],v9,s[52:55],s17 offen offset:2048 sc1\n"
"s_mov_b32 s17,28672\n"
"buffer_load_dwordx4 a[84:87],v9,s[52:55],s17 offen offset:0 sc1\n"
"s_mov_b32 s17,8192\n"
"buffer_load_dwordx4 a[88:91],v9,s[52:55],s17 offen offset:3072 sc1\n"
"s_mov_b32 s17,28672\n"
"buffer_load_dwordx4 a[92:95],v9,s[52:55],s17 offen offset:1024 sc1\n"
"s_waitcnt vmcnt(0)\n"
"v_mfma_f32_16x16x32_bf16 v[24:27],a[144:147],a[64:67],v[64:67]\n"
"v_mfma_f32_16x16x32_bf16 v[28:31],a[144:147],a[68:71],v[68:71]\n"
"s_nop 5\n"
"v_mfma_f32_16x16x32_bf16 v[16:19],a[148:151],a[72:75],v[24:27]\n"
"v_mfma_f32_16x16x32_bf16 v[20:23],a[148:151],a[76:79],v[28:31]\n"
"v_mfma_f32_16x16x32_bf16 v[16:19],a[152:155],a[80:83],v[16:19]\n"
"v_mfma_f32_16x16x32_bf16 v[20:23],a[152:155],a[84:87],v[20:23]\n"
"v_mfma_f32_16x16x32_bf16 v[16:19],a[156:159],a[88:91],v[16:19]\n"
"v_mfma_f32_16x16x32_bf16 v[20:23],a[156:159],a[92:95],v[20:23]\n"
"s_nop 7\n"
"v_cmp_u_f32 vcc,v16,v20\n"
"s_cbranch_vccnz L_fxc8_%=\n"
"L_fyc8_%=:\n"
"s_setpc_b64 s[18:19]\n"
"L_fzc8_0_%=:\n"
"s_call_b64 s[18:19],L_fxc8_%=\n"
"s_branch L_frc8_0_%=\n"
"L_fzc8_1_%=:\n"
"s_call_b64 s[18:19],L_fxc8_%=\n"
"s_branch L_frc8_1_%=\n"
"L_fxc12_%=:\n"
"s_cmp_eq_u32 s63,0\n"
"s_cbranch_scc1 L_fyc12_%=\n"
"s_sub_u32 s63,s63,1\n"
"s_mov_b32 s17,12288\n"
"buffer_load_dwordx4 a[96:99],v9,s[52:55],s17 offen offset:0 sc1\n"
"s_mov_b32 s17,28672\n"
"buffer_load_dwordx4 a[100:103],v9,s[52:55],s17 offen offset:2048 sc1\n"
"s_mov_b32 s17,12288\n"
"buffer_load_dwordx4 a[104:107],v9,s[52:55],s17 offen offset:1024 sc1\n"
"s_mov_b32 s17,28672\n"
"buffer_load_dwordx4 a[108:111],v9,s[52:55],s17 offen offset:3072 sc1\n"
"s_mov_b32 s17,12288\n"
"buffer_load_dwordx4 a[112:115],v9,s[52:55],s17 offen offset:2048 sc1\n"
"s_mov_b32 s17,32768\n"
"buffer_load_dwordx4 a[116:119],v9,s[52:55],s17 offen offset:0 sc1\n"
"s_mov_b32 s17,12288\n"
"buffer_load_dwordx4 a[120:123],v9,s[52:55],s17 offen offset:3072 sc1\n"
"s_mov_b32 s17,32768\n"
"buffer_load_dwordx4 a[124:127],v9,s[52:55],s17 offen offset:1024 sc1\n"
"s_waitcnt vmcnt(0)\n"
"v_mfma_f32_16x16x32_bf16 v[64:67],a[144:147],a[96:99],v[16:19]\n"
"v_mfma_f32_16x16x32_bf16 v[68:71],a[144:147],a[100:103],v[20:23]\n"
"s_nop 5\n"
"v_mfma_f32_16x16x32_bf16 v[24:27],a[148:151],a[104:107],v[64:67]\n"
"v_mfma_f32_16x16x32_bf16 v[28:31],a[148:151],a[108:111],v[68:71]\n"
"v_mfma_f32_16x16x32_bf16 v[24:27],a[152:155],a[112:115],v[24:27]\n"
"v_mfma_f32_16x16x32_bf16 v[28:31],a[152:155],a[116:119],v[28:31]\n"
"v_mfma_f32_16x16x32_bf16 v[24:27],a[156:159],a[120:123],v[24:27]\n"
"v_mfma_f32_16x16x32_bf16 v[28:31],a[156:159],a[124:127],v[28:31]\n"
"s_nop 7\n"
"v_cmp_u_f32 vcc,v24,v28\n"
"s_cbranch_vccnz L_fxc12_%=\n"
"L_fyc12_%=:\n"
"s_setpc_b64 s[18:19]\n"
"L_fzc12_0_%=:\n"
"s_call_b64 s[18:19],L_fxc12_%=\n"
"s_branch L_frc12_0_%=\n"
"L_fzc12_1_%=:\n"
"s_call_b64 s[18:19],L_fxc12_%=\n"
"s_branch L_frc12_1_%=\n"
"L_fxc16_%=:\n"
"s_cmp_eq_u32 s63,0\n"
"s_cbranch_scc1 L_fyc16_%=\n"
"s_sub_u32 s63,s63,1\n"
"s_mov_b32 s17,16384\n"
"buffer_load_dwordx4 a[128:131],v9,s[52:55],s17 offen offset:0 sc1\n"
"s_mov_b32 s17,32768\n"
"buffer_load_dwordx4 a[132:135],v9,s[52:55],s17 offen offset:2048 sc1\n"
"s_mov_b32 s17,16384\n"
"buffer_load_dwordx4 a[136:139],v9,s[52:55],s17 offen offset:1024 sc1\n"
"s_mov_b32 s17,32768\n"
"buffer_load_dwordx4 a[140:143],v9,s[52:55],s17 offen offset:3072 sc1\n"
"s_waitcnt vmcnt(0)\n"
"v_mfma_f32_16x16x32_bf16 v[16:19],a[144:147],a[128:131],v[24:27]\n"
"v_mfma_f32_16x16x32_bf16 v[20:23],a[144:147],a[132:135],v[28:31]\n"
"s_nop 5\n"
"v_mfma_f32_16x16x32_bf16 v[64:67],a[148:151],a[136:139],v[16:19]\n"
"v_mfma_f32_16x16x32_bf16 v[68:71],a[148:151],a[140:143],v[20:23]\n"
"s_nop 7\n"
"v_cmp_u_f32 vcc,v64,v68\n"
"s_cbranch_vccnz L_fxc16_%=\n"
"L_fyc16_%=:\n"
"s_setpc_b64 s[18:19]\n"
"L_fzc16_0_%=:\n"
"s_call_b64 s[18:19],L_fxc16_%=\n"
"s_branch L_frc16_0_%=\n"
"L_fzc16_1_%=:\n"
"s_call_b64 s[18:19],L_fxc16_%=\n"
"s_branch L_frc16_1_%=\n"
"L_ex_%=:\n"
"s_mov_b32 s79,0x40000\n"
"L_ey_%=:\n"
"buffer_load_dwordx2 v[16:17],v238,s[88:91],s77 offen offset:0 sc1\n"
"buffer_load_dwordx2 v[18:19],v238,s[88:91],s77 offen offset:512 sc1\n"
"buffer_load_dwordx2 v[20:21],v238,s[88:91],s77 offen offset:1024 sc1\n"
"buffer_load_dwordx2 v[22:23],v238,s[88:91],s77 offen offset:1536 sc1\n"
"buffer_load_dwordx2 v[24:25],v238,s[88:91],s77 offen offset:2048 sc1\n"
"buffer_load_dwordx2 v[26:27],v238,s[88:91],s77 offen offset:2560 sc1\n"
"buffer_load_dwordx2 v[28:29],v238,s[88:91],s77 offen offset:3072 sc1\n"
"buffer_load_dwordx2 v[30:31],v238,s[88:91],s77 offen offset:3584 sc1\n"
"buffer_load_dwordx2 v[32:33],v238,s[88:91],s76 offen offset:0 sc1\n"
"buffer_load_dwordx2 v[34:35],v238,s[88:91],s76 offen offset:512 sc1\n"
"buffer_load_dwordx2 v[36:37],v238,s[88:91],s76 offen offset:1024 sc1\n"
"buffer_load_dwordx2 v[38:39],v238,s[88:91],s76 offen offset:1536 sc1\n"
"buffer_load_dwordx2 v[40:41],v238,s[88:91],s76 offen offset:2048 sc1\n"
"buffer_load_dwordx2 v[42:43],v238,s[88:91],s76 offen offset:2560 sc1\n"
"buffer_load_dwordx2 v[44:45],v238,s[88:91],s76 offen offset:3072 sc1\n"
"buffer_load_dwordx2 v[46:47],v238,s[88:91],s76 offen offset:3584 sc1\n"
"s_sub_u32 s79,s79,1\n"
"s_waitcnt vmcnt(0)\n"
"v_min3_u32 v236,v17,v19,s38\n"
"v_max3_u32 v239,v17,v19,s38\n"
"v_min3_u32 v236,v21,v23,v236\n"
"v_max3_u32 v239,v21,v23,v239\n"
"v_min3_u32 v236,v25,v27,v236\n"
"v_max3_u32 v239,v25,v27,v239\n"
"v_min3_u32 v236,v29,v31,v236\n"
"v_max3_u32 v239,v29,v31,v239\n"
"v_min3_u32 v236,v33,v35,v236\n"
"v_max3_u32 v239,v33,v35,v239\n"
"v_min3_u32 v236,v37,v39,v236\n"
"v_max3_u32 v239,v37,v39,v239\n"
"v_min3_u32 v236,v41,v43,v236\n"
"v_max3_u32 v239,v41,v43,v239\n"
"v_min3_u32 v236,v45,v47,v236\n"
"v_max3_u32 v239,v45,v47,v239\n"
"v_cmp_ne_u32 vcc,v236,v239\n"
"s_cbranch_scc1 L_ok_%=\n"
"s_cbranch_vccnz L_ey_%=\n"
"s_branch L_ok_%=\n"
"L_ap5_%=:\n"
"s_mov_b32 s79,0x40000\n"
"L_pl5_%=:\n"
"buffer_load_dword v60,v61,s[88:91],s75 offen sc1\n"
"s_sub_u32 s79,s79,1\n"
"s_waitcnt vmcnt(0)\n"
"v_cmp_ne_u32 vcc,s38,v60\n"
"s_cbranch_scc1 L_pd5_%=\n"
"s_cbranch_vccnz L_pl5_%=\n"
"L_pd5_%=:\n"
"s_branch L_aq5_%=\n"
"L_ap6_%=:\n"
"s_mov_b32 s79,0x40000\n"
"L_pl6_%=:\n"
"buffer_load_dword v60,v61,s[88:91],s75 offen sc1\n"
"s_sub_u32 s79,s79,1\n"
"s_waitcnt vmcnt(0)\n"
"v_cmp_ne_u32 vcc,s38,v60\n"
"s_cbranch_scc1 L_pd6_%=\n"
"s_cbranch_vccnz L_pl6_%=\n"
"L_pd6_%=:\n"
"s_branch L_aq6_%=\n"
"L_fxe4_%=:\n"
"s_cmp_eq_u32 s63,0\n"
"s_cbranch_scc1 L_fye4_%=\n"
"s_sub_u32 s63,s63,1\n"
"s_add_u32 s17,s86,4\n"
"s_sub_u32 s12,s17,18\n"
"s_min_u32 s17,s17,s12\n"
"s_lshl_b32 s17,s17,10\n"
"buffer_load_dwordx4 a[72:75],v9,s[52:55],s17 offen sc1\n"
"s_add_u32 s17,s17,73728\n"
"buffer_load_dwordx4 a[76:79],v9,s[52:55],s17 offen sc1\n"
"s_add_u32 s17,s86,5\n"
"s_sub_u32 s12,s17,18\n"
"s_min_u32 s17,s17,s12\n"
"s_lshl_b32 s17,s17,10\n"
"buffer_load_dwordx4 a[80:83],v9,s[52:55],s17 offen sc1\n"
"s_add_u32 s17,s17,73728\n"
"buffer_load_dwordx4 a[84:87],v9,s[52:55],s17 offen sc1\n"
"s_add_u32 s17,s86,6\n"
"s_sub_u32 s12,s17,18\n"
"s_min_u32 s17,s17,s12\n"
"s_lshl_b32 s17,s17,10\n"
"buffer_load_dwordx4 a[88:91],v9,s[52:55],s17 offen sc1\n"
"s_add_u32 s17,s17,73728\n"
"buffer_load_dwordx4 a[92:95],v9,s[52:55],s17 offen sc1\n"
"s_add_u32 s17,s86,7\n"
"s_sub_u32 s12,s17,18\n"
"s_min_u32 s17,s17,s12\n"
"s_lshl_b32 s17,s17,10\n"
"buffer_load_dwordx4 a[96:99],v9,s[52:55],s17 offen sc1\n"
"s_add_u32 s17,s17,73728\n"
"buffer_load_dwordx4 a[100:103],v9,s[52:55],s17 offen sc1\n"
"s_waitcnt vmcnt(0)\n"
"v_mfma_f32_16x16x32_bf16 v[208:211],a[0:3],a[72:75],v[64:67]\n"
"v_mfma_f32_16x16x32_bf16 v[212:215],a[0:3],a[76:79],v[68:71]\n"
"s_nop 5\n"
"v_mfma_f32_16x16x32_bf16 v[136:139],a[4:7],a[80:83],v[208:211]\n"
"v_mfma_f32_16x16x32_bf16 v[140:143],a[4:7],a[84:87],v[212:215]\n"
"v_mfma_f32_16x16x32_bf16 v[136:139],a[8:11],a[88:91],v[136:139]\n"
"v_mfma_f32_16x16x32_bf16 v[140:143],a[8:11],a[92:95],v[140:143]\n"
"v_mfma_f32_16x16x32_bf16 v[136:139],a[12:15],a[96:99],v[136:139]\n"
"v_mfma_f32_16x16x32_bf16 v[140:143],a[12:15],a[100:103],v[140:143]\n"
"s_nop 7\n"
"v_cmp_u_f32 vcc,v136,v140\n"
"s_cbranch_vccnz L_fxe4_%=\n"
"L_fye4_%=:\n"
"s_setpc_b64 s[18:19]\n"
"L_fze4_0_%=:\n"
"s_call_b64 s[18:19],L_fxe4_%=\n"
"s_branch L_fre4_0_%=\n"
"L_fze4_1_%=:\n"
"s_call_b64 s[18:19],L_fxe4_%=\n"
"s_branch L_fre4_1_%=\n"
"L_fxe8_%=:\n"
"s_cmp_eq_u32 s63,0\n"
"s_cbranch_scc1 L_fye8_%=\n"
"s_sub_u32 s63,s63,1\n"
"s_add_u32 s17,s86,8\n"
"s_sub_u32 s12,s17,18\n"
"s_min_u32 s17,s17,s12\n"
"s_lshl_b32 s17,s17,10\n"
"buffer_load_dwordx4 a[104:107],v9,s[52:55],s17 offen sc1\n"
"s_add_u32 s17,s17,73728\n"
"buffer_load_dwordx4 a[108:111],v9,s[52:55],s17 offen sc1\n"
"s_add_u32 s17,s86,9\n"
"s_sub_u32 s12,s17,18\n"
"s_min_u32 s17,s17,s12\n"
"s_lshl_b32 s17,s17,10\n"
"buffer_load_dwordx4 a[112:115],v9,s[52:55],s17 offen sc1\n"
"s_add_u32 s17,s17,73728\n"
"buffer_load_dwordx4 a[116:119],v9,s[52:55],s17 offen sc1\n"
"s_add_u32 s17,s86,10\n"
"s_sub_u32 s12,s17,18\n"
"s_min_u32 s17,s17,s12\n"
"s_lshl_b32 s17,s17,10\n"
"buffer_load_dwordx4 a[120:123],v9,s[52:55],s17 offen sc1\n"
"s_add_u32 s17,s17,73728\n"
"buffer_load_dwordx4 a[124:127],v9,s[52:55],s17 offen sc1\n"
"s_add_u32 s17,s86,11\n"
"s_sub_u32 s12,s17,18\n"
"s_min_u32 s17,s17,s12\n"
"s_lshl_b32 s17,s17,10\n"
"buffer_load_dwordx4 a[128:131],v9,s[52:55],s17 offen sc1\n"
"s_add_u32 s17,s17,73728\n"
"buffer_load_dwordx4 a[132:135],v9,s[52:55],s17 offen sc1\n"
"s_waitcnt vmcnt(0)\n"
"v_mfma_f32_16x16x32_bf16 v[64:67],a[0:3],a[104:107],v[136:139]\n"
"v_mfma_f32_16x16x32_bf16 v[68:71],a[0:3],a[108:111],v[140:143]\n"
"s_nop 5\n"
"v_mfma_f32_16x16x32_bf16 v[208:211],a[4:7],a[112:115],v[64:67]\n"
"v_mfma_f32_16x16x32_bf16 v[212:215],a[4:7],a[116:119],v[68:71]\n"
"v_mfma_f32_16x16x32_bf16 v[208:211],a[8:11],a[120:123],v[208:211]\n"
"v_mfma_f32_16x16x32_bf16 v[212:215],a[8:11],a[124:127],v[212:215]\n"
"v_mfma_f32_16x16x32_bf16 v[208:211],a[12:15],a[128:131],v[208:211]\n"
"v_mfma_f32_16x16x32_bf16 v[212:215],a[12:15],a[132:135],v[212:215]\n"
"s_nop 7\n"
"v_cmp_u_f32 vcc,v208,v212\n"
"s_cbranch_vccnz L_fxe8_%=\n"
"L_fye8_%=:\n"
"s_setpc_b64 s[18:19]\n"
"L_fze8_0_%=:\n"
"s_call_b64 s[18:19],L_fxe8_%=\n"
"s_branch L_fre8_0_%=\n"
"L_fze8_1_%=:\n"
"s_call_b64 s[18:19],L_fxe8_%=\n"
"s_branch L_fre8_1_%=\n"
"L_fxe12_%=:\n"
"s_cmp_eq_u32 s63,0\n"
"s_cbranch_scc1 L_fye12_%=\n"
"s_sub_u32 s63,s63,1\n"
"s_add_u32 s17,s86,12\n"
"s_sub_u32 s12,s17,18\n"
"s_min_u32 s17,s17,s12\n"
"s_lshl_b32 s17,s17,10\n"
"buffer_load_dwordx4 a[136:139],v9,s[52:55],s17 offen sc1\n"
"s_add_u32 s17,s17,73728\n"
"buffer_load_dwordx4 a[140:143],v9,s[52:55],s17 offen sc1\n"
"s_add_u32 s17,s86,13\n"
"s_sub_u32 s12,s17,18\n"
"s_min_u32 s17,s17,s12\n"
"s_lshl_b32 s17,s17,10\n"
"buffer_load_dwordx4 a[144:147],v9,s[52:55],s17 offen sc1\n"
"s_add_u32 s17,s17,73728\n"
"buffer_load_dwordx4 a[148:151],v9,s[52:55],s17 offen sc1\n"
"s_add_u32 s17,s86,14\n"
"s_sub_u32 s12,s17,18\n"
"s_min_u32 s17,s17,s12\n"
"s_lshl_b32 s17,s17,10\n"
"buffer_load_dwordx4 a[152:155],v9,s[52:55],s17 offen sc1\n"
"s_add_u32 s17,s17,73728\n"
"buffer_load_dwordx4 a[156:159],v9,s[52:55],s17 offen sc1\n"
"s_add_u32 s17,s86,15\n"
"s_sub_u32 s12,s17,18\n"
"s_min_u32 s17,s17,s12\n"
"s_lshl_b32 s17,s17,10\n"
"buffer_load_dwordx4 a[160:163],v9,s[52:55],s17 offen sc1\n"
"s_add_u32 s17,s17,73728\n"
"buffer_load_dwordx4 a[164:167],v9,s[52:55],s17 offen sc1\n"
"s_add_u32 s17,s86,16\n"
"s_sub_u32 s12,s17,18\n"
"s_min_u32 s17,s17,s12\n"
"s_lshl_b32 s17,s17,10\n"
"buffer_load_dwordx4 a[168:171],v9,s[52:55],s17 offen sc1\n"
"s_add_u32 s17,s17,73728\n"
"buffer_load_dwordx4 a[172:175],v9,s[52:55],s17 offen sc1\n"
"s_add_u32 s17,s86,17\n"
"s_sub_u32 s12,s17,18\n"
"s_min_u32 s17,s17,s12\n"
"s_lshl_b32 s17,s17,10\n"
"buffer_load_dwordx4 a[176:179],v9,s[52:55],s17 offen sc1\n"
"s_add_u32 s17,s17,73728\n"
"buffer_load_dwordx4 a[180:183],v9,s[52:55],s17 offen sc1\n"
"s_waitcnt vmcnt(0)\n"
"v_mfma_f32_16x16x32_bf16 v[136:139],a[0:3],a[136:139],v[208:211]\n"
"v_mfma_f32_16x16x32_bf16 v[140:143],a[0:3],a[140:143],v[212:215]\n"
"s_nop 5\n"
"v_mfma_f32_16x16x32_bf16 v[64:67],a[4:7],a[144:147],v[136:139]\n"
"v_mfma_f32_16x16x32_bf16 v[68:71],a[4:7],a[148:151],v[140:143]\n"
"v_mfma_f32_16x16x32_bf16 v[64:67],a[8:11],a[152:155],v[64:67]\n"
"v_mfma_f32_16x16x32_bf16 v[68:71],a[8:11],a[156:159],v[68:71]\n"
"v_mfma_f32_16x16x32_bf16 v[64:67],a[12:15],a[160:163],v[64:67]\n"
"v_mfma_f32_16x16x32_bf16 v[68:71],a[12:15],a[164:167],v[68:71]\n"
"v_mfma_f32_16x16x32_bf16 v[64:67],a[16:19],a[168:171],v[64:67]\n"
"v_mfma_f32_16x16x32_bf16 v[68:71],a[16:19],a[172:175],v[68:71]\n"
"v_mfma_f32_16x16x32_bf16 v[64:67],a[20:23],a[176:179],v[64:67]\n"
"v_mfma_f32_16x16x32_bf16 v[68:71],a[20:23],a[180:183],v[68:71]\n"
"s_nop 7\n"
"v_cmp_u_f32 vcc,v64,v68\n"
"s_cbranch_vccnz L_fxe12_%=\n"
"L_fye12_%=:\n"
"s_setpc_b64 s[18:19]\n"
"L_fze12_0_%=:\n"
"s_call_b64 s[18:19],L_fxe12_%=\n"
"s_branch L_fre12_0_%=\n"
"L_rt7_%=:\n"
"s_cmp_eq_u32 s63,0\n"
"s_cbranch_scc1 L_rb7_%=\n"
"s_sub_u32 s63,s63,1\n"
"s_sleep 0\n"
"s_branch L_pe_%=\n"
"L_rt8_%=:\n"
"s_cmp_eq_u32 s63,0\n"
"s_cbranch_scc1 L_rb8_%=\n"
"s_sub_u32 s63,s63,1\n"
"s_sleep 0\n"
"s_branch L_pe_%=\n"
"k_main_e:\n"
    :: "v"(threadIdx.x), "s"(__builtin_amdgcn_kernarg_segment_ptr()), "s"(blockIdx.x)
    : "memory", "vcc", "scc", "v8", "v9", "v10", "v11", "v12", "v13", "v14", "v15", "v16", "v17", "v18", "v19", "v20", "v21", "v22", "v23", "v24", "v25", "v26", "v27", "v28", "v29", "v30", "v31", "v32", "v33", "v34", "v35", "v36", "v37", "v38", "v39", "v40", "v41", "v42", "v43", "v44", "v45", "v46", "v47", "v48", "v49", "v50", "v51", "v52", "v53", "v54", "v55", "v56", "v57", "v58", "v59", "v60", "v61", "v62", "v63", "v64", "v65", "v66", "v67", "v68", "v69", "v70", "v71", "v72", "v73", "v74", "v75", "v76", "v77", "v78", "v79", "v80", "v81", "v82", "v83", "v84", "v85", "v86", "v87", "v88", "v89", "v90", "v91", "v92", "v93", "v94", "v95", "v96", "v97", "v98", "v99", "v100", "v101", "v102", "v103", "v104", "v105", "v106", "v107", "v108", "v109", "v110", "v111", "v112", "v113", "v114", "v115", "v116", "v117", "v118", "v119", "v120", "v121", "v122", "v123", "v124", "v125", "v126", "v127", "v128", "v129", "v130", "v131", "v132", "v133", "v134", "v135", "v136", "v137", "v138", "v139", "v140", "v141", "v142", "v143", "v144", "v145", "v146", "v147", "v148", "v149", "v150", "v151", "v152", "v153", "v154", "v155", "v156", "v157", "v158", "v159", "v160", "v161", "v162", "v163", "v164", "v165", "v166", "v167", "v168", "v169", "v170", "v171", "v172", "v173", "v174", "v175", "v176", "v177", "v178", "v179", "v180", "v181", "v182", "v183", "v184", "v185", "v186", "v187", "v188", "v189", "v190", "v191", "v192", "v193", "v194", "v195", "v196", "v197", "v198", "v199", "v200", "v201", "v202", "v203", "v204", "v205", "v206", "v207", "v208", "v209", "v210", "v211", "v212", "v213", "v214", "v215", "v216", "v217", "v218", "v219", "v220", "v221", "v222", "v223", "v224", "v225", "v226", "v227", "v228", "v229", "v230", "v231", "v232", "v233", "v234", "v235", "v236", "v237", "v238", "v239", "v240", "v241", "v242", "v243", "v244", "v245", "v246", "v247", "v248", "v249", "v250", "v251", "v252", "v253", "v254", "v255", "a0", "a1", "a2", "a3", "a4", "a5", "a6", "a7", "a8", "a9", "a10", "a11", "a12", "a13", "a14", "a15", "a16", "a17", "a18", "a19", "a20", "a21", "a22", "a23", "a24", "a25", "a26", "a27", "a28", "a29", "a30", "a31", "a32", "a33", "a34", "a35", "a36", "a37", "a38", "a39", "a40", "a41", "a42", "a43", "a44", "a45", "a46", "a47", "a48", "a49", "a50", "a51", "a52", "a53", "a54", "a55", "a56", "a57", "a58", "a59", "a60", "a61", "a62", "a63", "a64", "a65", "a66", "a67", "a68", "a69", "a70", "a71", "a72", "a73", "a74", "a75", "a76", "a77", "a78", "a79", "a80", "a81", "a82", "a83", "a84", "a85", "a86", "a87", "a88", "a89", "a90", "a91", "a92", "a93", "a94", "a95", "a96", "a97", "a98", "a99", "a100", "a101", "a102", "a103", "a104", "a105", "a106", "a107", "a108", "a109", "a110", "a111", "a112", "a113", "a114", "a115", "a116", "a117", "a118", "a119", "a120", "a121", "a122", "a123", "a124", "a125", "a126", "a127", "a128", "a129", "a130", "a131", "a132", "a133", "a134", "a135", "a136", "a137", "a138", "a139", "a140", "a141", "a142", "a143", "a144", "a145", "a146", "a147", "a148", "a149", "a150", "a151", "a152", "a153", "a154", "a155", "a156", "a157", "a158", "a159", "a160", "a161", "a162", "a163", "a164", "a165", "a166", "a167", "a168", "a169", "a170", "a171", "a172", "a173", "a174", "a175", "a176", "a177", "a178", "a179", "a180", "a181", "a182", "a183", "a184", "a185", "a186", "a187", "a188", "a189", "a190", "a191", "a192", "a193", "a194", "a195", "a196", "a197", "a198", "a199", "a200", "a201", "a202", "a203", "a204", "a205", "a206", "a207", "a208", "a209", "a210", "a211", "a212", "a213", "a214", "a215", "a216", "a217", "a218", "a219", "a220", "a221", "a222", "a223", "a224", "a225", "a226", "a227", "a228", "a229", "a230", "a231", "a232", "a233", "a234", "a235", "a236", "a237", "a238", "a239", "a240", "a241", "a242", "a243", "a244", "a245", "a246", "a247", "a248", "a249", "a250", "a251", "a252", "a253", "a254", "a255", "s8", "s9", "s10", "s11", "s12", "s13", "s14", "s15", "s16", "s17", "s18", "s19", "s20", "s21", "s22", "s23", "s24", "s25", "s26", "s27", "s28", "s29", "s30", "s31", "s34", "s35", "s36", "s37", "s38", "s39", "s40", "s41", "s42", "s43", "s44", "s45", "s46", "s47", "s48", "s49", "s50", "s51", "s52", "s53", "s54", "s55", "s56", "s57", "s58", "s59", "s60", "s61", "s62", "s63", "s64", "s65", "s66", "s67", "s68", "s69", "s70", "s71", "s72", "s73", "s74", "s75", "s76", "s77", "s78", "s79", "s80", "s81", "s82", "s83", "s84", "s85", "s86", "s87", "s88", "s89", "s90", "s91", "s92", "s93", "s94", "s95", "s96", "s97", "s98", "s99");
}

extern "C" void kernel_launch(void* const* d_in, const int* in_sizes, int n_in, void* d_out, int out_size, void* d_ws, size_t ws_size, hipStream_t stream) {
  char* ws = (char*)d_ws;
  (void)hipFuncSetAttribute((const void*)k_main, hipFuncAttributeMaxDynamicSharedMemorySize, 163840);
  k_main<<<256, 256, 163840, stream>>>(ws, (const u16*)d_in[0], (const u16*)d_in[3], (const u16*)d_in[1], (const u16*)d_in[2], (const u16*)d_in[4], (const u16*)d_in[6], (const u16*)d_in[7], (const u16*)d_in[8],
                                       (const u16*)d_in[9], (u16*)d_out, (const u16*)d_in[10], (const u16*)d_in[11]);
}
```
